```python
import torch

def _softmax(x, axis=-1):
    return torch.softmax(x, dim=axis)
def _gelu(x):
    return torch.nn.functional.gelu(x, approximate="tanh")
def _silu(x):
    return torch.nn.functional.silu(x)
def _sigmoid(x):
    return torch.sigmoid(x)
def _layernorm(x, axis=-1, eps=1e-5):
    m = x.mean(dim=axis, keepdim=True)
    v = x.var(dim=axis, keepdim=True, unbiased=False)
    return (x - m) / torch.sqrt(v + eps)
def _scatter_add(tgt, idx, src, axis=0):
    return tgt.clone().index_add_(axis, idx, src)
def _segment_sum(src, seg, n):
    out = torch.zeros((n,) + src.shape[1:], dtype=src.dtype)
    return out.index_add_(0, seg, src)
def _pad(x, pw):
    flat = tuple(v for pair in reversed(pw) for v in pair)
    return torch.nn.functional.pad(x, flat)

kernel_name = 'gated_mix__linear_attn_block__compressed_kv_cla__prompt'

def setup_inputs():

    _w = (lambda a: (lambda t: t.to(torch.bfloat16) if t.is_floating_point() else t)(torch.as_tensor(a)))
    def _A_setup():
        B, S, D = 1, 2048, 512
        return {
            "x": _w((lambda *s: torch.randn(*s, dtype=torch.bfloat16) * 0.05)(B, S, D)),
            "wqkv": _w((lambda *s: torch.randn(*s, dtype=torch.bfloat16) * 0.8)(D, 3 * D)),
            "wo": _w((lambda *s: torch.randn(*s, dtype=torch.bfloat16) * 0.4)(D, D)),
        }
    _A_in = _A_setup()

    def _B_setup():
        B, H, Sq, Sk, D, M = 1, 32, 2048, 2048, 128, 512
        Dc = 4 * D
        return {
            "x": _w((lambda *s: torch.randn(*s, dtype=torch.bfloat16) * 0.05)(B, Sq, M)),
            "c_kv": _w((lambda *s: torch.randn(*s, dtype=torch.bfloat16) * 0.8)(B, Sk, Dc)),
            "w_q": _w((lambda *s: torch.randn(*s, dtype=torch.bfloat16) * 6.4)(M, H, D) * (M ** -0.5)),
            "w_uk": _w((lambda *s: torch.randn(*s, dtype=torch.bfloat16) * 6.4)(Dc, H, D) * (Dc ** -0.5)),
            "w_uv": _w((lambda *s: torch.randn(*s, dtype=torch.bfloat16) * 0.8)(Dc, H, D) * (Dc ** -0.5)),
            "w_o": _w((lambda *s: torch.randn(*s, dtype=torch.bfloat16) * 0.8)(H, D, M) * ((H * D) ** -0.5)),
        }
    _B_in = _B_setup()

    return {
        "A_x": _A_in["x"],
        "A_wqkv": _A_in["wqkv"],
        "A_wo": _A_in["wo"],
        "B_x": _B_in["x"],
        "B_c_kv": _B_in["c_kv"],
        "B_w_q": _B_in["w_q"],
        "B_w_uk": _B_in["w_uk"],
        "B_w_uv": _B_in["w_uv"],
        "B_w_o": _B_in["w_o"],
        "gate_w": _w((lambda *s: torch.randn(*s, dtype=torch.bfloat16) * 0.05)(512) * 16.0),
    }

def reference(A_x, A_wqkv, A_wo, B_x, B_c_kv, B_w_q, B_w_uk, B_w_uv, B_w_o, gate_w):
    def _A_ref(x, wqkv, wo):
        B, S, D = x.shape
        H = 32; Dh = D // H
        qkv = (lambda x, *d: x.permute(*d))((x @ wqkv).reshape(B, S, 3, H, Dh), 2, 0, 3, 1, 4)
        q, k, v = qkv[0], qkv[1], qkv[2]
        phi = lambda t: torch.where(t > 0, t + 1, torch.exp(t)) + 1e-6
        pq, pk = phi(q), phi(k)
        kv = torch.einsum("bhsd,bhse->bhde", pk, v)
        z = torch.einsum("bhsd->bhd", pk)
        num = torch.einsum("bhqd,bhde->bhqe", pq, kv)
        den = torch.einsum("bhqd,bhd->bhq", pq, z)[..., None] + 1e-6
        o = num / den
        return (lambda x, *d: x.permute(*d))(o, 0, 2, 1, 3).reshape(B, S, D) @ wo

    def _B_ref(x, c_kv, w_q, w_uk, w_uv, w_o):
        B, Sq, M = x.shape[0], x.shape[1], x.shape[2]
        Sk = c_kv.shape[1]
        H, D = w_q.shape[1], w_q.shape[2]
        q = torch.einsum("bqm,mhd->bhqd", x, w_q)
        k = torch.einsum("bkc,chd->bhkd", c_kv, w_uk)
        v = torch.einsum("bkc,chd->bhkd", c_kv, w_uv)
        scores = torch.einsum("bhqd,bhkd->bhqk", q, k) * (D ** -0.5)
        off = Sk - Sq
        qi = torch.arange(Sq)[:, None]
        kj = torch.arange(Sk)[None, :]
        cmask = kj <= (qi + off)
        scores = torch.where(cmask[None, None], scores, -1e9)
        p = _softmax(scores, axis=-1)
        ctx = torch.einsum("bhqk,bhkd->bhqd", p, v)
        return torch.einsum("bhqd,hdm->bqm", ctx, w_o)

    x = A_x
    g = _sigmoid((x * gate_w).sum(-1, keepdims=True))
    a = _A_ref(x, A_wqkv, A_wo)
    b = _B_ref(x, B_c_kv, B_w_q, B_w_uk, B_w_uv, B_w_o)
    return g * a + (1 - g) * b
```

```cpp
#include <hip/hip_runtime.h>
#include <hip/hip_bf16.h>
#include <stdint.h>

typedef short bf16x8 __attribute__((ext_vector_type(8)));
typedef short bf16x4 __attribute__((ext_vector_type(4)));
typedef float f32x4 __attribute__((ext_vector_type(4)));
typedef unsigned u32x4 __attribute__((ext_vector_type(4)));
typedef unsigned u32x2 __attribute__((ext_vector_type(2)));
typedef unsigned short u16;

constexpr int S = 2048, DM = 512, NH = 32, HD = 128;
constexpr size_t WS_QF = 0;
constexpr size_t WS_KV = WS_QF + (size_t)32 * 128 * 4 * 1024;
constexpr size_t WS_AF = WS_KV + (size_t)32 * 32 * 32768;
constexpr size_t WS_PQ = WS_AF + (size_t)128 * 144 * 1024;
constexpr size_t WS_KVP = WS_PQ + (size_t)16 * 128 * 1024;
constexpr size_t WS_ZP = WS_KVP + (size_t)32 * 32 * 1024;
constexpr size_t WS_G = WS_ZP + (size_t)32 * 32 * 64;
constexpr float QSCALE = 0.08838834764831845f * 1.4426950408889634f;

__device__ __forceinline__ unsigned cvtpk(float lo, float hi) {
  typedef float f2 __attribute__((ext_vector_type(2)));
  typedef __bf16 b2 __attribute__((ext_vector_type(2)));
  f2 v = {lo, hi}; b2 b = __builtin_convertvector(v, b2); return __builtin_bit_cast(unsigned, b);
}
__device__ __forceinline__ bf16x8 pack8(f32x4 a, f32x4 b) {
  u32x4 w = {cvtpk(a[0], a[1]), cvtpk(a[2], a[3]), cvtpk(b[0], b[1]), cvtpk(b[2], b[3])};
  return __builtin_bit_cast(bf16x8, w);
}
__device__ __forceinline__ float bf2f(u16 v) { return __uint_as_float((unsigned)v << 16); }
__device__ __forceinline__ float phi(float t) { return (t > 0.f ? t + 1.f : __expf(t)) + 1e-6f; }
#define MFMA16(a, b, c) __builtin_amdgcn_mfma_f32_16x16x32_bf16(a, b, c, 0, 0, 0)

__global__ __launch_bounds__(64) void k1_simple(const u16* x, const u16* wqkv, const u16* c, const u16* wq, const u16* wuk, const u16* wuv,
                                                 const u16* gw, char* ws) {
  const int lane = threadIdx.x, r = lane & 15, g = lane >> 4;
  const int rnd = blockIdx.x % 432, mt = blockIdx.x / 432;
  const u16* A; const u16* W; int ldw, col0, col1; int type;
  if (rnd < 128) { type = 0; A = x; W = wq; ldw = 4096; col0 = rnd * 32; col1 = col0 + 16; }
  else if (rnd < 256) { type = 1; A = c; W = wuk; ldw = 4096; col0 = (rnd - 128) * 32; col1 = col0 + 16; }
  else if (rnd < 384) { type = 2; A = c; W = wuv; ldw = 4096; col0 = (rnd - 256) * 32; col1 = col0 + 16; }
  else if (rnd < 400) { type = 3; A = x; W = wqkv; ldw = 1536; col0 = (rnd - 384) * 32; col1 = col0 + 16; }
  else { type = 4; A = x; W = wqkv; ldw = 1536; col0 = 512 + (rnd - 400) * 16; col1 = 1024 + (rnd - 400) * 16; }
  f32x4 acc[2][4] = {};
  const bool ns = (type == 0 || type == 1 || type == 3);
  for (int ks = 0; ks < 16; ++ks) {
    bf16x8 w0, w1;
    for (int j = 0; j < 8; ++j) { w0[j] = W[(size_t)(32 * ks + 8 * g + j) * ldw + col0 + r]; w1[j] = W[(size_t)(32 * ks + 8 * g + j) * ldw + col1 + r]; }
#pragma unroll
    for (int rb = 0; rb < 4; ++rb) {
      bf16x8 a = *(const bf16x8*)(A + (size_t)(mt * 64 + rb * 16 + r) * 512 + 32 * ks + 8 * g);
      if (ns) { acc[0][rb] = MFMA16(w0, a, acc[0][rb]); acc[1][rb] = MFMA16(w1, a, acc[1][rb]); }
      else { acc[0][rb] = MFMA16(a, w0, acc[0][rb]); acc[1][rb] = MFMA16(a, w1, acc[1][rb]); }
    }
  }
  if (type == 0) {
    const int h = rnd >> 2, ds = rnd & 3;
#pragma unroll
    for (int rb = 0; rb < 4; ++rb)
      *(bf16x8*)(ws + WS_QF + ((size_t)(h * 128 + mt * 4 + rb) * 4 + ds) * 1024 + lane * 16) = pack8(acc[0][rb] * QSCALE, acc[1][rb] * QSCALE);
  } else if (type == 1) {
    const int h = (rnd - 128) >> 2, ds = rnd & 3;
#pragma unroll
    for (int rb = 0; rb < 4; ++rb)
      *(bf16x8*)(ws + WS_KV + (size_t)(h * 32 + mt) * 32768 + (rb * 4 + ds) * 1024 + lane * 16) = pack8(acc[0][rb], acc[1][rb]);
  } else if (type == 2) {
    const int h = (rnd - 256) >> 2, dp = rnd & 3;
#pragma unroll
    for (int cb = 0; cb < 2; ++cb)
#pragma unroll
      for (int kp = 0; kp < 2; ++kp)
        *(bf16x8*)(ws + WS_KV + (size_t)(h * 32 + mt) * 32768 + 16384 + kp * 8192 + (dp * 2 + cb) * 1024 + lane * 16) = pack8(acc[cb][2 * kp], acc[cb][2 * kp + 1]);
  } else if (type == 3) {
    const int pr = rnd - 384;
#pragma unroll
    for (int rb = 0; rb < 4; ++rb) {
      f32x4 a, b;
      for (int e = 0; e < 4; ++e) { a[e] = phi(acc[0][rb][e]); b[e] = phi(acc[1][rb][e]); }
      *(bf16x8*)(ws + WS_PQ + (size_t)(pr * 128 + mt * 4 + rb) * 1024 + lane * 16) = pack8(a, b);
    }
  } else {
    const int h = rnd - 400;
    f32x4 kv = {}, z = {};
    const short one = 0x3f80; bf16x8 ones = {one, one, one, one, one, one, one, one};
#pragma unroll
    for (int kp = 0; kp < 2; ++kp) {
      f32x4 a, b;
      for (int e = 0; e < 4; ++e) { a[e] = phi(acc[0][2 * kp][e]); b[e] = phi(acc[0][2 * kp + 1][e]); }
      bf16x8 pk = pack8(a, b), v = pack8(acc[1][2 * kp], acc[1][2 * kp + 1]);
      kv = MFMA16(pk, v, kv); z = MFMA16(pk, ones, z);
    }
    *(f32x4*)(ws + WS_KVP + (size_t)(mt * 32 + h) * 1024 + lane * 16) = kv;
    if (r == 0) *(f32x4*)(ws + WS_ZP + (size_t)(mt * 32 + h) * 64 + g * 16) = z;
    if (h == 0) {
      const u16* xr = x + (size_t)(mt * 64 + lane) * 512; float s = 0.f;
      for (int k = 0; k < 512; ++k) s += bf2f(xr[k]) * bf2f(gw[k]);
      ((float*)(ws + WS_G))[mt * 64 + lane] = 1.f / (1.f + __expf(-s));
    }
  }
}

__device__ __noinline__ void attn_rows_exact(char* ws, int h, int rb) {
  const int lane = threadIdx.x & 63, r = lane & 15, g = lane >> 4;
  bf16x8 q[4];
#pragma unroll
  for (int ds = 0; ds < 4; ++ds) q[ds] = *(const bf16x8*)(ws + WS_QF + ((size_t)(h * 128 + rb) * 4 + ds) * 1024 + lane * 16);
  f32x4 o[8] = {}; float m = -1e30f, l = 0.f;
  const int qpos = rb * 16 + r, nu = (rb >> 1) + 1;
  for (int u = 0; u < nu; ++u) {
    const char* kt = ws + WS_KV + (size_t)(h * 32 + (u >> 1)) * 32768;
    f32x4 s[2] = {};
#pragma unroll
    for (int kb = 0; kb < 2; ++kb)
#pragma unroll
      for (int ds = 0; ds < 4; ++ds) s[kb] = MFMA16(*(const bf16x8*)(kt + (((u & 1) * 2 + kb) * 4 + ds) * 1024 + lane * 16), q[ds], s[kb]);
    float tm = -1e30f;
#pragma unroll
    for (int kb = 0; kb < 2; ++kb)
#pragma unroll
      for (int e = 0; e < 4; ++e) { if (u * 32 + kb * 16 + 4 * g + e > qpos) s[kb][e] = -__builtin_inff(); tm = fmaxf(tm, s[kb][e]); }
    tm = fmaxf(tm, __shfl_xor(tm, 16)); tm = fmaxf(tm, __shfl_xor(tm, 32));
    const float mn = fmaxf(m, tm), al = __builtin_amdgcn_exp2f(m - mn); m = mn;
    float ps = 0.f;
#pragma unroll
    for (int kb = 0; kb < 2; ++kb)
#pragma unroll
      for (int e = 0; e < 4; ++e) { s[kb][e] = __builtin_amdgcn_exp2f(s[kb][e] - mn); ps += s[kb][e]; }
    l = l * al + ps;
    const bf16x8 p = pack8(s[0], s[1]);
#pragma unroll
    for (int db = 0; db < 8; ++db) o[db] = MFMA16(*(const bf16x8*)(kt + 16384 + (u & 1) * 8192 + db * 1024 + lane * 16), p, o[db] * al);
  }
  l += __shfl_xor(l, 16); l += __shfl_xor(l, 32);
  const float f = (1.f - ((const float*)(ws + WS_G))[qpos]) / l;
#pragma unroll
  for (int p = 0; p < 4; ++p)
    *(bf16x8*)(ws + WS_AF + ((size_t)rb * 144 + h * 4 + p) * 1024 + lane * 16) = pack8(o[2 * p] * f, o[2 * p + 1] * f);
}
__global__ __launch_bounds__(64) void k2_simple(char* ws) { attn_rows_exact(ws, blockIdx.x & 31, 127 - (blockIdx.x >> 5)); }

__global__ __launch_bounds__(64) void k2_lin_simple(char* ws, int nparts) {
  const int lane = threadIdx.x, r = lane & 15, g = lane >> 4;
  const int pr = blockIdx.x & 15, rb = blockIdx.x >> 4;
  const bf16x8 pq = *(const bf16x8*)(ws + WS_PQ + (size_t)(pr * 128 + rb) * 1024 + lane * 16);
  const float gt = ((const float*)(ws + WS_G))[rb * 16 + r];
  f32x4 o[2];
#pragma unroll
  for (int hh = 0; hh < 2; ++hh) {
    f32x4 kv = {}, z = {};
    for (int p = 0; p < nparts; ++p) {
      kv += *(const f32x4*)(ws + WS_KVP + (size_t)(p * 32 + pr * 2 + hh) * 1024 + lane * 16);
      z += *(const f32x4*)(ws + WS_ZP + (size_t)(p * 32 + pr * 2 + hh) * 64 + g * 16);
    }
    const f32x4 zero = {};
    const bf16x8 akv = hh ? pack8(zero, kv) : pack8(kv, zero), az = hh ? pack8(zero, z) : pack8(z, zero);
    const f32x4 num = MFMA16(akv, pq, zero), den = MFMA16(az, pq, zero);
    o[hh] = num * (gt / (den[0] + 1e-6f));
  }
  *(bf16x8*)(ws + WS_AF + ((size_t)rb * 144 + 128 + pr) * 1024 + lane * 16) = pack8(o[0], o[1]);
}

__global__ __launch_bounds__(64) void k3_simple(const char* ws, const u16* wo_b, const u16* wo_a, u16* out) {
  const int lane = threadIdx.x, r = lane & 15, g = lane >> 4;
  const int nb = blockIdx.x & 15, rb = blockIdx.x >> 4;
  f32x4 acc[2] = {};
  for (int ks = 0; ks < 144; ++ks) {
    const u16* W = ks < 128 ? wo_b + (size_t)ks * 32 * 512 : wo_a + (size_t)(ks - 128) * 32 * 512;
    bf16x8 w0, w1;
    const int n0 = nb * 32 + 8 * (r >> 2) + (r & 3);
    for (int j = 0; j < 8; ++j) { const int k = 16 * (j >> 2) + 4 * g + (j & 3); w0[j] = W[k * 512 + n0]; w1[j] = W[k * 512 + n0 + 4]; }
    const bf16x8 a = *(const bf16x8*)(ws + WS_AF + ((size_t)rb * 144 + ks) * 1024 + lane * 16);
    acc[0] = MFMA16(w0, a, acc[0]); acc[1] = MFMA16(w1, a, acc[1]);
  }
  *(bf16x8*)(out + (size_t)(rb * 16 + r) * 512 + nb * 32 + 8 * g) = pack8(acc[0], acc[1]);
}

extern "C" void kernel_launch(void* const* d_in, const int* in_sizes, int n_in, void* d_out, int out_size, void* d_ws, size_t ws_size,
                              hipStream_t stream) {
  const u16* x = (const u16*)d_in[0]; const u16* wqkv = (const u16*)d_in[1]; const u16* wo_a = (const u16*)d_in[2];
  const u16* c = (const u16*)d_in[4]; const u16* wq = (const u16*)d_in[5]; const u16* wuk = (const u16*)d_in[6];
  const u16* wuv = (const u16*)d_in[7]; const u16* wo_b = (const u16*)d_in[8]; const u16* gw = (const u16*)d_in[9];
  char* ws = (char*)d_ws;
  k1_simple<<<432 * 32, 64, 0, stream>>>(x, wqkv, c, wq, wuk, wuv, gw, ws);
  k2_simple<<<32 * 128, 64, 0, stream>>>(ws);
  k2_lin_simple<<<16 * 128, 64, 0, stream>>>(ws, 32);
  k3_simple<<<16 * 128, 64, 0, stream>>>(ws, wo_b, wo_a, (u16*)d_out);
}
```

```cpp
#include <hip/hip_runtime.h>
#include <hip/hip_bf16.h>

typedef short bf16x8 __attribute__((ext_vector_type(8)));
typedef float f32x4 __attribute__((ext_vector_type(4)));
typedef unsigned u32x4 __attribute__((ext_vector_type(4)));
typedef unsigned short u16;
typedef const __attribute__((address_space(1))) u16* gu16;

constexpr size_t WS_KV = 0;
constexpr size_t WS_WF = 37748736;
constexpr size_t WS_XP = 42467328;
constexpr size_t WS_AF = 50855936;
constexpr size_t WS_KVP = 69730304;
constexpr size_t WS_ZP = 69992448;
constexpr size_t WS_G = 70025216;
constexpr size_t WS_F1 = 70033408;
constexpr size_t WS_EP = 70035456;
constexpr size_t WS_FE = 70037504;
constexpr size_t WS_FL = 70045696;
constexpr size_t WS_F4 = 70053888;
constexpr size_t WS_END = 70217728;

__device__ __forceinline__ unsigned cvtpk(float lo, float hi) {
  typedef float f2 __attribute__((ext_vector_type(2)));
  typedef __bf16 b2 __attribute__((ext_vector_type(2)));
  f2 v = {lo, hi}; b2 b = __builtin_convertvector(v, b2); return __builtin_bit_cast(unsigned, b);
}
__device__ __forceinline__ bf16x8 pack8(f32x4 a, f32x4 b) {
  u32x4 w = {cvtpk(a[0], a[1]), cvtpk(a[2], a[3]), cvtpk(b[0], b[1]), cvtpk(b[2], b[3])};
  return __builtin_bit_cast(bf16x8, w);
}
__device__ __forceinline__ void st16(char* ws, size_t off, bf16x8 v) {
  __builtin_amdgcn_raw_buffer_store_b128(__builtin_bit_cast(u32x4, v), __builtin_amdgcn_make_buffer_rsrc((void*)ws, (short)0, (int)WS_END, 0x20000), (unsigned)off, 0, 16);
}
__device__ __forceinline__ bf16x8 ld16(const char* ws, size_t off) {
  return __builtin_bit_cast(bf16x8, __builtin_amdgcn_raw_buffer_load_b128(__builtin_amdgcn_make_buffer_rsrc((void*)ws, (short)0, (int)WS_END, 0x20000), (unsigned)off, 0, 16));
}
__device__ __forceinline__ float ld4(const char* ws, size_t off) {
  return __uint_as_float(__builtin_amdgcn_raw_buffer_load_b32(__builtin_amdgcn_make_buffer_rsrc((void*)ws, (short)0, (int)WS_END, 0x20000), (unsigned)off, 0, 16));
}
#define MFMA16(a, b, c) __builtin_amdgcn_mfma_f32_16x16x32_bf16(a, b, c, 0, 0, 0)

__device__ __forceinline__ void attn_rows_exact(gu16 x, gu16 wq, char* ws, int h, int rb) {
  const int lane = __builtin_amdgcn_mbcnt_hi(~0u, __builtin_amdgcn_mbcnt_lo(~0u, 0u)), r = lane & 15, g = lane >> 4;
  bf16x8 q[4];
#pragma unroll
  for (int ds = 0; ds < 4; ++ds) {
    f32x4 a0 = {}, a1 = {};
    for (int ks = 0; ks < 16; ++ks) {
      bf16x8 w0, w1;
#pragma unroll
      for (int j = 0; j < 8; ++j) { gu16 wp = wq + (size_t)(32 * ks + 8 * g + j) * 4096 + h * 128 + ds * 32 + r; w0[j] = wp[0]; w1[j] = wp[16]; }
      const bf16x8 xv = *(const __attribute__((address_space(1))) bf16x8*)(x + (size_t)(rb * 16 + r) * 512 + 32 * ks + 8 * g);
      a0 = MFMA16(w0, xv, a0); a1 = MFMA16(w1, xv, a1);
    }
    q[ds] = pack8(a0, a1);
  }
  f32x4 o[8] = {}; float m = -1e30f, l = 0.f;
  const int qpos = rb * 16 + r, nu = (rb >> 1) + 1;
  for (int u = 0; u < nu; ++u) {
    const size_t kt = WS_KV + (size_t)(h * 32 + (u >> 1)) * 32768;
    f32x4 s[2] = {};
#pragma unroll
    for (int kb = 0; kb < 2; ++kb)
#pragma unroll
      for (int ds = 0; ds < 4; ++ds) s[kb] = MFMA16(ld16(ws, kt + (((u & 1) * 2 + kb) * 4 + ds) * 1024 + lane * 16), q[ds], s[kb]);
    float tm = -1e30f;
#pragma unroll
    for (int kb = 0; kb < 2; ++kb)
#pragma unroll
      for (int e = 0; e < 4; ++e) { if (u * 32 + kb * 16 + 4 * g + e > qpos) s[kb][e] = -__builtin_inff(); tm = fmaxf(tm, s[kb][e]); }
    tm = fmaxf(tm, __shfl_xor(tm, 16)); tm = fmaxf(tm, __shfl_xor(tm, 32));
    const float mn = fmaxf(m, tm), al = __builtin_amdgcn_exp2f(m - mn); m = mn;
    float ps = 0.f;
#pragma unroll
    for (int kb = 0; kb < 2; ++kb)
#pragma unroll
      for (int e = 0; e < 4; ++e) { s[kb][e] = __builtin_amdgcn_exp2f(s[kb][e] - mn); ps += s[kb][e]; }
    l = l * al + ps;
    const bf16x8 p = pack8(s[0], s[1]);
#pragma unroll
    for (int db = 0; db < 8; ++db) o[db] = MFMA16(ld16(ws, kt + 16384 + (u & 1) * 8192 + db * 1024 + lane * 16), p, o[db] * al);
  }
  l += __shfl_xor(l, 16); l += __shfl_xor(l, 32);
  const float f = (1.f - ld4(ws, WS_G + qpos * 4)) / l;
#pragma unroll
  for (int p = 0; p < 4; ++p) st16(ws, WS_AF + ((size_t)rb * 144 + h * 4 + p) * 1024 + lane * 16, pack8(o[2 * p] * f, o[2 * p + 1] * f));
}

__global__ __launch_bounds__(256, 1) void gated_mix_kernel(const u16* x_, const u16* wqkv_, const u16* wo_a_, const u16* c_, const u16* wq_, const u16* wuk_,
                                                           const u16* wuv_, const u16* wo_b_, const u16* gw_, char* ws_, u16* out_) {
  __shared__ __attribute__((aligned(1024))) char lds[163840];
  asm volatile("" :: "s"((unsigned)(size_t)lds));
  auto ka = __builtin_amdgcn_kernarg_segment_ptr();
  unsigned st;
  asm volatile(
"s_mov_b32 s4,%[bid_]\ns_mov_b32 s5,%[wid_]\ns_load_dwordx16 s[76:91],%[ka],0x0\ns_load_dwordx4 s[24:27],%[ka],0x40\ns_load_dwordx2 s[28:29],%[ka],0x50\ns_and_b32 s67,s4,7\ns_lshr_b32 s68,s4,3\ns_lshl_b32 s11,s68,2\n"
"s_add_u32 s11,s11,s5\ns_getpc_b64 s[36:37]\n.Lgm_code_start_pc:\ns_add_u32 s36,s36,.Lgm_code_start-.Lgm_code_start_pc\ns_addc_u32 s37,s37,0\ns_and_b32 s37,s37,0xffff\ns_mov_b32 s38,.Lgm_code_end-.Lgm_code_start\n"
"s_mov_b32 s39,0x20000\ns_lshl_b32 s71,s11,10\nv_mbcnt_lo_u32_b32 v161,-1,0\nv_mbcnt_hi_u32_b32 v161,-1,v161\nv_lshlrev_b32 v161,4,v161\nbuffer_load_dword v160,v161,s[36:39],s71 offen\n.Lgm_code_start:\ns_cmp_gt_u32 s67,5\n"
"s_subb_u32 s44,s67,0\ns_cmp_gt_u32 s67,7\ns_subb_u32 s44,s44,0\ns_and_b32 s10,s44,1\ns_lshl_b32 s10,s10,2\ns_and_b32 s45,s68,3\ns_or_b32 s10,s10,s45\ns_lshr_b32 s44,s44,1\ns_lshl_b32 s44,s44,3\ns_lshr_b32 s45,s68,2\n"
"s_add_u32 s9,s44,s45\ns_cmp_ge_u32 s9,12\ns_addc_u32 s9,s9,0\ns_lshr_b32 s44,s68,3\ns_and_b32 s68,s68,7\ns_add_u32 s45,s44,28\ns_cmp_eq_u32 s67,7\ns_cselect_b32 s9,s45,s9\ns_cselect_b32 s10,s68,s10\ns_add_u32 s45,s44,24\n"
"s_cmp_eq_u32 s44,0\ns_cselect_b32 s45,12,s45\ns_cmp_eq_u32 s67,5\ns_cselect_b32 s9,s45,s9\ns_cselect_b32 s10,s68,s10\ns_add_u32 s75,s9,0\ns_and_b32 s75,s75,1\ns_lshl_b32 s44,s9,9\ns_sub_u32 s44,s44,13312\ns_lshl_b32 s45,s9,8\n"
"s_sub_u32 s45,s45,6144\ns_cmp_ge_u32 s9,28\ns_cselect_b32 s63,s45,s44\ns_cselect_b32 s66,64,0x80\ns_cselect_b32 s73,960,0\ns_sub_u32 s44,s9,13\ns_min_u32 s44,s44,s9\ns_mul_i32 s44,s44,640\ns_movk_i32 s8,0x2000\n"
"s_cmp_lt_u32 s9,26\ns_cselect_b32 s63,s44,s63\ns_cselect_b32 s8,s8,0xc00\ns_lshl_b32 s38,s8,9\ns_mov_b32 s39,0x20000\ns_mov_b32 s42,0x10000\ns_mov_b32 s43,0x20000\ns_lshl_b32 s61,s5,12\ns_add_u32 s62,s61,0x10000\n"
"s_add_u32 s71,s75,0\ns_and_b32 s71,s71,3\ns_lshl_b32 s52,s71,8\ns_lshl_b32 s71,s71,7\ns_mul_i32 s48,s71,s8\ns_mov_b32 s56,0\ns_mov_b32 s57,16384\ns_mov_b32 s58,32768\ns_mov_b32 s59,49152\nv_mbcnt_lo_u32_b32 v244,-1,0\n"
"v_mbcnt_hi_u32_b32 v244,-1,v244\nv_lshrrev_b32 v245,3,v244\ns_lshl_b32 s71,s5,5\nv_add_u32 v245,s71,v245\nv_mul_lo_u32 v245,v245,s8\nv_lshrrev_b32 v246,3,v244\nv_and_b32 v246,2,v246\nv_and_b32 v247,7,v244\n"
"v_xor_b32 v246,v246,v247\nv_lshrrev_b32 v247,2,v246\nv_mul_lo_u32 v247,v247,s73\nv_lshl_add_u32 v247,v246,4,v247\nv_add_u32 v228,v245,v247\nv_xor_b32 v246,4,v246\nv_lshrrev_b32 v247,2,v246\nv_mul_lo_u32 v247,v247,s73\n"
"v_lshl_add_u32 v247,v246,4,v247\nv_add_u32 v229,v245,v247\ns_lshl_b32 s71,s8,3\ns_sub_u32 s71,s71,1024\nv_add_u32 v229,s71,v229\ns_lshl_b32 s71,s71,1\nv_add_u32 v230,s71,v228\nv_add_u32 v231,s71,v229\nv_lshrrev_b32 v245,4,v244\n"
"v_and_b32 v246,15,v244\nv_xor_b32 v246,v246,v245\nv_lshlrev_b32 v246,4,v246\nv_lshl_add_u32 v232,v245,10,v246\nv_xor_b32 v233,64,v232\nv_add_u32 v233,3072,v233\nv_xor_b32 v234,128,v232\nv_add_u32 v234,6144,v234\n"
"v_xor_b32 v235,192,v232\nv_add_u32 v235,9216,v235\ns_lshl_b32 s44,s10,2\ns_add_u32 s44,s44,s5\ns_lshl_b32 s44,s44,16\ns_waitcnt lgkmcnt(0)\ns_cmp_lt_u32 s9,13\ns_cselect_b64 s[36:37],s[86:87],s[88:89]\ns_cmp_lt_u32 s9,26\n"
"s_cselect_b64 s[36:37],s[36:37],s[78:79]\ns_cselect_b64 s[40:41],s[82:83],s[76:77]\ns_and_b32 s37,s37,0xffff\ns_add_u32 s40,s40,s44\ns_addc_u32 s41,s41,0\ns_and_b32 s41,s41,0xffff\ns_add_u32 s60,s48,s63\ns_add_u32 m0,s61,0\n"
"s_nop 0\nbuffer_load_dwordx4 v228,s[36:39],s60 offen offset:0 lds\nbuffer_load_dwordx4 v229,s[36:39],s60 offen offset:1024 lds\nbuffer_load_dwordx4 v230,s[36:39],s60 offen offset:2048 lds\n"
"buffer_load_dwordx4 v231,s[36:39],s60 offen offset:3072 lds\ns_add_u32 s60,s52,s56\ns_add_u32 m0,s62,0\ns_nop 0\nbuffer_load_dwordx4 v232,s[40:43],s60 offen offset:0 lds\n"
"buffer_load_dwordx4 v233,s[40:43],s60 offen offset:1024 lds\nbuffer_load_dwordx4 v234,s[40:43],s60 offen offset:2048 lds\nbuffer_load_dwordx4 v235,s[40:43],s60 offen offset:3072 lds\ns_add_u32 s60,s52,s57\ns_add_u32 m0,s62,16384\n"
"s_nop 0\nbuffer_load_dwordx4 v232,s[40:43],s60 offen offset:0 lds\nbuffer_load_dwordx4 v233,s[40:43],s60 offen offset:1024 lds\nbuffer_load_dwordx4 v234,s[40:43],s60 offen offset:2048 lds\n"
"buffer_load_dwordx4 v235,s[40:43],s60 offen offset:3072 lds\nv_and_b32 v245,15,v244\nv_lshl_add_u32 v245,v245,8,v246\nv_add_u32 v236,s62,v245\nv_xor_b32 v237,64,v236\nv_xor_b32 v238,128,v236\nv_xor_b32 v239,192,v236\n"
"v_add_u32 v188,0x10000,v236\nv_add_u32 v189,0x10000,v237\nv_add_u32 v190,0x10000,v238\nv_add_u32 v191,0x10000,v239\nv_lshrrev_b32 v245,4,v244\nv_lshlrev_b32 v245,10,v245\nv_bfe_u32 v246,v244,2,2\nv_lshl_add_u32 v245,v246,7,v245\n"
"v_and_b32 v246,3,v244\nv_lshl_add_u32 v245,v246,3,v245\nv_bfe_u32 v246,v244,3,2\nv_lshl_add_u32 v224,v246,5,v245\nv_xor_b32 v225,32,v224\nv_xor_b32 v226,64,v224\nv_xor_b32 v227,96,v224\ns_add_u32 s64,s63,s66\n"
"s_add_u32 s65,s64,s66\ns_add_u32 s71,s75,1\ns_and_b32 s71,s71,3\ns_lshl_b32 s53,s71,8\ns_lshl_b32 s71,s71,7\ns_mul_i32 s49,s71,s8\ns_add_u32 s71,s75,2\ns_and_b32 s71,s71,3\ns_lshl_b32 s54,s71,8\ns_lshl_b32 s71,s71,7\n"
"s_mul_i32 s50,s71,s8\ns_add_u32 s71,s75,3\ns_and_b32 s71,s71,3\ns_lshl_b32 s55,s71,8\ns_lshl_b32 s71,s71,7\ns_mul_i32 s51,s71,s8\ns_mov_b64 s[44:45],s[26:27]\ns_and_b32 s45,s45,0xffff\ns_mov_b32 s46,50855936\n"
"s_mov_b32 s47,0x20000\ns_mov_b64 s[30:31],s[76:77]\ns_mov_b64 s[34:35],s[84:85]\nv_lshlrev_b32 v240,4,v244\nv_add_u32 v241,4096,v240\nv_add_u32 v242,8192,v240\nv_add_u32 v243,12288,v240\ns_sub_u32 s13,s9,13\ns_cmp_lt_u32 s13,13\n"
"s_cselect_b32 s13,1,0\ns_cmp_ge_u32 s9,28\ns_cselect_b32 s13,2,s13\ns_add_u32 s60,s52,s58\ns_add_u32 m0,s62,32768\ns_nop 0\nbuffer_load_dwordx4 v232,s[40:43],s60 offen offset:0 lds\n"
"buffer_load_dwordx4 v233,s[40:43],s60 offen offset:1024 lds\nbuffer_load_dwordx4 v234,s[40:43],s60 offen offset:2048 lds\nbuffer_load_dwordx4 v235,s[40:43],s60 offen offset:3072 lds\ns_add_u32 s60,s52,s59\ns_add_u32 m0,s62,65536\n"
"s_nop 0\nbuffer_load_dwordx4 v232,s[40:43],s60 offen offset:0 lds\nbuffer_load_dwordx4 v233,s[40:43],s60 offen offset:1024 lds\nbuffer_load_dwordx4 v234,s[40:43],s60 offen offset:2048 lds\n"
"buffer_load_dwordx4 v235,s[40:43],s60 offen offset:3072 lds\ns_add_u32 s60,s48,s64\ns_add_u32 m0,s61,16384\ns_nop 0\nbuffer_load_dwordx4 v228,s[36:39],s60 offen offset:0 lds\n"
"buffer_load_dwordx4 v229,s[36:39],s60 offen offset:1024 lds\nbuffer_load_dwordx4 v230,s[36:39],s60 offen offset:2048 lds\nbuffer_load_dwordx4 v231,s[36:39],s60 offen offset:3072 lds\ns_add_u32 s60,s53,s56\ns_add_u32 m0,s62,81920\n"
"s_nop 0\nbuffer_load_dwordx4 v232,s[40:43],s60 offen offset:0 lds\nbuffer_load_dwordx4 v233,s[40:43],s60 offen offset:1024 lds\nbuffer_load_dwordx4 v234,s[40:43],s60 offen offset:2048 lds\n"
"buffer_load_dwordx4 v235,s[40:43],s60 offen offset:3072 lds\ns_cmp_eq_u32 s13,1\ns_cbranch_scc1 LS_%=\ns_cmp_eq_u32 s13,2\ns_cbranch_scc1 LK_%=\ns_waitcnt vmcnt(24)\ns_barrier\nds_read_b64_tr_b16 v[128:129],v224 offset:0\n"
"ds_read_b64_tr_b16 v[130:131],v224 offset:512\nds_read_b64_tr_b16 v[132:133],v224 offset:4096\nds_read_b64_tr_b16 v[134:135],v224 offset:4608\nds_read_b64_tr_b16 v[136:137],v224 offset:8192\n"
"ds_read_b64_tr_b16 v[138:139],v224 offset:8704\nds_read_b64_tr_b16 v[140:141],v224 offset:12288\nds_read_b64_tr_b16 v[142:143],v224 offset:12800\ns_waitcnt vmcnt(20)\nds_read_b128 a[0:3],v236 offset:0\n"
"ds_read_b128 a[4:7],v237 offset:0\nds_read_b128 a[8:11],v238 offset:0\nds_read_b128 a[12:15],v239 offset:0\ns_waitcnt vmcnt(16)\nds_read_b128 a[16:19],v236 offset:16384\nds_read_b128 a[20:23],v237 offset:16384\n"
"ds_read_b128 a[24:27],v238 offset:16384\nds_read_b128 a[28:31],v239 offset:16384\ns_waitcnt vmcnt(12)\nds_read_b128 a[32:35],v236 offset:32768\nds_read_b128 a[36:39],v237 offset:32768\nds_read_b128 a[40:43],v238 offset:32768\n"
"ds_read_b128 a[44:47],v239 offset:32768\ns_waitcnt vmcnt(8)\nds_read_b128 a[48:51],v188 offset:0\nds_read_b128 a[52:55],v189 offset:0\nds_read_b128 a[56:59],v190 offset:0\nds_read_b128 a[60:63],v191 offset:0\n"
"s_waitcnt lgkmcnt(0)\nv_mfma_f32_16x16x32_bf16 v[0:3],v[128:131],a[0:3],0\nds_read_b64_tr_b16 v[144:145],v225 offset:0\ns_and_b32 s71,s11,15\nv_mfma_f32_16x16x32_bf16 v[0:3],v[132:135],a[4:7],v[0:3]\n"
"ds_read_b64_tr_b16 v[146:147],v225 offset:512\ns_lshl_b32 s71,s71,5\nv_mfma_f32_16x16x32_bf16 v[0:3],v[136:139],a[8:11],v[0:3]\nds_read_b64_tr_b16 v[148:149],v225 offset:4096\ns_lshl_b32 s72,s8,4\n"
"v_mfma_f32_16x16x32_bf16 v[0:3],v[140:143],a[12:15],v[0:3]\ns_add_u32 s60,s53,s57\ns_add_u32 m0,s62,49152\nv_lshrrev_b32 v245,2,v244\nv_mfma_f32_16x16x32_bf16 v[4:7],v[140:143],a[28:31],0\n"
"ds_read_b64_tr_b16 v[150:151],v225 offset:4608\nv_add_u32 v245,s71,v245\nv_mfma_f32_16x16x32_bf16 v[4:7],v[136:139],a[24:27],v[4:7]\nds_read_b64_tr_b16 v[152:153],v225 offset:8192\nv_mul_lo_u32 v245,v245,s8\n"
"v_mfma_f32_16x16x32_bf16 v[4:7],v[132:135],a[20:23],v[4:7]\nds_read_b64_tr_b16 v[154:155],v225 offset:8704\nv_add_u32 v246,s72,v245\nv_mfma_f32_16x16x32_bf16 v[4:7],v[128:131],a[16:19],v[4:7]\n"
"buffer_load_dwordx4 v232,s[40:43],s60 offen offset:0 lds\nv_mfma_f32_16x16x32_bf16 v[8:11],v[128:131],a[32:35],0\nds_read_b64_tr_b16 v[156:157],v225 offset:12288\nv_mfma_f32_16x16x32_bf16 v[8:11],v[132:135],a[36:39],v[8:11]\n"
"ds_read_b64_tr_b16 v[158:159],v225 offset:12800\nv_mfma_f32_16x16x32_bf16 v[8:11],v[136:139],a[40:43],v[8:11]\nv_mfma_f32_16x16x32_bf16 v[8:11],v[140:143],a[44:47],v[8:11]\n"
"buffer_load_dwordx4 v233,s[40:43],s60 offen offset:1024 lds\nv_mfma_f32_16x16x32_bf16 v[12:15],v[140:143],a[60:63],0\ns_sub_u32 s88,s9,13\ns_cmp_lt_u32 s9,13\ns_cselect_b32 s88,s9,s88\n"
"v_mfma_f32_16x16x32_bf16 v[12:15],v[136:139],a[56:59],v[12:15]\ns_cmp_lt_u32 s88,12\ns_cselect_b32 s15,1,0\ns_add_u32 s70,s15,2\nv_mfma_f32_16x16x32_bf16 v[12:15],v[132:135],a[52:55],v[12:15]\ns_waitcnt lgkmcnt(0)\n"
"s_mul_i32 s67,s88,5\ns_lshl_b32 s6,s9,2\nv_mfma_f32_16x16x32_bf16 v[12:15],v[128:131],a[48:51],v[12:15]\nbuffer_load_dwordx4 v234,s[40:43],s60 offen offset:2048 lds\nv_mfma_f32_16x16x32_bf16 v[28:31],v[144:147],a[48:51],0\n"
"ds_read_b64_tr_b16 v[128:129],v226 offset:0\ns_sub_u32 s6,s6,40\nv_mfma_f32_16x16x32_bf16 v[28:31],v[148:151],a[52:55],v[28:31]\nds_read_b64_tr_b16 v[130:131],v226 offset:512\n"
"v_mfma_f32_16x16x32_bf16 v[28:31],v[152:155],a[56:59],v[28:31]\nds_read_b64_tr_b16 v[132:133],v226 offset:4096\nv_mfma_f32_16x16x32_bf16 v[28:31],v[156:159],a[60:63],v[28:31]\n"
"buffer_load_dwordx4 v235,s[40:43],s60 offen offset:3072 lds\nv_mfma_f32_16x16x32_bf16 v[24:27],v[156:159],a[44:47],0\nds_read_b64_tr_b16 v[134:135],v226 offset:4608\ns_add_u32 s60,s53,s58\n"
"v_mfma_f32_16x16x32_bf16 v[24:27],v[152:155],a[40:43],v[24:27]\nds_read_b64_tr_b16 v[136:137],v226 offset:8192\ns_add_u32 m0,s62,0\nv_mfma_f32_16x16x32_bf16 v[24:27],v[148:151],a[36:39],v[24:27]\n"
"ds_read_b64_tr_b16 v[138:139],v226 offset:8704\nv_mfma_f32_16x16x32_bf16 v[24:27],v[144:147],a[32:35],v[24:27]\nbuffer_load_dwordx4 v232,s[40:43],s60 offen offset:0 lds\nv_mfma_f32_16x16x32_bf16 v[20:23],v[144:147],a[16:19],0\n"
"ds_read_b64_tr_b16 v[140:141],v226 offset:12288\nv_mfma_f32_16x16x32_bf16 v[20:23],v[148:151],a[20:23],v[20:23]\nds_read_b64_tr_b16 v[142:143],v226 offset:12800\nv_mfma_f32_16x16x32_bf16 v[20:23],v[152:155],a[24:27],v[20:23]\n"
"s_cmp_lt_u32 s9,26\ns_cselect_b32 s67,s67,s6\ns_lshl_b32 s73,s10,2\nv_mfma_f32_16x16x32_bf16 v[20:23],v[156:159],a[28:31],v[20:23]\nbuffer_load_dwordx4 v233,s[40:43],s60 offen offset:1024 lds\n"
"v_mfma_f32_16x16x32_bf16 v[16:19],v[156:159],a[12:15],0\ns_add_u32 s73,s73,s5\ns_lshl_b32 s73,s73,15\ns_lshl_b32 s6,s13,14\nv_mfma_f32_16x16x32_bf16 v[16:19],v[152:155],a[8:11],v[16:19]\ns_add_u32 s73,s73,s6\n"
"s_mov_b32 s12,0x3e0293ee\nv_mfma_f32_16x16x32_bf16 v[16:19],v[148:151],a[4:7],v[16:19]\ns_waitcnt lgkmcnt(0)\ns_cmp_lt_u32 s9,13\ns_cselect_b32 s12,s12,0x3f800000\nv_mfma_f32_16x16x32_bf16 v[16:19],v[144:147],a[0:3],v[16:19]\n"
"buffer_load_dwordx4 v234,s[40:43],s60 offen offset:2048 lds\nv_mfma_f32_16x16x32_bf16 v[32:35],v[128:131],a[0:3],0\nds_read_b64_tr_b16 v[144:145],v227 offset:0\ns_and_b32 s14,s4,7\n"
"v_mfma_f32_16x16x32_bf16 v[32:35],v[132:135],a[4:7],v[32:35]\nds_read_b64_tr_b16 v[146:147],v227 offset:512\ns_lshl_b32 s14,s14,2\nv_mfma_f32_16x16x32_bf16 v[32:35],v[136:139],a[8:11],v[32:35]\n"
"ds_read_b64_tr_b16 v[148:149],v227 offset:4096\ns_lshr_b32 s6,s4,3\nv_mfma_f32_16x16x32_bf16 v[32:35],v[140:143],a[12:15],v[32:35]\nbuffer_load_dwordx4 v235,s[40:43],s60 offen offset:3072 lds\n"
"v_mfma_f32_16x16x32_bf16 v[36:39],v[140:143],a[28:31],0\nds_read_b64_tr_b16 v[150:151],v227 offset:4608\ns_add_u32 s60,s53,s59\nv_mfma_f32_16x16x32_bf16 v[36:39],v[136:139],a[24:27],v[36:39]\n"
"ds_read_b64_tr_b16 v[152:153],v227 offset:8192\ns_add_u32 m0,s62,16384\nv_mfma_f32_16x16x32_bf16 v[36:39],v[132:135],a[20:23],v[36:39]\nds_read_b64_tr_b16 v[154:155],v227 offset:8704\ns_and_b32 s6,s6,3\n"
"v_mfma_f32_16x16x32_bf16 v[36:39],v[128:131],a[16:19],v[36:39]\nbuffer_load_dwordx4 v232,s[40:43],s60 offen offset:0 lds\nv_mfma_f32_16x16x32_bf16 v[40:43],v[128:131],a[32:35],0\nds_read_b64_tr_b16 v[156:157],v227 offset:12288\n"
"s_waitcnt vmcnt(9)\nv_mfma_f32_16x16x32_bf16 v[40:43],v[132:135],a[36:39],v[40:43]\nds_read_b64_tr_b16 v[158:159],v227 offset:12800\ns_add_u32 s14,s14,s6\nv_mfma_f32_16x16x32_bf16 v[40:43],v[136:139],a[40:43],v[40:43]\n"
"ds_read_b128 a[64:67],v188 offset:16384\ns_lshl_b32 s14,s14,8\nv_mfma_f32_16x16x32_bf16 v[40:43],v[140:143],a[44:47],v[40:43]\nbuffer_load_dwordx4 v233,s[40:43],s60 offen offset:1024 lds\n"
"v_mfma_f32_16x16x32_bf16 v[44:47],v[140:143],a[60:63],0\nds_read_b128 a[68:71],v189 offset:16384\ns_lshr_b32 s16,s4,5\nv_mfma_f32_16x16x32_bf16 v[44:47],v[136:139],a[56:59],v[44:47]\nds_read_b128 a[72:75],v190 offset:16384\n"
"s_sub_u32 s16,15,s16\nv_mfma_f32_16x16x32_bf16 v[44:47],v[132:135],a[52:55],v[44:47]\ns_waitcnt vmcnt(14) lgkmcnt(3)\nds_read_b128 a[76:79],v191 offset:16384\nv_mfma_f32_16x16x32_bf16 v[44:47],v[128:131],a[48:51],v[44:47]\n"
"buffer_load_dwordx4 v234,s[40:43],s60 offen offset:2048 lds\nv_mfma_f32_16x16x32_bf16 v[60:63],v[144:147],a[48:51],0\ns_barrier\ns_lshl_b32 s16,s16,3\ns_and_b32 s6,s5,1\n"
"v_mfma_f32_16x16x32_bf16 v[60:63],v[148:151],a[52:55],v[60:63]\nds_read_b64_tr_b16 v[128:129],v224 offset:16384\ns_lshl_b32 s6,s6,2\nv_mfma_f32_16x16x32_bf16 v[60:63],v[152:155],a[56:59],v[60:63]\n"
"ds_read_b64_tr_b16 v[130:131],v224 offset:16896\ns_and_b32 s7,s5,2\nv_mfma_f32_16x16x32_bf16 v[60:63],v[156:159],a[60:63],v[60:63]\nbuffer_load_dwordx4 v235,s[40:43],s60 offen offset:3072 lds\n"
"v_mfma_f32_16x16x32_bf16 v[56:59],v[156:159],a[44:47],0\nds_read_b64_tr_b16 v[132:133],v224 offset:20480\ns_add_u32 s60,s49,s63\nv_mfma_f32_16x16x32_bf16 v[56:59],v[152:155],a[40:43],v[56:59]\n"
"ds_read_b64_tr_b16 v[134:135],v224 offset:20992\ns_add_u32 m0,s61,32768\nv_mfma_f32_16x16x32_bf16 v[56:59],v[148:151],a[36:39],v[56:59]\nds_read_b64_tr_b16 v[136:137],v224 offset:24576\ns_or_b32 s6,s6,s7\n"
"v_mfma_f32_16x16x32_bf16 v[56:59],v[144:147],a[32:35],v[56:59]\nbuffer_load_dwordx4 v228,s[36:39],s60 offen offset:0 lds\nv_mfma_f32_16x16x32_bf16 v[52:55],v[144:147],a[16:19],0\nds_read_b64_tr_b16 v[138:139],v224 offset:25088\n"
"s_waitcnt vmcnt(9)\nv_mfma_f32_16x16x32_bf16 v[52:55],v[148:151],a[20:23],v[52:55]\nds_read_b64_tr_b16 v[140:141],v224 offset:28672\ns_add_u32 s16,s16,s6\nv_mfma_f32_16x16x32_bf16 v[52:55],v[152:155],a[24:27],v[52:55]\n"
"ds_read_b64_tr_b16 v[142:143],v224 offset:29184\ns_lshr_b32 s17,s4,5\nv_mfma_f32_16x16x32_bf16 v[52:55],v[156:159],a[28:31],v[52:55]\nbuffer_load_dwordx4 v229,s[36:39],s60 offen offset:1024 lds\n"
"v_mfma_f32_16x16x32_bf16 v[48:51],v[156:159],a[12:15],0\nds_read_b128 a[80:83],v236 offset:49152\ns_lshl_b32 s17,s17,3\nv_mfma_f32_16x16x32_bf16 v[48:51],v[152:155],a[8:11],v[48:51]\nds_read_b128 a[84:87],v237 offset:49152\n"
"s_lshl_b32 s6,s5,1\nv_mfma_f32_16x16x32_bf16 v[48:51],v[148:151],a[4:7],v[48:51]\ns_waitcnt lgkmcnt(2)\nds_read_b128 a[88:91],v238 offset:49152\nv_mfma_f32_16x16x32_bf16 v[48:51],v[144:147],a[0:3],v[48:51]\n"
"buffer_load_dwordx4 v230,s[36:39],s60 offen offset:2048 lds\nv_mfma_f32_16x16x32_bf16 v[64:67],v[128:131],a[0:3],0\nds_read_b64_tr_b16 v[144:145],v225 offset:16384\ns_add_u32 s17,s17,s6\n"
"v_mfma_f32_16x16x32_bf16 v[64:67],v[132:135],a[4:7],v[64:67]\nds_read_b64_tr_b16 v[146:147],v225 offset:16896\ns_mul_i32 s89,s10,28\nv_mfma_f32_16x16x32_bf16 v[64:67],v[136:139],a[8:11],v[64:67]\n"
"ds_read_b64_tr_b16 v[148:149],v225 offset:20480\ns_add_u32 s89,s89,s9\nv_mfma_f32_16x16x32_bf16 v[64:67],v[140:143],a[12:15],v[64:67]\nbuffer_load_dwordx4 v231,s[36:39],s60 offen offset:3072 lds\n"
"v_mfma_f32_16x16x32_bf16 v[68:71],v[140:143],a[28:31],0\nds_read_b64_tr_b16 v[150:151],v225 offset:20992\ns_add_u32 s60,s54,s56\nv_mfma_f32_16x16x32_bf16 v[68:71],v[136:139],a[24:27],v[68:71]\n"
"ds_read_b64_tr_b16 v[152:153],v225 offset:24576\ns_add_u32 m0,s62,32768\nv_mfma_f32_16x16x32_bf16 v[68:71],v[132:135],a[20:23],v[68:71]\nds_read_b64_tr_b16 v[154:155],v225 offset:25088\ns_lshl_b32 s89,s89,2\n"
"v_mfma_f32_16x16x32_bf16 v[68:71],v[128:131],a[16:19],v[68:71]\nbuffer_load_dwordx4 v232,s[40:43],s60 offen offset:0 lds\nv_mfma_f32_16x16x32_bf16 v[72:75],v[128:131],a[32:35],0\nds_read_b64_tr_b16 v[156:157],v225 offset:28672\n"
"s_add_u32 s89,s89,s5\nv_mfma_f32_16x16x32_bf16 v[72:75],v[132:135],a[36:39],v[72:75]\nds_read_b64_tr_b16 v[158:159],v225 offset:29184\ns_and_b32 s89,s89,0x3ff\nv_mfma_f32_16x16x32_bf16 v[72:75],v[136:139],a[40:43],v[72:75]\n"
"ds_read_b128 a[92:95],v239 offset:49152\ns_lshr_b32 s83,0x249,s88\nv_mfma_f32_16x16x32_bf16 v[72:75],v[140:143],a[44:47],v[72:75]\nbuffer_load_dwordx4 v233,s[40:43],s60 offen offset:1024 lds\n"
"v_mfma_f32_16x16x32_bf16 v[76:79],v[140:143],a[60:63],0\ns_waitcnt vmcnt(10)\ns_and_b32 s83,s83,1\ns_lshl_b32 s6,s10,3\nv_mfma_f32_16x16x32_bf16 v[76:79],v[136:139],a[56:59],v[76:79]\nds_read_b128 a[96:99],v236 offset:0\n"
"s_lshl_b32 s7,s13,2\nv_mfma_f32_16x16x32_bf16 v[76:79],v[132:135],a[52:55],v[76:79]\ns_waitcnt lgkmcnt(2)\nds_read_b128 a[100:103],v237 offset:0\nv_mfma_f32_16x16x32_bf16 v[76:79],v[128:131],a[48:51],v[76:79]\n"
"buffer_load_dwordx4 v234,s[40:43],s60 offen offset:2048 lds\nv_mfma_f32_16x16x32_bf16 v[92:95],v[144:147],a[48:51],0\nds_read_b64_tr_b16 v[128:129],v226 offset:16384\ns_add_u32 s6,s6,s7\n"
"v_mfma_f32_16x16x32_bf16 v[92:95],v[148:151],a[52:55],v[92:95]\nds_read_b64_tr_b16 v[130:131],v226 offset:16896\ns_mul_i32 s7,s88,11\nv_mfma_f32_16x16x32_bf16 v[92:95],v[152:155],a[56:59],v[92:95]\n"
"ds_read_b64_tr_b16 v[132:133],v226 offset:20480\ns_lshr_b32 s7,s7,5\nv_mfma_f32_16x16x32_bf16 v[92:95],v[156:159],a[60:63],v[92:95]\nbuffer_load_dwordx4 v235,s[40:43],s60 offen offset:3072 lds\n"
"v_mfma_f32_16x16x32_bf16 v[88:91],v[156:159],a[44:47],0\nds_read_b64_tr_b16 v[134:135],v226 offset:20992\ns_add_u32 s60,s49,s64\nv_mfma_f32_16x16x32_bf16 v[88:91],v[152:155],a[40:43],v[88:91]\n"
"ds_read_b64_tr_b16 v[136:137],v226 offset:24576\ns_add_u32 m0,s61,49152\nv_mfma_f32_16x16x32_bf16 v[88:91],v[148:151],a[36:39],v[88:91]\nds_read_b64_tr_b16 v[138:139],v226 offset:25088\ns_add_u32 s6,s6,s7\n"
"v_mfma_f32_16x16x32_bf16 v[88:91],v[144:147],a[32:35],v[88:91]\nbuffer_load_dwordx4 v228,s[36:39],s60 offen offset:0 lds\nv_mfma_f32_16x16x32_bf16 v[84:87],v[144:147],a[16:19],0\nds_read_b64_tr_b16 v[140:141],v226 offset:28672\n"
"s_lshl_b32 s6,s6,2\nv_mfma_f32_16x16x32_bf16 v[84:87],v[148:151],a[20:23],v[84:87]\nds_read_b64_tr_b16 v[142:143],v226 offset:29184\ns_add_u32 s6,s6,s5\nv_mfma_f32_16x16x32_bf16 v[84:87],v[152:155],a[24:27],v[84:87]\n"
"ds_read_b128 a[104:107],v238 offset:0\ns_add_u32 s6,s6,0x380\nv_mfma_f32_16x16x32_bf16 v[84:87],v[156:159],a[28:31],v[84:87]\nbuffer_load_dwordx4 v229,s[36:39],s60 offen offset:1024 lds\n"
"v_mfma_f32_16x16x32_bf16 v[80:83],v[156:159],a[12:15],0\nds_read_b128 a[108:111],v239 offset:0\ns_waitcnt vmcnt(10)\nv_mfma_f32_16x16x32_bf16 v[80:83],v[152:155],a[8:11],v[80:83]\nds_read_b128 a[112:115],v236 offset:16384\n"
"v_mfma_f32_16x16x32_bf16 v[80:83],v[148:151],a[4:7],v[80:83]\ns_waitcnt lgkmcnt(3)\nds_read_b128 a[116:119],v237 offset:16384\nv_mfma_f32_16x16x32_bf16 v[80:83],v[144:147],a[0:3],v[80:83]\n"
"buffer_load_dwordx4 v230,s[36:39],s60 offen offset:2048 lds\nv_mfma_f32_16x16x32_bf16 v[96:99],v[128:131],a[0:3],0\nds_read_b64_tr_b16 v[144:145],v227 offset:16384\nv_mfma_f32_16x16x32_bf16 v[96:99],v[132:135],a[4:7],v[96:99]\n"
"ds_read_b64_tr_b16 v[146:147],v227 offset:16896\nv_mfma_f32_16x16x32_bf16 v[96:99],v[136:139],a[8:11],v[96:99]\nds_read_b64_tr_b16 v[148:149],v227 offset:20480\nv_mfma_f32_16x16x32_bf16 v[96:99],v[140:143],a[12:15],v[96:99]\n"
"buffer_load_dwordx4 v231,s[36:39],s60 offen offset:3072 lds\nv_mfma_f32_16x16x32_bf16 v[100:103],v[140:143],a[28:31],0\nds_read_b64_tr_b16 v[150:151],v227 offset:20992\ns_add_u32 s60,s54,s57\n"
"v_mfma_f32_16x16x32_bf16 v[100:103],v[136:139],a[24:27],v[100:103]\nds_read_b64_tr_b16 v[152:153],v227 offset:24576\ns_add_u32 m0,s62,65536\nv_mfma_f32_16x16x32_bf16 v[100:103],v[132:135],a[20:23],v[100:103]\n"
"ds_read_b64_tr_b16 v[154:155],v227 offset:25088\nv_mfma_f32_16x16x32_bf16 v[100:103],v[128:131],a[16:19],v[100:103]\nbuffer_load_dwordx4 v232,s[40:43],s60 offen offset:0 lds\n"
"v_mfma_f32_16x16x32_bf16 v[104:107],v[128:131],a[32:35],0\nds_read_b64_tr_b16 v[156:157],v227 offset:28672\nv_mfma_f32_16x16x32_bf16 v[104:107],v[132:135],a[36:39],v[104:107]\nds_read_b64_tr_b16 v[158:159],v227 offset:29184\n"
"v_mfma_f32_16x16x32_bf16 v[104:107],v[136:139],a[40:43],v[104:107]\nds_read_b128 a[120:123],v238 offset:16384\nv_mfma_f32_16x16x32_bf16 v[104:107],v[140:143],a[44:47],v[104:107]\n"
"buffer_load_dwordx4 v233,s[40:43],s60 offen offset:1024 lds\nv_mfma_f32_16x16x32_bf16 v[108:111],v[140:143],a[60:63],0\nds_read_b128 a[124:127],v239 offset:16384\n"
"v_mfma_f32_16x16x32_bf16 v[108:111],v[136:139],a[56:59],v[108:111]\ns_cmp_eq_u32 s83,1\ns_cselect_b32 s83,s6,0x7d0\ns_and_b32 s18,s89,7\nv_mfma_f32_16x16x32_bf16 v[108:111],v[132:135],a[52:55],v[108:111]\n"
"s_waitcnt vmcnt(10) lgkmcnt(2)\ns_lshr_b32 s18,s18,1\ns_mul_i32 s18,s18,0x90\nv_mfma_f32_16x16x32_bf16 v[108:111],v[128:131],a[48:51],v[108:111]\nbuffer_load_dwordx4 v234,s[40:43],s60 offen offset:2048 lds\n"
"v_mfma_f32_16x16x32_bf16 v[124:127],v[144:147],a[48:51],0\ns_barrier\ns_lshr_b32 s6,s89,3\ns_add_u32 s18,s18,s6\nv_mfma_f32_16x16x32_bf16 v[124:127],v[148:151],a[52:55],v[124:127]\nds_read_b64_tr_b16 v[128:129],v224 offset:32768\n"
"s_lshl_b32 s18,s18,13\nv_mfma_f32_16x16x32_bf16 v[124:127],v[152:155],a[56:59],v[124:127]\nds_read_b64_tr_b16 v[130:131],v224 offset:33280\ns_and_b32 s6,s89,1\nv_mfma_f32_16x16x32_bf16 v[124:127],v[156:159],a[60:63],v[124:127]\n"
"buffer_load_dwordx4 v235,s[40:43],s60 offen offset:3072 lds\nv_mfma_f32_16x16x32_bf16 v[120:123],v[156:159],a[44:47],0\nds_read_b64_tr_b16 v[132:133],v224 offset:36864\ns_add_u32 s60,s54,s58\n"
"v_mfma_f32_16x16x32_bf16 v[120:123],v[152:155],a[40:43],v[120:123]\nds_read_b64_tr_b16 v[134:135],v224 offset:37376\ns_add_u32 m0,s62,81920\nv_mfma_f32_16x16x32_bf16 v[120:123],v[148:151],a[36:39],v[120:123]\n"
"ds_read_b64_tr_b16 v[136:137],v224 offset:40960\ns_lshl_b32 s6,s6,12\nv_mfma_f32_16x16x32_bf16 v[120:123],v[144:147],a[32:35],v[120:123]\nbuffer_load_dwordx4 v232,s[40:43],s60 offen offset:0 lds\n"
"v_mfma_f32_16x16x32_bf16 v[116:119],v[144:147],a[16:19],0\nds_read_b64_tr_b16 v[138:139],v224 offset:41472\ns_add_u32 s18,s18,s6\nv_mfma_f32_16x16x32_bf16 v[116:119],v[148:151],a[20:23],v[116:119]\n"
"ds_read_b64_tr_b16 v[140:141],v224 offset:45056\ns_add_u32 s18,s18,0x2400000\nv_mfma_f32_16x16x32_bf16 v[116:119],v[152:155],a[24:27],v[116:119]\nds_read_b64_tr_b16 v[142:143],v224 offset:45568\ns_and_b32 s19,s83,7\n"
"v_mfma_f32_16x16x32_bf16 v[116:119],v[156:159],a[28:31],v[116:119]\nbuffer_load_dwordx4 v233,s[40:43],s60 offen offset:1024 lds\nv_mfma_f32_16x16x32_bf16 v[112:115],v[156:159],a[12:15],0\ns_lshr_b32 s19,s19,1\n"
"s_mul_i32 s19,s19,0x90\ns_lshr_b32 s6,s83,3\nv_mfma_f32_16x16x32_bf16 v[112:115],v[152:155],a[8:11],v[112:115]\ns_add_u32 s19,s19,s6\ns_lshl_b32 s19,s19,13\ns_and_b32 s6,s83,1\n"
"v_mfma_f32_16x16x32_bf16 v[112:115],v[148:151],a[4:7],v[112:115]\ns_waitcnt lgkmcnt(0)\ns_lshl_b32 s6,s6,12\ns_add_u32 s19,s19,s6\nv_mfma_f32_16x16x32_bf16 v[112:115],v[144:147],a[0:3],v[112:115]\n"
"buffer_load_dwordx4 v234,s[40:43],s60 offen offset:2048 lds\nv_mfma_f32_16x16x32_bf16 v[0:3],v[128:131],a[64:67],v[0:3]\nds_read_b64_tr_b16 v[144:145],v225 offset:32768\ns_add_u32 s19,s19,0x2400000\n"
"v_mfma_f32_16x16x32_bf16 v[0:3],v[132:135],a[68:71],v[0:3]\nds_read_b64_tr_b16 v[146:147],v225 offset:33280\nv_mfma_f32_16x16x32_bf16 v[0:3],v[136:139],a[72:75],v[0:3]\nds_read_b64_tr_b16 v[148:149],v225 offset:36864\n"
"v_mfma_f32_16x16x32_bf16 v[0:3],v[140:143],a[76:79],v[0:3]\nbuffer_load_dwordx4 v235,s[40:43],s60 offen offset:3072 lds\nv_mfma_f32_16x16x32_bf16 v[4:7],v[140:143],a[92:95],v[4:7]\nds_read_b64_tr_b16 v[150:151],v225 offset:37376\n"
"s_add_u32 s60,s54,s59\nv_mfma_f32_16x16x32_bf16 v[4:7],v[136:139],a[88:91],v[4:7]\nds_read_b64_tr_b16 v[152:153],v225 offset:40960\ns_add_u32 m0,s62,49152\nv_mfma_f32_16x16x32_bf16 v[4:7],v[132:135],a[84:87],v[4:7]\n"
"ds_read_b64_tr_b16 v[154:155],v225 offset:41472\ns_waitcnt vmcnt(12)\nv_mfma_f32_16x16x32_bf16 v[4:7],v[128:131],a[80:83],v[4:7]\nbuffer_load_dwordx4 v232,s[40:43],s60 offen offset:0 lds\n"
"v_mfma_f32_16x16x32_bf16 v[8:11],v[128:131],a[96:99],v[8:11]\nds_read_b64_tr_b16 v[156:157],v225 offset:45056\nv_mfma_f32_16x16x32_bf16 v[8:11],v[132:135],a[100:103],v[8:11]\nds_read_b64_tr_b16 v[158:159],v225 offset:45568\n"
"v_mfma_f32_16x16x32_bf16 v[8:11],v[136:139],a[104:107],v[8:11]\nds_read_b128 a[128:131],v236 offset:32768\nv_mfma_f32_16x16x32_bf16 v[8:11],v[140:143],a[108:111],v[8:11]\n"
"buffer_load_dwordx4 v233,s[40:43],s60 offen offset:1024 lds\nv_mfma_f32_16x16x32_bf16 v[12:15],v[140:143],a[124:127],v[12:15]\nds_read_b128 a[132:135],v237 offset:32768\n"
"v_mfma_f32_16x16x32_bf16 v[12:15],v[136:139],a[120:123],v[12:15]\nds_read_b128 a[136:139],v238 offset:32768\nv_mfma_f32_16x16x32_bf16 v[12:15],v[132:135],a[116:119],v[12:15]\ns_waitcnt lgkmcnt(3)\n"
"ds_read_b128 a[140:143],v239 offset:32768\nv_mfma_f32_16x16x32_bf16 v[12:15],v[128:131],a[112:115],v[12:15]\nbuffer_load_dwordx4 v234,s[40:43],s60 offen offset:2048 lds\n"
"v_mfma_f32_16x16x32_bf16 v[28:31],v[144:147],a[112:115],v[28:31]\nds_read_b64_tr_b16 v[128:129],v226 offset:32768\nv_mfma_f32_16x16x32_bf16 v[28:31],v[148:151],a[116:119],v[28:31]\nds_read_b64_tr_b16 v[130:131],v226 offset:33280\n"
"v_mfma_f32_16x16x32_bf16 v[28:31],v[152:155],a[120:123],v[28:31]\nds_read_b64_tr_b16 v[132:133],v226 offset:36864\nv_mfma_f32_16x16x32_bf16 v[28:31],v[156:159],a[124:127],v[28:31]\n"
"buffer_load_dwordx4 v235,s[40:43],s60 offen offset:3072 lds\nv_mfma_f32_16x16x32_bf16 v[24:27],v[156:159],a[108:111],v[24:27]\nds_read_b64_tr_b16 v[134:135],v226 offset:37376\ns_add_u32 s60,s50,s63\n"
"v_mfma_f32_16x16x32_bf16 v[24:27],v[152:155],a[104:107],v[24:27]\nds_read_b64_tr_b16 v[136:137],v226 offset:40960\ns_add_u32 m0,s61,0\nv_mfma_f32_16x16x32_bf16 v[24:27],v[148:151],a[100:103],v[24:27]\n"
"ds_read_b64_tr_b16 v[138:139],v226 offset:41472\nv_mfma_f32_16x16x32_bf16 v[24:27],v[144:147],a[96:99],v[24:27]\nbuffer_load_dwordx4 v228,s[36:39],s60 offen offset:0 lds\n"
"v_mfma_f32_16x16x32_bf16 v[20:23],v[144:147],a[80:83],v[20:23]\nds_read_b64_tr_b16 v[140:141],v226 offset:45056\nv_mfma_f32_16x16x32_bf16 v[20:23],v[148:151],a[84:87],v[20:23]\nds_read_b64_tr_b16 v[142:143],v226 offset:45568\n"
"v_mfma_f32_16x16x32_bf16 v[20:23],v[152:155],a[88:91],v[20:23]\ns_cmp_lt_u32 s83,0x480\ns_cselect_b32 s19,s19,0x40000000\nv_mfma_f32_16x16x32_bf16 v[20:23],v[156:159],a[92:95],v[20:23]\n"
"buffer_load_dwordx4 v229,s[36:39],s60 offen offset:1024 lds\nv_mfma_f32_16x16x32_bf16 v[16:19],v[156:159],a[76:79],v[16:19]\ns_cmp_lt_u32 s83,1024\ns_cselect_b64 s[80:81],s[90:91],s[80:81]\ns_lshr_b32 s6,s83,3\n"
"v_mfma_f32_16x16x32_bf16 v[16:19],v[152:155],a[72:75],v[16:19]\ns_cmp_lt_u32 s83,0x400\ns_cselect_b32 s7,0x7f,15\ns_and_b32 s6,s6,s7\nv_mfma_f32_16x16x32_bf16 v[16:19],v[148:151],a[68:71],v[16:19]\ns_waitcnt lgkmcnt(0)\n"
"s_lshl_b32 s6,s6,15\ns_and_b32 s7,s83,7\nv_mfma_f32_16x16x32_bf16 v[16:19],v[144:147],a[64:67],v[16:19]\nbuffer_load_dwordx4 v230,s[36:39],s60 offen offset:2048 lds\nv_mfma_f32_16x16x32_bf16 v[32:35],v[128:131],a[64:67],v[32:35]\n"
"ds_read_b64_tr_b16 v[144:145],v227 offset:32768\ns_lshl_b32 s7,s7,7\nv_mfma_f32_16x16x32_bf16 v[32:35],v[132:135],a[68:71],v[32:35]\nds_read_b64_tr_b16 v[146:147],v227 offset:33280\ns_add_u32 s6,s6,s7\n"
"v_mfma_f32_16x16x32_bf16 v[32:35],v[136:139],a[72:75],v[32:35]\nds_read_b64_tr_b16 v[148:149],v227 offset:36864\nv_mfma_f32_16x16x32_bf16 v[32:35],v[140:143],a[76:79],v[32:35]\n"
"buffer_load_dwordx4 v231,s[36:39],s60 offen offset:3072 lds\nv_mfma_f32_16x16x32_bf16 v[36:39],v[140:143],a[92:95],v[36:39]\nds_read_b64_tr_b16 v[150:151],v227 offset:37376\ns_add_u32 s60,s55,s56\n"
"v_mfma_f32_16x16x32_bf16 v[36:39],v[136:139],a[88:91],v[36:39]\nds_read_b64_tr_b16 v[152:153],v227 offset:40960\ns_add_u32 m0,s62,0\nv_mfma_f32_16x16x32_bf16 v[36:39],v[132:135],a[84:87],v[36:39]\n"
"ds_read_b64_tr_b16 v[154:155],v227 offset:41472\ns_waitcnt vmcnt(12)\nv_mfma_f32_16x16x32_bf16 v[36:39],v[128:131],a[80:83],v[36:39]\nbuffer_load_dwordx4 v232,s[40:43],s60 offen offset:0 lds\n"
"v_mfma_f32_16x16x32_bf16 v[40:43],v[128:131],a[96:99],v[40:43]\nds_read_b64_tr_b16 v[156:157],v227 offset:45056\nv_mfma_f32_16x16x32_bf16 v[40:43],v[132:135],a[100:103],v[40:43]\nds_read_b64_tr_b16 v[158:159],v227 offset:45568\n"
"v_mfma_f32_16x16x32_bf16 v[40:43],v[136:139],a[104:107],v[40:43]\nds_read_b128 a[144:147],v188 offset:0\nv_mfma_f32_16x16x32_bf16 v[40:43],v[140:143],a[108:111],v[40:43]\n"
"buffer_load_dwordx4 v233,s[40:43],s60 offen offset:1024 lds\nv_mfma_f32_16x16x32_bf16 v[44:47],v[140:143],a[124:127],v[44:47]\nds_read_b128 a[148:151],v189 offset:0\n"
"v_mfma_f32_16x16x32_bf16 v[44:47],v[136:139],a[120:123],v[44:47]\nds_read_b128 a[152:155],v190 offset:0\nv_mfma_f32_16x16x32_bf16 v[44:47],v[132:135],a[116:119],v[44:47]\ns_waitcnt vmcnt(18) lgkmcnt(3)\n"
"ds_read_b128 a[156:159],v191 offset:0\nv_mfma_f32_16x16x32_bf16 v[44:47],v[128:131],a[112:115],v[44:47]\nbuffer_load_dwordx4 v234,s[40:43],s60 offen offset:2048 lds\n"
"v_mfma_f32_16x16x32_bf16 v[60:63],v[144:147],a[112:115],v[60:63]\ns_barrier\ns_add_u32 s80,s80,s6\ns_addc_u32 s81,s81,0\nv_mfma_f32_16x16x32_bf16 v[60:63],v[148:151],a[116:119],v[60:63]\n"
"ds_read_b64_tr_b16 v[128:129],v224 offset:49152\ns_lshr_b32 s6,s89,3\nv_mfma_f32_16x16x32_bf16 v[60:63],v[152:155],a[120:123],v[60:63]\nds_read_b64_tr_b16 v[130:131],v224 offset:49664\ns_lshl_b32 s6,s6,15\n"
"v_mfma_f32_16x16x32_bf16 v[60:63],v[156:159],a[124:127],v[60:63]\nbuffer_load_dwordx4 v235,s[40:43],s60 offen offset:3072 lds\nv_mfma_f32_16x16x32_bf16 v[56:59],v[156:159],a[108:111],v[56:59]\n"
"ds_read_b64_tr_b16 v[132:133],v224 offset:53248\ns_add_u32 s60,s50,s64\nv_mfma_f32_16x16x32_bf16 v[56:59],v[152:155],a[104:107],v[56:59]\nds_read_b64_tr_b16 v[134:135],v224 offset:53760\ns_add_u32 m0,s61,16384\n"
"v_mfma_f32_16x16x32_bf16 v[56:59],v[148:151],a[100:103],v[56:59]\nds_read_b64_tr_b16 v[136:137],v224 offset:57344\ns_waitcnt vmcnt(12)\nv_mfma_f32_16x16x32_bf16 v[56:59],v[144:147],a[96:99],v[56:59]\n"
"buffer_load_dwordx4 v228,s[36:39],s60 offen offset:0 lds\nv_mfma_f32_16x16x32_bf16 v[52:55],v[144:147],a[80:83],v[52:55]\nds_read_b64_tr_b16 v[138:139],v224 offset:57856\ns_and_b32 s7,s89,7\n"
"v_mfma_f32_16x16x32_bf16 v[52:55],v[148:151],a[84:87],v[52:55]\nds_read_b64_tr_b16 v[140:141],v224 offset:61440\ns_lshl_b32 s7,s7,7\nv_mfma_f32_16x16x32_bf16 v[52:55],v[152:155],a[88:91],v[52:55]\n"
"ds_read_b64_tr_b16 v[142:143],v224 offset:61952\ns_add_u32 s6,s6,s7\nv_mfma_f32_16x16x32_bf16 v[52:55],v[156:159],a[92:95],v[52:55]\nbuffer_load_dwordx4 v229,s[36:39],s60 offen offset:1024 lds\n"
"v_mfma_f32_16x16x32_bf16 v[48:51],v[156:159],a[76:79],v[48:51]\nds_read_b128 a[160:163],v188 offset:16384\nv_mfma_f32_16x16x32_bf16 v[48:51],v[152:155],a[72:75],v[48:51]\nds_read_b128 a[164:167],v189 offset:16384\n"
"v_mfma_f32_16x16x32_bf16 v[48:51],v[148:151],a[68:71],v[48:51]\ns_waitcnt lgkmcnt(2)\nds_read_b128 a[168:171],v190 offset:16384\nv_mfma_f32_16x16x32_bf16 v[48:51],v[144:147],a[64:67],v[48:51]\n"
"buffer_load_dwordx4 v230,s[36:39],s60 offen offset:2048 lds\nv_mfma_f32_16x16x32_bf16 v[64:67],v[128:131],a[64:67],v[64:67]\nds_read_b64_tr_b16 v[144:145],v225 offset:49152\n"
"v_mfma_f32_16x16x32_bf16 v[64:67],v[132:135],a[68:71],v[64:67]\nds_read_b64_tr_b16 v[146:147],v225 offset:49664\nv_mfma_f32_16x16x32_bf16 v[64:67],v[136:139],a[72:75],v[64:67]\nds_read_b64_tr_b16 v[148:149],v225 offset:53248\n"
"v_mfma_f32_16x16x32_bf16 v[64:67],v[140:143],a[76:79],v[64:67]\nbuffer_load_dwordx4 v231,s[36:39],s60 offen offset:3072 lds\nv_mfma_f32_16x16x32_bf16 v[68:71],v[140:143],a[92:95],v[68:71]\n"
"ds_read_b64_tr_b16 v[150:151],v225 offset:53760\ns_add_u32 s60,s55,s57\nv_mfma_f32_16x16x32_bf16 v[68:71],v[136:139],a[88:91],v[68:71]\nds_read_b64_tr_b16 v[152:153],v225 offset:57344\ns_add_u32 m0,s62,16384\n"
"v_mfma_f32_16x16x32_bf16 v[68:71],v[132:135],a[84:87],v[68:71]\nds_read_b64_tr_b16 v[154:155],v225 offset:57856\nv_mfma_f32_16x16x32_bf16 v[68:71],v[128:131],a[80:83],v[68:71]\n"
"buffer_load_dwordx4 v232,s[40:43],s60 offen offset:0 lds\nv_mfma_f32_16x16x32_bf16 v[72:75],v[128:131],a[96:99],v[72:75]\nds_read_b64_tr_b16 v[156:157],v225 offset:61440\n"
"v_mfma_f32_16x16x32_bf16 v[72:75],v[132:135],a[100:103],v[72:75]\nds_read_b64_tr_b16 v[158:159],v225 offset:61952\nv_mfma_f32_16x16x32_bf16 v[72:75],v[136:139],a[104:107],v[72:75]\nds_read_b128 a[172:175],v191 offset:16384\n"
"s_waitcnt vmcnt(13)\nv_mfma_f32_16x16x32_bf16 v[72:75],v[140:143],a[108:111],v[72:75]\nbuffer_load_dwordx4 v233,s[40:43],s60 offen offset:1024 lds\nv_mfma_f32_16x16x32_bf16 v[76:79],v[140:143],a[124:127],v[76:79]\n"
"ds_read_b128 a[176:179],v236 offset:49152\nv_mfma_f32_16x16x32_bf16 v[76:79],v[136:139],a[120:123],v[76:79]\nds_read_b128 a[180:183],v237 offset:49152\nv_mfma_f32_16x16x32_bf16 v[76:79],v[132:135],a[116:119],v[76:79]\n"
"s_waitcnt lgkmcnt(3)\nds_read_b128 a[184:187],v238 offset:49152\nv_mfma_f32_16x16x32_bf16 v[76:79],v[128:131],a[112:115],v[76:79]\nbuffer_load_dwordx4 v234,s[40:43],s60 offen offset:2048 lds\n"
"v_mfma_f32_16x16x32_bf16 v[92:95],v[144:147],a[112:115],v[92:95]\nds_read_b64_tr_b16 v[128:129],v226 offset:49152\nv_mfma_f32_16x16x32_bf16 v[92:95],v[148:151],a[116:119],v[92:95]\nds_read_b64_tr_b16 v[130:131],v226 offset:49664\n"
"v_mfma_f32_16x16x32_bf16 v[92:95],v[152:155],a[120:123],v[92:95]\nds_read_b64_tr_b16 v[132:133],v226 offset:53248\nv_mfma_f32_16x16x32_bf16 v[92:95],v[156:159],a[124:127],v[92:95]\n"
"buffer_load_dwordx4 v235,s[40:43],s60 offen offset:3072 lds\nv_mfma_f32_16x16x32_bf16 v[88:91],v[156:159],a[108:111],v[88:91]\nds_read_b64_tr_b16 v[134:135],v226 offset:53760\ns_add_u32 s60,s55,s58\n"
"v_mfma_f32_16x16x32_bf16 v[88:91],v[152:155],a[104:107],v[88:91]\nds_read_b64_tr_b16 v[136:137],v226 offset:57344\ns_add_u32 m0,s62,32768\nv_mfma_f32_16x16x32_bf16 v[88:91],v[148:151],a[100:103],v[88:91]\n"
"ds_read_b64_tr_b16 v[138:139],v226 offset:57856\nv_mfma_f32_16x16x32_bf16 v[88:91],v[144:147],a[96:99],v[88:91]\nbuffer_load_dwordx4 v232,s[40:43],s60 offen offset:0 lds\n"
"v_mfma_f32_16x16x32_bf16 v[84:87],v[144:147],a[80:83],v[84:87]\nds_read_b64_tr_b16 v[140:141],v226 offset:61440\nv_mfma_f32_16x16x32_bf16 v[84:87],v[148:151],a[84:87],v[84:87]\nds_read_b64_tr_b16 v[142:143],v226 offset:61952\n"
"v_mfma_f32_16x16x32_bf16 v[84:87],v[152:155],a[88:91],v[84:87]\nds_read_b128 a[188:191],v239 offset:49152\nv_mfma_f32_16x16x32_bf16 v[84:87],v[156:159],a[92:95],v[84:87]\n"
"buffer_load_dwordx4 v233,s[40:43],s60 offen offset:1024 lds\nv_mfma_f32_16x16x32_bf16 v[80:83],v[156:159],a[76:79],v[80:83]\ns_add_u32 s90,s90,s6\ns_addc_u32 s91,s91,0\ns_lshl_b32 s6,s10,5\n"
"v_mfma_f32_16x16x32_bf16 v[80:83],v[152:155],a[72:75],v[80:83]\ns_and_b32 s7,s9,3\ns_lshl_b32 s7,s7,3\ns_add_u32 s6,s6,s7\nv_mfma_f32_16x16x32_bf16 v[80:83],v[148:151],a[68:71],v[80:83]\ns_waitcnt lgkmcnt(1)\ns_lshl_b32 s7,s5,1\n"
"s_add_u32 s6,s6,s7\nv_mfma_f32_16x16x32_bf16 v[80:83],v[144:147],a[64:67],v[80:83]\nbuffer_load_dwordx4 v234,s[40:43],s60 offen offset:2048 lds\nv_mfma_f32_16x16x32_bf16 v[96:99],v[128:131],a[64:67],v[96:99]\n"
"ds_read_b64_tr_b16 v[144:145],v227 offset:49152\ns_lshl_b32 s6,s6,10\nv_mfma_f32_16x16x32_bf16 v[96:99],v[132:135],a[68:71],v[96:99]\nds_read_b64_tr_b16 v[146:147],v227 offset:49664\ns_add_u32 s6,s6,0x4280000\n"
"v_mfma_f32_16x16x32_bf16 v[96:99],v[136:139],a[72:75],v[96:99]\nds_read_b64_tr_b16 v[148:149],v227 offset:53248\ns_lshl_b32 s7,s10,8\nv_mfma_f32_16x16x32_bf16 v[96:99],v[140:143],a[76:79],v[96:99]\n"
"buffer_load_dwordx4 v235,s[40:43],s60 offen offset:3072 lds\nv_mfma_f32_16x16x32_bf16 v[100:103],v[140:143],a[92:95],v[100:103]\nds_read_b64_tr_b16 v[150:151],v227 offset:53760\ns_add_u32 s60,s55,s59\n"
"v_mfma_f32_16x16x32_bf16 v[100:103],v[136:139],a[88:91],v[100:103]\nds_read_b64_tr_b16 v[152:153],v227 offset:57344\ns_add_u32 m0,s62,65536\nv_mfma_f32_16x16x32_bf16 v[100:103],v[132:135],a[84:87],v[100:103]\n"
"ds_read_b64_tr_b16 v[154:155],v227 offset:57856\ns_waitcnt vmcnt(12)\nv_mfma_f32_16x16x32_bf16 v[100:103],v[128:131],a[80:83],v[100:103]\nbuffer_load_dwordx4 v232,s[40:43],s60 offen offset:0 lds\n"
"v_mfma_f32_16x16x32_bf16 v[104:107],v[128:131],a[96:99],v[104:107]\nds_read_b64_tr_b16 v[156:157],v227 offset:61440\ns_lshl_b32 s84,s5,6\nv_mfma_f32_16x16x32_bf16 v[104:107],v[132:135],a[100:103],v[104:107]\n"
"ds_read_b64_tr_b16 v[158:159],v227 offset:61952\ns_add_u32 s7,s7,s84\nv_mfma_f32_16x16x32_bf16 v[104:107],v[136:139],a[104:107],v[104:107]\nds_read_b128 a[192:195],v236 offset:0\ns_and_b32 s84,s9,1\n"
"v_mfma_f32_16x16x32_bf16 v[104:107],v[140:143],a[108:111],v[104:107]\nbuffer_load_dwordx4 v233,s[40:43],s60 offen offset:1024 lds\nv_mfma_f32_16x16x32_bf16 v[108:111],v[140:143],a[124:127],v[108:111]\n"
"ds_read_b128 a[196:199],v237 offset:0\ns_lshl_b32 s84,s84,5\nv_mfma_f32_16x16x32_bf16 v[108:111],v[136:139],a[120:123],v[108:111]\nds_read_b128 a[200:203],v238 offset:0\ns_add_u32 s7,s7,s84\n"
"v_mfma_f32_16x16x32_bf16 v[108:111],v[132:135],a[116:119],v[108:111]\ns_waitcnt vmcnt(18) lgkmcnt(3)\nds_read_b128 a[204:207],v239 offset:0\nv_mfma_f32_16x16x32_bf16 v[108:111],v[128:131],a[112:115],v[108:111]\n"
"buffer_load_dwordx4 v234,s[40:43],s60 offen offset:2048 lds\nv_mfma_f32_16x16x32_bf16 v[124:127],v[144:147],a[112:115],v[124:127]\ns_barrier\ns_lshl_b32 s7,s7,2\ns_add_u32 s7,s7,0x42c8000\n"
"v_mfma_f32_16x16x32_bf16 v[124:127],v[148:151],a[116:119],v[124:127]\nds_read_b64_tr_b16 v[128:129],v224 offset:0\nv_mfma_f32_16x16x32_bf16 v[124:127],v[152:155],a[120:123],v[124:127]\n"
"ds_read_b64_tr_b16 v[130:131],v224 offset:512\nv_mfma_f32_16x16x32_bf16 v[124:127],v[156:159],a[124:127],v[124:127]\nbuffer_load_dwordx4 v235,s[40:43],s60 offen offset:3072 lds\n"
"v_mfma_f32_16x16x32_bf16 v[120:123],v[156:159],a[108:111],v[120:123]\nds_read_b64_tr_b16 v[132:133],v224 offset:4096\ns_add_u32 s60,s51,s63\nv_mfma_f32_16x16x32_bf16 v[120:123],v[152:155],a[104:107],v[120:123]\n"
"ds_read_b64_tr_b16 v[134:135],v224 offset:4608\ns_add_u32 m0,s61,32768\nv_mfma_f32_16x16x32_bf16 v[120:123],v[148:151],a[100:103],v[120:123]\nds_read_b64_tr_b16 v[136:137],v224 offset:8192\n"
"v_mfma_f32_16x16x32_bf16 v[120:123],v[144:147],a[96:99],v[120:123]\nbuffer_load_dwordx4 v228,s[36:39],s60 offen offset:0 lds\nv_mfma_f32_16x16x32_bf16 v[116:119],v[144:147],a[80:83],v[116:119]\n"
"ds_read_b64_tr_b16 v[138:139],v224 offset:8704\nv_mfma_f32_16x16x32_bf16 v[116:119],v[148:151],a[84:87],v[116:119]\nds_read_b64_tr_b16 v[140:141],v224 offset:12288\n"
"v_mfma_f32_16x16x32_bf16 v[116:119],v[152:155],a[88:91],v[116:119]\nds_read_b64_tr_b16 v[142:143],v224 offset:12800\nv_mfma_f32_16x16x32_bf16 v[116:119],v[156:159],a[92:95],v[116:119]\n"
"buffer_load_dwordx4 v229,s[36:39],s60 offen offset:1024 lds\nv_mfma_f32_16x16x32_bf16 v[112:115],v[156:159],a[76:79],v[112:115]\ns_cmp_ge_u32 s9,28\ns_cselect_b32 s6,s6,s7\n"
"v_mfma_f32_16x16x32_bf16 v[112:115],v[152:155],a[72:75],v[112:115]\ns_add_u32 s20,s26,s6\ns_addc_u32 s21,s27,0\ns_lshl_b32 s6,s10,2\nv_mfma_f32_16x16x32_bf16 v[112:115],v[148:151],a[68:71],v[112:115]\ns_waitcnt lgkmcnt(0)\n"
"s_and_b32 s7,s9,3\ns_add_u32 s6,s6,s7\nv_mfma_f32_16x16x32_bf16 v[112:115],v[144:147],a[64:67],v[112:115]\nbuffer_load_dwordx4 v230,s[36:39],s60 offen offset:2048 lds\nv_mfma_f32_16x16x32_bf16 v[0:3],v[128:131],a[128:131],v[0:3]\n"
"ds_read_b64_tr_b16 v[144:145],v225 offset:0\ns_lshl_b32 s6,s6,10\nv_mfma_f32_16x16x32_bf16 v[0:3],v[132:135],a[132:135],v[0:3]\nds_read_b64_tr_b16 v[146:147],v225 offset:512\ns_add_u32 s6,s6,0x42c0000\n"
"v_mfma_f32_16x16x32_bf16 v[0:3],v[136:139],a[136:139],v[0:3]\nds_read_b64_tr_b16 v[148:149],v225 offset:4096\nv_mfma_f32_16x16x32_bf16 v[0:3],v[140:143],a[140:143],v[0:3]\n"
"buffer_load_dwordx4 v231,s[36:39],s60 offen offset:3072 lds\nv_mfma_f32_16x16x32_bf16 v[4:7],v[140:143],a[156:159],v[4:7]\nds_read_b64_tr_b16 v[150:151],v225 offset:4608\ns_add_u32 s60,s51,s64\n"
"v_mfma_f32_16x16x32_bf16 v[4:7],v[136:139],a[152:155],v[4:7]\nds_read_b64_tr_b16 v[152:153],v225 offset:8192\ns_add_u32 m0,s61,49152\nv_mfma_f32_16x16x32_bf16 v[4:7],v[132:135],a[148:151],v[4:7]\n"
"ds_read_b64_tr_b16 v[154:155],v225 offset:8704\ns_waitcnt vmcnt(12)\nv_mfma_f32_16x16x32_bf16 v[4:7],v[128:131],a[144:147],v[4:7]\nbuffer_load_dwordx4 v228,s[36:39],s60 offen offset:0 lds\n"
"v_mfma_f32_16x16x32_bf16 v[8:11],v[128:131],a[160:163],v[8:11]\nds_read_b64_tr_b16 v[156:157],v225 offset:12288\nv_mfma_f32_16x16x32_bf16 v[8:11],v[132:135],a[164:167],v[8:11]\nds_read_b64_tr_b16 v[158:159],v225 offset:12800\n"
"v_mfma_f32_16x16x32_bf16 v[8:11],v[136:139],a[168:171],v[8:11]\nds_read_b128 a[208:211],v236 offset:16384\nv_mfma_f32_16x16x32_bf16 v[8:11],v[140:143],a[172:175],v[8:11]\n"
"buffer_load_dwordx4 v229,s[36:39],s60 offen offset:1024 lds\nv_mfma_f32_16x16x32_bf16 v[12:15],v[140:143],a[188:191],v[12:15]\nds_read_b128 a[212:215],v237 offset:16384\n"
"v_mfma_f32_16x16x32_bf16 v[12:15],v[136:139],a[184:187],v[12:15]\nds_read_b128 a[216:219],v238 offset:16384\nv_mfma_f32_16x16x32_bf16 v[12:15],v[132:135],a[180:183],v[12:15]\ns_waitcnt lgkmcnt(3)\n"
"ds_read_b128 a[220:223],v239 offset:16384\nv_mfma_f32_16x16x32_bf16 v[12:15],v[128:131],a[176:179],v[12:15]\nbuffer_load_dwordx4 v230,s[36:39],s60 offen offset:2048 lds\n"
"v_mfma_f32_16x16x32_bf16 v[28:31],v[144:147],a[176:179],v[28:31]\nds_read_b64_tr_b16 v[128:129],v226 offset:0\nv_mfma_f32_16x16x32_bf16 v[28:31],v[148:151],a[180:183],v[28:31]\nds_read_b64_tr_b16 v[130:131],v226 offset:512\n"
"v_mfma_f32_16x16x32_bf16 v[28:31],v[152:155],a[184:187],v[28:31]\nds_read_b64_tr_b16 v[132:133],v226 offset:4096\nv_mfma_f32_16x16x32_bf16 v[28:31],v[156:159],a[188:191],v[28:31]\n"
"buffer_load_dwordx4 v231,s[36:39],s60 offen offset:3072 lds\nv_mfma_f32_16x16x32_bf16 v[24:27],v[156:159],a[172:175],v[24:27]\nds_read_b64_tr_b16 v[134:135],v226 offset:4608\ns_waitcnt vmcnt(12)\n"
"v_mfma_f32_16x16x32_bf16 v[24:27],v[152:155],a[168:171],v[24:27]\nds_read_b64_tr_b16 v[136:137],v226 offset:8192\nv_mfma_f32_16x16x32_bf16 v[24:27],v[148:151],a[164:167],v[24:27]\nds_read_b64_tr_b16 v[138:139],v226 offset:8704\n"
"v_mfma_f32_16x16x32_bf16 v[24:27],v[144:147],a[160:163],v[24:27]\nbuffer_load_dword v235,v245,s[36:39],s65 offen offset:0\nv_mfma_f32_16x16x32_bf16 v[20:23],v[144:147],a[144:147],v[20:23]\n"
"ds_read_b64_tr_b16 v[140:141],v226 offset:12288\nv_mfma_f32_16x16x32_bf16 v[20:23],v[148:151],a[148:151],v[20:23]\nds_read_b64_tr_b16 v[142:143],v226 offset:12800\nv_mfma_f32_16x16x32_bf16 v[20:23],v[152:155],a[152:155],v[20:23]\n"
"ds_read_b128 a[224:227],v236 offset:32768\nv_mfma_f32_16x16x32_bf16 v[20:23],v[156:159],a[156:159],v[20:23]\nds_read_b128 a[228:231],v237 offset:32768\nv_mfma_f32_16x16x32_bf16 v[16:19],v[156:159],a[140:143],v[16:19]\n"
"ds_read_b128 a[232:235],v238 offset:32768\nv_mfma_f32_16x16x32_bf16 v[16:19],v[152:155],a[136:139],v[16:19]\nds_read_b128 a[236:239],v239 offset:32768\nv_mfma_f32_16x16x32_bf16 v[16:19],v[148:151],a[132:135],v[16:19]\n"
"s_waitcnt lgkmcnt(4)\ns_add_u32 s22,s26,s6\ns_addc_u32 s23,s27,0\nv_mfma_f32_16x16x32_bf16 v[16:19],v[144:147],a[128:131],v[16:19]\ns_mov_b32 s74,2048\ns_lshl_b32 s82,s17,16\ns_add_u32 s82,s82,s14\n"
"v_mfma_f32_16x16x32_bf16 v[32:35],v[128:131],a[128:131],v[32:35]\nds_read_b64_tr_b16 v[144:145],v227 offset:0\nv_mfma_f32_16x16x32_bf16 v[32:35],v[132:135],a[132:135],v[32:35]\nds_read_b64_tr_b16 v[146:147],v227 offset:512\n"
"v_mfma_f32_16x16x32_bf16 v[32:35],v[136:139],a[136:139],v[32:35]\nds_read_b64_tr_b16 v[148:149],v227 offset:4096\nv_mfma_f32_16x16x32_bf16 v[32:35],v[140:143],a[140:143],v[32:35]\n"
"buffer_load_dword v235,v246,s[36:39],s65 offen offset:0\nv_mfma_f32_16x16x32_bf16 v[36:39],v[140:143],a[156:159],v[36:39]\nds_read_b64_tr_b16 v[150:151],v227 offset:4608\ns_waitcnt vmcnt(10)\n"
"v_mfma_f32_16x16x32_bf16 v[36:39],v[136:139],a[152:155],v[36:39]\nds_read_b64_tr_b16 v[152:153],v227 offset:8192\nv_mfma_f32_16x16x32_bf16 v[36:39],v[132:135],a[148:151],v[36:39]\nds_read_b64_tr_b16 v[154:155],v227 offset:8704\n"
"v_mfma_f32_16x16x32_bf16 v[36:39],v[128:131],a[144:147],v[36:39]\nds_read_b128 a[240:243],v188 offset:0\nv_mfma_f32_16x16x32_bf16 v[40:43],v[128:131],a[160:163],v[40:43]\nds_read_b64_tr_b16 v[156:157],v227 offset:12288\n"
"v_mfma_f32_16x16x32_bf16 v[40:43],v[132:135],a[164:167],v[40:43]\nds_read_b64_tr_b16 v[158:159],v227 offset:12800\nv_mfma_f32_16x16x32_bf16 v[40:43],v[136:139],a[168:171],v[40:43]\nds_read_b128 a[244:247],v189 offset:0\n"
"v_mfma_f32_16x16x32_bf16 v[40:43],v[140:143],a[172:175],v[40:43]\nds_read_b128 a[248:251],v190 offset:0\nv_mfma_f32_16x16x32_bf16 v[44:47],v[140:143],a[188:191],v[44:47]\nbuffer_load_dword v235,v245,s[36:39],s65 offen offset:128\n"
"v_mfma_f32_16x16x32_bf16 v[44:47],v[136:139],a[184:187],v[44:47]\nds_read_b128 a[252:255],v191 offset:0\nv_mfma_f32_16x16x32_bf16 v[44:47],v[132:135],a[180:183],v[44:47]\ns_waitcnt vmcnt(23) lgkmcnt(3)\nv_lshrrev_b32 v238,7,v240\n"
"v_lshlrev_b32 v238,13,v238\nv_mfma_f32_16x16x32_bf16 v[44:47],v[128:131],a[176:179],v[44:47]\nv_and_b32 v239,64,v240\nv_lshl_add_u32 v238,v239,1,v238\nv_mfma_f32_16x16x32_bf16 v[60:63],v[144:147],a[176:179],v[60:63]\ns_barrier\n"
"v_add_u32 v238,s82,v238\nv_mfma_f32_16x16x32_bf16 v[60:63],v[148:151],a[180:183],v[60:63]\nds_read_b64_tr_b16 v[128:129],v224 offset:16384\nv_mfma_f32_16x16x32_bf16 v[60:63],v[152:155],a[184:187],v[60:63]\n"
"ds_read_b64_tr_b16 v[130:131],v224 offset:16896\nv_mfma_f32_16x16x32_bf16 v[60:63],v[156:159],a[188:191],v[60:63]\nv_mfma_f32_16x16x32_bf16 v[56:59],v[156:159],a[172:175],v[56:59]\nds_read_b64_tr_b16 v[132:133],v224 offset:20480\n"
"v_mfma_f32_16x16x32_bf16 v[56:59],v[152:155],a[168:171],v[56:59]\nds_read_b64_tr_b16 v[134:135],v224 offset:20992\nv_mfma_f32_16x16x32_bf16 v[56:59],v[148:151],a[164:167],v[56:59]\nds_read_b64_tr_b16 v[136:137],v224 offset:24576\n"
"v_mfma_f32_16x16x32_bf16 v[56:59],v[144:147],a[160:163],v[56:59]\nv_mfma_f32_16x16x32_bf16 v[52:55],v[144:147],a[144:147],v[52:55]\nds_read_b64_tr_b16 v[138:139],v224 offset:25088\n"
"v_mfma_f32_16x16x32_bf16 v[52:55],v[148:151],a[148:151],v[52:55]\nds_read_b64_tr_b16 v[140:141],v224 offset:28672\nv_mfma_f32_16x16x32_bf16 v[52:55],v[152:155],a[152:155],v[52:55]\nds_read_b64_tr_b16 v[142:143],v224 offset:29184\n"
"v_mfma_f32_16x16x32_bf16 v[52:55],v[156:159],a[156:159],v[52:55]\nbuffer_load_dword v235,v246,s[36:39],s65 offen offset:128\nv_mfma_f32_16x16x32_bf16 v[48:51],v[156:159],a[140:143],v[48:51]\ns_add_u32 s60,s48,s65\n"
"s_add_u32 m0,s61,65536\nv_mfma_f32_16x16x32_bf16 v[48:51],v[152:155],a[136:139],v[48:51]\nv_mfma_f32_16x16x32_bf16 v[48:51],v[148:151],a[132:135],v[48:51]\ns_waitcnt lgkmcnt(0)\n"
"v_mfma_f32_16x16x32_bf16 v[48:51],v[144:147],a[128:131],v[48:51]\nv_mfma_f32_16x16x32_bf16 v[64:67],v[128:131],a[128:131],v[64:67]\nds_read_b64_tr_b16 v[144:145],v225 offset:16384\n"
"v_mfma_f32_16x16x32_bf16 v[64:67],v[132:135],a[132:135],v[64:67]\nds_read_b64_tr_b16 v[146:147],v225 offset:16896\nv_mfma_f32_16x16x32_bf16 v[64:67],v[136:139],a[136:139],v[64:67]\nds_read_b64_tr_b16 v[148:149],v225 offset:20480\n"
"v_mfma_f32_16x16x32_bf16 v[64:67],v[140:143],a[140:143],v[64:67]\nv_mfma_f32_16x16x32_bf16 v[68:71],v[140:143],a[156:159],v[68:71]\nds_read_b64_tr_b16 v[150:151],v225 offset:20992\n"
"v_mfma_f32_16x16x32_bf16 v[68:71],v[136:139],a[152:155],v[68:71]\nds_read_b64_tr_b16 v[152:153],v225 offset:24576\nv_mfma_f32_16x16x32_bf16 v[68:71],v[132:135],a[148:151],v[68:71]\nds_read_b64_tr_b16 v[154:155],v225 offset:25088\n"
"v_mfma_f32_16x16x32_bf16 v[68:71],v[128:131],a[144:147],v[68:71]\nv_mfma_f32_16x16x32_bf16 v[72:75],v[128:131],a[160:163],v[72:75]\nds_read_b64_tr_b16 v[156:157],v225 offset:28672\n"
"v_mfma_f32_16x16x32_bf16 v[72:75],v[132:135],a[164:167],v[72:75]\nds_read_b64_tr_b16 v[158:159],v225 offset:29184\nv_mfma_f32_16x16x32_bf16 v[72:75],v[136:139],a[168:171],v[72:75]\n"
"buffer_load_dwordx4 v228,s[36:39],s60 offen offset:0 lds\nv_mfma_f32_16x16x32_bf16 v[72:75],v[140:143],a[172:175],v[72:75]\nv_mfma_f32_16x16x32_bf16 v[76:79],v[140:143],a[188:191],v[76:79]\n"
"v_mfma_f32_16x16x32_bf16 v[76:79],v[136:139],a[184:187],v[76:79]\nv_mfma_f32_16x16x32_bf16 v[76:79],v[132:135],a[180:183],v[76:79]\ns_waitcnt lgkmcnt(0)\nv_mfma_f32_16x16x32_bf16 v[76:79],v[128:131],a[176:179],v[76:79]\n"
"buffer_load_dwordx4 v229,s[36:39],s60 offen offset:1024 lds\nv_mfma_f32_16x16x32_bf16 v[92:95],v[144:147],a[176:179],v[92:95]\nds_read_b64_tr_b16 v[128:129],v226 offset:16384\n"
"v_mfma_f32_16x16x32_bf16 v[92:95],v[148:151],a[180:183],v[92:95]\nds_read_b64_tr_b16 v[130:131],v226 offset:16896\nv_mfma_f32_16x16x32_bf16 v[92:95],v[152:155],a[184:187],v[92:95]\nds_read_b64_tr_b16 v[132:133],v226 offset:20480\n"
"v_mfma_f32_16x16x32_bf16 v[92:95],v[156:159],a[188:191],v[92:95]\nbuffer_load_dwordx4 v230,s[36:39],s60 offen offset:2048 lds\nv_mfma_f32_16x16x32_bf16 v[88:91],v[156:159],a[172:175],v[88:91]\n"
"ds_read_b64_tr_b16 v[134:135],v226 offset:20992\nv_mfma_f32_16x16x32_bf16 v[88:91],v[152:155],a[168:171],v[88:91]\nds_read_b64_tr_b16 v[136:137],v226 offset:24576\nv_mfma_f32_16x16x32_bf16 v[88:91],v[148:151],a[164:167],v[88:91]\n"
"ds_read_b64_tr_b16 v[138:139],v226 offset:25088\nv_mfma_f32_16x16x32_bf16 v[88:91],v[144:147],a[160:163],v[88:91]\nbuffer_load_dwordx4 v231,s[36:39],s60 offen offset:3072 lds\n"
"v_mfma_f32_16x16x32_bf16 v[84:87],v[144:147],a[144:147],v[84:87]\nds_read_b64_tr_b16 v[140:141],v226 offset:28672\ns_add_u32 s60,s49,s65\nv_mfma_f32_16x16x32_bf16 v[84:87],v[148:151],a[148:151],v[84:87]\n"
"ds_read_b64_tr_b16 v[142:143],v226 offset:29184\ns_add_u32 m0,s61,81920\nv_mfma_f32_16x16x32_bf16 v[84:87],v[152:155],a[152:155],v[84:87]\nv_mfma_f32_16x16x32_bf16 v[84:87],v[156:159],a[156:159],v[84:87]\n"
"buffer_load_dwordx4 v228,s[36:39],s60 offen offset:0 lds\nv_mfma_f32_16x16x32_bf16 v[80:83],v[156:159],a[140:143],v[80:83]\nv_mfma_f32_16x16x32_bf16 v[80:83],v[152:155],a[136:139],v[80:83]\n"
"v_mfma_f32_16x16x32_bf16 v[80:83],v[148:151],a[132:135],v[80:83]\ns_waitcnt lgkmcnt(0)\nv_mfma_f32_16x16x32_bf16 v[80:83],v[144:147],a[128:131],v[80:83]\nbuffer_load_dwordx4 v229,s[36:39],s60 offen offset:1024 lds\n"
"v_mfma_f32_16x16x32_bf16 v[96:99],v[128:131],a[128:131],v[96:99]\nds_read_b64_tr_b16 v[144:145],v227 offset:16384\nv_mfma_f32_16x16x32_bf16 v[96:99],v[132:135],a[132:135],v[96:99]\nds_read_b64_tr_b16 v[146:147],v227 offset:16896\n"
"v_mfma_f32_16x16x32_bf16 v[96:99],v[136:139],a[136:139],v[96:99]\nds_read_b64_tr_b16 v[148:149],v227 offset:20480\nv_mfma_f32_16x16x32_bf16 v[96:99],v[140:143],a[140:143],v[96:99]\n"
"buffer_load_dwordx4 v230,s[36:39],s60 offen offset:2048 lds\nv_mfma_f32_16x16x32_bf16 v[100:103],v[140:143],a[156:159],v[100:103]\nds_read_b64_tr_b16 v[150:151],v227 offset:20992\n"
"v_mfma_f32_16x16x32_bf16 v[100:103],v[136:139],a[152:155],v[100:103]\nds_read_b64_tr_b16 v[152:153],v227 offset:24576\nv_mfma_f32_16x16x32_bf16 v[100:103],v[132:135],a[148:151],v[100:103]\n"
"ds_read_b64_tr_b16 v[154:155],v227 offset:25088\nv_mfma_f32_16x16x32_bf16 v[100:103],v[128:131],a[144:147],v[100:103]\nbuffer_load_dwordx4 v231,s[36:39],s60 offen offset:3072 lds\n"
"v_mfma_f32_16x16x32_bf16 v[104:107],v[128:131],a[160:163],v[104:107]\nds_read_b64_tr_b16 v[156:157],v227 offset:28672\ns_add_u32 s60,s50,s65\nv_mfma_f32_16x16x32_bf16 v[104:107],v[132:135],a[164:167],v[104:107]\n"
"ds_read_b64_tr_b16 v[158:159],v227 offset:29184\ns_add_u32 m0,s61,98304\nv_mfma_f32_16x16x32_bf16 v[104:107],v[136:139],a[168:171],v[104:107]\nv_mfma_f32_16x16x32_bf16 v[104:107],v[140:143],a[172:175],v[104:107]\n"
"buffer_load_dwordx4 v228,s[36:39],s60 offen offset:0 lds\nv_mfma_f32_16x16x32_bf16 v[108:111],v[140:143],a[188:191],v[108:111]\nv_mfma_f32_16x16x32_bf16 v[108:111],v[136:139],a[184:187],v[108:111]\n"
"v_mfma_f32_16x16x32_bf16 v[108:111],v[132:135],a[180:183],v[108:111]\ns_waitcnt vmcnt(17) lgkmcnt(0)\nv_mfma_f32_16x16x32_bf16 v[108:111],v[128:131],a[176:179],v[108:111]\n"
"buffer_load_dwordx4 v229,s[36:39],s60 offen offset:1024 lds\nv_mfma_f32_16x16x32_bf16 v[124:127],v[144:147],a[176:179],v[124:127]\ns_barrier\nv_mfma_f32_16x16x32_bf16 v[124:127],v[148:151],a[180:183],v[124:127]\n"
"ds_read_b64_tr_b16 v[128:129],v224 offset:32768\nv_mfma_f32_16x16x32_bf16 v[124:127],v[152:155],a[184:187],v[124:127]\nds_read_b64_tr_b16 v[130:131],v224 offset:33280\n"
"v_mfma_f32_16x16x32_bf16 v[124:127],v[156:159],a[188:191],v[124:127]\nbuffer_load_dwordx4 v230,s[36:39],s60 offen offset:2048 lds\nv_mfma_f32_16x16x32_bf16 v[120:123],v[156:159],a[172:175],v[120:123]\n"
"ds_read_b64_tr_b16 v[132:133],v224 offset:36864\nv_mfma_f32_16x16x32_bf16 v[120:123],v[152:155],a[168:171],v[120:123]\nds_read_b64_tr_b16 v[134:135],v224 offset:37376\n"
"v_mfma_f32_16x16x32_bf16 v[120:123],v[148:151],a[164:167],v[120:123]\nds_read_b64_tr_b16 v[136:137],v224 offset:40960\nv_mfma_f32_16x16x32_bf16 v[120:123],v[144:147],a[160:163],v[120:123]\n"
"buffer_load_dwordx4 v231,s[36:39],s60 offen offset:3072 lds\nv_mfma_f32_16x16x32_bf16 v[116:119],v[144:147],a[144:147],v[116:119]\nds_read_b64_tr_b16 v[138:139],v224 offset:41472\ns_add_u32 s60,s51,s65\n"
"v_mfma_f32_16x16x32_bf16 v[116:119],v[148:151],a[148:151],v[116:119]\nds_read_b64_tr_b16 v[140:141],v224 offset:45056\ns_add_u32 m0,s61,114688\nv_mfma_f32_16x16x32_bf16 v[116:119],v[152:155],a[152:155],v[116:119]\n"
"ds_read_b64_tr_b16 v[142:143],v224 offset:45568\nv_mfma_f32_16x16x32_bf16 v[116:119],v[156:159],a[156:159],v[116:119]\nbuffer_load_dwordx4 v228,s[36:39],s60 offen offset:0 lds\n"
"v_mfma_f32_16x16x32_bf16 v[112:115],v[156:159],a[140:143],v[112:115]\nv_mfma_f32_16x16x32_bf16 v[112:115],v[152:155],a[136:139],v[112:115]\nv_mfma_f32_16x16x32_bf16 v[112:115],v[148:151],a[132:135],v[112:115]\n"
"s_waitcnt lgkmcnt(0)\nv_mfma_f32_16x16x32_bf16 v[112:115],v[144:147],a[128:131],v[112:115]\nbuffer_load_dwordx4 v229,s[36:39],s60 offen offset:1024 lds\nv_mfma_f32_16x16x32_bf16 v[0:3],v[128:131],a[192:195],v[0:3]\n"
"ds_read_b64_tr_b16 v[144:145],v225 offset:32768\nv_mfma_f32_16x16x32_bf16 v[0:3],v[132:135],a[196:199],v[0:3]\nds_read_b64_tr_b16 v[146:147],v225 offset:33280\nv_mfma_f32_16x16x32_bf16 v[0:3],v[136:139],a[200:203],v[0:3]\n"
"ds_read_b64_tr_b16 v[148:149],v225 offset:36864\nv_mfma_f32_16x16x32_bf16 v[0:3],v[140:143],a[204:207],v[0:3]\nbuffer_load_dwordx4 v230,s[36:39],s60 offen offset:2048 lds\n"
"v_mfma_f32_16x16x32_bf16 v[4:7],v[140:143],a[220:223],v[4:7]\nds_read_b64_tr_b16 v[150:151],v225 offset:37376\nv_mfma_f32_16x16x32_bf16 v[4:7],v[136:139],a[216:219],v[4:7]\nds_read_b64_tr_b16 v[152:153],v225 offset:40960\n"
"v_mfma_f32_16x16x32_bf16 v[4:7],v[132:135],a[212:215],v[4:7]\nds_read_b64_tr_b16 v[154:155],v225 offset:41472\nv_mfma_f32_16x16x32_bf16 v[4:7],v[128:131],a[208:211],v[4:7]\n"
"buffer_load_dwordx4 v231,s[36:39],s60 offen offset:3072 lds\nv_mfma_f32_16x16x32_bf16 v[8:11],v[128:131],a[224:227],v[8:11]\nds_read_b64_tr_b16 v[156:157],v225 offset:45056\n"
"v_mfma_f32_16x16x32_bf16 v[8:11],v[132:135],a[228:231],v[8:11]\nds_read_b64_tr_b16 v[158:159],v225 offset:45568\nv_mfma_f32_16x16x32_bf16 v[8:11],v[136:139],a[232:235],v[8:11]\ns_lshl_b32 s62,s5,12\ns_add_u32 s62,s62,0x20000\n"
"v_lshrrev_b32 v233,6,v240\nv_mfma_f32_16x16x32_bf16 v[8:11],v[140:143],a[236:239],v[8:11]\ns_mov_b64 s[40:41],s[90:91]\ns_mov_b32 s42,0\ns_mov_b32 s56,0x40000000\nv_mfma_f32_16x16x32_bf16 v[12:15],v[140:143],a[252:255],v[12:15]\n"
"s_and_b32 s41,s41,0xffff\ns_mov_b32 s57,s18\nv_and_b32 v233,4,v233\nv_mfma_f32_16x16x32_bf16 v[12:15],v[136:139],a[248:251],v[12:15]\ns_lshr_b32 s71,s67,1\ns_lshl_b32 s71,s71,20\ns_add_u32 s71,s71,s73\n"
"v_mfma_f32_16x16x32_bf16 v[12:15],v[132:135],a[244:247],v[12:15]\ns_waitcnt lgkmcnt(0)\ns_bitcmp1_b32 s67,0\ns_cselect_b32 s72,s74,0\nv_mfma_f32_16x16x32_bf16 v[12:15],v[128:131],a[240:243],v[12:15]\ns_add_u32 s69,s71,s72\n"
"s_add_u32 s67,s67,1\nv_bfe_u32 v234,v240,4,3\nv_mfma_f32_16x16x32_bf16 v[28:31],v[144:147],a[240:243],v[28:31]\nds_read_b64_tr_b16 v[128:129],v226 offset:32768\nv_xor_b32 v233,v233,v234\n"
"v_mfma_f32_16x16x32_bf16 v[28:31],v[148:151],a[244:247],v[28:31]\nds_read_b64_tr_b16 v[130:131],v226 offset:33280\nv_lshlrev_b32 v233,4,v233\nv_mfma_f32_16x16x32_bf16 v[28:31],v[152:155],a[248:251],v[28:31]\n"
"ds_read_b64_tr_b16 v[132:133],v226 offset:36864\nv_lshrrev_b32 v234,7,v240\nv_mfma_f32_16x16x32_bf16 v[28:31],v[156:159],a[252:255],v[28:31]\nglobal_load_dword v235,v238,s[34:35]\n"
"v_mfma_f32_16x16x32_bf16 v[24:27],v[156:159],a[236:239],v[24:27]\nds_read_b64_tr_b16 v[134:135],v226 offset:37376\nv_lshl_add_u32 v232,v234,10,v233\nv_mfma_f32_16x16x32_bf16 v[24:27],v[152:155],a[232:235],v[24:27]\n"
"ds_read_b64_tr_b16 v[136:137],v226 offset:40960\nv_lshrrev_b32 v233,6,v240\nv_mfma_f32_16x16x32_bf16 v[24:27],v[148:151],a[228:231],v[24:27]\nds_read_b64_tr_b16 v[138:139],v226 offset:41472\nv_lshlrev_b32 v233,7,v233\n"
"v_mfma_f32_16x16x32_bf16 v[24:27],v[144:147],a[224:227],v[24:27]\nv_bfe_u32 v234,v240,7,1\nv_lshl_add_u32 v233,v234,6,v233\nv_mfma_f32_16x16x32_bf16 v[20:23],v[144:147],a[208:211],v[20:23]\n"
"ds_read_b64_tr_b16 v[140:141],v226 offset:45056\nv_and_b32 v234,48,v240\nv_mfma_f32_16x16x32_bf16 v[20:23],v[148:151],a[212:215],v[20:23]\nds_read_b64_tr_b16 v[142:143],v226 offset:45568\nv_add_u32 v233,v233,v234\n"
"v_mfma_f32_16x16x32_bf16 v[20:23],v[152:155],a[216:219],v[20:23]\nv_add_u32 v236,s62,v233\nv_xor_b32 v237,64,v236\nv_mfma_f32_16x16x32_bf16 v[20:23],v[156:159],a[220:223],v[20:23]\nv_mul_f32 v0,s12,v0\nv_mul_f32 v1,s12,v1\n"
"v_mfma_f32_16x16x32_bf16 v[16:19],v[156:159],a[204:207],v[16:19]\nv_mul_f32 v2,s12,v2\nv_mul_f32 v3,s12,v3\nv_mfma_f32_16x16x32_bf16 v[16:19],v[152:155],a[200:203],v[16:19]\nv_cvt_pk_bf16_f32 v192,v0,v1\n"
"v_cvt_pk_bf16_f32 v193,v2,v3\nv_mfma_f32_16x16x32_bf16 v[16:19],v[148:151],a[196:199],v[16:19]\ns_waitcnt lgkmcnt(0)\nv_mul_f32 v4,s12,v4\nv_mul_f32 v5,s12,v5\nv_mfma_f32_16x16x32_bf16 v[16:19],v[144:147],a[192:195],v[16:19]\n"
"v_mul_f32 v6,s12,v6\nv_mul_f32 v7,s12,v7\nv_mfma_f32_16x16x32_bf16 v[32:35],v[128:131],a[192:195],v[32:35]\nds_read_b64_tr_b16 v[144:145],v227 offset:32768\nv_cvt_pk_bf16_f32 v196,v4,v5\n"
"v_mfma_f32_16x16x32_bf16 v[32:35],v[132:135],a[196:199],v[32:35]\nds_read_b64_tr_b16 v[146:147],v227 offset:33280\nv_cvt_pk_bf16_f32 v197,v6,v7\nv_mfma_f32_16x16x32_bf16 v[32:35],v[136:139],a[200:203],v[32:35]\n"
"ds_read_b64_tr_b16 v[148:149],v227 offset:36864\nv_mul_f32 v8,s12,v8\nv_mfma_f32_16x16x32_bf16 v[32:35],v[140:143],a[204:207],v[32:35]\nv_mul_f32 v9,s12,v9\nv_mul_f32 v10,s12,v10\n"
"v_mfma_f32_16x16x32_bf16 v[36:39],v[140:143],a[220:223],v[36:39]\nds_read_b64_tr_b16 v[150:151],v227 offset:37376\nv_mul_f32 v11,s12,v11\nv_mfma_f32_16x16x32_bf16 v[36:39],v[136:139],a[216:219],v[36:39]\n"
"ds_read_b64_tr_b16 v[152:153],v227 offset:40960\nv_cvt_pk_bf16_f32 v200,v8,v9\nv_mfma_f32_16x16x32_bf16 v[36:39],v[132:135],a[212:215],v[36:39]\nds_read_b64_tr_b16 v[154:155],v227 offset:41472\nv_cvt_pk_bf16_f32 v201,v10,v11\n"
"v_mfma_f32_16x16x32_bf16 v[36:39],v[128:131],a[208:211],v[36:39]\nv_mul_f32 v12,s12,v12\nv_mul_f32 v13,s12,v13\nv_mfma_f32_16x16x32_bf16 v[40:43],v[128:131],a[224:227],v[40:43]\nds_read_b64_tr_b16 v[156:157],v227 offset:45056\n"
"v_mul_f32 v14,s12,v14\nv_mfma_f32_16x16x32_bf16 v[40:43],v[132:135],a[228:231],v[40:43]\nds_read_b64_tr_b16 v[158:159],v227 offset:45568\nv_mul_f32 v15,s12,v15\nv_mfma_f32_16x16x32_bf16 v[40:43],v[136:139],a[232:235],v[40:43]\n"
"v_cvt_pk_bf16_f32 v204,v12,v13\nv_cvt_pk_bf16_f32 v205,v14,v15\nv_mfma_f32_16x16x32_bf16 v[40:43],v[140:143],a[236:239],v[40:43]\nv_mul_f32 v28,s12,v28\nv_mul_f32 v29,s12,v29\n"
"v_mfma_f32_16x16x32_bf16 v[44:47],v[140:143],a[252:255],v[44:47]\nv_mul_f32 v30,s12,v30\nv_mul_f32 v31,s12,v31\nv_mfma_f32_16x16x32_bf16 v[44:47],v[136:139],a[248:251],v[44:47]\nv_cvt_pk_bf16_f32 v206,v28,v29\n"
"v_cvt_pk_bf16_f32 v207,v30,v31\nv_mfma_f32_16x16x32_bf16 v[44:47],v[132:135],a[244:247],v[44:47]\ns_waitcnt vmcnt(21) lgkmcnt(0)\nv_mul_f32 v24,s12,v24\nv_mul_f32 v25,s12,v25\n"
"v_mfma_f32_16x16x32_bf16 v[44:47],v[128:131],a[240:243],v[44:47]\nv_mul_f32 v26,s12,v26\nv_mul_f32 v27,s12,v27\nv_mfma_f32_16x16x32_bf16 v[60:63],v[144:147],a[240:243],v[60:63]\ns_barrier\nv_cvt_pk_bf16_f32 v202,v24,v25\n"
"v_cvt_pk_bf16_f32 v203,v26,v27\nv_mfma_f32_16x16x32_bf16 v[60:63],v[148:151],a[244:247],v[60:63]\nds_read_b64_tr_b16 v[128:129],v224 offset:49152\nv_mul_f32 v20,s12,v20\n"
"v_mfma_f32_16x16x32_bf16 v[60:63],v[152:155],a[248:251],v[60:63]\nds_read_b64_tr_b16 v[130:131],v224 offset:49664\nv_mul_f32 v21,s12,v21\nv_mfma_f32_16x16x32_bf16 v[60:63],v[156:159],a[252:255],v[60:63]\nv_mul_f32 v22,s12,v22\n"
"v_mul_f32 v23,s12,v23\nv_mfma_f32_16x16x32_bf16 v[56:59],v[156:159],a[236:239],v[56:59]\nds_read_b64_tr_b16 v[132:133],v224 offset:53248\nv_cvt_pk_bf16_f32 v198,v20,v21\n"
"v_mfma_f32_16x16x32_bf16 v[56:59],v[152:155],a[232:235],v[56:59]\nds_read_b64_tr_b16 v[134:135],v224 offset:53760\nv_cvt_pk_bf16_f32 v199,v22,v23\nv_mfma_f32_16x16x32_bf16 v[56:59],v[148:151],a[228:231],v[56:59]\n"
"ds_read_b64_tr_b16 v[136:137],v224 offset:57344\nv_mul_f32 v16,s12,v16\nv_mfma_f32_16x16x32_bf16 v[56:59],v[144:147],a[224:227],v[56:59]\nv_mul_f32 v17,s12,v17\nv_mul_f32 v18,s12,v18\n"
"v_mfma_f32_16x16x32_bf16 v[52:55],v[144:147],a[208:211],v[52:55]\nds_read_b64_tr_b16 v[138:139],v224 offset:57856\nv_mul_f32 v19,s12,v19\nv_mfma_f32_16x16x32_bf16 v[52:55],v[148:151],a[212:215],v[52:55]\n"
"ds_read_b64_tr_b16 v[140:141],v224 offset:61440\nv_cvt_pk_bf16_f32 v194,v16,v17\nv_mfma_f32_16x16x32_bf16 v[52:55],v[152:155],a[216:219],v[52:55]\nds_read_b64_tr_b16 v[142:143],v224 offset:61952\nv_cvt_pk_bf16_f32 v195,v18,v19\n"
"v_mfma_f32_16x16x32_bf16 v[52:55],v[156:159],a[220:223],v[52:55]\nbuffer_store_dwordx4 v[192:195],v240,s[44:47],s69 offen offset:0 sc1\nv_mfma_f32_16x16x32_bf16 v[48:51],v[156:159],a[204:207],v[48:51]\n"
"v_xor_b32 v238,0x10000,v238\nv_mfma_f32_16x16x32_bf16 v[48:51],v[152:155],a[200:203],v[48:51]\nv_mfma_f32_16x16x32_bf16 v[48:51],v[148:151],a[196:199],v[48:51]\ns_waitcnt lgkmcnt(0)\n"
"v_mfma_f32_16x16x32_bf16 v[48:51],v[144:147],a[192:195],v[48:51]\nglobal_load_dword v235,v238,s[34:35]\nbuffer_store_dwordx4 v[196:199],v241,s[44:47],s69 offen offset:0 sc1\n"
"buffer_store_dwordx4 v[200:203],v242,s[44:47],s69 offen offset:0 sc1\nbuffer_store_dwordx4 v[204:207],v243,s[44:47],s69 offen offset:0 sc1\n.p2align 6\nLLn_%=:\nv_mfma_f32_16x16x32_bf16 v[64:67],v[128:131],a[192:195],v[64:67]\n"
"ds_read_b64_tr_b16 v[144:145],v225 offset:49152\nv_mfma_f32_16x16x32_bf16 v[64:67],v[132:135],a[196:199],v[64:67]\nds_read_b64_tr_b16 v[146:147],v225 offset:49664\nv_xor_b32 v224,0x10000,v224\n"
"v_mfma_f32_16x16x32_bf16 v[64:67],v[136:139],a[200:203],v[64:67]\nds_read_b64_tr_b16 v[148:149],v225 offset:53248\nv_mfma_f32_16x16x32_bf16 v[64:67],v[140:143],a[204:207],v[64:67]\ns_add_u32 s65,s65,s66\ns_cmp_gt_u32 s70,1\n"
"s_cselect_b32 s75,s65,0x40000000\nv_mfma_f32_16x16x32_bf16 v[68:71],v[140:143],a[220:223],v[68:71]\nds_read_b64_tr_b16 v[150:151],v225 offset:53760\nv_mfma_f32_16x16x32_bf16 v[68:71],v[136:139],a[216:219],v[68:71]\n"
"ds_read_b64_tr_b16 v[152:153],v225 offset:57344\nv_mfma_f32_16x16x32_bf16 v[68:71],v[132:135],a[212:215],v[68:71]\nds_read_b64_tr_b16 v[154:155],v225 offset:57856\nv_mfma_f32_16x16x32_bf16 v[68:71],v[128:131],a[208:211],v[68:71]\n"
"s_cmp_eq_u32 s70,s15\ns_cselect_b32 s75,s14,s75\ns_cselect_b64 s[36:37],s[34:35],s[36:37]\nv_mfma_f32_16x16x32_bf16 v[72:75],v[128:131],a[224:227],v[72:75]\nds_read_b64_tr_b16 v[156:157],v225 offset:61440\n"
"v_mfma_f32_16x16x32_bf16 v[72:75],v[132:135],a[228:231],v[72:75]\nds_read_b64_tr_b16 v[158:159],v225 offset:61952\nv_mfma_f32_16x16x32_bf16 v[72:75],v[136:139],a[232:235],v[72:75]\ns_cmp_eq_u32 s70,s15\ns_cselect_b32 s48,0,s48\n"
"s_cselect_b32 s49,128,s49\nv_mfma_f32_16x16x32_bf16 v[72:75],v[140:143],a[236:239],v[72:75]\ns_cmp_eq_u32 s70,s15\ns_cselect_b32 s50,0x100000,s50\ns_cselect_b32 s51,0x100080,s51\n"
"v_mfma_f32_16x16x32_bf16 v[76:79],v[140:143],a[252:255],v[76:79]\ns_and_b32 s37,s37,0xffff\ns_add_u32 s60,s48,s75\ns_add_u32 m0,s61,0\nv_mfma_f32_16x16x32_bf16 v[76:79],v[136:139],a[248:251],v[76:79]\n"
"buffer_load_dwordx4 v228,s[36:39],s60 offen offset:0 lds\nv_mfma_f32_16x16x32_bf16 v[76:79],v[132:135],a[244:247],v[76:79]\ns_waitcnt lgkmcnt(0)\nv_mfma_f32_16x16x32_bf16 v[76:79],v[128:131],a[240:243],v[76:79]\n"
"s_lshr_b32 s71,s67,1\ns_lshl_b32 s71,s71,20\ns_add_u32 s71,s71,s73\nv_mfma_f32_16x16x32_bf16 v[92:95],v[144:147],a[240:243],v[92:95]\nds_read_b64_tr_b16 v[128:129],v226 offset:49152\n"
"v_mfma_f32_16x16x32_bf16 v[92:95],v[148:151],a[244:247],v[92:95]\nds_read_b64_tr_b16 v[130:131],v226 offset:49664\nv_xor_b32 v225,0x10000,v225\nv_mfma_f32_16x16x32_bf16 v[92:95],v[152:155],a[248:251],v[92:95]\n"
"ds_read_b64_tr_b16 v[132:133],v226 offset:53248\nv_mfma_f32_16x16x32_bf16 v[92:95],v[156:159],a[252:255],v[92:95]\nbuffer_load_dwordx4 v229,s[36:39],s60 offen offset:1024 lds\n"
"v_mfma_f32_16x16x32_bf16 v[88:91],v[156:159],a[236:239],v[88:91]\nds_read_b64_tr_b16 v[134:135],v226 offset:53760\nv_mul_f32 v64,s12,v64\nv_mfma_f32_16x16x32_bf16 v[88:91],v[152:155],a[232:235],v[88:91]\n"
"ds_read_b64_tr_b16 v[136:137],v226 offset:57344\nv_mul_f32 v65,s12,v65\nv_mfma_f32_16x16x32_bf16 v[88:91],v[148:151],a[228:231],v[88:91]\nds_read_b64_tr_b16 v[138:139],v226 offset:57856\nv_mul_f32 v66,s12,v66\n"
"v_mfma_f32_16x16x32_bf16 v[88:91],v[144:147],a[224:227],v[88:91]\nbuffer_load_dwordx4 v230,s[36:39],s60 offen offset:2048 lds\nv_mfma_f32_16x16x32_bf16 v[84:87],v[144:147],a[208:211],v[84:87]\n"
"ds_read_b64_tr_b16 v[140:141],v226 offset:61440\nv_mul_f32 v67,s12,v67\nv_mfma_f32_16x16x32_bf16 v[84:87],v[148:151],a[212:215],v[84:87]\nds_read_b64_tr_b16 v[142:143],v226 offset:61952\nv_cvt_pk_bf16_f32 v160,v64,v65\n"
"v_mfma_f32_16x16x32_bf16 v[84:87],v[152:155],a[216:219],v[84:87]\ns_bitcmp1_b32 s67,0\ns_cselect_b32 s72,s74,0\nv_cvt_pk_bf16_f32 v161,v66,v67\nv_mfma_f32_16x16x32_bf16 v[84:87],v[156:159],a[220:223],v[84:87]\n"
"buffer_load_dwordx4 v231,s[36:39],s60 offen offset:3072 lds\nv_mfma_f32_16x16x32_bf16 v[80:83],v[156:159],a[204:207],v[80:83]\ns_mov_b32 m0,s62\ns_add_u32 s68,s71,s72\ns_add_u32 s67,s67,1\n"
"v_mfma_f32_16x16x32_bf16 v[80:83],v[152:155],a[200:203],v[80:83]\ns_mov_b32 s60,0\ns_cmp_gt_u32 s70,2\ns_cselect_b32 s59,s65,0x40000000\nv_mfma_f32_16x16x32_bf16 v[80:83],v[148:151],a[196:199],v[80:83]\ns_waitcnt lgkmcnt(0)\n"
"v_mul_f32 v68,s12,v68\nv_mul_f32 v69,s12,v69\nv_mfma_f32_16x16x32_bf16 v[80:83],v[144:147],a[192:195],v[80:83]\nbuffer_load_dwordx4 v232,s[40:43],s60 offen offset:0 lds\n"
"v_mfma_f32_16x16x32_bf16 v[96:99],v[128:131],a[192:195],v[96:99]\nds_read_b64_tr_b16 v[144:145],v227 offset:49152\ns_mov_b32 s60,7168\nv_mfma_f32_16x16x32_bf16 v[96:99],v[132:135],a[196:199],v[96:99]\n"
"ds_read_b64_tr_b16 v[146:147],v227 offset:49664\nv_xor_b32 v226,0x10000,v226\nv_mfma_f32_16x16x32_bf16 v[96:99],v[136:139],a[200:203],v[96:99]\nds_read_b64_tr_b16 v[148:149],v227 offset:53248\nv_mul_f32 v70,s12,v70\n"
"v_mfma_f32_16x16x32_bf16 v[96:99],v[140:143],a[204:207],v[96:99]\nbuffer_load_dwordx4 v232,s[40:43],s60 offen offset:1024 lds\nv_mfma_f32_16x16x32_bf16 v[100:103],v[140:143],a[220:223],v[100:103]\n"
"ds_read_b64_tr_b16 v[150:151],v227 offset:53760\ns_mov_b32 s60,14336\nv_mfma_f32_16x16x32_bf16 v[100:103],v[136:139],a[216:219],v[100:103]\nds_read_b64_tr_b16 v[152:153],v227 offset:57344\nv_mul_f32 v71,s12,v71\n"
"v_mfma_f32_16x16x32_bf16 v[100:103],v[132:135],a[212:215],v[100:103]\nds_read_b64_tr_b16 v[154:155],v227 offset:57856\nv_cvt_pk_bf16_f32 v164,v68,v69\nv_mfma_f32_16x16x32_bf16 v[100:103],v[128:131],a[208:211],v[100:103]\n"
"buffer_load_dwordx4 v232,s[40:43],s60 offen offset:2048 lds\nv_mfma_f32_16x16x32_bf16 v[104:107],v[128:131],a[224:227],v[104:107]\nds_read_b64_tr_b16 v[156:157],v227 offset:61440\ns_mov_b32 s60,21504\n"
"v_mfma_f32_16x16x32_bf16 v[104:107],v[132:135],a[228:231],v[104:107]\nds_read_b64_tr_b16 v[158:159],v227 offset:61952\nv_cvt_pk_bf16_f32 v165,v70,v71\nv_mfma_f32_16x16x32_bf16 v[104:107],v[136:139],a[232:235],v[104:107]\n"
"v_mul_f32 v72,s12,v72\nv_mul_f32 v73,s12,v73\nv_mfma_f32_16x16x32_bf16 v[104:107],v[140:143],a[236:239],v[104:107]\nbuffer_load_dwordx4 v232,s[40:43],s60 offen offset:3072 lds\n"
"v_mfma_f32_16x16x32_bf16 v[108:111],v[140:143],a[252:255],v[108:111]\ns_add_u32 s60,s49,s75\ns_add_u32 m0,s61,16384\nv_mul_f32 v74,s12,v74\nv_mfma_f32_16x16x32_bf16 v[108:111],v[136:139],a[248:251],v[108:111]\n"
"v_mul_f32 v75,s12,v75\nv_cvt_pk_bf16_f32 v168,v72,v73\nv_mfma_f32_16x16x32_bf16 v[108:111],v[132:135],a[244:247],v[108:111]\ns_waitcnt vmcnt(26) lgkmcnt(0)\nv_cvt_pk_bf16_f32 v169,v74,v75\nv_mul_f32 v76,s12,v76\n"
"v_mfma_f32_16x16x32_bf16 v[108:111],v[128:131],a[240:243],v[108:111]\nv_mul_f32 v77,s12,v77\nv_mul_f32 v78,s12,v78\nv_mfma_f32_16x16x32_bf16 v[124:127],v[144:147],a[240:243],v[124:127]\ns_barrier\nv_mul_f32 v79,s12,v79\n"
"v_cvt_pk_bf16_f32 v172,v76,v77\nv_mfma_f32_16x16x32_bf16 v[124:127],v[148:151],a[244:247],v[124:127]\nds_read_b64_tr_b16 v[128:129],v224 offset:0\nv_xor_b32 v227,0x10000,v227\n"
"v_mfma_f32_16x16x32_bf16 v[124:127],v[152:155],a[248:251],v[124:127]\nds_read_b64_tr_b16 v[130:131],v224 offset:512\nv_cvt_pk_bf16_f32 v173,v78,v79\nv_mfma_f32_16x16x32_bf16 v[124:127],v[156:159],a[252:255],v[124:127]\n"
"v_mul_f32 v92,s12,v92\nv_mul_f32 v93,s12,v93\nv_mfma_f32_16x16x32_bf16 v[120:123],v[156:159],a[236:239],v[120:123]\nds_read_b64_tr_b16 v[132:133],v224 offset:4096\nv_mul_f32 v94,s12,v94\n"
"v_mfma_f32_16x16x32_bf16 v[120:123],v[152:155],a[232:235],v[120:123]\nds_read_b64_tr_b16 v[134:135],v224 offset:4608\nv_mul_f32 v95,s12,v95\nv_mfma_f32_16x16x32_bf16 v[120:123],v[148:151],a[228:231],v[120:123]\n"
"ds_read_b64_tr_b16 v[136:137],v224 offset:8192\nv_cvt_pk_bf16_f32 v174,v92,v93\nv_mfma_f32_16x16x32_bf16 v[120:123],v[144:147],a[224:227],v[120:123]\nv_cvt_pk_bf16_f32 v175,v94,v95\nv_mul_f32 v88,s12,v88\n"
"v_mfma_f32_16x16x32_bf16 v[116:119],v[144:147],a[208:211],v[116:119]\nds_read_b64_tr_b16 v[138:139],v224 offset:8704\nv_mul_f32 v89,s12,v89\nv_mfma_f32_16x16x32_bf16 v[116:119],v[148:151],a[212:215],v[116:119]\n"
"ds_read_b64_tr_b16 v[140:141],v224 offset:12288\nv_mul_f32 v90,s12,v90\nv_mfma_f32_16x16x32_bf16 v[116:119],v[152:155],a[216:219],v[116:119]\nds_read_b64_tr_b16 v[142:143],v224 offset:12800\nv_mul_f32 v91,s12,v91\n"
"v_mfma_f32_16x16x32_bf16 v[116:119],v[156:159],a[220:223],v[116:119]\nv_cvt_pk_bf16_f32 v170,v88,v89\nv_cvt_pk_bf16_f32 v171,v90,v91\nv_mfma_f32_16x16x32_bf16 v[112:115],v[156:159],a[204:207],v[112:115]\nv_mul_f32 v84,s12,v84\n"
"v_mul_f32 v85,s12,v85\nv_mfma_f32_16x16x32_bf16 v[112:115],v[152:155],a[200:203],v[112:115]\nv_mul_f32 v86,s12,v86\nv_mul_f32 v87,s12,v87\nv_mfma_f32_16x16x32_bf16 v[112:115],v[148:151],a[196:199],v[112:115]\n"
"s_waitcnt lgkmcnt(0)\nv_cvt_pk_bf16_f32 v166,v84,v85\nv_cvt_pk_bf16_f32 v167,v86,v87\nv_mfma_f32_16x16x32_bf16 v[112:115],v[144:147],a[192:195],v[112:115]\nv_mul_f32 v80,s12,v80\nv_mul_f32 v81,s12,v81\n"
"v_mfma_f32_16x16x32_bf16 v[64:67],v[128:131],a[0:3],0\nds_read_b64_tr_b16 v[144:145],v225 offset:0\nv_mul_f32 v82,s12,v82\nv_mfma_f32_16x16x32_bf16 v[64:67],v[132:135],a[4:7],v[64:67]\n"
"ds_read_b64_tr_b16 v[146:147],v225 offset:512\nv_mul_f32 v83,s12,v83\nv_mfma_f32_16x16x32_bf16 v[64:67],v[136:139],a[8:11],v[64:67]\nds_read_b64_tr_b16 v[148:149],v225 offset:4096\nv_cvt_pk_bf16_f32 v162,v80,v81\n"
"v_mfma_f32_16x16x32_bf16 v[64:67],v[140:143],a[12:15],v[64:67]\nbuffer_load_dwordx4 v228,s[36:39],s60 offen offset:0 lds\nv_mfma_f32_16x16x32_bf16 v[68:71],v[140:143],a[28:31],0\nds_read_b64_tr_b16 v[150:151],v225 offset:4608\n"
"v_cvt_pk_bf16_f32 v163,v82,v83\nv_mfma_f32_16x16x32_bf16 v[68:71],v[136:139],a[24:27],v[68:71]\nds_read_b64_tr_b16 v[152:153],v225 offset:8192\nv_mul_f32 v96,s12,v96\n"
"v_mfma_f32_16x16x32_bf16 v[68:71],v[132:135],a[20:23],v[68:71]\nds_read_b64_tr_b16 v[154:155],v225 offset:8704\nv_mul_f32 v97,s12,v97\nv_mfma_f32_16x16x32_bf16 v[68:71],v[128:131],a[16:19],v[68:71]\n"
"buffer_store_dwordx4 v[160:163],v240,s[44:47],s68 offen offset:0 sc1\nv_mfma_f32_16x16x32_bf16 v[72:75],v[128:131],a[32:35],0\nds_read_b64_tr_b16 v[156:157],v225 offset:12288\nv_mul_f32 v98,s12,v98\n"
"v_mfma_f32_16x16x32_bf16 v[72:75],v[132:135],a[36:39],v[72:75]\nds_read_b64_tr_b16 v[158:159],v225 offset:12800\nv_mul_f32 v99,s12,v99\nv_mfma_f32_16x16x32_bf16 v[72:75],v[136:139],a[40:43],v[72:75]\n"
"v_cvt_pk_bf16_f32 v176,v96,v97\nv_cvt_pk_bf16_f32 v177,v98,v99\nv_mfma_f32_16x16x32_bf16 v[72:75],v[140:143],a[44:47],v[72:75]\nbuffer_store_dwordx4 v[164:167],v241,s[44:47],s68 offen offset:0 sc1\n"
"v_mfma_f32_16x16x32_bf16 v[76:79],v[140:143],a[60:63],0\nv_mul_f32 v100,s12,v100\nv_mul_f32 v101,s12,v101\nv_mfma_f32_16x16x32_bf16 v[76:79],v[136:139],a[56:59],v[76:79]\nv_mul_f32 v102,s12,v102\nv_mul_f32 v103,s12,v103\n"
"v_mfma_f32_16x16x32_bf16 v[76:79],v[132:135],a[52:55],v[76:79]\ns_waitcnt lgkmcnt(0)\nv_cvt_pk_bf16_f32 v180,v100,v101\nv_cvt_pk_bf16_f32 v181,v102,v103\nv_mfma_f32_16x16x32_bf16 v[76:79],v[128:131],a[48:51],v[76:79]\n"
"v_mul_f32 v104,s12,v104\nv_mul_f32 v105,s12,v105\nv_mfma_f32_16x16x32_bf16 v[92:95],v[144:147],a[48:51],0\nds_read_b64_tr_b16 v[128:129],v226 offset:0\nv_mul_f32 v106,s12,v106\n"
"v_mfma_f32_16x16x32_bf16 v[92:95],v[148:151],a[52:55],v[92:95]\nds_read_b64_tr_b16 v[130:131],v226 offset:512\nv_mul_f32 v107,s12,v107\nv_mfma_f32_16x16x32_bf16 v[92:95],v[152:155],a[56:59],v[92:95]\n"
"ds_read_b64_tr_b16 v[132:133],v226 offset:4096\nv_cvt_pk_bf16_f32 v184,v104,v105\nv_mfma_f32_16x16x32_bf16 v[92:95],v[156:159],a[60:63],v[92:95]\nbuffer_load_dwordx4 v229,s[36:39],s60 offen offset:1024 lds\n"
"v_mfma_f32_16x16x32_bf16 v[88:91],v[156:159],a[44:47],0\nds_read_b64_tr_b16 v[134:135],v226 offset:4608\nv_cvt_pk_bf16_f32 v185,v106,v107\nv_mfma_f32_16x16x32_bf16 v[88:91],v[152:155],a[40:43],v[88:91]\n"
"ds_read_b64_tr_b16 v[136:137],v226 offset:8192\nv_mul_f32 v108,s12,v108\nv_mfma_f32_16x16x32_bf16 v[88:91],v[148:151],a[36:39],v[88:91]\nds_read_b64_tr_b16 v[138:139],v226 offset:8704\nv_mul_f32 v109,s12,v109\n"
"v_mfma_f32_16x16x32_bf16 v[88:91],v[144:147],a[32:35],v[88:91]\nbuffer_store_dwordx4 v[168:171],v242,s[44:47],s68 offen offset:0 sc1\nv_mfma_f32_16x16x32_bf16 v[84:87],v[144:147],a[16:19],0\n"
"ds_read_b64_tr_b16 v[140:141],v226 offset:12288\nv_mul_f32 v110,s12,v110\nv_mfma_f32_16x16x32_bf16 v[84:87],v[148:151],a[20:23],v[84:87]\nds_read_b64_tr_b16 v[142:143],v226 offset:12800\nv_mul_f32 v111,s12,v111\n"
"v_mfma_f32_16x16x32_bf16 v[84:87],v[152:155],a[24:27],v[84:87]\nv_cvt_pk_bf16_f32 v188,v108,v109\nv_cvt_pk_bf16_f32 v189,v110,v111\nv_mfma_f32_16x16x32_bf16 v[84:87],v[156:159],a[28:31],v[84:87]\nv_mul_f32 v124,s12,v124\n"
"v_mul_f32 v125,s12,v125\nv_mfma_f32_16x16x32_bf16 v[80:83],v[156:159],a[12:15],0\nbuffer_load_dword v250,v245,s[36:39],s59 offen offset:128\nv_mfma_f32_16x16x32_bf16 v[80:83],v[152:155],a[8:11],v[80:83]\nv_mul_f32 v126,s12,v126\n"
"v_mul_f32 v127,s12,v127\nv_mfma_f32_16x16x32_bf16 v[80:83],v[148:151],a[4:7],v[80:83]\ns_waitcnt lgkmcnt(0)\nv_cvt_pk_bf16_f32 v190,v124,v125\nv_cvt_pk_bf16_f32 v191,v126,v127\n"
"v_mfma_f32_16x16x32_bf16 v[80:83],v[144:147],a[0:3],v[80:83]\nv_mul_f32 v120,s12,v120\nv_mul_f32 v121,s12,v121\nv_mfma_f32_16x16x32_bf16 v[96:99],v[128:131],a[0:3],0\nds_read_b64_tr_b16 v[144:145],v227 offset:0\n"
"v_mul_f32 v122,s12,v122\nv_mfma_f32_16x16x32_bf16 v[96:99],v[132:135],a[4:7],v[96:99]\nds_read_b64_tr_b16 v[146:147],v227 offset:512\nv_mul_f32 v123,s12,v123\nv_mfma_f32_16x16x32_bf16 v[96:99],v[136:139],a[8:11],v[96:99]\n"
"ds_read_b64_tr_b16 v[148:149],v227 offset:4096\nv_cvt_pk_bf16_f32 v186,v120,v121\nv_mfma_f32_16x16x32_bf16 v[96:99],v[140:143],a[12:15],v[96:99]\nbuffer_load_dwordx4 v230,s[36:39],s60 offen offset:2048 lds\n"
"v_mfma_f32_16x16x32_bf16 v[100:103],v[140:143],a[28:31],0\nds_read_b64_tr_b16 v[150:151],v227 offset:4608\nv_cvt_pk_bf16_f32 v187,v122,v123\nv_mfma_f32_16x16x32_bf16 v[100:103],v[136:139],a[24:27],v[100:103]\n"
"ds_read_b64_tr_b16 v[152:153],v227 offset:8192\nv_mul_f32 v116,s12,v116\nv_mfma_f32_16x16x32_bf16 v[100:103],v[132:135],a[20:23],v[100:103]\nds_read_b64_tr_b16 v[154:155],v227 offset:8704\nv_mul_f32 v117,s12,v117\n"
"v_mfma_f32_16x16x32_bf16 v[100:103],v[128:131],a[16:19],v[100:103]\nbuffer_store_dwordx4 v[172:175],v243,s[44:47],s68 offen offset:0 sc1\nv_mfma_f32_16x16x32_bf16 v[104:107],v[128:131],a[32:35],0\n"
"ds_read_b64_tr_b16 v[156:157],v227 offset:12288\nv_mul_f32 v118,s12,v118\nv_mfma_f32_16x16x32_bf16 v[104:107],v[132:135],a[36:39],v[104:107]\nds_read_b64_tr_b16 v[158:159],v227 offset:12800\nv_mul_f32 v119,s12,v119\n"
"v_mfma_f32_16x16x32_bf16 v[104:107],v[136:139],a[40:43],v[104:107]\nv_cvt_pk_bf16_f32 v182,v116,v117\nv_cvt_pk_bf16_f32 v183,v118,v119\nv_mfma_f32_16x16x32_bf16 v[104:107],v[140:143],a[44:47],v[104:107]\nv_mul_f32 v112,s12,v112\n"
"v_mul_f32 v113,s12,v113\nv_mfma_f32_16x16x32_bf16 v[108:111],v[140:143],a[60:63],0\nbuffer_load_dword v250,v246,s[36:39],s59 offen offset:128\nv_mfma_f32_16x16x32_bf16 v[108:111],v[136:139],a[56:59],v[108:111]\n"
"v_mul_f32 v114,s12,v114\nv_mul_f32 v115,s12,v115\nv_mfma_f32_16x16x32_bf16 v[108:111],v[132:135],a[52:55],v[108:111]\ns_waitcnt vmcnt(31) lgkmcnt(0)\nv_cvt_pk_bf16_f32 v178,v112,v113\nv_cvt_pk_bf16_f32 v179,v114,v115\n"
"v_mfma_f32_16x16x32_bf16 v[108:111],v[128:131],a[48:51],v[108:111]\nv_mfma_f32_16x16x32_bf16 v[124:127],v[144:147],a[48:51],0\ns_barrier\nv_mfma_f32_16x16x32_bf16 v[124:127],v[148:151],a[52:55],v[124:127]\n"
"ds_read_b64_tr_b16 v[128:129],v224 offset:16384\nv_mfma_f32_16x16x32_bf16 v[124:127],v[152:155],a[56:59],v[124:127]\nds_read_b64_tr_b16 v[130:131],v224 offset:16896\n"
"v_mfma_f32_16x16x32_bf16 v[124:127],v[156:159],a[60:63],v[124:127]\nbuffer_load_dwordx4 v231,s[36:39],s60 offen offset:3072 lds\nv_mfma_f32_16x16x32_bf16 v[120:123],v[156:159],a[44:47],0\n"
"ds_read_b64_tr_b16 v[132:133],v224 offset:20480\ns_add_u32 s60,s50,s75\nv_mfma_f32_16x16x32_bf16 v[120:123],v[152:155],a[40:43],v[120:123]\nds_read_b64_tr_b16 v[134:135],v224 offset:20992\ns_add_u32 m0,s61,32768\n"
"v_mfma_f32_16x16x32_bf16 v[120:123],v[148:151],a[36:39],v[120:123]\nds_read_b64_tr_b16 v[136:137],v224 offset:24576\nv_mfma_f32_16x16x32_bf16 v[120:123],v[144:147],a[32:35],v[120:123]\n"
"v_mfma_f32_16x16x32_bf16 v[116:119],v[144:147],a[16:19],0\nds_read_b64_tr_b16 v[138:139],v224 offset:25088\nv_mfma_f32_16x16x32_bf16 v[116:119],v[148:151],a[20:23],v[116:119]\nds_read_b64_tr_b16 v[140:141],v224 offset:28672\n"
"v_mfma_f32_16x16x32_bf16 v[116:119],v[152:155],a[24:27],v[116:119]\nds_read_b64_tr_b16 v[142:143],v224 offset:29184\nv_mfma_f32_16x16x32_bf16 v[116:119],v[156:159],a[28:31],v[116:119]\n"
"buffer_store_dwordx4 v[176:179],v240,s[44:47],s68 offen offset:1024 sc1\nv_mfma_f32_16x16x32_bf16 v[112:115],v[156:159],a[12:15],0\nv_mfma_f32_16x16x32_bf16 v[112:115],v[152:155],a[8:11],v[112:115]\n"
"v_mfma_f32_16x16x32_bf16 v[112:115],v[148:151],a[4:7],v[112:115]\ns_waitcnt lgkmcnt(0)\nv_mfma_f32_16x16x32_bf16 v[112:115],v[144:147],a[0:3],v[112:115]\nv_mfma_f32_16x16x32_bf16 v[64:67],v[128:131],a[64:67],v[64:67]\n"
"ds_read_b64_tr_b16 v[144:145],v225 offset:16384\nv_mfma_f32_16x16x32_bf16 v[64:67],v[132:135],a[68:71],v[64:67]\nds_read_b64_tr_b16 v[146:147],v225 offset:16896\nv_mfma_f32_16x16x32_bf16 v[64:67],v[136:139],a[72:75],v[64:67]\n"
"ds_read_b64_tr_b16 v[148:149],v225 offset:20480\nv_mfma_f32_16x16x32_bf16 v[64:67],v[140:143],a[76:79],v[64:67]\nbuffer_load_dwordx4 v228,s[36:39],s60 offen offset:0 lds\n"
"v_mfma_f32_16x16x32_bf16 v[68:71],v[140:143],a[92:95],v[68:71]\nds_read_b64_tr_b16 v[150:151],v225 offset:20992\nv_mfma_f32_16x16x32_bf16 v[68:71],v[136:139],a[88:91],v[68:71]\nds_read_b64_tr_b16 v[152:153],v225 offset:24576\n"
"v_mfma_f32_16x16x32_bf16 v[68:71],v[132:135],a[84:87],v[68:71]\nds_read_b64_tr_b16 v[154:155],v225 offset:25088\nv_mfma_f32_16x16x32_bf16 v[68:71],v[128:131],a[80:83],v[68:71]\n"
"v_mfma_f32_16x16x32_bf16 v[72:75],v[128:131],a[96:99],v[72:75]\nds_read_b64_tr_b16 v[156:157],v225 offset:28672\nv_mfma_f32_16x16x32_bf16 v[72:75],v[132:135],a[100:103],v[72:75]\nds_read_b64_tr_b16 v[158:159],v225 offset:29184\n"
"v_mfma_f32_16x16x32_bf16 v[72:75],v[136:139],a[104:107],v[72:75]\nv_mfma_f32_16x16x32_bf16 v[72:75],v[140:143],a[108:111],v[72:75]\nv_mfma_f32_16x16x32_bf16 v[76:79],v[140:143],a[124:127],v[76:79]\n"
"buffer_store_dwordx4 v[180:183],v241,s[44:47],s68 offen offset:1024 sc1\nv_mfma_f32_16x16x32_bf16 v[76:79],v[136:139],a[120:123],v[76:79]\nv_mfma_f32_16x16x32_bf16 v[76:79],v[132:135],a[116:119],v[76:79]\ns_waitcnt lgkmcnt(0)\n"
"v_mfma_f32_16x16x32_bf16 v[76:79],v[128:131],a[112:115],v[76:79]\nv_mfma_f32_16x16x32_bf16 v[92:95],v[144:147],a[112:115],v[92:95]\nds_read_b64_tr_b16 v[128:129],v226 offset:16384\n"
"v_mfma_f32_16x16x32_bf16 v[92:95],v[148:151],a[116:119],v[92:95]\nds_read_b64_tr_b16 v[130:131],v226 offset:16896\nv_mfma_f32_16x16x32_bf16 v[92:95],v[152:155],a[120:123],v[92:95]\nds_read_b64_tr_b16 v[132:133],v226 offset:20480\n"
"v_mfma_f32_16x16x32_bf16 v[92:95],v[156:159],a[124:127],v[92:95]\nbuffer_load_dwordx4 v229,s[36:39],s60 offen offset:1024 lds\nv_mfma_f32_16x16x32_bf16 v[88:91],v[156:159],a[108:111],v[88:91]\n"
"ds_read_b64_tr_b16 v[134:135],v226 offset:20992\nv_mfma_f32_16x16x32_bf16 v[88:91],v[152:155],a[104:107],v[88:91]\nds_read_b64_tr_b16 v[136:137],v226 offset:24576\nv_mfma_f32_16x16x32_bf16 v[88:91],v[148:151],a[100:103],v[88:91]\n"
"ds_read_b64_tr_b16 v[138:139],v226 offset:25088\nv_mfma_f32_16x16x32_bf16 v[88:91],v[144:147],a[96:99],v[88:91]\nv_mfma_f32_16x16x32_bf16 v[84:87],v[144:147],a[80:83],v[84:87]\nds_read_b64_tr_b16 v[140:141],v226 offset:28672\n"
"v_mfma_f32_16x16x32_bf16 v[84:87],v[148:151],a[84:87],v[84:87]\nds_read_b64_tr_b16 v[142:143],v226 offset:29184\nv_mfma_f32_16x16x32_bf16 v[84:87],v[152:155],a[88:91],v[84:87]\n"
"v_mfma_f32_16x16x32_bf16 v[84:87],v[156:159],a[92:95],v[84:87]\nv_mfma_f32_16x16x32_bf16 v[80:83],v[156:159],a[76:79],v[80:83]\nv_mfma_f32_16x16x32_bf16 v[80:83],v[152:155],a[72:75],v[80:83]\n"
"v_mfma_f32_16x16x32_bf16 v[80:83],v[148:151],a[68:71],v[80:83]\ns_waitcnt lgkmcnt(0)\nv_mfma_f32_16x16x32_bf16 v[80:83],v[144:147],a[64:67],v[80:83]\nv_mfma_f32_16x16x32_bf16 v[96:99],v[128:131],a[64:67],v[96:99]\n"
"ds_read_b64_tr_b16 v[144:145],v227 offset:16384\nv_mfma_f32_16x16x32_bf16 v[96:99],v[132:135],a[68:71],v[96:99]\nds_read_b64_tr_b16 v[146:147],v227 offset:16896\nv_mfma_f32_16x16x32_bf16 v[96:99],v[136:139],a[72:75],v[96:99]\n"
"ds_read_b64_tr_b16 v[148:149],v227 offset:20480\nv_mfma_f32_16x16x32_bf16 v[96:99],v[140:143],a[76:79],v[96:99]\nbuffer_load_dwordx4 v230,s[36:39],s60 offen offset:2048 lds\n"
"v_mfma_f32_16x16x32_bf16 v[100:103],v[140:143],a[92:95],v[100:103]\nds_read_b64_tr_b16 v[150:151],v227 offset:20992\nv_mfma_f32_16x16x32_bf16 v[100:103],v[136:139],a[88:91],v[100:103]\n"
"ds_read_b64_tr_b16 v[152:153],v227 offset:24576\nv_mfma_f32_16x16x32_bf16 v[100:103],v[132:135],a[84:87],v[100:103]\nds_read_b64_tr_b16 v[154:155],v227 offset:25088\n"
"v_mfma_f32_16x16x32_bf16 v[100:103],v[128:131],a[80:83],v[100:103]\nbuffer_store_dwordx4 v[184:187],v242,s[44:47],s68 offen offset:1024 sc1\nv_mfma_f32_16x16x32_bf16 v[104:107],v[128:131],a[96:99],v[104:107]\n"
"ds_read_b64_tr_b16 v[156:157],v227 offset:28672\nv_mfma_f32_16x16x32_bf16 v[104:107],v[132:135],a[100:103],v[104:107]\nds_read_b64_tr_b16 v[158:159],v227 offset:29184\n"
"v_mfma_f32_16x16x32_bf16 v[104:107],v[136:139],a[104:107],v[104:107]\nv_mfma_f32_16x16x32_bf16 v[104:107],v[140:143],a[108:111],v[104:107]\nv_mfma_f32_16x16x32_bf16 v[108:111],v[140:143],a[124:127],v[108:111]\n"
"v_mfma_f32_16x16x32_bf16 v[108:111],v[136:139],a[120:123],v[108:111]\nv_mfma_f32_16x16x32_bf16 v[108:111],v[132:135],a[116:119],v[108:111]\ns_waitcnt vmcnt(33) lgkmcnt(0)\n"
"v_mfma_f32_16x16x32_bf16 v[108:111],v[128:131],a[112:115],v[108:111]\nv_mfma_f32_16x16x32_bf16 v[124:127],v[144:147],a[112:115],v[124:127]\ns_barrier\nv_mfma_f32_16x16x32_bf16 v[124:127],v[148:151],a[116:119],v[124:127]\n"
"ds_read_b64_tr_b16 v[128:129],v224 offset:32768\nv_mfma_f32_16x16x32_bf16 v[124:127],v[152:155],a[120:123],v[124:127]\nds_read_b64_tr_b16 v[130:131],v224 offset:33280\n"
"v_mfma_f32_16x16x32_bf16 v[124:127],v[156:159],a[124:127],v[124:127]\nbuffer_load_dwordx4 v231,s[36:39],s60 offen offset:3072 lds\nv_mfma_f32_16x16x32_bf16 v[120:123],v[156:159],a[108:111],v[120:123]\n"
"ds_read_b64_tr_b16 v[132:133],v224 offset:36864\ns_add_u32 s60,s51,s75\nv_mfma_f32_16x16x32_bf16 v[120:123],v[152:155],a[104:107],v[120:123]\nds_read_b64_tr_b16 v[134:135],v224 offset:37376\ns_add_u32 m0,s61,49152\n"
"v_mfma_f32_16x16x32_bf16 v[120:123],v[148:151],a[100:103],v[120:123]\nds_read_b64_tr_b16 v[136:137],v224 offset:40960\nv_mfma_f32_16x16x32_bf16 v[120:123],v[144:147],a[96:99],v[120:123]\n"
"buffer_store_dwordx4 v[188:191],v243,s[44:47],s68 offen offset:1024 sc1\nv_mfma_f32_16x16x32_bf16 v[116:119],v[144:147],a[80:83],v[116:119]\nds_read_b64_tr_b16 v[138:139],v224 offset:41472\n"
"v_mfma_f32_16x16x32_bf16 v[116:119],v[148:151],a[84:87],v[116:119]\nds_read_b64_tr_b16 v[140:141],v224 offset:45056\nv_mfma_f32_16x16x32_bf16 v[116:119],v[152:155],a[88:91],v[116:119]\n"
"ds_read_b64_tr_b16 v[142:143],v224 offset:45568\nv_mfma_f32_16x16x32_bf16 v[116:119],v[156:159],a[92:95],v[116:119]\nv_mfma_f32_16x16x32_bf16 v[112:115],v[156:159],a[76:79],v[112:115]\n"
"v_mfma_f32_16x16x32_bf16 v[112:115],v[152:155],a[72:75],v[112:115]\nv_mfma_f32_16x16x32_bf16 v[112:115],v[148:151],a[68:71],v[112:115]\ns_waitcnt lgkmcnt(0)\ns_waitcnt vmcnt(18)\n"
"v_mfma_f32_16x16x32_bf16 v[112:115],v[144:147],a[64:67],v[112:115]\nds_read_b64_tr_b16 v[192:193],v236 offset:0\nv_mfma_f32_16x16x32_bf16 v[64:67],v[128:131],a[128:131],v[64:67]\nds_read_b64_tr_b16 v[144:145],v225 offset:32768\n"
"v_mfma_f32_16x16x32_bf16 v[64:67],v[132:135],a[132:135],v[64:67]\nds_read_b64_tr_b16 v[146:147],v225 offset:33280\nv_mfma_f32_16x16x32_bf16 v[64:67],v[136:139],a[136:139],v[64:67]\nds_read_b64_tr_b16 v[148:149],v225 offset:36864\n"
"v_mfma_f32_16x16x32_bf16 v[64:67],v[140:143],a[140:143],v[64:67]\nbuffer_load_dwordx4 v228,s[36:39],s60 offen offset:0 lds\nv_mfma_f32_16x16x32_bf16 v[68:71],v[140:143],a[156:159],v[68:71]\n"
"ds_read_b64_tr_b16 v[150:151],v225 offset:37376\nv_mfma_f32_16x16x32_bf16 v[68:71],v[136:139],a[152:155],v[68:71]\nds_read_b64_tr_b16 v[152:153],v225 offset:40960\nv_mfma_f32_16x16x32_bf16 v[68:71],v[132:135],a[148:151],v[68:71]\n"
"ds_read_b64_tr_b16 v[154:155],v225 offset:41472\nv_mfma_f32_16x16x32_bf16 v[68:71],v[128:131],a[144:147],v[68:71]\nds_read_b64_tr_b16 v[194:195],v236 offset:2048\nv_mfma_f32_16x16x32_bf16 v[72:75],v[128:131],a[160:163],v[72:75]\n"
"ds_read_b64_tr_b16 v[156:157],v225 offset:45056\nv_mfma_f32_16x16x32_bf16 v[72:75],v[132:135],a[164:167],v[72:75]\nds_read_b64_tr_b16 v[158:159],v225 offset:45568\nv_mfma_f32_16x16x32_bf16 v[72:75],v[136:139],a[168:171],v[72:75]\n"
"ds_read_b64_tr_b16 v[196:197],v236 offset:8\nv_mfma_f32_16x16x32_bf16 v[72:75],v[140:143],a[172:175],v[72:75]\nds_read_b64_tr_b16 v[198:199],v236 offset:2056\nv_mfma_f32_16x16x32_bf16 v[76:79],v[140:143],a[188:191],v[76:79]\n"
"ds_read_b64_tr_b16 v[200:201],v237 offset:0\nv_mfma_f32_16x16x32_bf16 v[76:79],v[136:139],a[184:187],v[76:79]\nds_read_b64_tr_b16 v[202:203],v237 offset:2048\nv_mfma_f32_16x16x32_bf16 v[76:79],v[132:135],a[180:183],v[76:79]\n"
"s_waitcnt lgkmcnt(4)\nds_read_b64_tr_b16 v[204:205],v237 offset:8\nv_mfma_f32_16x16x32_bf16 v[76:79],v[128:131],a[176:179],v[76:79]\nds_read_b64_tr_b16 v[206:207],v237 offset:2056\n"
"v_mfma_f32_16x16x32_bf16 v[92:95],v[144:147],a[176:179],v[92:95]\nds_read_b64_tr_b16 v[128:129],v226 offset:32768\nv_mfma_f32_16x16x32_bf16 v[92:95],v[148:151],a[180:183],v[92:95]\nds_read_b64_tr_b16 v[130:131],v226 offset:33280\n"
"v_mfma_f32_16x16x32_bf16 v[92:95],v[152:155],a[184:187],v[92:95]\nds_read_b64_tr_b16 v[132:133],v226 offset:36864\nv_mfma_f32_16x16x32_bf16 v[92:95],v[156:159],a[188:191],v[92:95]\n"
"buffer_load_dwordx4 v229,s[36:39],s60 offen offset:1024 lds\nv_mfma_f32_16x16x32_bf16 v[88:91],v[156:159],a[172:175],v[88:91]\nds_read_b64_tr_b16 v[134:135],v226 offset:37376\n"
"v_mfma_f32_16x16x32_bf16 v[88:91],v[152:155],a[168:171],v[88:91]\nds_read_b64_tr_b16 v[136:137],v226 offset:40960\nv_mfma_f32_16x16x32_bf16 v[88:91],v[148:151],a[164:167],v[88:91]\nds_read_b64_tr_b16 v[138:139],v226 offset:41472\n"
"v_mfma_f32_16x16x32_bf16 v[88:91],v[144:147],a[160:163],v[88:91]\nv_mfma_f32_16x16x32_bf16 v[84:87],v[144:147],a[144:147],v[84:87]\nds_read_b64_tr_b16 v[140:141],v226 offset:45056\n"
"v_mfma_f32_16x16x32_bf16 v[84:87],v[148:151],a[148:151],v[84:87]\nds_read_b64_tr_b16 v[142:143],v226 offset:45568\nv_mfma_f32_16x16x32_bf16 v[84:87],v[152:155],a[152:155],v[84:87]\n"
"v_mfma_f32_16x16x32_bf16 v[84:87],v[156:159],a[156:159],v[84:87]\nv_mfma_f32_16x16x32_bf16 v[80:83],v[156:159],a[140:143],v[80:83]\nv_mfma_f32_16x16x32_bf16 v[80:83],v[152:155],a[136:139],v[80:83]\n"
"v_mfma_f32_16x16x32_bf16 v[80:83],v[148:151],a[132:135],v[80:83]\ns_waitcnt lgkmcnt(0)\nv_mfma_f32_16x16x32_bf16 v[80:83],v[144:147],a[128:131],v[80:83]\nbuffer_store_dwordx4 v[192:195],v240,s[44:47],s56 offen offset:0 sc1\n"
"v_mfma_f32_16x16x32_bf16 v[96:99],v[128:131],a[128:131],v[96:99]\nds_read_b64_tr_b16 v[144:145],v227 offset:32768\nv_mfma_f32_16x16x32_bf16 v[96:99],v[132:135],a[132:135],v[96:99]\nds_read_b64_tr_b16 v[146:147],v227 offset:33280\n"
"v_mfma_f32_16x16x32_bf16 v[96:99],v[136:139],a[136:139],v[96:99]\nds_read_b64_tr_b16 v[148:149],v227 offset:36864\nv_mfma_f32_16x16x32_bf16 v[96:99],v[140:143],a[140:143],v[96:99]\n"
"buffer_load_dwordx4 v230,s[36:39],s60 offen offset:2048 lds\nv_mfma_f32_16x16x32_bf16 v[100:103],v[140:143],a[156:159],v[100:103]\nds_read_b64_tr_b16 v[150:151],v227 offset:37376\n"
"v_mfma_f32_16x16x32_bf16 v[100:103],v[136:139],a[152:155],v[100:103]\nds_read_b64_tr_b16 v[152:153],v227 offset:40960\nv_mfma_f32_16x16x32_bf16 v[100:103],v[132:135],a[148:151],v[100:103]\n"
"ds_read_b64_tr_b16 v[154:155],v227 offset:41472\nv_mfma_f32_16x16x32_bf16 v[100:103],v[128:131],a[144:147],v[100:103]\nbuffer_store_dwordx4 v[196:199],v240,s[44:47],s56 offen offset:1024 sc1\n"
"v_mfma_f32_16x16x32_bf16 v[104:107],v[128:131],a[160:163],v[104:107]\nds_read_b64_tr_b16 v[156:157],v227 offset:45056\nv_mfma_f32_16x16x32_bf16 v[104:107],v[132:135],a[164:167],v[104:107]\n"
"ds_read_b64_tr_b16 v[158:159],v227 offset:45568\nv_mfma_f32_16x16x32_bf16 v[104:107],v[136:139],a[168:171],v[104:107]\nv_mfma_f32_16x16x32_bf16 v[104:107],v[140:143],a[172:175],v[104:107]\n"
"buffer_store_dwordx4 v[200:203],v240,s[44:47],s56 offen offset:2048 sc1\nv_mfma_f32_16x16x32_bf16 v[108:111],v[140:143],a[188:191],v[108:111]\nv_mfma_f32_16x16x32_bf16 v[108:111],v[136:139],a[184:187],v[108:111]\n"
"v_mfma_f32_16x16x32_bf16 v[108:111],v[132:135],a[180:183],v[108:111]\ns_waitcnt vmcnt(32) lgkmcnt(0)\nv_mfma_f32_16x16x32_bf16 v[108:111],v[128:131],a[176:179],v[108:111]\n"
"buffer_store_dwordx4 v[204:207],v240,s[44:47],s56 offen offset:3072 sc1\nv_mfma_f32_16x16x32_bf16 v[124:127],v[144:147],a[176:179],v[124:127]\ns_barrier\nv_mfma_f32_16x16x32_bf16 v[124:127],v[148:151],a[180:183],v[124:127]\n"
"ds_read_b64_tr_b16 v[128:129],v224 offset:49152\nv_mfma_f32_16x16x32_bf16 v[124:127],v[152:155],a[184:187],v[124:127]\nds_read_b64_tr_b16 v[130:131],v224 offset:49664\n"
"v_mfma_f32_16x16x32_bf16 v[124:127],v[156:159],a[188:191],v[124:127]\nbuffer_load_dwordx4 v231,s[36:39],s60 offen offset:3072 lds\nv_mfma_f32_16x16x32_bf16 v[120:123],v[156:159],a[172:175],v[120:123]\n"
"ds_read_b64_tr_b16 v[132:133],v224 offset:53248\ns_xor_b32 s61,s61,0x10000\nv_mfma_f32_16x16x32_bf16 v[120:123],v[152:155],a[168:171],v[120:123]\nds_read_b64_tr_b16 v[134:135],v224 offset:53760\n"
"v_mfma_f32_16x16x32_bf16 v[120:123],v[148:151],a[164:167],v[120:123]\nds_read_b64_tr_b16 v[136:137],v224 offset:57344\nv_mfma_f32_16x16x32_bf16 v[120:123],v[144:147],a[160:163],v[120:123]\ns_cmp_eq_u32 s42,0\n"
"s_cselect_b64 s[40:41],s[40:41],s[80:81]\ns_mov_b32 s56,s57\nv_mfma_f32_16x16x32_bf16 v[116:119],v[144:147],a[144:147],v[116:119]\nds_read_b64_tr_b16 v[138:139],v224 offset:57856\ns_and_b32 s41,s41,0xffff\n"
"v_mfma_f32_16x16x32_bf16 v[116:119],v[148:151],a[148:151],v[116:119]\nds_read_b64_tr_b16 v[140:141],v224 offset:61440\nv_mfma_f32_16x16x32_bf16 v[116:119],v[152:155],a[152:155],v[116:119]\n"
"ds_read_b64_tr_b16 v[142:143],v224 offset:61952\nv_mfma_f32_16x16x32_bf16 v[116:119],v[156:159],a[156:159],v[116:119]\ns_cmp_lt_u32 s56,0x40000000\ns_cselect_b32 s42,0x8000,0\ns_mov_b32 s57,s19\n"
"v_mfma_f32_16x16x32_bf16 v[112:115],v[156:159],a[140:143],v[112:115]\nv_mfma_f32_16x16x32_bf16 v[112:115],v[152:155],a[136:139],v[112:115]\nv_mfma_f32_16x16x32_bf16 v[112:115],v[148:151],a[132:135],v[112:115]\n"
"s_waitcnt lgkmcnt(0)\nv_mfma_f32_16x16x32_bf16 v[112:115],v[144:147],a[128:131],v[112:115]\ns_sub_u32 s70,s70,1\ns_cmp_lg_u32 s70,0\ns_cbranch_scc1 LLn_%=\ns_cmp_lt_u32 s9,26\ns_cbranch_scc1 LNG_%=\ns_mov_b64 s[36:37],s[24:25]\n"
"s_and_b32 s37,s37,0xffff\ns_mov_b32 s38,1024\ns_lshl_b32 s75,s5,12\ns_add_u32 s75,s75,0x1c000\ns_mov_b32 m0,s75\ns_nop 0\nbuffer_load_dwordx4 v240,s[36:39],0 offen lds\nLNG_%=:\nv_lshrrev_b32 v229,8,v240\nv_bfe_u32 v230,v240,4,4\n"
"v_xor_b32 v230,v230,v229\nv_lshlrev_b32 v230,4,v230\nv_lshl_add_u32 v232,v229,10,v230\nv_xor_b32 v233,64,v232\nv_add_u32 v233,3072,v233\nv_xor_b32 v234,128,v232\nv_add_u32 v234,6144,v234\nv_xor_b32 v235,192,v232\n"
"v_add_u32 v235,9216,v235\ns_mov_b64 s[40:41],s[30:31]\ns_mov_b32 s42,0x200000\ns_and_b32 s41,s41,0xffff\ns_mov_b32 s82,0\ns_lshl_b32 s56,s16,14\ns_add_u32 s57,s56,0x4000\ns_lshl_b32 s58,s17,14\ns_add_u32 s59,s58,0x4000\n"
"s_lshl_b32 s62,s5,12\ns_add_u32 s62,s62,0x10000\nv_mfma_f32_16x16x32_bf16 v[64:67],v[128:131],a[192:195],v[64:67]\nds_read_b64_tr_b16 v[144:145],v225 offset:49152\ns_add_u32 s60,s82,s56\n"
"v_mfma_f32_16x16x32_bf16 v[64:67],v[132:135],a[196:199],v[64:67]\nds_read_b64_tr_b16 v[146:147],v225 offset:49664\ns_add_u32 m0,s62,0\nv_mfma_f32_16x16x32_bf16 v[64:67],v[136:139],a[200:203],v[64:67]\n"
"ds_read_b64_tr_b16 v[148:149],v225 offset:53248\nv_mul_f32 v32,s12,v32\nv_mfma_f32_16x16x32_bf16 v[64:67],v[140:143],a[204:207],v[64:67]\nbuffer_load_dwordx4 v232,s[40:43],s60 offen offset:0 lds\n"
"v_mfma_f32_16x16x32_bf16 v[68:71],v[140:143],a[220:223],v[68:71]\nds_read_b64_tr_b16 v[150:151],v225 offset:53760\nv_mul_f32 v33,s12,v33\nv_mfma_f32_16x16x32_bf16 v[68:71],v[136:139],a[216:219],v[68:71]\n"
"ds_read_b64_tr_b16 v[152:153],v225 offset:57344\nv_mul_f32 v34,s12,v34\nv_mfma_f32_16x16x32_bf16 v[68:71],v[132:135],a[212:215],v[68:71]\nds_read_b64_tr_b16 v[154:155],v225 offset:57856\nv_mul_f32 v35,s12,v35\n"
"v_mfma_f32_16x16x32_bf16 v[68:71],v[128:131],a[208:211],v[68:71]\nbuffer_load_dwordx4 v233,s[40:43],s60 offen offset:1024 lds\nv_mfma_f32_16x16x32_bf16 v[72:75],v[128:131],a[224:227],v[72:75]\n"
"ds_read_b64_tr_b16 v[156:157],v225 offset:61440\nv_cvt_pk_bf16_f32 v208,v32,v33\nv_mfma_f32_16x16x32_bf16 v[72:75],v[132:135],a[228:231],v[72:75]\nds_read_b64_tr_b16 v[158:159],v225 offset:61952\nv_cvt_pk_bf16_f32 v209,v34,v35\n"
"v_mfma_f32_16x16x32_bf16 v[72:75],v[136:139],a[232:235],v[72:75]\ns_lshr_b32 s71,s67,1\ns_lshl_b32 s71,s71,20\ns_add_u32 s71,s71,s73\nv_mfma_f32_16x16x32_bf16 v[72:75],v[140:143],a[236:239],v[72:75]\n"
"buffer_load_dwordx4 v234,s[40:43],s60 offen offset:2048 lds\nv_mfma_f32_16x16x32_bf16 v[76:79],v[140:143],a[252:255],v[76:79]\ns_bitcmp1_b32 s67,0\ns_cselect_b32 s72,s74,0\nv_mul_f32 v36,s12,v36\n"
"v_mfma_f32_16x16x32_bf16 v[76:79],v[136:139],a[248:251],v[76:79]\ns_add_u32 s68,s71,s72\ns_add_u32 s67,s67,1\nv_mul_f32 v37,s12,v37\nv_mfma_f32_16x16x32_bf16 v[76:79],v[132:135],a[244:247],v[76:79]\ns_waitcnt lgkmcnt(0)\n"
"v_mul_f32 v38,s12,v38\nv_mul_f32 v39,s12,v39\nv_mfma_f32_16x16x32_bf16 v[76:79],v[128:131],a[240:243],v[76:79]\nbuffer_load_dwordx4 v235,s[40:43],s60 offen offset:3072 lds\n"
"v_mfma_f32_16x16x32_bf16 v[92:95],v[144:147],a[240:243],v[92:95]\nds_read_b64_tr_b16 v[128:129],v226 offset:49152\ns_add_u32 s60,s82,s57\nv_mfma_f32_16x16x32_bf16 v[92:95],v[148:151],a[244:247],v[92:95]\n"
"ds_read_b64_tr_b16 v[130:131],v226 offset:49664\ns_add_u32 m0,s62,16384\nv_mfma_f32_16x16x32_bf16 v[92:95],v[152:155],a[248:251],v[92:95]\nds_read_b64_tr_b16 v[132:133],v226 offset:53248\nv_cvt_pk_bf16_f32 v212,v36,v37\n"
"v_mfma_f32_16x16x32_bf16 v[92:95],v[156:159],a[252:255],v[92:95]\nbuffer_load_dwordx4 v232,s[40:43],s60 offen offset:0 lds\nv_mfma_f32_16x16x32_bf16 v[88:91],v[156:159],a[236:239],v[88:91]\n"
"ds_read_b64_tr_b16 v[134:135],v226 offset:53760\nv_cvt_pk_bf16_f32 v213,v38,v39\nv_mfma_f32_16x16x32_bf16 v[88:91],v[152:155],a[232:235],v[88:91]\nds_read_b64_tr_b16 v[136:137],v226 offset:57344\nv_mul_f32 v40,s12,v40\n"
"v_mfma_f32_16x16x32_bf16 v[88:91],v[148:151],a[228:231],v[88:91]\nds_read_b64_tr_b16 v[138:139],v226 offset:57856\nv_mul_f32 v41,s12,v41\nv_mfma_f32_16x16x32_bf16 v[88:91],v[144:147],a[224:227],v[88:91]\n"
"buffer_load_dwordx4 v233,s[40:43],s60 offen offset:1024 lds\nv_mfma_f32_16x16x32_bf16 v[84:87],v[144:147],a[208:211],v[84:87]\nds_read_b64_tr_b16 v[140:141],v226 offset:61440\nv_mul_f32 v42,s12,v42\n"
"v_mfma_f32_16x16x32_bf16 v[84:87],v[148:151],a[212:215],v[84:87]\nds_read_b64_tr_b16 v[142:143],v226 offset:61952\nv_mul_f32 v43,s12,v43\nv_mfma_f32_16x16x32_bf16 v[84:87],v[152:155],a[216:219],v[84:87]\n"
"v_cvt_pk_bf16_f32 v216,v40,v41\nv_cvt_pk_bf16_f32 v217,v42,v43\nv_mfma_f32_16x16x32_bf16 v[84:87],v[156:159],a[220:223],v[84:87]\nbuffer_load_dwordx4 v234,s[40:43],s60 offen offset:2048 lds\n"
"v_mfma_f32_16x16x32_bf16 v[80:83],v[156:159],a[204:207],v[80:83]\nv_mul_f32 v44,s12,v44\nv_mul_f32 v45,s12,v45\nv_mfma_f32_16x16x32_bf16 v[80:83],v[152:155],a[200:203],v[80:83]\nv_mul_f32 v46,s12,v46\nv_mul_f32 v47,s12,v47\n"
"v_mfma_f32_16x16x32_bf16 v[80:83],v[148:151],a[196:199],v[80:83]\ns_waitcnt lgkmcnt(0)\nv_cvt_pk_bf16_f32 v220,v44,v45\nv_cvt_pk_bf16_f32 v221,v46,v47\nv_mfma_f32_16x16x32_bf16 v[80:83],v[144:147],a[192:195],v[80:83]\n"
"buffer_load_dwordx4 v235,s[40:43],s60 offen offset:3072 lds\nv_mfma_f32_16x16x32_bf16 v[96:99],v[128:131],a[192:195],v[96:99]\nds_read_b64_tr_b16 v[144:145],v227 offset:49152\ns_add_u32 s60,s82,s58\n"
"v_mfma_f32_16x16x32_bf16 v[96:99],v[132:135],a[196:199],v[96:99]\nds_read_b64_tr_b16 v[146:147],v227 offset:49664\ns_add_u32 m0,s62,32768\nv_mfma_f32_16x16x32_bf16 v[96:99],v[136:139],a[200:203],v[96:99]\n"
"ds_read_b64_tr_b16 v[148:149],v227 offset:53248\nv_mul_f32 v60,s12,v60\nv_mfma_f32_16x16x32_bf16 v[96:99],v[140:143],a[204:207],v[96:99]\nbuffer_load_dwordx4 v232,s[40:43],s60 offen offset:0 lds\n"
"v_mfma_f32_16x16x32_bf16 v[100:103],v[140:143],a[220:223],v[100:103]\nds_read_b64_tr_b16 v[150:151],v227 offset:53760\nv_mul_f32 v61,s12,v61\nv_mfma_f32_16x16x32_bf16 v[100:103],v[136:139],a[216:219],v[100:103]\n"
"ds_read_b64_tr_b16 v[152:153],v227 offset:57344\nv_mul_f32 v62,s12,v62\nv_mfma_f32_16x16x32_bf16 v[100:103],v[132:135],a[212:215],v[100:103]\nds_read_b64_tr_b16 v[154:155],v227 offset:57856\nv_mul_f32 v63,s12,v63\n"
"v_mfma_f32_16x16x32_bf16 v[100:103],v[128:131],a[208:211],v[100:103]\nbuffer_load_dwordx4 v233,s[40:43],s60 offen offset:1024 lds\nv_mfma_f32_16x16x32_bf16 v[104:107],v[128:131],a[224:227],v[104:107]\n"
"ds_read_b64_tr_b16 v[156:157],v227 offset:61440\nv_cvt_pk_bf16_f32 v222,v60,v61\nv_mfma_f32_16x16x32_bf16 v[104:107],v[132:135],a[228:231],v[104:107]\nds_read_b64_tr_b16 v[158:159],v227 offset:61952\n"
"v_cvt_pk_bf16_f32 v223,v62,v63\nv_mfma_f32_16x16x32_bf16 v[104:107],v[136:139],a[232:235],v[104:107]\nv_mul_f32 v56,s12,v56\nv_mul_f32 v57,s12,v57\nv_mfma_f32_16x16x32_bf16 v[104:107],v[140:143],a[236:239],v[104:107]\n"
"buffer_load_dwordx4 v234,s[40:43],s60 offen offset:2048 lds\nv_mfma_f32_16x16x32_bf16 v[108:111],v[140:143],a[252:255],v[108:111]\nv_mul_f32 v58,s12,v58\nv_mul_f32 v59,s12,v59\n"
"v_mfma_f32_16x16x32_bf16 v[108:111],v[136:139],a[248:251],v[108:111]\nv_cvt_pk_bf16_f32 v218,v56,v57\nv_cvt_pk_bf16_f32 v219,v58,v59\nv_mfma_f32_16x16x32_bf16 v[108:111],v[132:135],a[244:247],v[108:111]\ns_waitcnt lgkmcnt(0)\n"
"v_mul_f32 v52,s12,v52\nv_mul_f32 v53,s12,v53\nv_mfma_f32_16x16x32_bf16 v[108:111],v[128:131],a[240:243],v[108:111]\nbuffer_load_dwordx4 v235,s[40:43],s60 offen offset:3072 lds\n"
"v_mfma_f32_16x16x32_bf16 v[124:127],v[144:147],a[240:243],v[124:127]\ns_add_u32 s60,s82,s59\ns_add_u32 m0,s62,65536\nv_mul_f32 v54,s12,v54\nv_mfma_f32_16x16x32_bf16 v[124:127],v[148:151],a[244:247],v[124:127]\n"
"v_mul_f32 v55,s12,v55\nv_cvt_pk_bf16_f32 v214,v52,v53\nv_mfma_f32_16x16x32_bf16 v[124:127],v[152:155],a[248:251],v[124:127]\nv_cvt_pk_bf16_f32 v215,v54,v55\nv_mul_f32 v48,s12,v48\n"
"v_mfma_f32_16x16x32_bf16 v[124:127],v[156:159],a[252:255],v[124:127]\nbuffer_load_dwordx4 v232,s[40:43],s60 offen offset:0 lds\nv_mfma_f32_16x16x32_bf16 v[120:123],v[156:159],a[236:239],v[120:123]\nv_mul_f32 v49,s12,v49\n"
"v_mul_f32 v50,s12,v50\nv_mfma_f32_16x16x32_bf16 v[120:123],v[152:155],a[232:235],v[120:123]\nv_mul_f32 v51,s12,v51\nv_cvt_pk_bf16_f32 v210,v48,v49\nv_mfma_f32_16x16x32_bf16 v[120:123],v[148:151],a[228:231],v[120:123]\n"
"v_cvt_pk_bf16_f32 v211,v50,v51\nv_mul_f32 v64,s12,v64\nv_mfma_f32_16x16x32_bf16 v[120:123],v[144:147],a[224:227],v[120:123]\nbuffer_load_dwordx4 v233,s[40:43],s60 offen offset:1024 lds\n"
"v_mfma_f32_16x16x32_bf16 v[116:119],v[144:147],a[208:211],v[116:119]\nv_mul_f32 v65,s12,v65\nv_mul_f32 v66,s12,v66\nv_mfma_f32_16x16x32_bf16 v[116:119],v[148:151],a[212:215],v[116:119]\nv_mul_f32 v67,s12,v67\n"
"v_cvt_pk_bf16_f32 v160,v64,v65\nv_mfma_f32_16x16x32_bf16 v[116:119],v[152:155],a[216:219],v[116:119]\nv_cvt_pk_bf16_f32 v161,v66,v67\nv_mul_f32 v68,s12,v68\nv_mfma_f32_16x16x32_bf16 v[116:119],v[156:159],a[220:223],v[116:119]\n"
"buffer_load_dwordx4 v234,s[40:43],s60 offen offset:2048 lds\nv_mfma_f32_16x16x32_bf16 v[112:115],v[156:159],a[204:207],v[112:115]\nv_mul_f32 v69,s12,v69\nv_mul_f32 v70,s12,v70\n"
"v_mfma_f32_16x16x32_bf16 v[112:115],v[152:155],a[200:203],v[112:115]\nv_mul_f32 v71,s12,v71\nv_cvt_pk_bf16_f32 v164,v68,v69\nv_mfma_f32_16x16x32_bf16 v[112:115],v[148:151],a[196:199],v[112:115]\nv_cvt_pk_bf16_f32 v165,v70,v71\n"
"v_mul_f32 v72,s12,v72\nv_mfma_f32_16x16x32_bf16 v[112:115],v[144:147],a[192:195],v[112:115]\nbuffer_load_dwordx4 v235,s[40:43],s60 offen offset:3072 lds\ns_nop 7\nv_mul_f32 v73,s12,v73\nv_mul_f32 v74,s12,v74\n"
"v_mul_f32 v75,s12,v75\nv_cvt_pk_bf16_f32 v168,v72,v73\nv_cvt_pk_bf16_f32 v169,v74,v75\nv_mul_f32 v76,s12,v76\nv_mul_f32 v77,s12,v77\nv_mul_f32 v78,s12,v78\nv_mul_f32 v79,s12,v79\nv_cvt_pk_bf16_f32 v172,v76,v77\n"
"v_cvt_pk_bf16_f32 v173,v78,v79\nv_mul_f32 v92,s12,v92\nv_mul_f32 v93,s12,v93\nv_mul_f32 v94,s12,v94\nv_mul_f32 v95,s12,v95\nv_cvt_pk_bf16_f32 v174,v92,v93\nv_cvt_pk_bf16_f32 v175,v94,v95\nv_mul_f32 v88,s12,v88\n"
"v_mul_f32 v89,s12,v89\nv_mul_f32 v90,s12,v90\nv_mul_f32 v91,s12,v91\nv_cvt_pk_bf16_f32 v170,v88,v89\nv_cvt_pk_bf16_f32 v171,v90,v91\nv_mul_f32 v84,s12,v84\nv_mul_f32 v85,s12,v85\nv_mul_f32 v86,s12,v86\nv_mul_f32 v87,s12,v87\n"
"v_cvt_pk_bf16_f32 v166,v84,v85\nv_cvt_pk_bf16_f32 v167,v86,v87\nv_mul_f32 v80,s12,v80\nv_mul_f32 v81,s12,v81\nv_mul_f32 v82,s12,v82\nv_mul_f32 v83,s12,v83\nv_cvt_pk_bf16_f32 v162,v80,v81\nv_cvt_pk_bf16_f32 v163,v82,v83\n"
"v_mul_f32 v96,s12,v96\nv_mul_f32 v97,s12,v97\nv_mul_f32 v98,s12,v98\nv_mul_f32 v99,s12,v99\nv_cvt_pk_bf16_f32 v176,v96,v97\nv_cvt_pk_bf16_f32 v177,v98,v99\nv_mul_f32 v100,s12,v100\nv_mul_f32 v101,s12,v101\n"
"v_mul_f32 v102,s12,v102\nv_mul_f32 v103,s12,v103\nv_cvt_pk_bf16_f32 v180,v100,v101\nv_cvt_pk_bf16_f32 v181,v102,v103\nv_mul_f32 v104,s12,v104\nv_mul_f32 v105,s12,v105\nv_mul_f32 v106,s12,v106\nv_mul_f32 v107,s12,v107\n"
"v_cvt_pk_bf16_f32 v184,v104,v105\nv_cvt_pk_bf16_f32 v185,v106,v107\nv_mul_f32 v108,s12,v108\nv_mul_f32 v109,s12,v109\nv_mul_f32 v110,s12,v110\nv_mul_f32 v111,s12,v111\nv_cvt_pk_bf16_f32 v188,v108,v109\n"
"v_cvt_pk_bf16_f32 v189,v110,v111\nv_mul_f32 v124,s12,v124\nv_mul_f32 v125,s12,v125\nv_mul_f32 v126,s12,v126\nv_mul_f32 v127,s12,v127\nv_cvt_pk_bf16_f32 v190,v124,v125\nv_cvt_pk_bf16_f32 v191,v126,v127\nv_mul_f32 v120,s12,v120\n"
"v_mul_f32 v121,s12,v121\nv_mul_f32 v122,s12,v122\nv_mul_f32 v123,s12,v123\nv_cvt_pk_bf16_f32 v186,v120,v121\nv_cvt_pk_bf16_f32 v187,v122,v123\nv_mul_f32 v116,s12,v116\nv_mul_f32 v117,s12,v117\nv_mul_f32 v118,s12,v118\n"
"v_mul_f32 v119,s12,v119\nv_cvt_pk_bf16_f32 v182,v116,v117\nv_cvt_pk_bf16_f32 v183,v118,v119\nv_mul_f32 v112,s12,v112\nv_mul_f32 v113,s12,v113\nv_mul_f32 v114,s12,v114\nv_mul_f32 v115,s12,v115\nv_cvt_pk_bf16_f32 v178,v112,v113\n"
"v_cvt_pk_bf16_f32 v179,v114,v115\nbuffer_store_dwordx4 v[208:211],v240,s[44:47],s69 offen offset:1024 sc1\nbuffer_store_dwordx4 v[212:215],v241,s[44:47],s69 offen offset:1024 sc1\n"
"buffer_store_dwordx4 v[216:219],v242,s[44:47],s69 offen offset:1024 sc1\nbuffer_store_dwordx4 v[220:223],v243,s[44:47],s69 offen offset:1024 sc1\nbuffer_store_dwordx4 v[160:163],v240,s[44:47],s68 offen offset:0 sc1\n"
"buffer_store_dwordx4 v[164:167],v241,s[44:47],s68 offen offset:0 sc1\nbuffer_store_dwordx4 v[168:171],v242,s[44:47],s68 offen offset:0 sc1\nbuffer_store_dwordx4 v[172:175],v243,s[44:47],s68 offen offset:0 sc1\n"
"buffer_store_dwordx4 v[176:179],v240,s[44:47],s68 offen offset:1024 sc1\nbuffer_store_dwordx4 v[180:183],v241,s[44:47],s68 offen offset:1024 sc1\nbuffer_store_dwordx4 v[184:187],v242,s[44:47],s68 offen offset:1024 sc1\n"
"buffer_store_dwordx4 v[188:191],v243,s[44:47],s68 offen offset:1024 sc1\ns_waitcnt vmcnt(28)\ns_cmp_lt_u32 s9,26\ns_cbranch_scc1 LEND_%=\nv_lshrrev_b32 v244,4,v240\nv_and_b32 v245,48,v244\nv_add_u32 v245,s75,v245\n"
"v_add_u32 v246,s52,v245\nds_read_b128 v[0:3],v246 offset:0\nds_read_b128 v[4:7],v246 offset:64\nds_read_b128 v[8:11],v246 offset:128\nds_read_b128 v[12:15],v246 offset:192\nv_add_u32 v246,s53,v245\n"
"ds_read_b128 v[16:19],v246 offset:0\nds_read_b128 v[20:23],v246 offset:64\nds_read_b128 v[24:27],v246 offset:128\nds_read_b128 v[28:31],v246 offset:192\nv_add_u32 v246,s54,v245\nds_read_b128 v[32:35],v246 offset:0\n"
"ds_read_b128 v[36:39],v246 offset:64\nds_read_b128 v[40:43],v246 offset:128\nds_read_b128 v[44:47],v246 offset:192\nv_add_u32 v246,s55,v245\nds_read_b128 v[48:51],v246 offset:0\nds_read_b128 v[52:55],v246 offset:64\n"
"ds_read_b128 v[56:59],v246 offset:128\nds_read_b128 v[60:63],v246 offset:192\ns_waitcnt lgkmcnt(0)\ns_cmp_eq_u32 s9,27\ns_cbranch_scc1 LGB_%=\nv_mfma_f32_16x16x32_bf16 v[64:67],v[0:3],a[0:3],0\n"
"v_mfma_f32_16x16x32_bf16 v[68:71],v[0:3],a[16:19],0\nv_mfma_f32_16x16x32_bf16 v[64:67],v[4:7],a[4:7],v[64:67]\nv_mfma_f32_16x16x32_bf16 v[68:71],v[4:7],a[20:23],v[68:71]\n"
"v_mfma_f32_16x16x32_bf16 v[64:67],v[8:11],a[8:11],v[64:67]\nv_mfma_f32_16x16x32_bf16 v[68:71],v[8:11],a[24:27],v[68:71]\nv_mfma_f32_16x16x32_bf16 v[64:67],v[12:15],a[12:15],v[64:67]\n"
"v_mfma_f32_16x16x32_bf16 v[68:71],v[12:15],a[28:31],v[68:71]\nv_mfma_f32_16x16x32_bf16 v[64:67],v[16:19],a[64:67],v[64:67]\nv_mfma_f32_16x16x32_bf16 v[68:71],v[16:19],a[80:83],v[68:71]\n"
"v_mfma_f32_16x16x32_bf16 v[64:67],v[20:23],a[68:71],v[64:67]\nv_mfma_f32_16x16x32_bf16 v[68:71],v[20:23],a[84:87],v[68:71]\nv_mfma_f32_16x16x32_bf16 v[64:67],v[24:27],a[72:75],v[64:67]\n"
"v_mfma_f32_16x16x32_bf16 v[68:71],v[24:27],a[88:91],v[68:71]\nv_mfma_f32_16x16x32_bf16 v[64:67],v[28:31],a[76:79],v[64:67]\nv_mfma_f32_16x16x32_bf16 v[68:71],v[28:31],a[92:95],v[68:71]\n"
"v_mfma_f32_16x16x32_bf16 v[64:67],v[32:35],a[128:131],v[64:67]\nv_mfma_f32_16x16x32_bf16 v[68:71],v[32:35],a[144:147],v[68:71]\nv_mfma_f32_16x16x32_bf16 v[64:67],v[36:39],a[132:135],v[64:67]\n"
"v_mfma_f32_16x16x32_bf16 v[68:71],v[36:39],a[148:151],v[68:71]\nv_mfma_f32_16x16x32_bf16 v[64:67],v[40:43],a[136:139],v[64:67]\nv_mfma_f32_16x16x32_bf16 v[68:71],v[40:43],a[152:155],v[68:71]\n"
"v_mfma_f32_16x16x32_bf16 v[64:67],v[44:47],a[140:143],v[64:67]\nv_mfma_f32_16x16x32_bf16 v[68:71],v[44:47],a[156:159],v[68:71]\nv_mfma_f32_16x16x32_bf16 v[64:67],v[48:51],a[192:195],v[64:67]\n"
"v_mfma_f32_16x16x32_bf16 v[68:71],v[48:51],a[208:211],v[68:71]\nv_mfma_f32_16x16x32_bf16 v[64:67],v[52:55],a[196:199],v[64:67]\nv_mfma_f32_16x16x32_bf16 v[68:71],v[52:55],a[212:215],v[68:71]\n"
"v_mfma_f32_16x16x32_bf16 v[64:67],v[56:59],a[200:203],v[64:67]\nv_mfma_f32_16x16x32_bf16 v[68:71],v[56:59],a[216:219],v[68:71]\nv_mfma_f32_16x16x32_bf16 v[64:67],v[60:63],a[204:207],v[64:67]\n"
"v_mfma_f32_16x16x32_bf16 v[68:71],v[60:63],a[220:223],v[68:71]\ns_branch LGC_%=\nLGB_%=:\nv_mfma_f32_16x16x32_bf16 v[64:67],v[0:3],a[32:35],0\nv_mfma_f32_16x16x32_bf16 v[68:71],v[0:3],a[48:51],0\n"
"v_mfma_f32_16x16x32_bf16 v[64:67],v[4:7],a[36:39],v[64:67]\nv_mfma_f32_16x16x32_bf16 v[68:71],v[4:7],a[52:55],v[68:71]\nv_mfma_f32_16x16x32_bf16 v[64:67],v[8:11],a[40:43],v[64:67]\n"
"v_mfma_f32_16x16x32_bf16 v[68:71],v[8:11],a[56:59],v[68:71]\nv_mfma_f32_16x16x32_bf16 v[64:67],v[12:15],a[44:47],v[64:67]\nv_mfma_f32_16x16x32_bf16 v[68:71],v[12:15],a[60:63],v[68:71]\n"
"v_mfma_f32_16x16x32_bf16 v[64:67],v[16:19],a[96:99],v[64:67]\nv_mfma_f32_16x16x32_bf16 v[68:71],v[16:19],a[112:115],v[68:71]\nv_mfma_f32_16x16x32_bf16 v[64:67],v[20:23],a[100:103],v[64:67]\n"
"v_mfma_f32_16x16x32_bf16 v[68:71],v[20:23],a[116:119],v[68:71]\nv_mfma_f32_16x16x32_bf16 v[64:67],v[24:27],a[104:107],v[64:67]\nv_mfma_f32_16x16x32_bf16 v[68:71],v[24:27],a[120:123],v[68:71]\n"
"v_mfma_f32_16x16x32_bf16 v[64:67],v[28:31],a[108:111],v[64:67]\nv_mfma_f32_16x16x32_bf16 v[68:71],v[28:31],a[124:127],v[68:71]\nv_mfma_f32_16x16x32_bf16 v[64:67],v[32:35],a[160:163],v[64:67]\n"
"v_mfma_f32_16x16x32_bf16 v[68:71],v[32:35],a[176:179],v[68:71]\nv_mfma_f32_16x16x32_bf16 v[64:67],v[36:39],a[164:167],v[64:67]\nv_mfma_f32_16x16x32_bf16 v[68:71],v[36:39],a[180:183],v[68:71]\n"
"v_mfma_f32_16x16x32_bf16 v[64:67],v[40:43],a[168:171],v[64:67]\nv_mfma_f32_16x16x32_bf16 v[68:71],v[40:43],a[184:187],v[68:71]\nv_mfma_f32_16x16x32_bf16 v[64:67],v[44:47],a[172:175],v[64:67]\n"
"v_mfma_f32_16x16x32_bf16 v[68:71],v[44:47],a[188:191],v[68:71]\nv_mfma_f32_16x16x32_bf16 v[64:67],v[48:51],a[224:227],v[64:67]\nv_mfma_f32_16x16x32_bf16 v[68:71],v[48:51],a[240:243],v[68:71]\n"
"v_mfma_f32_16x16x32_bf16 v[64:67],v[52:55],a[228:231],v[64:67]\nv_mfma_f32_16x16x32_bf16 v[68:71],v[52:55],a[244:247],v[68:71]\nv_mfma_f32_16x16x32_bf16 v[64:67],v[56:59],a[232:235],v[64:67]\n"
"v_mfma_f32_16x16x32_bf16 v[68:71],v[56:59],a[248:251],v[68:71]\nv_mfma_f32_16x16x32_bf16 v[64:67],v[60:63],a[236:239],v[64:67]\nv_mfma_f32_16x16x32_bf16 v[68:71],v[60:63],a[252:255],v[68:71]\nLGC_%=:\nv_and_b32 v245,16,v244\n"
"v_cmp_ne_u32 vcc,0,v245\nv_and_b32 v244,31,v244\nv_lshlrev_b32 v244,2,v244\ns_nop 4\nv_cndmask_b32 v64,v64,v68,vcc\nv_mul_f32 v64,0xbfb8aa3b,v64\nv_exp_f32 v64,v64\ns_nop 0\nv_add_f32 v64,1.0,v64\nv_rcp_f32 v64,v64\ns_nop 0\n"
"global_store_dword v244,v64,s[20:21] sc1\ns_branch LEND_%=\nLS_%=:\ns_waitcnt vmcnt(24)\ns_barrier\nds_read_b64_tr_b16 v[128:129],v224 offset:0\nds_read_b64_tr_b16 v[130:131],v224 offset:512\n"
"ds_read_b64_tr_b16 v[132:133],v224 offset:4096\nds_read_b64_tr_b16 v[134:135],v224 offset:4608\nds_read_b64_tr_b16 v[136:137],v224 offset:8192\nds_read_b64_tr_b16 v[138:139],v224 offset:8704\n"
"ds_read_b64_tr_b16 v[140:141],v224 offset:12288\nds_read_b64_tr_b16 v[142:143],v224 offset:12800\ns_waitcnt vmcnt(20)\nds_read_b128 a[0:3],v236 offset:0\nds_read_b128 a[4:7],v237 offset:0\nds_read_b128 a[8:11],v238 offset:0\n"
"ds_read_b128 a[12:15],v239 offset:0\ns_waitcnt vmcnt(16)\nds_read_b128 a[16:19],v236 offset:16384\nds_read_b128 a[20:23],v237 offset:16384\nds_read_b128 a[24:27],v238 offset:16384\nds_read_b128 a[28:31],v239 offset:16384\n"
"s_waitcnt vmcnt(12)\nds_read_b128 a[32:35],v236 offset:32768\nds_read_b128 a[36:39],v237 offset:32768\nds_read_b128 a[40:43],v238 offset:32768\nds_read_b128 a[44:47],v239 offset:32768\ns_waitcnt vmcnt(8)\n"
"ds_read_b128 a[48:51],v188 offset:0\nds_read_b128 a[52:55],v189 offset:0\nds_read_b128 a[56:59],v190 offset:0\nds_read_b128 a[60:63],v191 offset:0\ns_waitcnt lgkmcnt(0)\nv_mfma_f32_16x16x32_bf16 v[0:3],a[0:3],v[128:131],0\n"
"ds_read_b64_tr_b16 v[144:145],v225 offset:0\ns_and_b32 s71,s11,15\nv_mfma_f32_16x16x32_bf16 v[0:3],a[4:7],v[132:135],v[0:3]\nds_read_b64_tr_b16 v[146:147],v225 offset:512\ns_lshl_b32 s71,s71,5\n"
"v_mfma_f32_16x16x32_bf16 v[0:3],a[8:11],v[136:139],v[0:3]\nds_read_b64_tr_b16 v[148:149],v225 offset:4096\ns_lshl_b32 s72,s8,4\nv_mfma_f32_16x16x32_bf16 v[0:3],a[12:15],v[140:143],v[0:3]\ns_add_u32 s60,s53,s57\n"
"s_add_u32 m0,s62,49152\nv_lshrrev_b32 v245,2,v244\nv_mfma_f32_16x16x32_bf16 v[4:7],a[28:31],v[140:143],0\nds_read_b64_tr_b16 v[150:151],v225 offset:4608\nv_add_u32 v245,s71,v245\n"
"v_mfma_f32_16x16x32_bf16 v[4:7],a[24:27],v[136:139],v[4:7]\nds_read_b64_tr_b16 v[152:153],v225 offset:8192\nv_mul_lo_u32 v245,v245,s8\nv_mfma_f32_16x16x32_bf16 v[4:7],a[20:23],v[132:135],v[4:7]\n"
"ds_read_b64_tr_b16 v[154:155],v225 offset:8704\nv_add_u32 v246,s72,v245\nv_mfma_f32_16x16x32_bf16 v[4:7],a[16:19],v[128:131],v[4:7]\nbuffer_load_dwordx4 v232,s[40:43],s60 offen offset:0 lds\n"
"v_mfma_f32_16x16x32_bf16 v[8:11],a[32:35],v[128:131],0\nds_read_b64_tr_b16 v[156:157],v225 offset:12288\nv_mfma_f32_16x16x32_bf16 v[8:11],a[36:39],v[132:135],v[8:11]\nds_read_b64_tr_b16 v[158:159],v225 offset:12800\n"
"v_mfma_f32_16x16x32_bf16 v[8:11],a[40:43],v[136:139],v[8:11]\nv_mfma_f32_16x16x32_bf16 v[8:11],a[44:47],v[140:143],v[8:11]\nbuffer_load_dwordx4 v233,s[40:43],s60 offen offset:1024 lds\n"
"v_mfma_f32_16x16x32_bf16 v[12:15],a[60:63],v[140:143],0\ns_sub_u32 s88,s9,13\ns_cmp_lt_u32 s9,13\ns_cselect_b32 s88,s9,s88\nv_mfma_f32_16x16x32_bf16 v[12:15],a[56:59],v[136:139],v[12:15]\ns_cmp_lt_u32 s88,12\n"
"s_cselect_b32 s15,1,0\ns_add_u32 s70,s15,2\nv_mfma_f32_16x16x32_bf16 v[12:15],a[52:55],v[132:135],v[12:15]\ns_waitcnt lgkmcnt(0)\ns_mul_i32 s67,s88,5\ns_lshl_b32 s6,s9,2\n"
"v_mfma_f32_16x16x32_bf16 v[12:15],a[48:51],v[128:131],v[12:15]\nbuffer_load_dwordx4 v234,s[40:43],s60 offen offset:2048 lds\nv_mfma_f32_16x16x32_bf16 v[28:31],a[48:51],v[144:147],0\nds_read_b64_tr_b16 v[128:129],v226 offset:0\n"
"s_sub_u32 s6,s6,40\nv_mfma_f32_16x16x32_bf16 v[28:31],a[52:55],v[148:151],v[28:31]\nds_read_b64_tr_b16 v[130:131],v226 offset:512\nv_mfma_f32_16x16x32_bf16 v[28:31],a[56:59],v[152:155],v[28:31]\n"
"ds_read_b64_tr_b16 v[132:133],v226 offset:4096\nv_mfma_f32_16x16x32_bf16 v[28:31],a[60:63],v[156:159],v[28:31]\nbuffer_load_dwordx4 v235,s[40:43],s60 offen offset:3072 lds\nv_mfma_f32_16x16x32_bf16 v[24:27],a[44:47],v[156:159],0\n"
"ds_read_b64_tr_b16 v[134:135],v226 offset:4608\ns_add_u32 s60,s53,s58\nv_mfma_f32_16x16x32_bf16 v[24:27],a[40:43],v[152:155],v[24:27]\nds_read_b64_tr_b16 v[136:137],v226 offset:8192\ns_add_u32 m0,s62,0\n"
"v_mfma_f32_16x16x32_bf16 v[24:27],a[36:39],v[148:151],v[24:27]\nds_read_b64_tr_b16 v[138:139],v226 offset:8704\nv_mfma_f32_16x16x32_bf16 v[24:27],a[32:35],v[144:147],v[24:27]\n"
"buffer_load_dwordx4 v232,s[40:43],s60 offen offset:0 lds\nv_mfma_f32_16x16x32_bf16 v[20:23],a[16:19],v[144:147],0\nds_read_b64_tr_b16 v[140:141],v226 offset:12288\nv_mfma_f32_16x16x32_bf16 v[20:23],a[20:23],v[148:151],v[20:23]\n"
"ds_read_b64_tr_b16 v[142:143],v226 offset:12800\nv_mfma_f32_16x16x32_bf16 v[20:23],a[24:27],v[152:155],v[20:23]\ns_cmp_lt_u32 s9,26\ns_cselect_b32 s67,s67,s6\ns_lshl_b32 s73,s10,2\n"
"v_mfma_f32_16x16x32_bf16 v[20:23],a[28:31],v[156:159],v[20:23]\nbuffer_load_dwordx4 v233,s[40:43],s60 offen offset:1024 lds\nv_mfma_f32_16x16x32_bf16 v[16:19],a[12:15],v[156:159],0\ns_add_u32 s73,s73,s5\ns_lshl_b32 s73,s73,15\n"
"s_lshl_b32 s6,s13,14\nv_mfma_f32_16x16x32_bf16 v[16:19],a[8:11],v[152:155],v[16:19]\ns_add_u32 s73,s73,s6\ns_mov_b32 s12,0x3e0293ee\nv_mfma_f32_16x16x32_bf16 v[16:19],a[4:7],v[148:151],v[16:19]\ns_waitcnt lgkmcnt(0)\n"
"s_cmp_lt_u32 s9,13\ns_cselect_b32 s12,s12,0x3f800000\nv_mfma_f32_16x16x32_bf16 v[16:19],a[0:3],v[144:147],v[16:19]\nbuffer_load_dwordx4 v234,s[40:43],s60 offen offset:2048 lds\n"
"v_mfma_f32_16x16x32_bf16 v[32:35],a[0:3],v[128:131],0\nds_read_b64_tr_b16 v[144:145],v227 offset:0\ns_and_b32 s14,s4,7\nv_mfma_f32_16x16x32_bf16 v[32:35],a[4:7],v[132:135],v[32:35]\nds_read_b64_tr_b16 v[146:147],v227 offset:512\n"
"s_lshl_b32 s14,s14,2\nv_mfma_f32_16x16x32_bf16 v[32:35],a[8:11],v[136:139],v[32:35]\nds_read_b64_tr_b16 v[148:149],v227 offset:4096\ns_lshr_b32 s6,s4,3\nv_mfma_f32_16x16x32_bf16 v[32:35],a[12:15],v[140:143],v[32:35]\n"
"buffer_load_dwordx4 v235,s[40:43],s60 offen offset:3072 lds\nv_mfma_f32_16x16x32_bf16 v[36:39],a[28:31],v[140:143],0\nds_read_b64_tr_b16 v[150:151],v227 offset:4608\ns_add_u32 s60,s53,s59\n"
"v_mfma_f32_16x16x32_bf16 v[36:39],a[24:27],v[136:139],v[36:39]\nds_read_b64_tr_b16 v[152:153],v227 offset:8192\ns_add_u32 m0,s62,16384\nv_mfma_f32_16x16x32_bf16 v[36:39],a[20:23],v[132:135],v[36:39]\n"
"ds_read_b64_tr_b16 v[154:155],v227 offset:8704\ns_and_b32 s6,s6,3\nv_mfma_f32_16x16x32_bf16 v[36:39],a[16:19],v[128:131],v[36:39]\nbuffer_load_dwordx4 v232,s[40:43],s60 offen offset:0 lds\n"
"v_mfma_f32_16x16x32_bf16 v[40:43],a[32:35],v[128:131],0\nds_read_b64_tr_b16 v[156:157],v227 offset:12288\ns_waitcnt vmcnt(9)\nv_mfma_f32_16x16x32_bf16 v[40:43],a[36:39],v[132:135],v[40:43]\n"
"ds_read_b64_tr_b16 v[158:159],v227 offset:12800\ns_add_u32 s14,s14,s6\nv_mfma_f32_16x16x32_bf16 v[40:43],a[40:43],v[136:139],v[40:43]\nds_read_b128 a[64:67],v188 offset:16384\ns_lshl_b32 s14,s14,8\n"
"v_mfma_f32_16x16x32_bf16 v[40:43],a[44:47],v[140:143],v[40:43]\nbuffer_load_dwordx4 v233,s[40:43],s60 offen offset:1024 lds\nv_mfma_f32_16x16x32_bf16 v[44:47],a[60:63],v[140:143],0\nds_read_b128 a[68:71],v189 offset:16384\n"
"s_lshr_b32 s16,s4,5\nv_mfma_f32_16x16x32_bf16 v[44:47],a[56:59],v[136:139],v[44:47]\nds_read_b128 a[72:75],v190 offset:16384\ns_sub_u32 s16,15,s16\nv_mfma_f32_16x16x32_bf16 v[44:47],a[52:55],v[132:135],v[44:47]\n"
"s_waitcnt vmcnt(14) lgkmcnt(3)\nds_read_b128 a[76:79],v191 offset:16384\nv_mfma_f32_16x16x32_bf16 v[44:47],a[48:51],v[128:131],v[44:47]\nbuffer_load_dwordx4 v234,s[40:43],s60 offen offset:2048 lds\n"
"v_mfma_f32_16x16x32_bf16 v[60:63],a[48:51],v[144:147],0\ns_barrier\ns_lshl_b32 s16,s16,3\ns_and_b32 s6,s5,1\nv_mfma_f32_16x16x32_bf16 v[60:63],a[52:55],v[148:151],v[60:63]\nds_read_b64_tr_b16 v[128:129],v224 offset:16384\n"
"s_lshl_b32 s6,s6,2\nv_mfma_f32_16x16x32_bf16 v[60:63],a[56:59],v[152:155],v[60:63]\nds_read_b64_tr_b16 v[130:131],v224 offset:16896\ns_and_b32 s7,s5,2\nv_mfma_f32_16x16x32_bf16 v[60:63],a[60:63],v[156:159],v[60:63]\n"
"buffer_load_dwordx4 v235,s[40:43],s60 offen offset:3072 lds\nv_mfma_f32_16x16x32_bf16 v[56:59],a[44:47],v[156:159],0\nds_read_b64_tr_b16 v[132:133],v224 offset:20480\ns_add_u32 s60,s49,s63\n"
"v_mfma_f32_16x16x32_bf16 v[56:59],a[40:43],v[152:155],v[56:59]\nds_read_b64_tr_b16 v[134:135],v224 offset:20992\ns_add_u32 m0,s61,32768\nv_mfma_f32_16x16x32_bf16 v[56:59],a[36:39],v[148:151],v[56:59]\n"
"ds_read_b64_tr_b16 v[136:137],v224 offset:24576\ns_or_b32 s6,s6,s7\nv_mfma_f32_16x16x32_bf16 v[56:59],a[32:35],v[144:147],v[56:59]\nbuffer_load_dwordx4 v228,s[36:39],s60 offen offset:0 lds\n"
"v_mfma_f32_16x16x32_bf16 v[52:55],a[16:19],v[144:147],0\nds_read_b64_tr_b16 v[138:139],v224 offset:25088\ns_waitcnt vmcnt(9)\nv_mfma_f32_16x16x32_bf16 v[52:55],a[20:23],v[148:151],v[52:55]\n"
"ds_read_b64_tr_b16 v[140:141],v224 offset:28672\ns_add_u32 s16,s16,s6\nv_mfma_f32_16x16x32_bf16 v[52:55],a[24:27],v[152:155],v[52:55]\nds_read_b64_tr_b16 v[142:143],v224 offset:29184\ns_lshr_b32 s17,s4,5\n"
"v_mfma_f32_16x16x32_bf16 v[52:55],a[28:31],v[156:159],v[52:55]\nbuffer_load_dwordx4 v229,s[36:39],s60 offen offset:1024 lds\nv_mfma_f32_16x16x32_bf16 v[48:51],a[12:15],v[156:159],0\nds_read_b128 a[80:83],v236 offset:49152\n"
"s_lshl_b32 s17,s17,3\nv_mfma_f32_16x16x32_bf16 v[48:51],a[8:11],v[152:155],v[48:51]\nds_read_b128 a[84:87],v237 offset:49152\ns_lshl_b32 s6,s5,1\nv_mfma_f32_16x16x32_bf16 v[48:51],a[4:7],v[148:151],v[48:51]\ns_waitcnt lgkmcnt(2)\n"
"ds_read_b128 a[88:91],v238 offset:49152\nv_mfma_f32_16x16x32_bf16 v[48:51],a[0:3],v[144:147],v[48:51]\nbuffer_load_dwordx4 v230,s[36:39],s60 offen offset:2048 lds\nv_mfma_f32_16x16x32_bf16 v[64:67],a[0:3],v[128:131],0\n"
"ds_read_b64_tr_b16 v[144:145],v225 offset:16384\ns_add_u32 s17,s17,s6\nv_mfma_f32_16x16x32_bf16 v[64:67],a[4:7],v[132:135],v[64:67]\nds_read_b64_tr_b16 v[146:147],v225 offset:16896\ns_mul_i32 s89,s10,28\n"
"v_mfma_f32_16x16x32_bf16 v[64:67],a[8:11],v[136:139],v[64:67]\nds_read_b64_tr_b16 v[148:149],v225 offset:20480\ns_add_u32 s89,s89,s9\nv_mfma_f32_16x16x32_bf16 v[64:67],a[12:15],v[140:143],v[64:67]\n"
"buffer_load_dwordx4 v231,s[36:39],s60 offen offset:3072 lds\nv_mfma_f32_16x16x32_bf16 v[68:71],a[28:31],v[140:143],0\nds_read_b64_tr_b16 v[150:151],v225 offset:20992\ns_add_u32 s60,s54,s56\n"
"v_mfma_f32_16x16x32_bf16 v[68:71],a[24:27],v[136:139],v[68:71]\nds_read_b64_tr_b16 v[152:153],v225 offset:24576\ns_add_u32 m0,s62,32768\nv_mfma_f32_16x16x32_bf16 v[68:71],a[20:23],v[132:135],v[68:71]\n"
"ds_read_b64_tr_b16 v[154:155],v225 offset:25088\ns_lshl_b32 s89,s89,2\nv_mfma_f32_16x16x32_bf16 v[68:71],a[16:19],v[128:131],v[68:71]\nbuffer_load_dwordx4 v232,s[40:43],s60 offen offset:0 lds\n"
"v_mfma_f32_16x16x32_bf16 v[72:75],a[32:35],v[128:131],0\nds_read_b64_tr_b16 v[156:157],v225 offset:28672\ns_add_u32 s89,s89,s5\nv_mfma_f32_16x16x32_bf16 v[72:75],a[36:39],v[132:135],v[72:75]\n"
"ds_read_b64_tr_b16 v[158:159],v225 offset:29184\ns_and_b32 s89,s89,0x3ff\nv_mfma_f32_16x16x32_bf16 v[72:75],a[40:43],v[136:139],v[72:75]\nds_read_b128 a[92:95],v239 offset:49152\ns_lshr_b32 s83,0x249,s88\n"
"v_mfma_f32_16x16x32_bf16 v[72:75],a[44:47],v[140:143],v[72:75]\nbuffer_load_dwordx4 v233,s[40:43],s60 offen offset:1024 lds\nv_mfma_f32_16x16x32_bf16 v[76:79],a[60:63],v[140:143],0\ns_waitcnt vmcnt(10)\ns_and_b32 s83,s83,1\n"
"s_lshl_b32 s6,s10,3\nv_mfma_f32_16x16x32_bf16 v[76:79],a[56:59],v[136:139],v[76:79]\nds_read_b128 a[96:99],v236 offset:0\ns_lshl_b32 s7,s13,2\nv_mfma_f32_16x16x32_bf16 v[76:79],a[52:55],v[132:135],v[76:79]\ns_waitcnt lgkmcnt(2)\n"
"ds_read_b128 a[100:103],v237 offset:0\nv_mfma_f32_16x16x32_bf16 v[76:79],a[48:51],v[128:131],v[76:79]\nbuffer_load_dwordx4 v234,s[40:43],s60 offen offset:2048 lds\nv_mfma_f32_16x16x32_bf16 v[92:95],a[48:51],v[144:147],0\n"
"ds_read_b64_tr_b16 v[128:129],v226 offset:16384\ns_add_u32 s6,s6,s7\nv_mfma_f32_16x16x32_bf16 v[92:95],a[52:55],v[148:151],v[92:95]\nds_read_b64_tr_b16 v[130:131],v226 offset:16896\ns_mul_i32 s7,s88,11\n"
"v_mfma_f32_16x16x32_bf16 v[92:95],a[56:59],v[152:155],v[92:95]\nds_read_b64_tr_b16 v[132:133],v226 offset:20480\ns_lshr_b32 s7,s7,5\nv_mfma_f32_16x16x32_bf16 v[92:95],a[60:63],v[156:159],v[92:95]\n"
"buffer_load_dwordx4 v235,s[40:43],s60 offen offset:3072 lds\nv_mfma_f32_16x16x32_bf16 v[88:91],a[44:47],v[156:159],0\nds_read_b64_tr_b16 v[134:135],v226 offset:20992\ns_add_u32 s60,s49,s64\n"
"v_mfma_f32_16x16x32_bf16 v[88:91],a[40:43],v[152:155],v[88:91]\nds_read_b64_tr_b16 v[136:137],v226 offset:24576\ns_add_u32 m0,s61,49152\nv_mfma_f32_16x16x32_bf16 v[88:91],a[36:39],v[148:151],v[88:91]\n"
"ds_read_b64_tr_b16 v[138:139],v226 offset:25088\ns_add_u32 s6,s6,s7\nv_mfma_f32_16x16x32_bf16 v[88:91],a[32:35],v[144:147],v[88:91]\nbuffer_load_dwordx4 v228,s[36:39],s60 offen offset:0 lds\n"
"v_mfma_f32_16x16x32_bf16 v[84:87],a[16:19],v[144:147],0\nds_read_b64_tr_b16 v[140:141],v226 offset:28672\ns_lshl_b32 s6,s6,2\nv_mfma_f32_16x16x32_bf16 v[84:87],a[20:23],v[148:151],v[84:87]\n"
"ds_read_b64_tr_b16 v[142:143],v226 offset:29184\ns_add_u32 s6,s6,s5\nv_mfma_f32_16x16x32_bf16 v[84:87],a[24:27],v[152:155],v[84:87]\nds_read_b128 a[104:107],v238 offset:0\ns_add_u32 s6,s6,0x380\n"
"v_mfma_f32_16x16x32_bf16 v[84:87],a[28:31],v[156:159],v[84:87]\nbuffer_load_dwordx4 v229,s[36:39],s60 offen offset:1024 lds\nv_mfma_f32_16x16x32_bf16 v[80:83],a[12:15],v[156:159],0\nds_read_b128 a[108:111],v239 offset:0\n"
"s_waitcnt vmcnt(10)\nv_mfma_f32_16x16x32_bf16 v[80:83],a[8:11],v[152:155],v[80:83]\nds_read_b128 a[112:115],v236 offset:16384\nv_mfma_f32_16x16x32_bf16 v[80:83],a[4:7],v[148:151],v[80:83]\ns_waitcnt lgkmcnt(3)\n"
"ds_read_b128 a[116:119],v237 offset:16384\nv_mfma_f32_16x16x32_bf16 v[80:83],a[0:3],v[144:147],v[80:83]\nbuffer_load_dwordx4 v230,s[36:39],s60 offen offset:2048 lds\nv_mfma_f32_16x16x32_bf16 v[96:99],a[0:3],v[128:131],0\n"
"ds_read_b64_tr_b16 v[144:145],v227 offset:16384\nv_mfma_f32_16x16x32_bf16 v[96:99],a[4:7],v[132:135],v[96:99]\nds_read_b64_tr_b16 v[146:147],v227 offset:16896\nv_mfma_f32_16x16x32_bf16 v[96:99],a[8:11],v[136:139],v[96:99]\n"
"ds_read_b64_tr_b16 v[148:149],v227 offset:20480\nv_mfma_f32_16x16x32_bf16 v[96:99],a[12:15],v[140:143],v[96:99]\nbuffer_load_dwordx4 v231,s[36:39],s60 offen offset:3072 lds\n"
"v_mfma_f32_16x16x32_bf16 v[100:103],a[28:31],v[140:143],0\nds_read_b64_tr_b16 v[150:151],v227 offset:20992\ns_add_u32 s60,s54,s57\nv_mfma_f32_16x16x32_bf16 v[100:103],a[24:27],v[136:139],v[100:103]\n"
"ds_read_b64_tr_b16 v[152:153],v227 offset:24576\ns_add_u32 m0,s62,65536\nv_mfma_f32_16x16x32_bf16 v[100:103],a[20:23],v[132:135],v[100:103]\nds_read_b64_tr_b16 v[154:155],v227 offset:25088\n"
"v_mfma_f32_16x16x32_bf16 v[100:103],a[16:19],v[128:131],v[100:103]\nbuffer_load_dwordx4 v232,s[40:43],s60 offen offset:0 lds\nv_mfma_f32_16x16x32_bf16 v[104:107],a[32:35],v[128:131],0\n"
"ds_read_b64_tr_b16 v[156:157],v227 offset:28672\nv_mfma_f32_16x16x32_bf16 v[104:107],a[36:39],v[132:135],v[104:107]\nds_read_b64_tr_b16 v[158:159],v227 offset:29184\n"
"v_mfma_f32_16x16x32_bf16 v[104:107],a[40:43],v[136:139],v[104:107]\nds_read_b128 a[120:123],v238 offset:16384\nv_mfma_f32_16x16x32_bf16 v[104:107],a[44:47],v[140:143],v[104:107]\n"
"buffer_load_dwordx4 v233,s[40:43],s60 offen offset:1024 lds\nv_mfma_f32_16x16x32_bf16 v[108:111],a[60:63],v[140:143],0\nds_read_b128 a[124:127],v239 offset:16384\n"
"v_mfma_f32_16x16x32_bf16 v[108:111],a[56:59],v[136:139],v[108:111]\ns_cmp_eq_u32 s83,1\ns_cselect_b32 s83,s6,0x7d0\ns_and_b32 s18,s89,7\nv_mfma_f32_16x16x32_bf16 v[108:111],a[52:55],v[132:135],v[108:111]\n"
"s_waitcnt vmcnt(10) lgkmcnt(2)\ns_lshr_b32 s18,s18,1\ns_mul_i32 s18,s18,0x90\nv_mfma_f32_16x16x32_bf16 v[108:111],a[48:51],v[128:131],v[108:111]\nbuffer_load_dwordx4 v234,s[40:43],s60 offen offset:2048 lds\n"
"v_mfma_f32_16x16x32_bf16 v[124:127],a[48:51],v[144:147],0\ns_barrier\ns_lshr_b32 s6,s89,3\ns_add_u32 s18,s18,s6\nv_mfma_f32_16x16x32_bf16 v[124:127],a[52:55],v[148:151],v[124:127]\nds_read_b64_tr_b16 v[128:129],v224 offset:32768\n"
"s_lshl_b32 s18,s18,13\nv_mfma_f32_16x16x32_bf16 v[124:127],a[56:59],v[152:155],v[124:127]\nds_read_b64_tr_b16 v[130:131],v224 offset:33280\ns_and_b32 s6,s89,1\nv_mfma_f32_16x16x32_bf16 v[124:127],a[60:63],v[156:159],v[124:127]\n"
"buffer_load_dwordx4 v235,s[40:43],s60 offen offset:3072 lds\nv_mfma_f32_16x16x32_bf16 v[120:123],a[44:47],v[156:159],0\nds_read_b64_tr_b16 v[132:133],v224 offset:36864\ns_add_u32 s60,s54,s58\n"
"v_mfma_f32_16x16x32_bf16 v[120:123],a[40:43],v[152:155],v[120:123]\nds_read_b64_tr_b16 v[134:135],v224 offset:37376\ns_add_u32 m0,s62,81920\nv_mfma_f32_16x16x32_bf16 v[120:123],a[36:39],v[148:151],v[120:123]\n"
"ds_read_b64_tr_b16 v[136:137],v224 offset:40960\ns_lshl_b32 s6,s6,12\nv_mfma_f32_16x16x32_bf16 v[120:123],a[32:35],v[144:147],v[120:123]\nbuffer_load_dwordx4 v232,s[40:43],s60 offen offset:0 lds\n"
"v_mfma_f32_16x16x32_bf16 v[116:119],a[16:19],v[144:147],0\nds_read_b64_tr_b16 v[138:139],v224 offset:41472\ns_add_u32 s18,s18,s6\nv_mfma_f32_16x16x32_bf16 v[116:119],a[20:23],v[148:151],v[116:119]\n"
"ds_read_b64_tr_b16 v[140:141],v224 offset:45056\ns_add_u32 s18,s18,0x2400000\nv_mfma_f32_16x16x32_bf16 v[116:119],a[24:27],v[152:155],v[116:119]\nds_read_b64_tr_b16 v[142:143],v224 offset:45568\ns_and_b32 s19,s83,7\n"
"v_mfma_f32_16x16x32_bf16 v[116:119],a[28:31],v[156:159],v[116:119]\nbuffer_load_dwordx4 v233,s[40:43],s60 offen offset:1024 lds\nv_mfma_f32_16x16x32_bf16 v[112:115],a[12:15],v[156:159],0\ns_lshr_b32 s19,s19,1\n"
"s_mul_i32 s19,s19,0x90\ns_lshr_b32 s6,s83,3\nv_mfma_f32_16x16x32_bf16 v[112:115],a[8:11],v[152:155],v[112:115]\ns_add_u32 s19,s19,s6\ns_lshl_b32 s19,s19,13\ns_and_b32 s6,s83,1\n"
"v_mfma_f32_16x16x32_bf16 v[112:115],a[4:7],v[148:151],v[112:115]\ns_waitcnt lgkmcnt(0)\ns_lshl_b32 s6,s6,12\ns_add_u32 s19,s19,s6\nv_mfma_f32_16x16x32_bf16 v[112:115],a[0:3],v[144:147],v[112:115]\n"
"buffer_load_dwordx4 v234,s[40:43],s60 offen offset:2048 lds\nv_mfma_f32_16x16x32_bf16 v[0:3],a[64:67],v[128:131],v[0:3]\nds_read_b64_tr_b16 v[144:145],v225 offset:32768\ns_add_u32 s19,s19,0x2400000\n"
"v_mfma_f32_16x16x32_bf16 v[0:3],a[68:71],v[132:135],v[0:3]\nds_read_b64_tr_b16 v[146:147],v225 offset:33280\nv_mfma_f32_16x16x32_bf16 v[0:3],a[72:75],v[136:139],v[0:3]\nds_read_b64_tr_b16 v[148:149],v225 offset:36864\n"
"v_mfma_f32_16x16x32_bf16 v[0:3],a[76:79],v[140:143],v[0:3]\nbuffer_load_dwordx4 v235,s[40:43],s60 offen offset:3072 lds\nv_mfma_f32_16x16x32_bf16 v[4:7],a[92:95],v[140:143],v[4:7]\nds_read_b64_tr_b16 v[150:151],v225 offset:37376\n"
"s_add_u32 s60,s54,s59\nv_mfma_f32_16x16x32_bf16 v[4:7],a[88:91],v[136:139],v[4:7]\nds_read_b64_tr_b16 v[152:153],v225 offset:40960\ns_add_u32 m0,s62,49152\nv_mfma_f32_16x16x32_bf16 v[4:7],a[84:87],v[132:135],v[4:7]\n"
"ds_read_b64_tr_b16 v[154:155],v225 offset:41472\ns_waitcnt vmcnt(12)\nv_mfma_f32_16x16x32_bf16 v[4:7],a[80:83],v[128:131],v[4:7]\nbuffer_load_dwordx4 v232,s[40:43],s60 offen offset:0 lds\n"
"v_mfma_f32_16x16x32_bf16 v[8:11],a[96:99],v[128:131],v[8:11]\nds_read_b64_tr_b16 v[156:157],v225 offset:45056\nv_mfma_f32_16x16x32_bf16 v[8:11],a[100:103],v[132:135],v[8:11]\nds_read_b64_tr_b16 v[158:159],v225 offset:45568\n"
"v_mfma_f32_16x16x32_bf16 v[8:11],a[104:107],v[136:139],v[8:11]\nds_read_b128 a[128:131],v236 offset:32768\nv_mfma_f32_16x16x32_bf16 v[8:11],a[108:111],v[140:143],v[8:11]\n"
"buffer_load_dwordx4 v233,s[40:43],s60 offen offset:1024 lds\nv_mfma_f32_16x16x32_bf16 v[12:15],a[124:127],v[140:143],v[12:15]\nds_read_b128 a[132:135],v237 offset:32768\n"
"v_mfma_f32_16x16x32_bf16 v[12:15],a[120:123],v[136:139],v[12:15]\nds_read_b128 a[136:139],v238 offset:32768\nv_mfma_f32_16x16x32_bf16 v[12:15],a[116:119],v[132:135],v[12:15]\ns_waitcnt lgkmcnt(3)\n"
"ds_read_b128 a[140:143],v239 offset:32768\nv_mfma_f32_16x16x32_bf16 v[12:15],a[112:115],v[128:131],v[12:15]\nbuffer_load_dwordx4 v234,s[40:43],s60 offen offset:2048 lds\n"
"v_mfma_f32_16x16x32_bf16 v[28:31],a[112:115],v[144:147],v[28:31]\nds_read_b64_tr_b16 v[128:129],v226 offset:32768\nv_mfma_f32_16x16x32_bf16 v[28:31],a[116:119],v[148:151],v[28:31]\nds_read_b64_tr_b16 v[130:131],v226 offset:33280\n"
"v_mfma_f32_16x16x32_bf16 v[28:31],a[120:123],v[152:155],v[28:31]\nds_read_b64_tr_b16 v[132:133],v226 offset:36864\nv_mfma_f32_16x16x32_bf16 v[28:31],a[124:127],v[156:159],v[28:31]\n"
"buffer_load_dwordx4 v235,s[40:43],s60 offen offset:3072 lds\nv_mfma_f32_16x16x32_bf16 v[24:27],a[108:111],v[156:159],v[24:27]\nds_read_b64_tr_b16 v[134:135],v226 offset:37376\ns_add_u32 s60,s50,s63\n"
"v_mfma_f32_16x16x32_bf16 v[24:27],a[104:107],v[152:155],v[24:27]\nds_read_b64_tr_b16 v[136:137],v226 offset:40960\ns_add_u32 m0,s61,0\nv_mfma_f32_16x16x32_bf16 v[24:27],a[100:103],v[148:151],v[24:27]\n"
"ds_read_b64_tr_b16 v[138:139],v226 offset:41472\nv_mfma_f32_16x16x32_bf16 v[24:27],a[96:99],v[144:147],v[24:27]\nbuffer_load_dwordx4 v228,s[36:39],s60 offen offset:0 lds\n"
"v_mfma_f32_16x16x32_bf16 v[20:23],a[80:83],v[144:147],v[20:23]\nds_read_b64_tr_b16 v[140:141],v226 offset:45056\nv_mfma_f32_16x16x32_bf16 v[20:23],a[84:87],v[148:151],v[20:23]\nds_read_b64_tr_b16 v[142:143],v226 offset:45568\n"
"v_mfma_f32_16x16x32_bf16 v[20:23],a[88:91],v[152:155],v[20:23]\ns_cmp_lt_u32 s83,0x480\ns_cselect_b32 s19,s19,0x40000000\nv_mfma_f32_16x16x32_bf16 v[20:23],a[92:95],v[156:159],v[20:23]\n"
"buffer_load_dwordx4 v229,s[36:39],s60 offen offset:1024 lds\nv_mfma_f32_16x16x32_bf16 v[16:19],a[76:79],v[156:159],v[16:19]\ns_cmp_lt_u32 s83,1024\ns_cselect_b64 s[80:81],s[90:91],s[80:81]\ns_lshr_b32 s6,s83,3\n"
"v_mfma_f32_16x16x32_bf16 v[16:19],a[72:75],v[152:155],v[16:19]\ns_cmp_lt_u32 s83,0x400\ns_cselect_b32 s7,0x7f,15\ns_and_b32 s6,s6,s7\nv_mfma_f32_16x16x32_bf16 v[16:19],a[68:71],v[148:151],v[16:19]\ns_waitcnt lgkmcnt(0)\n"
"s_lshl_b32 s6,s6,15\ns_and_b32 s7,s83,7\nv_mfma_f32_16x16x32_bf16 v[16:19],a[64:67],v[144:147],v[16:19]\nbuffer_load_dwordx4 v230,s[36:39],s60 offen offset:2048 lds\nv_mfma_f32_16x16x32_bf16 v[32:35],a[64:67],v[128:131],v[32:35]\n"
"ds_read_b64_tr_b16 v[144:145],v227 offset:32768\ns_lshl_b32 s7,s7,7\nv_mfma_f32_16x16x32_bf16 v[32:35],a[68:71],v[132:135],v[32:35]\nds_read_b64_tr_b16 v[146:147],v227 offset:33280\ns_add_u32 s6,s6,s7\n"
"v_mfma_f32_16x16x32_bf16 v[32:35],a[72:75],v[136:139],v[32:35]\nds_read_b64_tr_b16 v[148:149],v227 offset:36864\nv_mfma_f32_16x16x32_bf16 v[32:35],a[76:79],v[140:143],v[32:35]\n"
"buffer_load_dwordx4 v231,s[36:39],s60 offen offset:3072 lds\nv_mfma_f32_16x16x32_bf16 v[36:39],a[92:95],v[140:143],v[36:39]\nds_read_b64_tr_b16 v[150:151],v227 offset:37376\ns_add_u32 s60,s55,s56\n"
"v_mfma_f32_16x16x32_bf16 v[36:39],a[88:91],v[136:139],v[36:39]\nds_read_b64_tr_b16 v[152:153],v227 offset:40960\ns_add_u32 m0,s62,0\nv_mfma_f32_16x16x32_bf16 v[36:39],a[84:87],v[132:135],v[36:39]\n"
"ds_read_b64_tr_b16 v[154:155],v227 offset:41472\ns_waitcnt vmcnt(12)\nv_mfma_f32_16x16x32_bf16 v[36:39],a[80:83],v[128:131],v[36:39]\nbuffer_load_dwordx4 v232,s[40:43],s60 offen offset:0 lds\n"
"v_mfma_f32_16x16x32_bf16 v[40:43],a[96:99],v[128:131],v[40:43]\nds_read_b64_tr_b16 v[156:157],v227 offset:45056\nv_mfma_f32_16x16x32_bf16 v[40:43],a[100:103],v[132:135],v[40:43]\nds_read_b64_tr_b16 v[158:159],v227 offset:45568\n"
"v_mfma_f32_16x16x32_bf16 v[40:43],a[104:107],v[136:139],v[40:43]\nds_read_b128 a[144:147],v188 offset:0\nv_mfma_f32_16x16x32_bf16 v[40:43],a[108:111],v[140:143],v[40:43]\n"
"buffer_load_dwordx4 v233,s[40:43],s60 offen offset:1024 lds\nv_mfma_f32_16x16x32_bf16 v[44:47],a[124:127],v[140:143],v[44:47]\nds_read_b128 a[148:151],v189 offset:0\n"
"v_mfma_f32_16x16x32_bf16 v[44:47],a[120:123],v[136:139],v[44:47]\nds_read_b128 a[152:155],v190 offset:0\nv_mfma_f32_16x16x32_bf16 v[44:47],a[116:119],v[132:135],v[44:47]\ns_waitcnt vmcnt(18) lgkmcnt(3)\n"
"ds_read_b128 a[156:159],v191 offset:0\nv_mfma_f32_16x16x32_bf16 v[44:47],a[112:115],v[128:131],v[44:47]\nbuffer_load_dwordx4 v234,s[40:43],s60 offen offset:2048 lds\n"
"v_mfma_f32_16x16x32_bf16 v[60:63],a[112:115],v[144:147],v[60:63]\ns_barrier\ns_add_u32 s80,s80,s6\ns_addc_u32 s81,s81,0\nv_mfma_f32_16x16x32_bf16 v[60:63],a[116:119],v[148:151],v[60:63]\n"
"ds_read_b64_tr_b16 v[128:129],v224 offset:49152\ns_lshr_b32 s6,s89,3\nv_mfma_f32_16x16x32_bf16 v[60:63],a[120:123],v[152:155],v[60:63]\nds_read_b64_tr_b16 v[130:131],v224 offset:49664\ns_lshl_b32 s6,s6,15\n"
"v_mfma_f32_16x16x32_bf16 v[60:63],a[124:127],v[156:159],v[60:63]\nbuffer_load_dwordx4 v235,s[40:43],s60 offen offset:3072 lds\nv_mfma_f32_16x16x32_bf16 v[56:59],a[108:111],v[156:159],v[56:59]\n"
"ds_read_b64_tr_b16 v[132:133],v224 offset:53248\ns_add_u32 s60,s50,s64\nv_mfma_f32_16x16x32_bf16 v[56:59],a[104:107],v[152:155],v[56:59]\nds_read_b64_tr_b16 v[134:135],v224 offset:53760\ns_add_u32 m0,s61,16384\n"
"v_mfma_f32_16x16x32_bf16 v[56:59],a[100:103],v[148:151],v[56:59]\nds_read_b64_tr_b16 v[136:137],v224 offset:57344\ns_waitcnt vmcnt(12)\nv_mfma_f32_16x16x32_bf16 v[56:59],a[96:99],v[144:147],v[56:59]\n"
"buffer_load_dwordx4 v228,s[36:39],s60 offen offset:0 lds\nv_mfma_f32_16x16x32_bf16 v[52:55],a[80:83],v[144:147],v[52:55]\nds_read_b64_tr_b16 v[138:139],v224 offset:57856\ns_and_b32 s7,s89,7\n"
"v_mfma_f32_16x16x32_bf16 v[52:55],a[84:87],v[148:151],v[52:55]\nds_read_b64_tr_b16 v[140:141],v224 offset:61440\ns_lshl_b32 s7,s7,7\nv_mfma_f32_16x16x32_bf16 v[52:55],a[88:91],v[152:155],v[52:55]\n"
"ds_read_b64_tr_b16 v[142:143],v224 offset:61952\ns_add_u32 s6,s6,s7\nv_mfma_f32_16x16x32_bf16 v[52:55],a[92:95],v[156:159],v[52:55]\nbuffer_load_dwordx4 v229,s[36:39],s60 offen offset:1024 lds\n"
"v_mfma_f32_16x16x32_bf16 v[48:51],a[76:79],v[156:159],v[48:51]\nds_read_b128 a[160:163],v188 offset:16384\nv_mfma_f32_16x16x32_bf16 v[48:51],a[72:75],v[152:155],v[48:51]\nds_read_b128 a[164:167],v189 offset:16384\n"
"v_mfma_f32_16x16x32_bf16 v[48:51],a[68:71],v[148:151],v[48:51]\ns_waitcnt lgkmcnt(2)\nds_read_b128 a[168:171],v190 offset:16384\nv_mfma_f32_16x16x32_bf16 v[48:51],a[64:67],v[144:147],v[48:51]\n"
"buffer_load_dwordx4 v230,s[36:39],s60 offen offset:2048 lds\nv_mfma_f32_16x16x32_bf16 v[64:67],a[64:67],v[128:131],v[64:67]\nds_read_b64_tr_b16 v[144:145],v225 offset:49152\n"
"v_mfma_f32_16x16x32_bf16 v[64:67],a[68:71],v[132:135],v[64:67]\nds_read_b64_tr_b16 v[146:147],v225 offset:49664\nv_mfma_f32_16x16x32_bf16 v[64:67],a[72:75],v[136:139],v[64:67]\nds_read_b64_tr_b16 v[148:149],v225 offset:53248\n"
"v_mfma_f32_16x16x32_bf16 v[64:67],a[76:79],v[140:143],v[64:67]\nbuffer_load_dwordx4 v231,s[36:39],s60 offen offset:3072 lds\nv_mfma_f32_16x16x32_bf16 v[68:71],a[92:95],v[140:143],v[68:71]\n"
"ds_read_b64_tr_b16 v[150:151],v225 offset:53760\ns_add_u32 s60,s55,s57\nv_mfma_f32_16x16x32_bf16 v[68:71],a[88:91],v[136:139],v[68:71]\nds_read_b64_tr_b16 v[152:153],v225 offset:57344\ns_add_u32 m0,s62,16384\n"
"v_mfma_f32_16x16x32_bf16 v[68:71],a[84:87],v[132:135],v[68:71]\nds_read_b64_tr_b16 v[154:155],v225 offset:57856\nv_mfma_f32_16x16x32_bf16 v[68:71],a[80:83],v[128:131],v[68:71]\n"
"buffer_load_dwordx4 v232,s[40:43],s60 offen offset:0 lds\nv_mfma_f32_16x16x32_bf16 v[72:75],a[96:99],v[128:131],v[72:75]\nds_read_b64_tr_b16 v[156:157],v225 offset:61440\n"
"v_mfma_f32_16x16x32_bf16 v[72:75],a[100:103],v[132:135],v[72:75]\nds_read_b64_tr_b16 v[158:159],v225 offset:61952\nv_mfma_f32_16x16x32_bf16 v[72:75],a[104:107],v[136:139],v[72:75]\nds_read_b128 a[172:175],v191 offset:16384\n"
"s_waitcnt vmcnt(13)\nv_mfma_f32_16x16x32_bf16 v[72:75],a[108:111],v[140:143],v[72:75]\nbuffer_load_dwordx4 v233,s[40:43],s60 offen offset:1024 lds\nv_mfma_f32_16x16x32_bf16 v[76:79],a[124:127],v[140:143],v[76:79]\n"
"ds_read_b128 a[176:179],v236 offset:49152\nv_mfma_f32_16x16x32_bf16 v[76:79],a[120:123],v[136:139],v[76:79]\nds_read_b128 a[180:183],v237 offset:49152\nv_mfma_f32_16x16x32_bf16 v[76:79],a[116:119],v[132:135],v[76:79]\n"
"s_waitcnt lgkmcnt(3)\nds_read_b128 a[184:187],v238 offset:49152\nv_mfma_f32_16x16x32_bf16 v[76:79],a[112:115],v[128:131],v[76:79]\nbuffer_load_dwordx4 v234,s[40:43],s60 offen offset:2048 lds\n"
"v_mfma_f32_16x16x32_bf16 v[92:95],a[112:115],v[144:147],v[92:95]\nds_read_b64_tr_b16 v[128:129],v226 offset:49152\nv_mfma_f32_16x16x32_bf16 v[92:95],a[116:119],v[148:151],v[92:95]\nds_read_b64_tr_b16 v[130:131],v226 offset:49664\n"
"v_mfma_f32_16x16x32_bf16 v[92:95],a[120:123],v[152:155],v[92:95]\nds_read_b64_tr_b16 v[132:133],v226 offset:53248\nv_mfma_f32_16x16x32_bf16 v[92:95],a[124:127],v[156:159],v[92:95]\n"
"buffer_load_dwordx4 v235,s[40:43],s60 offen offset:3072 lds\nv_mfma_f32_16x16x32_bf16 v[88:91],a[108:111],v[156:159],v[88:91]\nds_read_b64_tr_b16 v[134:135],v226 offset:53760\ns_add_u32 s60,s55,s58\n"
"v_mfma_f32_16x16x32_bf16 v[88:91],a[104:107],v[152:155],v[88:91]\nds_read_b64_tr_b16 v[136:137],v226 offset:57344\ns_add_u32 m0,s62,32768\nv_mfma_f32_16x16x32_bf16 v[88:91],a[100:103],v[148:151],v[88:91]\n"
"ds_read_b64_tr_b16 v[138:139],v226 offset:57856\nv_mfma_f32_16x16x32_bf16 v[88:91],a[96:99],v[144:147],v[88:91]\nbuffer_load_dwordx4 v232,s[40:43],s60 offen offset:0 lds\n"
"v_mfma_f32_16x16x32_bf16 v[84:87],a[80:83],v[144:147],v[84:87]\nds_read_b64_tr_b16 v[140:141],v226 offset:61440\nv_mfma_f32_16x16x32_bf16 v[84:87],a[84:87],v[148:151],v[84:87]\nds_read_b64_tr_b16 v[142:143],v226 offset:61952\n"
"v_mfma_f32_16x16x32_bf16 v[84:87],a[88:91],v[152:155],v[84:87]\nds_read_b128 a[188:191],v239 offset:49152\nv_mfma_f32_16x16x32_bf16 v[84:87],a[92:95],v[156:159],v[84:87]\n"
"buffer_load_dwordx4 v233,s[40:43],s60 offen offset:1024 lds\nv_mfma_f32_16x16x32_bf16 v[80:83],a[76:79],v[156:159],v[80:83]\ns_add_u32 s90,s90,s6\ns_addc_u32 s91,s91,0\ns_lshl_b32 s6,s10,5\n"
"v_mfma_f32_16x16x32_bf16 v[80:83],a[72:75],v[152:155],v[80:83]\ns_and_b32 s7,s9,3\ns_lshl_b32 s7,s7,3\ns_add_u32 s6,s6,s7\nv_mfma_f32_16x16x32_bf16 v[80:83],a[68:71],v[148:151],v[80:83]\ns_waitcnt lgkmcnt(1)\ns_lshl_b32 s7,s5,1\n"
"s_add_u32 s6,s6,s7\nv_mfma_f32_16x16x32_bf16 v[80:83],a[64:67],v[144:147],v[80:83]\nbuffer_load_dwordx4 v234,s[40:43],s60 offen offset:2048 lds\nv_mfma_f32_16x16x32_bf16 v[96:99],a[64:67],v[128:131],v[96:99]\n"
"ds_read_b64_tr_b16 v[144:145],v227 offset:49152\ns_lshl_b32 s6,s6,10\nv_mfma_f32_16x16x32_bf16 v[96:99],a[68:71],v[132:135],v[96:99]\nds_read_b64_tr_b16 v[146:147],v227 offset:49664\ns_add_u32 s6,s6,0x4280000\n"
"v_mfma_f32_16x16x32_bf16 v[96:99],a[72:75],v[136:139],v[96:99]\nds_read_b64_tr_b16 v[148:149],v227 offset:53248\ns_lshl_b32 s7,s10,8\nv_mfma_f32_16x16x32_bf16 v[96:99],a[76:79],v[140:143],v[96:99]\n"
"buffer_load_dwordx4 v235,s[40:43],s60 offen offset:3072 lds\nv_mfma_f32_16x16x32_bf16 v[100:103],a[92:95],v[140:143],v[100:103]\nds_read_b64_tr_b16 v[150:151],v227 offset:53760\ns_add_u32 s60,s55,s59\n"
"v_mfma_f32_16x16x32_bf16 v[100:103],a[88:91],v[136:139],v[100:103]\nds_read_b64_tr_b16 v[152:153],v227 offset:57344\ns_add_u32 m0,s62,65536\nv_mfma_f32_16x16x32_bf16 v[100:103],a[84:87],v[132:135],v[100:103]\n"
"ds_read_b64_tr_b16 v[154:155],v227 offset:57856\ns_waitcnt vmcnt(12)\nv_mfma_f32_16x16x32_bf16 v[100:103],a[80:83],v[128:131],v[100:103]\nbuffer_load_dwordx4 v232,s[40:43],s60 offen offset:0 lds\n"
"v_mfma_f32_16x16x32_bf16 v[104:107],a[96:99],v[128:131],v[104:107]\nds_read_b64_tr_b16 v[156:157],v227 offset:61440\ns_lshl_b32 s84,s5,6\nv_mfma_f32_16x16x32_bf16 v[104:107],a[100:103],v[132:135],v[104:107]\n"
"ds_read_b64_tr_b16 v[158:159],v227 offset:61952\ns_add_u32 s7,s7,s84\nv_mfma_f32_16x16x32_bf16 v[104:107],a[104:107],v[136:139],v[104:107]\nds_read_b128 a[192:195],v236 offset:0\ns_and_b32 s84,s9,1\n"
"v_mfma_f32_16x16x32_bf16 v[104:107],a[108:111],v[140:143],v[104:107]\nbuffer_load_dwordx4 v233,s[40:43],s60 offen offset:1024 lds\nv_mfma_f32_16x16x32_bf16 v[108:111],a[124:127],v[140:143],v[108:111]\n"
"ds_read_b128 a[196:199],v237 offset:0\ns_lshl_b32 s84,s84,5\nv_mfma_f32_16x16x32_bf16 v[108:111],a[120:123],v[136:139],v[108:111]\nds_read_b128 a[200:203],v238 offset:0\ns_add_u32 s7,s7,s84\n"
"v_mfma_f32_16x16x32_bf16 v[108:111],a[116:119],v[132:135],v[108:111]\ns_waitcnt vmcnt(18) lgkmcnt(3)\nds_read_b128 a[204:207],v239 offset:0\nv_mfma_f32_16x16x32_bf16 v[108:111],a[112:115],v[128:131],v[108:111]\n"
"buffer_load_dwordx4 v234,s[40:43],s60 offen offset:2048 lds\nv_mfma_f32_16x16x32_bf16 v[124:127],a[112:115],v[144:147],v[124:127]\ns_barrier\ns_lshl_b32 s7,s7,2\ns_add_u32 s7,s7,0x42c8000\n"
"v_mfma_f32_16x16x32_bf16 v[124:127],a[116:119],v[148:151],v[124:127]\nds_read_b64_tr_b16 v[128:129],v224 offset:0\nv_mfma_f32_16x16x32_bf16 v[124:127],a[120:123],v[152:155],v[124:127]\n"
"ds_read_b64_tr_b16 v[130:131],v224 offset:512\nv_mfma_f32_16x16x32_bf16 v[124:127],a[124:127],v[156:159],v[124:127]\nbuffer_load_dwordx4 v235,s[40:43],s60 offen offset:3072 lds\n"
"v_mfma_f32_16x16x32_bf16 v[120:123],a[108:111],v[156:159],v[120:123]\nds_read_b64_tr_b16 v[132:133],v224 offset:4096\ns_add_u32 s60,s51,s63\nv_mfma_f32_16x16x32_bf16 v[120:123],a[104:107],v[152:155],v[120:123]\n"
"ds_read_b64_tr_b16 v[134:135],v224 offset:4608\ns_add_u32 m0,s61,32768\nv_mfma_f32_16x16x32_bf16 v[120:123],a[100:103],v[148:151],v[120:123]\nds_read_b64_tr_b16 v[136:137],v224 offset:8192\n"
"v_mfma_f32_16x16x32_bf16 v[120:123],a[96:99],v[144:147],v[120:123]\nbuffer_load_dwordx4 v228,s[36:39],s60 offen offset:0 lds\nv_mfma_f32_16x16x32_bf16 v[116:119],a[80:83],v[144:147],v[116:119]\n"
"ds_read_b64_tr_b16 v[138:139],v224 offset:8704\nv_mfma_f32_16x16x32_bf16 v[116:119],a[84:87],v[148:151],v[116:119]\nds_read_b64_tr_b16 v[140:141],v224 offset:12288\n"
"v_mfma_f32_16x16x32_bf16 v[116:119],a[88:91],v[152:155],v[116:119]\nds_read_b64_tr_b16 v[142:143],v224 offset:12800\nv_mfma_f32_16x16x32_bf16 v[116:119],a[92:95],v[156:159],v[116:119]\n"
"buffer_load_dwordx4 v229,s[36:39],s60 offen offset:1024 lds\nv_mfma_f32_16x16x32_bf16 v[112:115],a[76:79],v[156:159],v[112:115]\ns_cmp_ge_u32 s9,28\ns_cselect_b32 s6,s6,s7\n"
"v_mfma_f32_16x16x32_bf16 v[112:115],a[72:75],v[152:155],v[112:115]\ns_add_u32 s20,s26,s6\ns_addc_u32 s21,s27,0\ns_lshl_b32 s6,s10,2\nv_mfma_f32_16x16x32_bf16 v[112:115],a[68:71],v[148:151],v[112:115]\ns_waitcnt lgkmcnt(0)\n"
"s_and_b32 s7,s9,3\ns_add_u32 s6,s6,s7\nv_mfma_f32_16x16x32_bf16 v[112:115],a[64:67],v[144:147],v[112:115]\nbuffer_load_dwordx4 v230,s[36:39],s60 offen offset:2048 lds\nv_mfma_f32_16x16x32_bf16 v[0:3],a[128:131],v[128:131],v[0:3]\n"
"ds_read_b64_tr_b16 v[144:145],v225 offset:0\ns_lshl_b32 s6,s6,10\nv_mfma_f32_16x16x32_bf16 v[0:3],a[132:135],v[132:135],v[0:3]\nds_read_b64_tr_b16 v[146:147],v225 offset:512\ns_add_u32 s6,s6,0x42c0000\n"
"v_mfma_f32_16x16x32_bf16 v[0:3],a[136:139],v[136:139],v[0:3]\nds_read_b64_tr_b16 v[148:149],v225 offset:4096\nv_mfma_f32_16x16x32_bf16 v[0:3],a[140:143],v[140:143],v[0:3]\n"
"buffer_load_dwordx4 v231,s[36:39],s60 offen offset:3072 lds\nv_mfma_f32_16x16x32_bf16 v[4:7],a[156:159],v[140:143],v[4:7]\nds_read_b64_tr_b16 v[150:151],v225 offset:4608\ns_add_u32 s60,s51,s64\n"
"v_mfma_f32_16x16x32_bf16 v[4:7],a[152:155],v[136:139],v[4:7]\nds_read_b64_tr_b16 v[152:153],v225 offset:8192\ns_add_u32 m0,s61,49152\nv_mfma_f32_16x16x32_bf16 v[4:7],a[148:151],v[132:135],v[4:7]\n"
"ds_read_b64_tr_b16 v[154:155],v225 offset:8704\ns_waitcnt vmcnt(12)\nv_mfma_f32_16x16x32_bf16 v[4:7],a[144:147],v[128:131],v[4:7]\nbuffer_load_dwordx4 v228,s[36:39],s60 offen offset:0 lds\n"
"v_mfma_f32_16x16x32_bf16 v[8:11],a[160:163],v[128:131],v[8:11]\nds_read_b64_tr_b16 v[156:157],v225 offset:12288\nv_mfma_f32_16x16x32_bf16 v[8:11],a[164:167],v[132:135],v[8:11]\nds_read_b64_tr_b16 v[158:159],v225 offset:12800\n"
"v_mfma_f32_16x16x32_bf16 v[8:11],a[168:171],v[136:139],v[8:11]\nds_read_b128 a[208:211],v236 offset:16384\nv_mfma_f32_16x16x32_bf16 v[8:11],a[172:175],v[140:143],v[8:11]\n"
"buffer_load_dwordx4 v229,s[36:39],s60 offen offset:1024 lds\nv_mfma_f32_16x16x32_bf16 v[12:15],a[188:191],v[140:143],v[12:15]\nds_read_b128 a[212:215],v237 offset:16384\n"
"v_mfma_f32_16x16x32_bf16 v[12:15],a[184:187],v[136:139],v[12:15]\nds_read_b128 a[216:219],v238 offset:16384\nv_mfma_f32_16x16x32_bf16 v[12:15],a[180:183],v[132:135],v[12:15]\ns_waitcnt lgkmcnt(3)\n"
"ds_read_b128 a[220:223],v239 offset:16384\nv_mfma_f32_16x16x32_bf16 v[12:15],a[176:179],v[128:131],v[12:15]\nbuffer_load_dwordx4 v230,s[36:39],s60 offen offset:2048 lds\n"
"v_mfma_f32_16x16x32_bf16 v[28:31],a[176:179],v[144:147],v[28:31]\nds_read_b64_tr_b16 v[128:129],v226 offset:0\nv_mfma_f32_16x16x32_bf16 v[28:31],a[180:183],v[148:151],v[28:31]\nds_read_b64_tr_b16 v[130:131],v226 offset:512\n"
"v_mfma_f32_16x16x32_bf16 v[28:31],a[184:187],v[152:155],v[28:31]\nds_read_b64_tr_b16 v[132:133],v226 offset:4096\nv_mfma_f32_16x16x32_bf16 v[28:31],a[188:191],v[156:159],v[28:31]\n"
"buffer_load_dwordx4 v231,s[36:39],s60 offen offset:3072 lds\nv_mfma_f32_16x16x32_bf16 v[24:27],a[172:175],v[156:159],v[24:27]\nds_read_b64_tr_b16 v[134:135],v226 offset:4608\ns_waitcnt vmcnt(12)\n"
"v_mfma_f32_16x16x32_bf16 v[24:27],a[168:171],v[152:155],v[24:27]\nds_read_b64_tr_b16 v[136:137],v226 offset:8192\nv_mfma_f32_16x16x32_bf16 v[24:27],a[164:167],v[148:151],v[24:27]\nds_read_b64_tr_b16 v[138:139],v226 offset:8704\n"
"v_mfma_f32_16x16x32_bf16 v[24:27],a[160:163],v[144:147],v[24:27]\nbuffer_load_dword v235,v245,s[36:39],s65 offen offset:0\nv_mfma_f32_16x16x32_bf16 v[20:23],a[144:147],v[144:147],v[20:23]\n"
"ds_read_b64_tr_b16 v[140:141],v226 offset:12288\nv_mfma_f32_16x16x32_bf16 v[20:23],a[148:151],v[148:151],v[20:23]\nds_read_b64_tr_b16 v[142:143],v226 offset:12800\nv_mfma_f32_16x16x32_bf16 v[20:23],a[152:155],v[152:155],v[20:23]\n"
"ds_read_b128 a[224:227],v236 offset:32768\nv_mfma_f32_16x16x32_bf16 v[20:23],a[156:159],v[156:159],v[20:23]\nds_read_b128 a[228:231],v237 offset:32768\nv_mfma_f32_16x16x32_bf16 v[16:19],a[140:143],v[156:159],v[16:19]\n"
"ds_read_b128 a[232:235],v238 offset:32768\nv_mfma_f32_16x16x32_bf16 v[16:19],a[136:139],v[152:155],v[16:19]\nds_read_b128 a[236:239],v239 offset:32768\nv_mfma_f32_16x16x32_bf16 v[16:19],a[132:135],v[148:151],v[16:19]\n"
"s_waitcnt lgkmcnt(4)\ns_add_u32 s22,s26,s6\ns_addc_u32 s23,s27,0\nv_mfma_f32_16x16x32_bf16 v[16:19],a[128:131],v[144:147],v[16:19]\ns_mov_b32 s74,4096\ns_lshl_b32 s82,s17,16\ns_add_u32 s82,s82,s14\n"
"v_mfma_f32_16x16x32_bf16 v[32:35],a[128:131],v[128:131],v[32:35]\nds_read_b64_tr_b16 v[144:145],v227 offset:0\nv_mfma_f32_16x16x32_bf16 v[32:35],a[132:135],v[132:135],v[32:35]\nds_read_b64_tr_b16 v[146:147],v227 offset:512\n"
"v_mfma_f32_16x16x32_bf16 v[32:35],a[136:139],v[136:139],v[32:35]\nds_read_b64_tr_b16 v[148:149],v227 offset:4096\nv_mfma_f32_16x16x32_bf16 v[32:35],a[140:143],v[140:143],v[32:35]\n"
"buffer_load_dword v235,v246,s[36:39],s65 offen offset:0\nv_mfma_f32_16x16x32_bf16 v[36:39],a[156:159],v[140:143],v[36:39]\nds_read_b64_tr_b16 v[150:151],v227 offset:4608\ns_waitcnt vmcnt(10)\n"
"v_mfma_f32_16x16x32_bf16 v[36:39],a[152:155],v[136:139],v[36:39]\nds_read_b64_tr_b16 v[152:153],v227 offset:8192\nv_mfma_f32_16x16x32_bf16 v[36:39],a[148:151],v[132:135],v[36:39]\nds_read_b64_tr_b16 v[154:155],v227 offset:8704\n"
"v_mfma_f32_16x16x32_bf16 v[36:39],a[144:147],v[128:131],v[36:39]\nds_read_b128 a[240:243],v188 offset:0\nv_mfma_f32_16x16x32_bf16 v[40:43],a[160:163],v[128:131],v[40:43]\nds_read_b64_tr_b16 v[156:157],v227 offset:12288\n"
"v_mfma_f32_16x16x32_bf16 v[40:43],a[164:167],v[132:135],v[40:43]\nds_read_b64_tr_b16 v[158:159],v227 offset:12800\nv_mfma_f32_16x16x32_bf16 v[40:43],a[168:171],v[136:139],v[40:43]\nds_read_b128 a[244:247],v189 offset:0\n"
"v_mfma_f32_16x16x32_bf16 v[40:43],a[172:175],v[140:143],v[40:43]\nds_read_b128 a[248:251],v190 offset:0\nv_mfma_f32_16x16x32_bf16 v[44:47],a[188:191],v[140:143],v[44:47]\nbuffer_load_dword v235,v245,s[36:39],s65 offen offset:128\n"
"v_mfma_f32_16x16x32_bf16 v[44:47],a[184:187],v[136:139],v[44:47]\nds_read_b128 a[252:255],v191 offset:0\nv_mfma_f32_16x16x32_bf16 v[44:47],a[180:183],v[132:135],v[44:47]\ns_waitcnt vmcnt(23) lgkmcnt(3)\nv_lshrrev_b32 v238,7,v240\n"
"v_lshlrev_b32 v238,13,v238\nv_mfma_f32_16x16x32_bf16 v[44:47],a[176:179],v[128:131],v[44:47]\nv_and_b32 v239,64,v240\nv_lshl_add_u32 v238,v239,1,v238\nv_mfma_f32_16x16x32_bf16 v[60:63],a[176:179],v[144:147],v[60:63]\ns_barrier\n"
"v_add_u32 v238,s82,v238\nv_mfma_f32_16x16x32_bf16 v[60:63],a[180:183],v[148:151],v[60:63]\nds_read_b64_tr_b16 v[128:129],v224 offset:16384\nv_mfma_f32_16x16x32_bf16 v[60:63],a[184:187],v[152:155],v[60:63]\n"
"ds_read_b64_tr_b16 v[130:131],v224 offset:16896\nv_mfma_f32_16x16x32_bf16 v[60:63],a[188:191],v[156:159],v[60:63]\nv_mfma_f32_16x16x32_bf16 v[56:59],a[172:175],v[156:159],v[56:59]\nds_read_b64_tr_b16 v[132:133],v224 offset:20480\n"
"v_mfma_f32_16x16x32_bf16 v[56:59],a[168:171],v[152:155],v[56:59]\nds_read_b64_tr_b16 v[134:135],v224 offset:20992\nv_mfma_f32_16x16x32_bf16 v[56:59],a[164:167],v[148:151],v[56:59]\nds_read_b64_tr_b16 v[136:137],v224 offset:24576\n"
"v_mfma_f32_16x16x32_bf16 v[56:59],a[160:163],v[144:147],v[56:59]\nv_mfma_f32_16x16x32_bf16 v[52:55],a[144:147],v[144:147],v[52:55]\nds_read_b64_tr_b16 v[138:139],v224 offset:25088\n"
"v_mfma_f32_16x16x32_bf16 v[52:55],a[148:151],v[148:151],v[52:55]\nds_read_b64_tr_b16 v[140:141],v224 offset:28672\nv_mfma_f32_16x16x32_bf16 v[52:55],a[152:155],v[152:155],v[52:55]\nds_read_b64_tr_b16 v[142:143],v224 offset:29184\n"
"v_mfma_f32_16x16x32_bf16 v[52:55],a[156:159],v[156:159],v[52:55]\nbuffer_load_dword v235,v246,s[36:39],s65 offen offset:128\nv_mfma_f32_16x16x32_bf16 v[48:51],a[140:143],v[156:159],v[48:51]\ns_add_u32 s60,s48,s65\n"
"s_add_u32 m0,s61,65536\nv_mfma_f32_16x16x32_bf16 v[48:51],a[136:139],v[152:155],v[48:51]\nv_mfma_f32_16x16x32_bf16 v[48:51],a[132:135],v[148:151],v[48:51]\ns_waitcnt lgkmcnt(0)\n"
"v_mfma_f32_16x16x32_bf16 v[48:51],a[128:131],v[144:147],v[48:51]\nv_mfma_f32_16x16x32_bf16 v[64:67],a[128:131],v[128:131],v[64:67]\nds_read_b64_tr_b16 v[144:145],v225 offset:16384\n"
"v_mfma_f32_16x16x32_bf16 v[64:67],a[132:135],v[132:135],v[64:67]\nds_read_b64_tr_b16 v[146:147],v225 offset:16896\nv_mfma_f32_16x16x32_bf16 v[64:67],a[136:139],v[136:139],v[64:67]\nds_read_b64_tr_b16 v[148:149],v225 offset:20480\n"
"v_mfma_f32_16x16x32_bf16 v[64:67],a[140:143],v[140:143],v[64:67]\nv_mfma_f32_16x16x32_bf16 v[68:71],a[156:159],v[140:143],v[68:71]\nds_read_b64_tr_b16 v[150:151],v225 offset:20992\n"
"v_mfma_f32_16x16x32_bf16 v[68:71],a[152:155],v[136:139],v[68:71]\nds_read_b64_tr_b16 v[152:153],v225 offset:24576\nv_mfma_f32_16x16x32_bf16 v[68:71],a[148:151],v[132:135],v[68:71]\nds_read_b64_tr_b16 v[154:155],v225 offset:25088\n"
"v_mfma_f32_16x16x32_bf16 v[68:71],a[144:147],v[128:131],v[68:71]\nv_mfma_f32_16x16x32_bf16 v[72:75],a[160:163],v[128:131],v[72:75]\nds_read_b64_tr_b16 v[156:157],v225 offset:28672\n"
"v_mfma_f32_16x16x32_bf16 v[72:75],a[164:167],v[132:135],v[72:75]\nds_read_b64_tr_b16 v[158:159],v225 offset:29184\nv_mfma_f32_16x16x32_bf16 v[72:75],a[168:171],v[136:139],v[72:75]\n"
"buffer_load_dwordx4 v228,s[36:39],s60 offen offset:0 lds\nv_mfma_f32_16x16x32_bf16 v[72:75],a[172:175],v[140:143],v[72:75]\nv_mfma_f32_16x16x32_bf16 v[76:79],a[188:191],v[140:143],v[76:79]\n"
"v_mfma_f32_16x16x32_bf16 v[76:79],a[184:187],v[136:139],v[76:79]\nv_mfma_f32_16x16x32_bf16 v[76:79],a[180:183],v[132:135],v[76:79]\ns_waitcnt lgkmcnt(0)\nv_mfma_f32_16x16x32_bf16 v[76:79],a[176:179],v[128:131],v[76:79]\n"
"buffer_load_dwordx4 v229,s[36:39],s60 offen offset:1024 lds\nv_mfma_f32_16x16x32_bf16 v[92:95],a[176:179],v[144:147],v[92:95]\nds_read_b64_tr_b16 v[128:129],v226 offset:16384\n"
"v_mfma_f32_16x16x32_bf16 v[92:95],a[180:183],v[148:151],v[92:95]\nds_read_b64_tr_b16 v[130:131],v226 offset:16896\nv_mfma_f32_16x16x32_bf16 v[92:95],a[184:187],v[152:155],v[92:95]\nds_read_b64_tr_b16 v[132:133],v226 offset:20480\n"
"v_mfma_f32_16x16x32_bf16 v[92:95],a[188:191],v[156:159],v[92:95]\nbuffer_load_dwordx4 v230,s[36:39],s60 offen offset:2048 lds\nv_mfma_f32_16x16x32_bf16 v[88:91],a[172:175],v[156:159],v[88:91]\n"
"ds_read_b64_tr_b16 v[134:135],v226 offset:20992\nv_mfma_f32_16x16x32_bf16 v[88:91],a[168:171],v[152:155],v[88:91]\nds_read_b64_tr_b16 v[136:137],v226 offset:24576\nv_mfma_f32_16x16x32_bf16 v[88:91],a[164:167],v[148:151],v[88:91]\n"
"ds_read_b64_tr_b16 v[138:139],v226 offset:25088\nv_mfma_f32_16x16x32_bf16 v[88:91],a[160:163],v[144:147],v[88:91]\nbuffer_load_dwordx4 v231,s[36:39],s60 offen offset:3072 lds\n"
"v_mfma_f32_16x16x32_bf16 v[84:87],a[144:147],v[144:147],v[84:87]\nds_read_b64_tr_b16 v[140:141],v226 offset:28672\ns_add_u32 s60,s49,s65\nv_mfma_f32_16x16x32_bf16 v[84:87],a[148:151],v[148:151],v[84:87]\n"
"ds_read_b64_tr_b16 v[142:143],v226 offset:29184\ns_add_u32 m0,s61,81920\nv_mfma_f32_16x16x32_bf16 v[84:87],a[152:155],v[152:155],v[84:87]\nv_mfma_f32_16x16x32_bf16 v[84:87],a[156:159],v[156:159],v[84:87]\n"
"buffer_load_dwordx4 v228,s[36:39],s60 offen offset:0 lds\nv_mfma_f32_16x16x32_bf16 v[80:83],a[140:143],v[156:159],v[80:83]\nv_mfma_f32_16x16x32_bf16 v[80:83],a[136:139],v[152:155],v[80:83]\n"
"v_mfma_f32_16x16x32_bf16 v[80:83],a[132:135],v[148:151],v[80:83]\ns_waitcnt lgkmcnt(0)\nv_mfma_f32_16x16x32_bf16 v[80:83],a[128:131],v[144:147],v[80:83]\nbuffer_load_dwordx4 v229,s[36:39],s60 offen offset:1024 lds\n"
"v_mfma_f32_16x16x32_bf16 v[96:99],a[128:131],v[128:131],v[96:99]\nds_read_b64_tr_b16 v[144:145],v227 offset:16384\nv_mfma_f32_16x16x32_bf16 v[96:99],a[132:135],v[132:135],v[96:99]\nds_read_b64_tr_b16 v[146:147],v227 offset:16896\n"
"v_mfma_f32_16x16x32_bf16 v[96:99],a[136:139],v[136:139],v[96:99]\nds_read_b64_tr_b16 v[148:149],v227 offset:20480\nv_mfma_f32_16x16x32_bf16 v[96:99],a[140:143],v[140:143],v[96:99]\n"
"buffer_load_dwordx4 v230,s[36:39],s60 offen offset:2048 lds\nv_mfma_f32_16x16x32_bf16 v[100:103],a[156:159],v[140:143],v[100:103]\nds_read_b64_tr_b16 v[150:151],v227 offset:20992\n"
"v_mfma_f32_16x16x32_bf16 v[100:103],a[152:155],v[136:139],v[100:103]\nds_read_b64_tr_b16 v[152:153],v227 offset:24576\nv_mfma_f32_16x16x32_bf16 v[100:103],a[148:151],v[132:135],v[100:103]\n"
"ds_read_b64_tr_b16 v[154:155],v227 offset:25088\nv_mfma_f32_16x16x32_bf16 v[100:103],a[144:147],v[128:131],v[100:103]\nbuffer_load_dwordx4 v231,s[36:39],s60 offen offset:3072 lds\n"
"v_mfma_f32_16x16x32_bf16 v[104:107],a[160:163],v[128:131],v[104:107]\nds_read_b64_tr_b16 v[156:157],v227 offset:28672\ns_add_u32 s60,s50,s65\nv_mfma_f32_16x16x32_bf16 v[104:107],a[164:167],v[132:135],v[104:107]\n"
"ds_read_b64_tr_b16 v[158:159],v227 offset:29184\ns_add_u32 m0,s61,98304\nv_mfma_f32_16x16x32_bf16 v[104:107],a[168:171],v[136:139],v[104:107]\nv_mfma_f32_16x16x32_bf16 v[104:107],a[172:175],v[140:143],v[104:107]\n"
"buffer_load_dwordx4 v228,s[36:39],s60 offen offset:0 lds\nv_mfma_f32_16x16x32_bf16 v[108:111],a[188:191],v[140:143],v[108:111]\nv_mfma_f32_16x16x32_bf16 v[108:111],a[184:187],v[136:139],v[108:111]\n"
"v_mfma_f32_16x16x32_bf16 v[108:111],a[180:183],v[132:135],v[108:111]\ns_waitcnt vmcnt(17) lgkmcnt(0)\nv_mfma_f32_16x16x32_bf16 v[108:111],a[176:179],v[128:131],v[108:111]\n"
"buffer_load_dwordx4 v229,s[36:39],s60 offen offset:1024 lds\nv_mfma_f32_16x16x32_bf16 v[124:127],a[176:179],v[144:147],v[124:127]\ns_barrier\nv_mfma_f32_16x16x32_bf16 v[124:127],a[180:183],v[148:151],v[124:127]\n"
"ds_read_b64_tr_b16 v[128:129],v224 offset:32768\nv_mfma_f32_16x16x32_bf16 v[124:127],a[184:187],v[152:155],v[124:127]\nds_read_b64_tr_b16 v[130:131],v224 offset:33280\n"
"v_mfma_f32_16x16x32_bf16 v[124:127],a[188:191],v[156:159],v[124:127]\nbuffer_load_dwordx4 v230,s[36:39],s60 offen offset:2048 lds\nv_mfma_f32_16x16x32_bf16 v[120:123],a[172:175],v[156:159],v[120:123]\n"
"ds_read_b64_tr_b16 v[132:133],v224 offset:36864\nv_mfma_f32_16x16x32_bf16 v[120:123],a[168:171],v[152:155],v[120:123]\nds_read_b64_tr_b16 v[134:135],v224 offset:37376\n"
"v_mfma_f32_16x16x32_bf16 v[120:123],a[164:167],v[148:151],v[120:123]\nds_read_b64_tr_b16 v[136:137],v224 offset:40960\nv_mfma_f32_16x16x32_bf16 v[120:123],a[160:163],v[144:147],v[120:123]\n"
"buffer_load_dwordx4 v231,s[36:39],s60 offen offset:3072 lds\nv_mfma_f32_16x16x32_bf16 v[116:119],a[144:147],v[144:147],v[116:119]\nds_read_b64_tr_b16 v[138:139],v224 offset:41472\ns_add_u32 s60,s51,s65\n"
"v_mfma_f32_16x16x32_bf16 v[116:119],a[148:151],v[148:151],v[116:119]\nds_read_b64_tr_b16 v[140:141],v224 offset:45056\ns_add_u32 m0,s61,114688\nv_mfma_f32_16x16x32_bf16 v[116:119],a[152:155],v[152:155],v[116:119]\n"
"ds_read_b64_tr_b16 v[142:143],v224 offset:45568\nv_mfma_f32_16x16x32_bf16 v[116:119],a[156:159],v[156:159],v[116:119]\nbuffer_load_dwordx4 v228,s[36:39],s60 offen offset:0 lds\n"
"v_mfma_f32_16x16x32_bf16 v[112:115],a[140:143],v[156:159],v[112:115]\nv_mfma_f32_16x16x32_bf16 v[112:115],a[136:139],v[152:155],v[112:115]\nv_mfma_f32_16x16x32_bf16 v[112:115],a[132:135],v[148:151],v[112:115]\n"
"s_waitcnt lgkmcnt(0)\nv_mfma_f32_16x16x32_bf16 v[112:115],a[128:131],v[144:147],v[112:115]\nbuffer_load_dwordx4 v229,s[36:39],s60 offen offset:1024 lds\nv_mfma_f32_16x16x32_bf16 v[0:3],a[192:195],v[128:131],v[0:3]\n"
"ds_read_b64_tr_b16 v[144:145],v225 offset:32768\nv_mfma_f32_16x16x32_bf16 v[0:3],a[196:199],v[132:135],v[0:3]\nds_read_b64_tr_b16 v[146:147],v225 offset:33280\nv_mfma_f32_16x16x32_bf16 v[0:3],a[200:203],v[136:139],v[0:3]\n"
"ds_read_b64_tr_b16 v[148:149],v225 offset:36864\nv_mfma_f32_16x16x32_bf16 v[0:3],a[204:207],v[140:143],v[0:3]\nbuffer_load_dwordx4 v230,s[36:39],s60 offen offset:2048 lds\n"
"v_mfma_f32_16x16x32_bf16 v[4:7],a[220:223],v[140:143],v[4:7]\nds_read_b64_tr_b16 v[150:151],v225 offset:37376\nv_mfma_f32_16x16x32_bf16 v[4:7],a[216:219],v[136:139],v[4:7]\nds_read_b64_tr_b16 v[152:153],v225 offset:40960\n"
"v_mfma_f32_16x16x32_bf16 v[4:7],a[212:215],v[132:135],v[4:7]\nds_read_b64_tr_b16 v[154:155],v225 offset:41472\nv_mfma_f32_16x16x32_bf16 v[4:7],a[208:211],v[128:131],v[4:7]\n"
"buffer_load_dwordx4 v231,s[36:39],s60 offen offset:3072 lds\nv_mfma_f32_16x16x32_bf16 v[8:11],a[224:227],v[128:131],v[8:11]\nds_read_b64_tr_b16 v[156:157],v225 offset:45056\n"
"v_mfma_f32_16x16x32_bf16 v[8:11],a[228:231],v[132:135],v[8:11]\nds_read_b64_tr_b16 v[158:159],v225 offset:45568\nv_mfma_f32_16x16x32_bf16 v[8:11],a[232:235],v[136:139],v[8:11]\ns_lshl_b32 s62,s5,12\ns_add_u32 s62,s62,0x20000\n"
"v_lshrrev_b32 v233,6,v240\nv_mfma_f32_16x16x32_bf16 v[8:11],a[236:239],v[140:143],v[8:11]\ns_mov_b64 s[40:41],s[90:91]\ns_mov_b32 s42,0\ns_mov_b32 s56,0x40000000\nv_mfma_f32_16x16x32_bf16 v[12:15],a[252:255],v[140:143],v[12:15]\n"
"s_and_b32 s41,s41,0xffff\ns_mov_b32 s57,s18\nv_and_b32 v233,4,v233\nv_mfma_f32_16x16x32_bf16 v[12:15],a[248:251],v[136:139],v[12:15]\ns_lshr_b32 s71,s67,1\ns_lshl_b32 s71,s71,20\ns_add_u32 s71,s71,s73\n"
"v_mfma_f32_16x16x32_bf16 v[12:15],a[244:247],v[132:135],v[12:15]\ns_waitcnt lgkmcnt(0)\ns_bitcmp1_b32 s67,0\ns_cselect_b32 s72,s74,0\nv_mfma_f32_16x16x32_bf16 v[12:15],a[240:243],v[128:131],v[12:15]\ns_add_u32 s69,s71,s72\n"
"s_add_u32 s67,s67,1\nv_bfe_u32 v234,v240,4,3\nv_mfma_f32_16x16x32_bf16 v[28:31],a[240:243],v[144:147],v[28:31]\nds_read_b64_tr_b16 v[128:129],v226 offset:32768\nv_xor_b32 v233,v233,v234\n"
"v_mfma_f32_16x16x32_bf16 v[28:31],a[244:247],v[148:151],v[28:31]\nds_read_b64_tr_b16 v[130:131],v226 offset:33280\nv_lshlrev_b32 v233,4,v233\nv_mfma_f32_16x16x32_bf16 v[28:31],a[248:251],v[152:155],v[28:31]\n"
"ds_read_b64_tr_b16 v[132:133],v226 offset:36864\nv_lshrrev_b32 v234,7,v240\nv_mfma_f32_16x16x32_bf16 v[28:31],a[252:255],v[156:159],v[28:31]\nglobal_load_dword v235,v238,s[34:35]\n"
"v_mfma_f32_16x16x32_bf16 v[24:27],a[236:239],v[156:159],v[24:27]\nds_read_b64_tr_b16 v[134:135],v226 offset:37376\nv_lshl_add_u32 v232,v234,10,v233\nv_mfma_f32_16x16x32_bf16 v[24:27],a[232:235],v[152:155],v[24:27]\n"
"ds_read_b64_tr_b16 v[136:137],v226 offset:40960\nv_lshrrev_b32 v233,6,v240\nv_mfma_f32_16x16x32_bf16 v[24:27],a[228:231],v[148:151],v[24:27]\nds_read_b64_tr_b16 v[138:139],v226 offset:41472\nv_lshlrev_b32 v233,7,v233\n"
"v_mfma_f32_16x16x32_bf16 v[24:27],a[224:227],v[144:147],v[24:27]\nv_bfe_u32 v234,v240,7,1\nv_lshl_add_u32 v233,v234,6,v233\nv_mfma_f32_16x16x32_bf16 v[20:23],a[208:211],v[144:147],v[20:23]\n"
"ds_read_b64_tr_b16 v[140:141],v226 offset:45056\nv_and_b32 v234,48,v240\nv_mfma_f32_16x16x32_bf16 v[20:23],a[212:215],v[148:151],v[20:23]\nds_read_b64_tr_b16 v[142:143],v226 offset:45568\nv_add_u32 v233,v233,v234\n"
"v_mfma_f32_16x16x32_bf16 v[20:23],a[216:219],v[152:155],v[20:23]\nv_add_u32 v236,s62,v233\nv_xor_b32 v237,64,v236\nv_mfma_f32_16x16x32_bf16 v[20:23],a[220:223],v[156:159],v[20:23]\nv_cvt_pk_bf16_f32 v192,v0,v1\n"
"v_cvt_pk_bf16_f32 v193,v2,v3\nv_mfma_f32_16x16x32_bf16 v[16:19],a[204:207],v[156:159],v[16:19]\nv_cvt_pk_bf16_f32 v194,v4,v5\nv_cvt_pk_bf16_f32 v195,v6,v7\nv_mfma_f32_16x16x32_bf16 v[16:19],a[200:203],v[152:155],v[16:19]\n"
"buffer_store_dwordx4 v[192:195],v240,s[44:47],s69 offen offset:0 sc1\nv_mfma_f32_16x16x32_bf16 v[16:19],a[196:199],v[148:151],v[16:19]\ns_waitcnt lgkmcnt(0)\nv_cvt_pk_bf16_f32 v196,v8,v9\nv_cvt_pk_bf16_f32 v197,v10,v11\n"
"v_mfma_f32_16x16x32_bf16 v[16:19],a[192:195],v[144:147],v[16:19]\nv_cvt_pk_bf16_f32 v198,v12,v13\nv_cvt_pk_bf16_f32 v199,v14,v15\nv_mfma_f32_16x16x32_bf16 v[32:35],a[192:195],v[128:131],v[32:35]\n"
"ds_read_b64_tr_b16 v[144:145],v227 offset:32768\nv_xor_b32 v238,0x10000,v238\nv_mfma_f32_16x16x32_bf16 v[32:35],a[196:199],v[132:135],v[32:35]\nds_read_b64_tr_b16 v[146:147],v227 offset:33280\n"
"v_mfma_f32_16x16x32_bf16 v[32:35],a[200:203],v[136:139],v[32:35]\nds_read_b64_tr_b16 v[148:149],v227 offset:36864\nv_mfma_f32_16x16x32_bf16 v[32:35],a[204:207],v[140:143],v[32:35]\nglobal_load_dword v235,v238,s[34:35]\n"
"v_mfma_f32_16x16x32_bf16 v[36:39],a[220:223],v[140:143],v[36:39]\nds_read_b64_tr_b16 v[150:151],v227 offset:37376\nv_cvt_pk_bf16_f32 v206,v28,v29\nv_mfma_f32_16x16x32_bf16 v[36:39],a[216:219],v[136:139],v[36:39]\n"
"ds_read_b64_tr_b16 v[152:153],v227 offset:40960\nv_cvt_pk_bf16_f32 v207,v30,v31\nv_mfma_f32_16x16x32_bf16 v[36:39],a[212:215],v[132:135],v[36:39]\nds_read_b64_tr_b16 v[154:155],v227 offset:41472\nv_cvt_pk_bf16_f32 v204,v24,v25\n"
"v_mfma_f32_16x16x32_bf16 v[36:39],a[208:211],v[128:131],v[36:39]\nbuffer_store_dwordx4 v[196:199],v242,s[44:47],s69 offen offset:0 sc1\nv_mfma_f32_16x16x32_bf16 v[40:43],a[224:227],v[128:131],v[40:43]\n"
"ds_read_b64_tr_b16 v[156:157],v227 offset:45056\nv_cvt_pk_bf16_f32 v205,v26,v27\nv_mfma_f32_16x16x32_bf16 v[40:43],a[228:231],v[132:135],v[40:43]\nds_read_b64_tr_b16 v[158:159],v227 offset:45568\nv_cvt_pk_bf16_f32 v202,v20,v21\n"
"v_mfma_f32_16x16x32_bf16 v[40:43],a[232:235],v[136:139],v[40:43]\nv_cvt_pk_bf16_f32 v203,v22,v23\nv_cvt_pk_bf16_f32 v200,v16,v17\nv_mfma_f32_16x16x32_bf16 v[40:43],a[236:239],v[140:143],v[40:43]\nv_cvt_pk_bf16_f32 v201,v18,v19\n"
"v_mfma_f32_16x16x32_bf16 v[44:47],a[252:255],v[140:143],v[44:47]\nbuffer_store_dwordx4 v[200:203],v240,s[44:47],s69 offen offset:1024 sc1\nv_mfma_f32_16x16x32_bf16 v[44:47],a[248:251],v[136:139],v[44:47]\n"
"v_mfma_f32_16x16x32_bf16 v[44:47],a[244:247],v[132:135],v[44:47]\ns_waitcnt vmcnt(25) lgkmcnt(0)\nv_mfma_f32_16x16x32_bf16 v[44:47],a[240:243],v[128:131],v[44:47]\nv_mfma_f32_16x16x32_bf16 v[60:63],a[240:243],v[144:147],v[60:63]\n"
"s_barrier\nv_mfma_f32_16x16x32_bf16 v[60:63],a[244:247],v[148:151],v[60:63]\nds_read_b64_tr_b16 v[128:129],v224 offset:49152\nv_mfma_f32_16x16x32_bf16 v[60:63],a[248:251],v[152:155],v[60:63]\n"
"ds_read_b64_tr_b16 v[130:131],v224 offset:49664\nv_mfma_f32_16x16x32_bf16 v[60:63],a[252:255],v[156:159],v[60:63]\nbuffer_store_dwordx4 v[204:207],v242,s[44:47],s69 offen offset:1024 sc1\n"
"v_mfma_f32_16x16x32_bf16 v[56:59],a[236:239],v[156:159],v[56:59]\nds_read_b64_tr_b16 v[132:133],v224 offset:53248\nv_cvt_pk_bf16_f32 v208,v32,v33\nv_mfma_f32_16x16x32_bf16 v[56:59],a[232:235],v[152:155],v[56:59]\n"
"ds_read_b64_tr_b16 v[134:135],v224 offset:53760\nv_cvt_pk_bf16_f32 v209,v34,v35\nv_mfma_f32_16x16x32_bf16 v[56:59],a[228:231],v[148:151],v[56:59]\nds_read_b64_tr_b16 v[136:137],v224 offset:57344\nv_cvt_pk_bf16_f32 v210,v36,v37\n"
"v_mfma_f32_16x16x32_bf16 v[56:59],a[224:227],v[144:147],v[56:59]\nv_cvt_pk_bf16_f32 v211,v38,v39\nv_cvt_pk_bf16_f32 v212,v40,v41\nv_mfma_f32_16x16x32_bf16 v[52:55],a[208:211],v[144:147],v[52:55]\n"
"ds_read_b64_tr_b16 v[138:139],v224 offset:57856\nv_cvt_pk_bf16_f32 v213,v42,v43\nv_mfma_f32_16x16x32_bf16 v[52:55],a[212:215],v[148:151],v[52:55]\nds_read_b64_tr_b16 v[140:141],v224 offset:61440\nv_cvt_pk_bf16_f32 v214,v44,v45\n"
"v_mfma_f32_16x16x32_bf16 v[52:55],a[216:219],v[152:155],v[52:55]\nds_read_b64_tr_b16 v[142:143],v224 offset:61952\nv_cvt_pk_bf16_f32 v215,v46,v47\nv_mfma_f32_16x16x32_bf16 v[52:55],a[220:223],v[156:159],v[52:55]\n"
"buffer_store_dwordx4 v[208:211],v240,s[44:47],s69 offen offset:2048 sc1\nv_mfma_f32_16x16x32_bf16 v[48:51],a[204:207],v[156:159],v[48:51]\nv_mfma_f32_16x16x32_bf16 v[48:51],a[200:203],v[152:155],v[48:51]\n"
"v_mfma_f32_16x16x32_bf16 v[48:51],a[196:199],v[148:151],v[48:51]\ns_waitcnt lgkmcnt(0)\nv_mfma_f32_16x16x32_bf16 v[48:51],a[192:195],v[144:147],v[48:51]\nbuffer_store_dwordx4 v[212:215],v242,s[44:47],s69 offen offset:2048 sc1\n"
".p2align 6\nLLs_%=:\nv_mfma_f32_16x16x32_bf16 v[64:67],a[192:195],v[128:131],v[64:67]\nds_read_b64_tr_b16 v[144:145],v225 offset:49152\nv_mfma_f32_16x16x32_bf16 v[64:67],a[196:199],v[132:135],v[64:67]\n"
"ds_read_b64_tr_b16 v[146:147],v225 offset:49664\nv_xor_b32 v224,0x10000,v224\nv_mfma_f32_16x16x32_bf16 v[64:67],a[200:203],v[136:139],v[64:67]\nds_read_b64_tr_b16 v[148:149],v225 offset:53248\n"
"v_mfma_f32_16x16x32_bf16 v[64:67],a[204:207],v[140:143],v[64:67]\ns_add_u32 s65,s65,s66\ns_cmp_gt_u32 s70,1\ns_cselect_b32 s75,s65,0x40000000\nv_mfma_f32_16x16x32_bf16 v[68:71],a[220:223],v[140:143],v[68:71]\n"
"ds_read_b64_tr_b16 v[150:151],v225 offset:53760\nv_mfma_f32_16x16x32_bf16 v[68:71],a[216:219],v[136:139],v[68:71]\nds_read_b64_tr_b16 v[152:153],v225 offset:57344\nv_mfma_f32_16x16x32_bf16 v[68:71],a[212:215],v[132:135],v[68:71]\n"
"ds_read_b64_tr_b16 v[154:155],v225 offset:57856\nv_mfma_f32_16x16x32_bf16 v[68:71],a[208:211],v[128:131],v[68:71]\ns_cmp_eq_u32 s70,s15\ns_cselect_b32 s75,s14,s75\ns_cselect_b64 s[36:37],s[34:35],s[36:37]\n"
"v_mfma_f32_16x16x32_bf16 v[72:75],a[224:227],v[128:131],v[72:75]\nds_read_b64_tr_b16 v[156:157],v225 offset:61440\nv_mfma_f32_16x16x32_bf16 v[72:75],a[228:231],v[132:135],v[72:75]\nds_read_b64_tr_b16 v[158:159],v225 offset:61952\n"
"v_mfma_f32_16x16x32_bf16 v[72:75],a[232:235],v[136:139],v[72:75]\ns_cmp_eq_u32 s70,s15\ns_cselect_b32 s48,0,s48\ns_cselect_b32 s49,128,s49\nv_mfma_f32_16x16x32_bf16 v[72:75],a[236:239],v[140:143],v[72:75]\ns_cmp_eq_u32 s70,s15\n"
"s_cselect_b32 s50,0x100000,s50\ns_cselect_b32 s51,0x100080,s51\nv_mfma_f32_16x16x32_bf16 v[76:79],a[252:255],v[140:143],v[76:79]\ns_and_b32 s37,s37,0xffff\ns_add_u32 s60,s48,s75\ns_add_u32 m0,s61,0\n"
"v_mfma_f32_16x16x32_bf16 v[76:79],a[248:251],v[136:139],v[76:79]\nbuffer_load_dwordx4 v228,s[36:39],s60 offen offset:0 lds\nv_mfma_f32_16x16x32_bf16 v[76:79],a[244:247],v[132:135],v[76:79]\ns_waitcnt lgkmcnt(0)\n"
"v_mfma_f32_16x16x32_bf16 v[76:79],a[240:243],v[128:131],v[76:79]\ns_lshr_b32 s71,s67,1\ns_lshl_b32 s71,s71,20\ns_add_u32 s71,s71,s73\nv_mfma_f32_16x16x32_bf16 v[92:95],a[240:243],v[144:147],v[92:95]\n"
"ds_read_b64_tr_b16 v[128:129],v226 offset:49152\nv_mfma_f32_16x16x32_bf16 v[92:95],a[244:247],v[148:151],v[92:95]\nds_read_b64_tr_b16 v[130:131],v226 offset:49664\nv_xor_b32 v225,0x10000,v225\n"
"v_mfma_f32_16x16x32_bf16 v[92:95],a[248:251],v[152:155],v[92:95]\nds_read_b64_tr_b16 v[132:133],v226 offset:53248\nv_mfma_f32_16x16x32_bf16 v[92:95],a[252:255],v[156:159],v[92:95]\n"
"buffer_load_dwordx4 v229,s[36:39],s60 offen offset:1024 lds\nv_mfma_f32_16x16x32_bf16 v[88:91],a[236:239],v[156:159],v[88:91]\nds_read_b64_tr_b16 v[134:135],v226 offset:53760\nv_cvt_pk_bf16_f32 v160,v64,v65\n"
"v_mfma_f32_16x16x32_bf16 v[88:91],a[232:235],v[152:155],v[88:91]\nds_read_b64_tr_b16 v[136:137],v226 offset:57344\nv_cvt_pk_bf16_f32 v161,v66,v67\nv_mfma_f32_16x16x32_bf16 v[88:91],a[228:231],v[148:151],v[88:91]\n"
"ds_read_b64_tr_b16 v[138:139],v226 offset:57856\nv_cvt_pk_bf16_f32 v162,v68,v69\nv_mfma_f32_16x16x32_bf16 v[88:91],a[224:227],v[144:147],v[88:91]\nbuffer_load_dwordx4 v230,s[36:39],s60 offen offset:2048 lds\n"
"v_mfma_f32_16x16x32_bf16 v[84:87],a[208:211],v[144:147],v[84:87]\nds_read_b64_tr_b16 v[140:141],v226 offset:61440\nv_cvt_pk_bf16_f32 v163,v70,v71\nv_mfma_f32_16x16x32_bf16 v[84:87],a[212:215],v[148:151],v[84:87]\n"
"ds_read_b64_tr_b16 v[142:143],v226 offset:61952\nv_cvt_pk_bf16_f32 v164,v72,v73\nv_mfma_f32_16x16x32_bf16 v[84:87],a[216:219],v[152:155],v[84:87]\ns_bitcmp1_b32 s67,0\ns_cselect_b32 s72,s74,0\nv_cvt_pk_bf16_f32 v165,v74,v75\n"
"v_mfma_f32_16x16x32_bf16 v[84:87],a[220:223],v[156:159],v[84:87]\nbuffer_load_dwordx4 v231,s[36:39],s60 offen offset:3072 lds\nv_mfma_f32_16x16x32_bf16 v[80:83],a[204:207],v[156:159],v[80:83]\ns_mov_b32 m0,s62\n"
"s_add_u32 s68,s71,s72\ns_add_u32 s67,s67,1\nv_mfma_f32_16x16x32_bf16 v[80:83],a[200:203],v[152:155],v[80:83]\ns_mov_b32 s60,0\ns_cmp_gt_u32 s70,2\ns_cselect_b32 s59,s65,0x40000000\n"
"v_mfma_f32_16x16x32_bf16 v[80:83],a[196:199],v[148:151],v[80:83]\ns_waitcnt lgkmcnt(0)\nv_cvt_pk_bf16_f32 v166,v76,v77\nv_cvt_pk_bf16_f32 v167,v78,v79\nv_mfma_f32_16x16x32_bf16 v[80:83],a[192:195],v[144:147],v[80:83]\n"
"buffer_load_dwordx4 v232,s[40:43],s60 offen offset:0 lds\nv_mfma_f32_16x16x32_bf16 v[96:99],a[192:195],v[128:131],v[96:99]\nds_read_b64_tr_b16 v[144:145],v227 offset:49152\ns_mov_b32 s60,7168\n"
"v_mfma_f32_16x16x32_bf16 v[96:99],a[196:199],v[132:135],v[96:99]\nds_read_b64_tr_b16 v[146:147],v227 offset:49664\nv_xor_b32 v226,0x10000,v226\nv_mfma_f32_16x16x32_bf16 v[96:99],a[200:203],v[136:139],v[96:99]\n"
"ds_read_b64_tr_b16 v[148:149],v227 offset:53248\nv_mfma_f32_16x16x32_bf16 v[96:99],a[204:207],v[140:143],v[96:99]\nbuffer_load_dwordx4 v232,s[40:43],s60 offen offset:1024 lds\n"
"v_mfma_f32_16x16x32_bf16 v[100:103],a[220:223],v[140:143],v[100:103]\nds_read_b64_tr_b16 v[150:151],v227 offset:53760\ns_mov_b32 s60,14336\nv_mfma_f32_16x16x32_bf16 v[100:103],a[216:219],v[136:139],v[100:103]\n"
"ds_read_b64_tr_b16 v[152:153],v227 offset:57344\nv_cvt_pk_bf16_f32 v174,v92,v93\nv_mfma_f32_16x16x32_bf16 v[100:103],a[212:215],v[132:135],v[100:103]\nds_read_b64_tr_b16 v[154:155],v227 offset:57856\n"
"v_cvt_pk_bf16_f32 v175,v94,v95\nv_mfma_f32_16x16x32_bf16 v[100:103],a[208:211],v[128:131],v[100:103]\nbuffer_load_dwordx4 v232,s[40:43],s60 offen offset:2048 lds\n"
"v_mfma_f32_16x16x32_bf16 v[104:107],a[224:227],v[128:131],v[104:107]\nds_read_b64_tr_b16 v[156:157],v227 offset:61440\ns_mov_b32 s60,21504\nv_mfma_f32_16x16x32_bf16 v[104:107],a[228:231],v[132:135],v[104:107]\n"
"ds_read_b64_tr_b16 v[158:159],v227 offset:61952\nv_cvt_pk_bf16_f32 v172,v88,v89\nv_mfma_f32_16x16x32_bf16 v[104:107],a[232:235],v[136:139],v[104:107]\nv_cvt_pk_bf16_f32 v173,v90,v91\nv_cvt_pk_bf16_f32 v170,v84,v85\n"
"v_mfma_f32_16x16x32_bf16 v[104:107],a[236:239],v[140:143],v[104:107]\nbuffer_load_dwordx4 v232,s[40:43],s60 offen offset:3072 lds\nv_mfma_f32_16x16x32_bf16 v[108:111],a[252:255],v[140:143],v[108:111]\ns_add_u32 s60,s49,s75\n"
"s_add_u32 m0,s61,16384\nv_cvt_pk_bf16_f32 v171,v86,v87\nv_mfma_f32_16x16x32_bf16 v[108:111],a[248:251],v[136:139],v[108:111]\nv_cvt_pk_bf16_f32 v168,v80,v81\nv_cvt_pk_bf16_f32 v169,v82,v83\n"
"v_mfma_f32_16x16x32_bf16 v[108:111],a[244:247],v[132:135],v[108:111]\ns_waitcnt vmcnt(28) lgkmcnt(0)\nv_mfma_f32_16x16x32_bf16 v[108:111],a[240:243],v[128:131],v[108:111]\n"
"buffer_store_dwordx4 v[160:163],v240,s[44:47],s68 offen offset:0 sc1\nv_mfma_f32_16x16x32_bf16 v[124:127],a[240:243],v[144:147],v[124:127]\ns_barrier\nv_mfma_f32_16x16x32_bf16 v[124:127],a[244:247],v[148:151],v[124:127]\n"
"ds_read_b64_tr_b16 v[128:129],v224 offset:0\nv_xor_b32 v227,0x10000,v227\nv_mfma_f32_16x16x32_bf16 v[124:127],a[248:251],v[152:155],v[124:127]\nds_read_b64_tr_b16 v[130:131],v224 offset:512\n"
"v_mfma_f32_16x16x32_bf16 v[124:127],a[252:255],v[156:159],v[124:127]\nv_cvt_pk_bf16_f32 v176,v96,v97\nv_cvt_pk_bf16_f32 v177,v98,v99\nv_mfma_f32_16x16x32_bf16 v[120:123],a[236:239],v[156:159],v[120:123]\n"
"ds_read_b64_tr_b16 v[132:133],v224 offset:4096\nv_cvt_pk_bf16_f32 v178,v100,v101\nv_mfma_f32_16x16x32_bf16 v[120:123],a[232:235],v[152:155],v[120:123]\nds_read_b64_tr_b16 v[134:135],v224 offset:4608\n"
"v_cvt_pk_bf16_f32 v179,v102,v103\nv_mfma_f32_16x16x32_bf16 v[120:123],a[228:231],v[148:151],v[120:123]\nds_read_b64_tr_b16 v[136:137],v224 offset:8192\nv_cvt_pk_bf16_f32 v180,v104,v105\n"
"v_mfma_f32_16x16x32_bf16 v[120:123],a[224:227],v[144:147],v[120:123]\nv_cvt_pk_bf16_f32 v181,v106,v107\nv_cvt_pk_bf16_f32 v182,v108,v109\nv_mfma_f32_16x16x32_bf16 v[116:119],a[208:211],v[144:147],v[116:119]\n"
"ds_read_b64_tr_b16 v[138:139],v224 offset:8704\nv_cvt_pk_bf16_f32 v183,v110,v111\nv_mfma_f32_16x16x32_bf16 v[116:119],a[212:215],v[148:151],v[116:119]\nds_read_b64_tr_b16 v[140:141],v224 offset:12288\n"
"v_mfma_f32_16x16x32_bf16 v[116:119],a[216:219],v[152:155],v[116:119]\nds_read_b64_tr_b16 v[142:143],v224 offset:12800\nv_mfma_f32_16x16x32_bf16 v[116:119],a[220:223],v[156:159],v[116:119]\n"
"v_mfma_f32_16x16x32_bf16 v[112:115],a[204:207],v[156:159],v[112:115]\nbuffer_store_dwordx4 v[164:167],v242,s[44:47],s68 offen offset:0 sc1\nv_mfma_f32_16x16x32_bf16 v[112:115],a[200:203],v[152:155],v[112:115]\n"
"v_mfma_f32_16x16x32_bf16 v[112:115],a[196:199],v[148:151],v[112:115]\ns_waitcnt lgkmcnt(0)\nv_mfma_f32_16x16x32_bf16 v[112:115],a[192:195],v[144:147],v[112:115]\nv_mfma_f32_16x16x32_bf16 v[64:67],a[0:3],v[128:131],0\n"
"ds_read_b64_tr_b16 v[144:145],v225 offset:0\nv_mfma_f32_16x16x32_bf16 v[64:67],a[4:7],v[132:135],v[64:67]\nds_read_b64_tr_b16 v[146:147],v225 offset:512\nv_mfma_f32_16x16x32_bf16 v[64:67],a[8:11],v[136:139],v[64:67]\n"
"ds_read_b64_tr_b16 v[148:149],v225 offset:4096\nv_mfma_f32_16x16x32_bf16 v[64:67],a[12:15],v[140:143],v[64:67]\nbuffer_load_dwordx4 v228,s[36:39],s60 offen offset:0 lds\nv_mfma_f32_16x16x32_bf16 v[68:71],a[28:31],v[140:143],0\n"
"ds_read_b64_tr_b16 v[150:151],v225 offset:4608\nv_cvt_pk_bf16_f32 v190,v124,v125\nv_mfma_f32_16x16x32_bf16 v[68:71],a[24:27],v[136:139],v[68:71]\nds_read_b64_tr_b16 v[152:153],v225 offset:8192\nv_cvt_pk_bf16_f32 v191,v126,v127\n"
"v_mfma_f32_16x16x32_bf16 v[68:71],a[20:23],v[132:135],v[68:71]\nds_read_b64_tr_b16 v[154:155],v225 offset:8704\nv_cvt_pk_bf16_f32 v188,v120,v121\nv_mfma_f32_16x16x32_bf16 v[68:71],a[16:19],v[128:131],v[68:71]\n"
"v_cvt_pk_bf16_f32 v189,v122,v123\nv_cvt_pk_bf16_f32 v186,v116,v117\nv_mfma_f32_16x16x32_bf16 v[72:75],a[32:35],v[128:131],0\nds_read_b64_tr_b16 v[156:157],v225 offset:12288\nv_cvt_pk_bf16_f32 v187,v118,v119\n"
"v_mfma_f32_16x16x32_bf16 v[72:75],a[36:39],v[132:135],v[72:75]\nds_read_b64_tr_b16 v[158:159],v225 offset:12800\nv_cvt_pk_bf16_f32 v184,v112,v113\nv_mfma_f32_16x16x32_bf16 v[72:75],a[40:43],v[136:139],v[72:75]\n"
"v_cvt_pk_bf16_f32 v185,v114,v115\nv_mfma_f32_16x16x32_bf16 v[72:75],a[44:47],v[140:143],v[72:75]\nv_mfma_f32_16x16x32_bf16 v[76:79],a[60:63],v[140:143],0\nv_mfma_f32_16x16x32_bf16 v[76:79],a[56:59],v[136:139],v[76:79]\n"
"v_mfma_f32_16x16x32_bf16 v[76:79],a[52:55],v[132:135],v[76:79]\ns_waitcnt lgkmcnt(0)\nv_mfma_f32_16x16x32_bf16 v[76:79],a[48:51],v[128:131],v[76:79]\nv_mfma_f32_16x16x32_bf16 v[92:95],a[48:51],v[144:147],0\n"
"ds_read_b64_tr_b16 v[128:129],v226 offset:0\nv_mfma_f32_16x16x32_bf16 v[92:95],a[52:55],v[148:151],v[92:95]\nds_read_b64_tr_b16 v[130:131],v226 offset:512\nv_mfma_f32_16x16x32_bf16 v[92:95],a[56:59],v[152:155],v[92:95]\n"
"ds_read_b64_tr_b16 v[132:133],v226 offset:4096\nv_mfma_f32_16x16x32_bf16 v[92:95],a[60:63],v[156:159],v[92:95]\nbuffer_load_dwordx4 v229,s[36:39],s60 offen offset:1024 lds\nv_mfma_f32_16x16x32_bf16 v[88:91],a[44:47],v[156:159],0\n"
"ds_read_b64_tr_b16 v[134:135],v226 offset:4608\nv_mfma_f32_16x16x32_bf16 v[88:91],a[40:43],v[152:155],v[88:91]\nds_read_b64_tr_b16 v[136:137],v226 offset:8192\nv_mfma_f32_16x16x32_bf16 v[88:91],a[36:39],v[148:151],v[88:91]\n"
"ds_read_b64_tr_b16 v[138:139],v226 offset:8704\nv_mfma_f32_16x16x32_bf16 v[88:91],a[32:35],v[144:147],v[88:91]\nbuffer_store_dwordx4 v[168:171],v240,s[44:47],s68 offen offset:1024 sc1\n"
"v_mfma_f32_16x16x32_bf16 v[84:87],a[16:19],v[144:147],0\nds_read_b64_tr_b16 v[140:141],v226 offset:12288\nv_mfma_f32_16x16x32_bf16 v[84:87],a[20:23],v[148:151],v[84:87]\nds_read_b64_tr_b16 v[142:143],v226 offset:12800\n"
"v_mfma_f32_16x16x32_bf16 v[84:87],a[24:27],v[152:155],v[84:87]\nv_mfma_f32_16x16x32_bf16 v[84:87],a[28:31],v[156:159],v[84:87]\nv_mfma_f32_16x16x32_bf16 v[80:83],a[12:15],v[156:159],0\n"
"buffer_load_dword v250,v245,s[36:39],s59 offen offset:128\nv_mfma_f32_16x16x32_bf16 v[80:83],a[8:11],v[152:155],v[80:83]\nv_mfma_f32_16x16x32_bf16 v[80:83],a[4:7],v[148:151],v[80:83]\ns_waitcnt lgkmcnt(0)\n"
"v_mfma_f32_16x16x32_bf16 v[80:83],a[0:3],v[144:147],v[80:83]\nv_mfma_f32_16x16x32_bf16 v[96:99],a[0:3],v[128:131],0\nds_read_b64_tr_b16 v[144:145],v227 offset:0\nv_mfma_f32_16x16x32_bf16 v[96:99],a[4:7],v[132:135],v[96:99]\n"
"ds_read_b64_tr_b16 v[146:147],v227 offset:512\nv_mfma_f32_16x16x32_bf16 v[96:99],a[8:11],v[136:139],v[96:99]\nds_read_b64_tr_b16 v[148:149],v227 offset:4096\nv_mfma_f32_16x16x32_bf16 v[96:99],a[12:15],v[140:143],v[96:99]\n"
"buffer_load_dwordx4 v230,s[36:39],s60 offen offset:2048 lds\nv_mfma_f32_16x16x32_bf16 v[100:103],a[28:31],v[140:143],0\nds_read_b64_tr_b16 v[150:151],v227 offset:4608\n"
"v_mfma_f32_16x16x32_bf16 v[100:103],a[24:27],v[136:139],v[100:103]\nds_read_b64_tr_b16 v[152:153],v227 offset:8192\nv_mfma_f32_16x16x32_bf16 v[100:103],a[20:23],v[132:135],v[100:103]\n"
"ds_read_b64_tr_b16 v[154:155],v227 offset:8704\nv_mfma_f32_16x16x32_bf16 v[100:103],a[16:19],v[128:131],v[100:103]\nbuffer_store_dwordx4 v[172:175],v242,s[44:47],s68 offen offset:1024 sc1\n"
"v_mfma_f32_16x16x32_bf16 v[104:107],a[32:35],v[128:131],0\nds_read_b64_tr_b16 v[156:157],v227 offset:12288\nv_mfma_f32_16x16x32_bf16 v[104:107],a[36:39],v[132:135],v[104:107]\nds_read_b64_tr_b16 v[158:159],v227 offset:12800\n"
"v_mfma_f32_16x16x32_bf16 v[104:107],a[40:43],v[136:139],v[104:107]\nv_mfma_f32_16x16x32_bf16 v[104:107],a[44:47],v[140:143],v[104:107]\nv_mfma_f32_16x16x32_bf16 v[108:111],a[60:63],v[140:143],0\n"
"buffer_load_dword v250,v246,s[36:39],s59 offen offset:128\nv_mfma_f32_16x16x32_bf16 v[108:111],a[56:59],v[136:139],v[108:111]\nv_mfma_f32_16x16x32_bf16 v[108:111],a[52:55],v[132:135],v[108:111]\ns_waitcnt vmcnt(33) lgkmcnt(0)\n"
"v_mfma_f32_16x16x32_bf16 v[108:111],a[48:51],v[128:131],v[108:111]\nv_mfma_f32_16x16x32_bf16 v[124:127],a[48:51],v[144:147],0\ns_barrier\nv_mfma_f32_16x16x32_bf16 v[124:127],a[52:55],v[148:151],v[124:127]\n"
"ds_read_b64_tr_b16 v[128:129],v224 offset:16384\nv_mfma_f32_16x16x32_bf16 v[124:127],a[56:59],v[152:155],v[124:127]\nds_read_b64_tr_b16 v[130:131],v224 offset:16896\n"
"v_mfma_f32_16x16x32_bf16 v[124:127],a[60:63],v[156:159],v[124:127]\nbuffer_load_dwordx4 v231,s[36:39],s60 offen offset:3072 lds\nv_mfma_f32_16x16x32_bf16 v[120:123],a[44:47],v[156:159],0\n"
"ds_read_b64_tr_b16 v[132:133],v224 offset:20480\ns_add_u32 s60,s50,s75\nv_mfma_f32_16x16x32_bf16 v[120:123],a[40:43],v[152:155],v[120:123]\nds_read_b64_tr_b16 v[134:135],v224 offset:20992\ns_add_u32 m0,s61,32768\n"
"v_mfma_f32_16x16x32_bf16 v[120:123],a[36:39],v[148:151],v[120:123]\nds_read_b64_tr_b16 v[136:137],v224 offset:24576\nv_mfma_f32_16x16x32_bf16 v[120:123],a[32:35],v[144:147],v[120:123]\n"
"v_mfma_f32_16x16x32_bf16 v[116:119],a[16:19],v[144:147],0\nds_read_b64_tr_b16 v[138:139],v224 offset:25088\nv_mfma_f32_16x16x32_bf16 v[116:119],a[20:23],v[148:151],v[116:119]\nds_read_b64_tr_b16 v[140:141],v224 offset:28672\n"
"v_mfma_f32_16x16x32_bf16 v[116:119],a[24:27],v[152:155],v[116:119]\nds_read_b64_tr_b16 v[142:143],v224 offset:29184\nv_mfma_f32_16x16x32_bf16 v[116:119],a[28:31],v[156:159],v[116:119]\n"
"buffer_store_dwordx4 v[176:179],v240,s[44:47],s68 offen offset:2048 sc1\nv_mfma_f32_16x16x32_bf16 v[112:115],a[12:15],v[156:159],0\nv_mfma_f32_16x16x32_bf16 v[112:115],a[8:11],v[152:155],v[112:115]\n"
"v_mfma_f32_16x16x32_bf16 v[112:115],a[4:7],v[148:151],v[112:115]\ns_waitcnt lgkmcnt(0)\nv_mfma_f32_16x16x32_bf16 v[112:115],a[0:3],v[144:147],v[112:115]\nv_mfma_f32_16x16x32_bf16 v[64:67],a[64:67],v[128:131],v[64:67]\n"
"ds_read_b64_tr_b16 v[144:145],v225 offset:16384\nv_mfma_f32_16x16x32_bf16 v[64:67],a[68:71],v[132:135],v[64:67]\nds_read_b64_tr_b16 v[146:147],v225 offset:16896\nv_mfma_f32_16x16x32_bf16 v[64:67],a[72:75],v[136:139],v[64:67]\n"
"ds_read_b64_tr_b16 v[148:149],v225 offset:20480\nv_mfma_f32_16x16x32_bf16 v[64:67],a[76:79],v[140:143],v[64:67]\nbuffer_load_dwordx4 v228,s[36:39],s60 offen offset:0 lds\n"
"v_mfma_f32_16x16x32_bf16 v[68:71],a[92:95],v[140:143],v[68:71]\nds_read_b64_tr_b16 v[150:151],v225 offset:20992\nv_mfma_f32_16x16x32_bf16 v[68:71],a[88:91],v[136:139],v[68:71]\nds_read_b64_tr_b16 v[152:153],v225 offset:24576\n"
"v_mfma_f32_16x16x32_bf16 v[68:71],a[84:87],v[132:135],v[68:71]\nds_read_b64_tr_b16 v[154:155],v225 offset:25088\nv_mfma_f32_16x16x32_bf16 v[68:71],a[80:83],v[128:131],v[68:71]\n"
"v_mfma_f32_16x16x32_bf16 v[72:75],a[96:99],v[128:131],v[72:75]\nds_read_b64_tr_b16 v[156:157],v225 offset:28672\nv_mfma_f32_16x16x32_bf16 v[72:75],a[100:103],v[132:135],v[72:75]\nds_read_b64_tr_b16 v[158:159],v225 offset:29184\n"
"v_mfma_f32_16x16x32_bf16 v[72:75],a[104:107],v[136:139],v[72:75]\nv_mfma_f32_16x16x32_bf16 v[72:75],a[108:111],v[140:143],v[72:75]\nv_mfma_f32_16x16x32_bf16 v[76:79],a[124:127],v[140:143],v[76:79]\n"
"buffer_store_dwordx4 v[180:183],v242,s[44:47],s68 offen offset:2048 sc1\nv_mfma_f32_16x16x32_bf16 v[76:79],a[120:123],v[136:139],v[76:79]\nv_mfma_f32_16x16x32_bf16 v[76:79],a[116:119],v[132:135],v[76:79]\ns_waitcnt lgkmcnt(0)\n"
"v_mfma_f32_16x16x32_bf16 v[76:79],a[112:115],v[128:131],v[76:79]\nv_mfma_f32_16x16x32_bf16 v[92:95],a[112:115],v[144:147],v[92:95]\nds_read_b64_tr_b16 v[128:129],v226 offset:16384\n"
"v_mfma_f32_16x16x32_bf16 v[92:95],a[116:119],v[148:151],v[92:95]\nds_read_b64_tr_b16 v[130:131],v226 offset:16896\nv_mfma_f32_16x16x32_bf16 v[92:95],a[120:123],v[152:155],v[92:95]\nds_read_b64_tr_b16 v[132:133],v226 offset:20480\n"
"v_mfma_f32_16x16x32_bf16 v[92:95],a[124:127],v[156:159],v[92:95]\nbuffer_load_dwordx4 v229,s[36:39],s60 offen offset:1024 lds\nv_mfma_f32_16x16x32_bf16 v[88:91],a[108:111],v[156:159],v[88:91]\n"
"ds_read_b64_tr_b16 v[134:135],v226 offset:20992\nv_mfma_f32_16x16x32_bf16 v[88:91],a[104:107],v[152:155],v[88:91]\nds_read_b64_tr_b16 v[136:137],v226 offset:24576\nv_mfma_f32_16x16x32_bf16 v[88:91],a[100:103],v[148:151],v[88:91]\n"
"ds_read_b64_tr_b16 v[138:139],v226 offset:25088\nv_mfma_f32_16x16x32_bf16 v[88:91],a[96:99],v[144:147],v[88:91]\nv_mfma_f32_16x16x32_bf16 v[84:87],a[80:83],v[144:147],v[84:87]\nds_read_b64_tr_b16 v[140:141],v226 offset:28672\n"
"v_mfma_f32_16x16x32_bf16 v[84:87],a[84:87],v[148:151],v[84:87]\nds_read_b64_tr_b16 v[142:143],v226 offset:29184\nv_mfma_f32_16x16x32_bf16 v[84:87],a[88:91],v[152:155],v[84:87]\n"
"v_mfma_f32_16x16x32_bf16 v[84:87],a[92:95],v[156:159],v[84:87]\nv_mfma_f32_16x16x32_bf16 v[80:83],a[76:79],v[156:159],v[80:83]\nv_mfma_f32_16x16x32_bf16 v[80:83],a[72:75],v[152:155],v[80:83]\n"
"v_mfma_f32_16x16x32_bf16 v[80:83],a[68:71],v[148:151],v[80:83]\ns_waitcnt lgkmcnt(0)\nv_mfma_f32_16x16x32_bf16 v[80:83],a[64:67],v[144:147],v[80:83]\nv_mfma_f32_16x16x32_bf16 v[96:99],a[64:67],v[128:131],v[96:99]\n"
"ds_read_b64_tr_b16 v[144:145],v227 offset:16384\nv_mfma_f32_16x16x32_bf16 v[96:99],a[68:71],v[132:135],v[96:99]\nds_read_b64_tr_b16 v[146:147],v227 offset:16896\nv_mfma_f32_16x16x32_bf16 v[96:99],a[72:75],v[136:139],v[96:99]\n"
"ds_read_b64_tr_b16 v[148:149],v227 offset:20480\nv_mfma_f32_16x16x32_bf16 v[96:99],a[76:79],v[140:143],v[96:99]\nbuffer_load_dwordx4 v230,s[36:39],s60 offen offset:2048 lds\n"
"v_mfma_f32_16x16x32_bf16 v[100:103],a[92:95],v[140:143],v[100:103]\nds_read_b64_tr_b16 v[150:151],v227 offset:20992\nv_mfma_f32_16x16x32_bf16 v[100:103],a[88:91],v[136:139],v[100:103]\n"
"ds_read_b64_tr_b16 v[152:153],v227 offset:24576\nv_mfma_f32_16x16x32_bf16 v[100:103],a[84:87],v[132:135],v[100:103]\nds_read_b64_tr_b16 v[154:155],v227 offset:25088\n"
"v_mfma_f32_16x16x32_bf16 v[100:103],a[80:83],v[128:131],v[100:103]\nbuffer_store_dwordx4 v[184:187],v240,s[44:47],s68 offen offset:3072 sc1\nv_mfma_f32_16x16x32_bf16 v[104:107],a[96:99],v[128:131],v[104:107]\n"
"ds_read_b64_tr_b16 v[156:157],v227 offset:28672\nv_mfma_f32_16x16x32_bf16 v[104:107],a[100:103],v[132:135],v[104:107]\nds_read_b64_tr_b16 v[158:159],v227 offset:29184\n"
"v_mfma_f32_16x16x32_bf16 v[104:107],a[104:107],v[136:139],v[104:107]\nv_mfma_f32_16x16x32_bf16 v[104:107],a[108:111],v[140:143],v[104:107]\nv_mfma_f32_16x16x32_bf16 v[108:111],a[124:127],v[140:143],v[108:111]\n"
"v_mfma_f32_16x16x32_bf16 v[108:111],a[120:123],v[136:139],v[108:111]\nv_mfma_f32_16x16x32_bf16 v[108:111],a[116:119],v[132:135],v[108:111]\ns_waitcnt vmcnt(33) lgkmcnt(0)\n"
"v_mfma_f32_16x16x32_bf16 v[108:111],a[112:115],v[128:131],v[108:111]\nv_mfma_f32_16x16x32_bf16 v[124:127],a[112:115],v[144:147],v[124:127]\ns_barrier\nv_mfma_f32_16x16x32_bf16 v[124:127],a[116:119],v[148:151],v[124:127]\n"
"ds_read_b64_tr_b16 v[128:129],v224 offset:32768\nv_mfma_f32_16x16x32_bf16 v[124:127],a[120:123],v[152:155],v[124:127]\nds_read_b64_tr_b16 v[130:131],v224 offset:33280\n"
"v_mfma_f32_16x16x32_bf16 v[124:127],a[124:127],v[156:159],v[124:127]\nbuffer_load_dwordx4 v231,s[36:39],s60 offen offset:3072 lds\nv_mfma_f32_16x16x32_bf16 v[120:123],a[108:111],v[156:159],v[120:123]\n"
"ds_read_b64_tr_b16 v[132:133],v224 offset:36864\ns_add_u32 s60,s51,s75\nv_mfma_f32_16x16x32_bf16 v[120:123],a[104:107],v[152:155],v[120:123]\nds_read_b64_tr_b16 v[134:135],v224 offset:37376\ns_add_u32 m0,s61,49152\n"
"v_mfma_f32_16x16x32_bf16 v[120:123],a[100:103],v[148:151],v[120:123]\nds_read_b64_tr_b16 v[136:137],v224 offset:40960\nv_mfma_f32_16x16x32_bf16 v[120:123],a[96:99],v[144:147],v[120:123]\n"
"buffer_store_dwordx4 v[188:191],v242,s[44:47],s68 offen offset:3072 sc1\nv_mfma_f32_16x16x32_bf16 v[116:119],a[80:83],v[144:147],v[116:119]\nds_read_b64_tr_b16 v[138:139],v224 offset:41472\n"
"v_mfma_f32_16x16x32_bf16 v[116:119],a[84:87],v[148:151],v[116:119]\nds_read_b64_tr_b16 v[140:141],v224 offset:45056\nv_mfma_f32_16x16x32_bf16 v[116:119],a[88:91],v[152:155],v[116:119]\n"
"ds_read_b64_tr_b16 v[142:143],v224 offset:45568\nv_mfma_f32_16x16x32_bf16 v[116:119],a[92:95],v[156:159],v[116:119]\nv_mfma_f32_16x16x32_bf16 v[112:115],a[76:79],v[156:159],v[112:115]\n"
"v_mfma_f32_16x16x32_bf16 v[112:115],a[72:75],v[152:155],v[112:115]\nv_mfma_f32_16x16x32_bf16 v[112:115],a[68:71],v[148:151],v[112:115]\ns_waitcnt lgkmcnt(0)\ns_waitcnt vmcnt(18)\n"
"v_mfma_f32_16x16x32_bf16 v[112:115],a[64:67],v[144:147],v[112:115]\nds_read_b64_tr_b16 v[192:193],v236 offset:0\nv_mfma_f32_16x16x32_bf16 v[64:67],a[128:131],v[128:131],v[64:67]\nds_read_b64_tr_b16 v[144:145],v225 offset:32768\n"
"v_mfma_f32_16x16x32_bf16 v[64:67],a[132:135],v[132:135],v[64:67]\nds_read_b64_tr_b16 v[146:147],v225 offset:33280\nv_mfma_f32_16x16x32_bf16 v[64:67],a[136:139],v[136:139],v[64:67]\nds_read_b64_tr_b16 v[148:149],v225 offset:36864\n"
"v_mfma_f32_16x16x32_bf16 v[64:67],a[140:143],v[140:143],v[64:67]\nbuffer_load_dwordx4 v228,s[36:39],s60 offen offset:0 lds\nv_mfma_f32_16x16x32_bf16 v[68:71],a[156:159],v[140:143],v[68:71]\n"
"ds_read_b64_tr_b16 v[150:151],v225 offset:37376\nv_mfma_f32_16x16x32_bf16 v[68:71],a[152:155],v[136:139],v[68:71]\nds_read_b64_tr_b16 v[152:153],v225 offset:40960\nv_mfma_f32_16x16x32_bf16 v[68:71],a[148:151],v[132:135],v[68:71]\n"
"ds_read_b64_tr_b16 v[154:155],v225 offset:41472\nv_mfma_f32_16x16x32_bf16 v[68:71],a[144:147],v[128:131],v[68:71]\nds_read_b64_tr_b16 v[194:195],v236 offset:2048\nv_mfma_f32_16x16x32_bf16 v[72:75],a[160:163],v[128:131],v[72:75]\n"
"ds_read_b64_tr_b16 v[156:157],v225 offset:45056\nv_mfma_f32_16x16x32_bf16 v[72:75],a[164:167],v[132:135],v[72:75]\nds_read_b64_tr_b16 v[158:159],v225 offset:45568\nv_mfma_f32_16x16x32_bf16 v[72:75],a[168:171],v[136:139],v[72:75]\n"
"ds_read_b64_tr_b16 v[196:197],v236 offset:8\nv_mfma_f32_16x16x32_bf16 v[72:75],a[172:175],v[140:143],v[72:75]\nds_read_b64_tr_b16 v[198:199],v236 offset:2056\nv_mfma_f32_16x16x32_bf16 v[76:79],a[188:191],v[140:143],v[76:79]\n"
"ds_read_b64_tr_b16 v[200:201],v237 offset:0\nv_mfma_f32_16x16x32_bf16 v[76:79],a[184:187],v[136:139],v[76:79]\nds_read_b64_tr_b16 v[202:203],v237 offset:2048\nv_mfma_f32_16x16x32_bf16 v[76:79],a[180:183],v[132:135],v[76:79]\n"
"s_waitcnt lgkmcnt(4)\nds_read_b64_tr_b16 v[204:205],v237 offset:8\nv_mfma_f32_16x16x32_bf16 v[76:79],a[176:179],v[128:131],v[76:79]\nds_read_b64_tr_b16 v[206:207],v237 offset:2056\n"
"v_mfma_f32_16x16x32_bf16 v[92:95],a[176:179],v[144:147],v[92:95]\nds_read_b64_tr_b16 v[128:129],v226 offset:32768\nv_mfma_f32_16x16x32_bf16 v[92:95],a[180:183],v[148:151],v[92:95]\nds_read_b64_tr_b16 v[130:131],v226 offset:33280\n"
"v_mfma_f32_16x16x32_bf16 v[92:95],a[184:187],v[152:155],v[92:95]\nds_read_b64_tr_b16 v[132:133],v226 offset:36864\nv_mfma_f32_16x16x32_bf16 v[92:95],a[188:191],v[156:159],v[92:95]\n"
"buffer_load_dwordx4 v229,s[36:39],s60 offen offset:1024 lds\nv_mfma_f32_16x16x32_bf16 v[88:91],a[172:175],v[156:159],v[88:91]\nds_read_b64_tr_b16 v[134:135],v226 offset:37376\n"
"v_mfma_f32_16x16x32_bf16 v[88:91],a[168:171],v[152:155],v[88:91]\nds_read_b64_tr_b16 v[136:137],v226 offset:40960\nv_mfma_f32_16x16x32_bf16 v[88:91],a[164:167],v[148:151],v[88:91]\nds_read_b64_tr_b16 v[138:139],v226 offset:41472\n"
"v_mfma_f32_16x16x32_bf16 v[88:91],a[160:163],v[144:147],v[88:91]\nv_mfma_f32_16x16x32_bf16 v[84:87],a[144:147],v[144:147],v[84:87]\nds_read_b64_tr_b16 v[140:141],v226 offset:45056\n"
"v_mfma_f32_16x16x32_bf16 v[84:87],a[148:151],v[148:151],v[84:87]\nds_read_b64_tr_b16 v[142:143],v226 offset:45568\nv_mfma_f32_16x16x32_bf16 v[84:87],a[152:155],v[152:155],v[84:87]\n"
"v_mfma_f32_16x16x32_bf16 v[84:87],a[156:159],v[156:159],v[84:87]\nv_mfma_f32_16x16x32_bf16 v[80:83],a[140:143],v[156:159],v[80:83]\nv_mfma_f32_16x16x32_bf16 v[80:83],a[136:139],v[152:155],v[80:83]\n"
"v_mfma_f32_16x16x32_bf16 v[80:83],a[132:135],v[148:151],v[80:83]\ns_waitcnt lgkmcnt(0)\nv_mfma_f32_16x16x32_bf16 v[80:83],a[128:131],v[144:147],v[80:83]\nbuffer_store_dwordx4 v[192:195],v240,s[44:47],s56 offen offset:0 sc1\n"
"v_mfma_f32_16x16x32_bf16 v[96:99],a[128:131],v[128:131],v[96:99]\nds_read_b64_tr_b16 v[144:145],v227 offset:32768\nv_mfma_f32_16x16x32_bf16 v[96:99],a[132:135],v[132:135],v[96:99]\nds_read_b64_tr_b16 v[146:147],v227 offset:33280\n"
"v_mfma_f32_16x16x32_bf16 v[96:99],a[136:139],v[136:139],v[96:99]\nds_read_b64_tr_b16 v[148:149],v227 offset:36864\nv_mfma_f32_16x16x32_bf16 v[96:99],a[140:143],v[140:143],v[96:99]\n"
"buffer_load_dwordx4 v230,s[36:39],s60 offen offset:2048 lds\nv_mfma_f32_16x16x32_bf16 v[100:103],a[156:159],v[140:143],v[100:103]\nds_read_b64_tr_b16 v[150:151],v227 offset:37376\n"
"v_mfma_f32_16x16x32_bf16 v[100:103],a[152:155],v[136:139],v[100:103]\nds_read_b64_tr_b16 v[152:153],v227 offset:40960\nv_mfma_f32_16x16x32_bf16 v[100:103],a[148:151],v[132:135],v[100:103]\n"
"ds_read_b64_tr_b16 v[154:155],v227 offset:41472\nv_mfma_f32_16x16x32_bf16 v[100:103],a[144:147],v[128:131],v[100:103]\nbuffer_store_dwordx4 v[196:199],v240,s[44:47],s56 offen offset:1024 sc1\n"
"v_mfma_f32_16x16x32_bf16 v[104:107],a[160:163],v[128:131],v[104:107]\nds_read_b64_tr_b16 v[156:157],v227 offset:45056\nv_mfma_f32_16x16x32_bf16 v[104:107],a[164:167],v[132:135],v[104:107]\n"
"ds_read_b64_tr_b16 v[158:159],v227 offset:45568\nv_mfma_f32_16x16x32_bf16 v[104:107],a[168:171],v[136:139],v[104:107]\nv_mfma_f32_16x16x32_bf16 v[104:107],a[172:175],v[140:143],v[104:107]\n"
"buffer_store_dwordx4 v[200:203],v240,s[44:47],s56 offen offset:2048 sc1\nv_mfma_f32_16x16x32_bf16 v[108:111],a[188:191],v[140:143],v[108:111]\nv_mfma_f32_16x16x32_bf16 v[108:111],a[184:187],v[136:139],v[108:111]\n"
"v_mfma_f32_16x16x32_bf16 v[108:111],a[180:183],v[132:135],v[108:111]\ns_waitcnt vmcnt(32) lgkmcnt(0)\nv_mfma_f32_16x16x32_bf16 v[108:111],a[176:179],v[128:131],v[108:111]\n"
"buffer_store_dwordx4 v[204:207],v240,s[44:47],s56 offen offset:3072 sc1\nv_mfma_f32_16x16x32_bf16 v[124:127],a[176:179],v[144:147],v[124:127]\ns_barrier\nv_mfma_f32_16x16x32_bf16 v[124:127],a[180:183],v[148:151],v[124:127]\n"
"ds_read_b64_tr_b16 v[128:129],v224 offset:49152\nv_mfma_f32_16x16x32_bf16 v[124:127],a[184:187],v[152:155],v[124:127]\nds_read_b64_tr_b16 v[130:131],v224 offset:49664\n"
"v_mfma_f32_16x16x32_bf16 v[124:127],a[188:191],v[156:159],v[124:127]\nbuffer_load_dwordx4 v231,s[36:39],s60 offen offset:3072 lds\nv_mfma_f32_16x16x32_bf16 v[120:123],a[172:175],v[156:159],v[120:123]\n"
"ds_read_b64_tr_b16 v[132:133],v224 offset:53248\ns_xor_b32 s61,s61,0x10000\nv_mfma_f32_16x16x32_bf16 v[120:123],a[168:171],v[152:155],v[120:123]\nds_read_b64_tr_b16 v[134:135],v224 offset:53760\n"
"v_mfma_f32_16x16x32_bf16 v[120:123],a[164:167],v[148:151],v[120:123]\nds_read_b64_tr_b16 v[136:137],v224 offset:57344\nv_mfma_f32_16x16x32_bf16 v[120:123],a[160:163],v[144:147],v[120:123]\ns_cmp_eq_u32 s42,0\n"
"s_cselect_b64 s[40:41],s[40:41],s[80:81]\ns_mov_b32 s56,s57\nv_mfma_f32_16x16x32_bf16 v[116:119],a[144:147],v[144:147],v[116:119]\nds_read_b64_tr_b16 v[138:139],v224 offset:57856\ns_and_b32 s41,s41,0xffff\n"
"v_mfma_f32_16x16x32_bf16 v[116:119],a[148:151],v[148:151],v[116:119]\nds_read_b64_tr_b16 v[140:141],v224 offset:61440\nv_mfma_f32_16x16x32_bf16 v[116:119],a[152:155],v[152:155],v[116:119]\n"
"ds_read_b64_tr_b16 v[142:143],v224 offset:61952\nv_mfma_f32_16x16x32_bf16 v[116:119],a[156:159],v[156:159],v[116:119]\ns_cmp_lt_u32 s56,0x40000000\ns_cselect_b32 s42,0x8000,0\ns_mov_b32 s57,s19\n"
"v_mfma_f32_16x16x32_bf16 v[112:115],a[140:143],v[156:159],v[112:115]\nv_mfma_f32_16x16x32_bf16 v[112:115],a[136:139],v[152:155],v[112:115]\nv_mfma_f32_16x16x32_bf16 v[112:115],a[132:135],v[148:151],v[112:115]\n"
"s_waitcnt lgkmcnt(0)\nv_mfma_f32_16x16x32_bf16 v[112:115],a[128:131],v[144:147],v[112:115]\ns_sub_u32 s70,s70,1\ns_cmp_lg_u32 s70,0\ns_cbranch_scc1 LLs_%=\nv_lshrrev_b32 v229,8,v240\nv_bfe_u32 v230,v240,4,4\n"
"v_xor_b32 v230,v230,v229\nv_lshlrev_b32 v230,4,v230\nv_lshl_add_u32 v232,v229,10,v230\nv_xor_b32 v233,64,v232\nv_add_u32 v233,3072,v233\nv_xor_b32 v234,128,v232\nv_add_u32 v234,6144,v234\nv_xor_b32 v235,192,v232\n"
"v_add_u32 v235,9216,v235\ns_mov_b64 s[40:41],s[30:31]\ns_mov_b32 s42,0x200000\ns_and_b32 s41,s41,0xffff\ns_mov_b32 s82,0\ns_lshl_b32 s56,s16,14\ns_add_u32 s57,s56,0x4000\ns_lshl_b32 s58,s17,14\ns_add_u32 s59,s58,0x4000\n"
"s_lshl_b32 s62,s5,12\ns_add_u32 s62,s62,0x10000\nv_mfma_f32_16x16x32_bf16 v[64:67],a[192:195],v[128:131],v[64:67]\nds_read_b64_tr_b16 v[144:145],v225 offset:49152\ns_add_u32 s60,s82,s56\n"
"v_mfma_f32_16x16x32_bf16 v[64:67],a[196:199],v[132:135],v[64:67]\nds_read_b64_tr_b16 v[146:147],v225 offset:49664\ns_add_u32 m0,s62,0\nv_mfma_f32_16x16x32_bf16 v[64:67],a[200:203],v[136:139],v[64:67]\n"
"ds_read_b64_tr_b16 v[148:149],v225 offset:53248\nv_cvt_pk_bf16_f32 v222,v60,v61\nv_mfma_f32_16x16x32_bf16 v[64:67],a[204:207],v[140:143],v[64:67]\nbuffer_load_dwordx4 v232,s[40:43],s60 offen offset:0 lds\n"
"v_mfma_f32_16x16x32_bf16 v[68:71],a[220:223],v[140:143],v[68:71]\nds_read_b64_tr_b16 v[150:151],v225 offset:53760\nv_cvt_pk_bf16_f32 v223,v62,v63\nv_mfma_f32_16x16x32_bf16 v[68:71],a[216:219],v[136:139],v[68:71]\n"
"ds_read_b64_tr_b16 v[152:153],v225 offset:57344\nv_cvt_pk_bf16_f32 v220,v56,v57\nv_mfma_f32_16x16x32_bf16 v[68:71],a[212:215],v[132:135],v[68:71]\nds_read_b64_tr_b16 v[154:155],v225 offset:57856\nv_cvt_pk_bf16_f32 v221,v58,v59\n"
"v_mfma_f32_16x16x32_bf16 v[68:71],a[208:211],v[128:131],v[68:71]\nbuffer_load_dwordx4 v233,s[40:43],s60 offen offset:1024 lds\nv_mfma_f32_16x16x32_bf16 v[72:75],a[224:227],v[128:131],v[72:75]\n"
"ds_read_b64_tr_b16 v[156:157],v225 offset:61440\nv_cvt_pk_bf16_f32 v218,v52,v53\nv_mfma_f32_16x16x32_bf16 v[72:75],a[228:231],v[132:135],v[72:75]\nds_read_b64_tr_b16 v[158:159],v225 offset:61952\nv_cvt_pk_bf16_f32 v219,v54,v55\n"
"v_mfma_f32_16x16x32_bf16 v[72:75],a[232:235],v[136:139],v[72:75]\ns_lshr_b32 s71,s67,1\ns_lshl_b32 s71,s71,20\ns_add_u32 s71,s71,s73\nv_mfma_f32_16x16x32_bf16 v[72:75],a[236:239],v[140:143],v[72:75]\n"
"buffer_load_dwordx4 v234,s[40:43],s60 offen offset:2048 lds\nv_mfma_f32_16x16x32_bf16 v[76:79],a[252:255],v[140:143],v[76:79]\ns_bitcmp1_b32 s67,0\ns_cselect_b32 s72,s74,0\nv_cvt_pk_bf16_f32 v216,v48,v49\n"
"v_mfma_f32_16x16x32_bf16 v[76:79],a[248:251],v[136:139],v[76:79]\ns_add_u32 s68,s71,s72\ns_add_u32 s67,s67,1\nv_cvt_pk_bf16_f32 v217,v50,v51\nv_mfma_f32_16x16x32_bf16 v[76:79],a[244:247],v[132:135],v[76:79]\ns_waitcnt lgkmcnt(0)\n"
"v_mfma_f32_16x16x32_bf16 v[76:79],a[240:243],v[128:131],v[76:79]\nbuffer_load_dwordx4 v235,s[40:43],s60 offen offset:3072 lds\nv_mfma_f32_16x16x32_bf16 v[92:95],a[240:243],v[144:147],v[92:95]\n"
"ds_read_b64_tr_b16 v[128:129],v226 offset:49152\ns_add_u32 s60,s82,s57\nv_mfma_f32_16x16x32_bf16 v[92:95],a[244:247],v[148:151],v[92:95]\nds_read_b64_tr_b16 v[130:131],v226 offset:49664\ns_add_u32 m0,s62,16384\n"
"v_mfma_f32_16x16x32_bf16 v[92:95],a[248:251],v[152:155],v[92:95]\nds_read_b64_tr_b16 v[132:133],v226 offset:53248\nv_mfma_f32_16x16x32_bf16 v[92:95],a[252:255],v[156:159],v[92:95]\n"
"buffer_load_dwordx4 v232,s[40:43],s60 offen offset:0 lds\nv_mfma_f32_16x16x32_bf16 v[88:91],a[236:239],v[156:159],v[88:91]\nds_read_b64_tr_b16 v[134:135],v226 offset:53760\nv_cvt_pk_bf16_f32 v160,v64,v65\n"
"v_mfma_f32_16x16x32_bf16 v[88:91],a[232:235],v[152:155],v[88:91]\nds_read_b64_tr_b16 v[136:137],v226 offset:57344\nv_cvt_pk_bf16_f32 v161,v66,v67\nv_mfma_f32_16x16x32_bf16 v[88:91],a[228:231],v[148:151],v[88:91]\n"
"ds_read_b64_tr_b16 v[138:139],v226 offset:57856\nv_cvt_pk_bf16_f32 v162,v68,v69\nv_mfma_f32_16x16x32_bf16 v[88:91],a[224:227],v[144:147],v[88:91]\nbuffer_load_dwordx4 v233,s[40:43],s60 offen offset:1024 lds\n"
"v_mfma_f32_16x16x32_bf16 v[84:87],a[208:211],v[144:147],v[84:87]\nds_read_b64_tr_b16 v[140:141],v226 offset:61440\nv_cvt_pk_bf16_f32 v163,v70,v71\nv_mfma_f32_16x16x32_bf16 v[84:87],a[212:215],v[148:151],v[84:87]\n"
"ds_read_b64_tr_b16 v[142:143],v226 offset:61952\nv_cvt_pk_bf16_f32 v164,v72,v73\nv_mfma_f32_16x16x32_bf16 v[84:87],a[216:219],v[152:155],v[84:87]\nv_cvt_pk_bf16_f32 v165,v74,v75\nv_cvt_pk_bf16_f32 v166,v76,v77\n"
"v_mfma_f32_16x16x32_bf16 v[84:87],a[220:223],v[156:159],v[84:87]\nbuffer_load_dwordx4 v234,s[40:43],s60 offen offset:2048 lds\nv_mfma_f32_16x16x32_bf16 v[80:83],a[204:207],v[156:159],v[80:83]\nv_cvt_pk_bf16_f32 v167,v78,v79\n"
"v_mfma_f32_16x16x32_bf16 v[80:83],a[200:203],v[152:155],v[80:83]\nv_mfma_f32_16x16x32_bf16 v[80:83],a[196:199],v[148:151],v[80:83]\ns_waitcnt lgkmcnt(0)\nv_mfma_f32_16x16x32_bf16 v[80:83],a[192:195],v[144:147],v[80:83]\n"
"buffer_load_dwordx4 v235,s[40:43],s60 offen offset:3072 lds\nv_mfma_f32_16x16x32_bf16 v[96:99],a[192:195],v[128:131],v[96:99]\nds_read_b64_tr_b16 v[144:145],v227 offset:49152\ns_add_u32 s60,s82,s58\n"
"v_mfma_f32_16x16x32_bf16 v[96:99],a[196:199],v[132:135],v[96:99]\nds_read_b64_tr_b16 v[146:147],v227 offset:49664\ns_add_u32 m0,s62,32768\nv_mfma_f32_16x16x32_bf16 v[96:99],a[200:203],v[136:139],v[96:99]\n"
"ds_read_b64_tr_b16 v[148:149],v227 offset:53248\nv_mfma_f32_16x16x32_bf16 v[96:99],a[204:207],v[140:143],v[96:99]\nbuffer_load_dwordx4 v232,s[40:43],s60 offen offset:0 lds\n"
"v_mfma_f32_16x16x32_bf16 v[100:103],a[220:223],v[140:143],v[100:103]\nds_read_b64_tr_b16 v[150:151],v227 offset:53760\nv_cvt_pk_bf16_f32 v174,v92,v93\nv_mfma_f32_16x16x32_bf16 v[100:103],a[216:219],v[136:139],v[100:103]\n"
"ds_read_b64_tr_b16 v[152:153],v227 offset:57344\nv_cvt_pk_bf16_f32 v175,v94,v95\nv_mfma_f32_16x16x32_bf16 v[100:103],a[212:215],v[132:135],v[100:103]\nds_read_b64_tr_b16 v[154:155],v227 offset:57856\n"
"v_cvt_pk_bf16_f32 v172,v88,v89\nv_mfma_f32_16x16x32_bf16 v[100:103],a[208:211],v[128:131],v[100:103]\nbuffer_load_dwordx4 v233,s[40:43],s60 offen offset:1024 lds\n"
"v_mfma_f32_16x16x32_bf16 v[104:107],a[224:227],v[128:131],v[104:107]\nds_read_b64_tr_b16 v[156:157],v227 offset:61440\nv_cvt_pk_bf16_f32 v173,v90,v91\nv_mfma_f32_16x16x32_bf16 v[104:107],a[228:231],v[132:135],v[104:107]\n"
"ds_read_b64_tr_b16 v[158:159],v227 offset:61952\nv_cvt_pk_bf16_f32 v170,v84,v85\nv_mfma_f32_16x16x32_bf16 v[104:107],a[232:235],v[136:139],v[104:107]\nv_cvt_pk_bf16_f32 v171,v86,v87\nv_cvt_pk_bf16_f32 v168,v80,v81\n"
"v_mfma_f32_16x16x32_bf16 v[104:107],a[236:239],v[140:143],v[104:107]\nbuffer_load_dwordx4 v234,s[40:43],s60 offen offset:2048 lds\nv_mfma_f32_16x16x32_bf16 v[108:111],a[252:255],v[140:143],v[108:111]\n"
"v_cvt_pk_bf16_f32 v169,v82,v83\nv_mfma_f32_16x16x32_bf16 v[108:111],a[248:251],v[136:139],v[108:111]\nv_mfma_f32_16x16x32_bf16 v[108:111],a[244:247],v[132:135],v[108:111]\ns_waitcnt lgkmcnt(0)\n"
"v_mfma_f32_16x16x32_bf16 v[108:111],a[240:243],v[128:131],v[108:111]\nbuffer_load_dwordx4 v235,s[40:43],s60 offen offset:3072 lds\nv_mfma_f32_16x16x32_bf16 v[124:127],a[240:243],v[144:147],v[124:127]\ns_add_u32 s60,s82,s59\n"
"s_add_u32 m0,s62,65536\nv_mfma_f32_16x16x32_bf16 v[124:127],a[244:247],v[148:151],v[124:127]\nv_mfma_f32_16x16x32_bf16 v[124:127],a[248:251],v[152:155],v[124:127]\n"
"v_mfma_f32_16x16x32_bf16 v[124:127],a[252:255],v[156:159],v[124:127]\nbuffer_load_dwordx4 v232,s[40:43],s60 offen offset:0 lds\nv_mfma_f32_16x16x32_bf16 v[120:123],a[236:239],v[156:159],v[120:123]\nv_cvt_pk_bf16_f32 v176,v96,v97\n"
"v_cvt_pk_bf16_f32 v177,v98,v99\nv_mfma_f32_16x16x32_bf16 v[120:123],a[232:235],v[152:155],v[120:123]\nv_cvt_pk_bf16_f32 v178,v100,v101\nv_cvt_pk_bf16_f32 v179,v102,v103\n"
"v_mfma_f32_16x16x32_bf16 v[120:123],a[228:231],v[148:151],v[120:123]\nv_cvt_pk_bf16_f32 v180,v104,v105\nv_cvt_pk_bf16_f32 v181,v106,v107\nv_mfma_f32_16x16x32_bf16 v[120:123],a[224:227],v[144:147],v[120:123]\n"
"buffer_load_dwordx4 v233,s[40:43],s60 offen offset:1024 lds\nv_mfma_f32_16x16x32_bf16 v[116:119],a[208:211],v[144:147],v[116:119]\nv_cvt_pk_bf16_f32 v182,v108,v109\nv_cvt_pk_bf16_f32 v183,v110,v111\n"
"v_mfma_f32_16x16x32_bf16 v[116:119],a[212:215],v[148:151],v[116:119]\nv_mfma_f32_16x16x32_bf16 v[116:119],a[216:219],v[152:155],v[116:119]\nv_mfma_f32_16x16x32_bf16 v[116:119],a[220:223],v[156:159],v[116:119]\n"
"buffer_load_dwordx4 v234,s[40:43],s60 offen offset:2048 lds\nv_mfma_f32_16x16x32_bf16 v[112:115],a[204:207],v[156:159],v[112:115]\nv_mfma_f32_16x16x32_bf16 v[112:115],a[200:203],v[152:155],v[112:115]\n"
"v_mfma_f32_16x16x32_bf16 v[112:115],a[196:199],v[148:151],v[112:115]\nv_mfma_f32_16x16x32_bf16 v[112:115],a[192:195],v[144:147],v[112:115]\nbuffer_load_dwordx4 v235,s[40:43],s60 offen offset:3072 lds\ns_nop 7\n"
"v_cvt_pk_bf16_f32 v190,v124,v125\nv_cvt_pk_bf16_f32 v191,v126,v127\nv_cvt_pk_bf16_f32 v188,v120,v121\nv_cvt_pk_bf16_f32 v189,v122,v123\nv_cvt_pk_bf16_f32 v186,v116,v117\nv_cvt_pk_bf16_f32 v187,v118,v119\n"
"v_cvt_pk_bf16_f32 v184,v112,v113\nv_cvt_pk_bf16_f32 v185,v114,v115\nbuffer_store_dwordx4 v[216:219],v240,s[44:47],s69 offen offset:3072 sc1\nbuffer_store_dwordx4 v[220:223],v242,s[44:47],s69 offen offset:3072 sc1\n"
"buffer_store_dwordx4 v[160:163],v240,s[44:47],s68 offen offset:0 sc1\nbuffer_store_dwordx4 v[164:167],v242,s[44:47],s68 offen offset:0 sc1\nbuffer_store_dwordx4 v[168:171],v240,s[44:47],s68 offen offset:1024 sc1\n"
"buffer_store_dwordx4 v[172:175],v242,s[44:47],s68 offen offset:1024 sc1\nbuffer_store_dwordx4 v[176:179],v240,s[44:47],s68 offen offset:2048 sc1\nbuffer_store_dwordx4 v[180:183],v242,s[44:47],s68 offen offset:2048 sc1\n"
"buffer_store_dwordx4 v[184:187],v240,s[44:47],s68 offen offset:3072 sc1\nbuffer_store_dwordx4 v[188:191],v242,s[44:47],s68 offen offset:3072 sc1\ns_waitcnt vmcnt(26)\ns_branch LEND_%=\nLK_%=:\nv_bfe_u32 v241,v244,1,3\n"
"s_lshl_b32 s71,s5,10\ns_add_u32 s71,s71,0x20000\nv_add_u32 v242,s71,v240\nv_mov_b32 v243,0x3f803f80\ns_mov_b32 s74,0\nv_mov_b32 v248,0\nv_mov_b32 v249,0\nv_mov_b32 v250,0\nv_mov_b32 v251,0\ns_waitcnt vmcnt(24)\ns_barrier\n"
"ds_read_b64_tr_b16 v[128:129],v224 offset:0\nds_read_b64_tr_b16 v[130:131],v224 offset:512\nds_read_b64_tr_b16 v[132:133],v224 offset:4096\nds_read_b64_tr_b16 v[134:135],v224 offset:4608\n"
"ds_read_b64_tr_b16 v[136:137],v224 offset:8192\nds_read_b64_tr_b16 v[138:139],v224 offset:8704\nds_read_b64_tr_b16 v[140:141],v224 offset:12288\nds_read_b64_tr_b16 v[142:143],v224 offset:12800\ns_waitcnt vmcnt(20)\n"
"ds_read_b128 a[0:3],v236 offset:0\nds_read_b128 a[4:7],v237 offset:0\nds_read_b128 a[8:11],v238 offset:0\nds_read_b128 a[12:15],v239 offset:0\ns_waitcnt vmcnt(16)\nds_read_b128 a[16:19],v236 offset:16384\n"
"ds_read_b128 a[20:23],v237 offset:16384\nds_read_b128 a[24:27],v238 offset:16384\nds_read_b128 a[28:31],v239 offset:16384\ns_waitcnt vmcnt(12)\nds_read_b128 a[32:35],v236 offset:32768\nds_read_b128 a[36:39],v237 offset:32768\n"
"ds_read_b128 a[40:43],v238 offset:32768\nds_read_b128 a[44:47],v239 offset:32768\ns_waitcnt vmcnt(8)\nds_read_b128 a[48:51],v188 offset:0\nds_read_b128 a[52:55],v189 offset:0\nds_read_b128 a[56:59],v190 offset:0\n"
"ds_read_b128 a[60:63],v191 offset:0\ns_waitcnt lgkmcnt(0)\nv_mfma_f32_16x16x32_bf16 v[0:3],a[0:3],v[128:131],0\nds_read_b64_tr_b16 v[144:145],v225 offset:0\ns_and_b32 s71,s11,15\n"
"v_mfma_f32_16x16x32_bf16 v[0:3],a[4:7],v[132:135],v[0:3]\nds_read_b64_tr_b16 v[146:147],v225 offset:512\ns_lshl_b32 s71,s71,5\nv_mfma_f32_16x16x32_bf16 v[0:3],a[8:11],v[136:139],v[0:3]\n"
"ds_read_b64_tr_b16 v[148:149],v225 offset:4096\ns_lshl_b32 s72,s8,4\nv_mfma_f32_16x16x32_bf16 v[0:3],a[12:15],v[140:143],v[0:3]\ns_add_u32 s60,s53,s57\ns_add_u32 m0,s62,49152\nv_lshrrev_b32 v245,2,v244\n"
"v_mfma_f32_16x16x32_bf16 v[4:7],a[28:31],v[140:143],0\nds_read_b64_tr_b16 v[150:151],v225 offset:4608\nv_add_u32 v245,s71,v245\nv_mfma_f32_16x16x32_bf16 v[4:7],a[24:27],v[136:139],v[4:7]\n"
"ds_read_b64_tr_b16 v[152:153],v225 offset:8192\nv_mul_lo_u32 v245,v245,s8\nv_mfma_f32_16x16x32_bf16 v[4:7],a[20:23],v[132:135],v[4:7]\nds_read_b64_tr_b16 v[154:155],v225 offset:8704\nv_add_u32 v246,s72,v245\n"
"v_mfma_f32_16x16x32_bf16 v[4:7],a[16:19],v[128:131],v[4:7]\nbuffer_load_dwordx4 v232,s[40:43],s60 offen offset:0 lds\nv_mfma_f32_16x16x32_bf16 v[8:11],a[32:35],v[128:131],0\nds_read_b64_tr_b16 v[156:157],v225 offset:12288\n"
"v_mfma_f32_16x16x32_bf16 v[8:11],a[36:39],v[132:135],v[8:11]\nds_read_b64_tr_b16 v[158:159],v225 offset:12800\nv_mfma_f32_16x16x32_bf16 v[8:11],a[40:43],v[136:139],v[8:11]\n"
"v_mfma_f32_16x16x32_bf16 v[8:11],a[44:47],v[140:143],v[8:11]\nbuffer_load_dwordx4 v233,s[40:43],s60 offen offset:1024 lds\nv_mfma_f32_16x16x32_bf16 v[12:15],a[60:63],v[140:143],0\ns_sub_u32 s88,s9,13\ns_cmp_lt_u32 s9,13\n"
"s_cselect_b32 s88,s9,s88\nv_mfma_f32_16x16x32_bf16 v[12:15],a[56:59],v[136:139],v[12:15]\ns_cmp_lt_u32 s88,12\ns_cselect_b32 s15,1,0\ns_add_u32 s70,s15,2\nv_mfma_f32_16x16x32_bf16 v[12:15],a[52:55],v[132:135],v[12:15]\n"
"s_waitcnt lgkmcnt(0)\ns_mul_i32 s67,s88,5\ns_lshl_b32 s6,s9,2\nv_mfma_f32_16x16x32_bf16 v[12:15],a[48:51],v[128:131],v[12:15]\nbuffer_load_dwordx4 v234,s[40:43],s60 offen offset:2048 lds\n"
"v_mfma_f32_16x16x32_bf16 v[28:31],a[48:51],v[144:147],0\nds_read_b64_tr_b16 v[128:129],v226 offset:0\ns_sub_u32 s6,s6,40\nv_mfma_f32_16x16x32_bf16 v[28:31],a[52:55],v[148:151],v[28:31]\n"
"ds_read_b64_tr_b16 v[130:131],v226 offset:512\nv_mfma_f32_16x16x32_bf16 v[28:31],a[56:59],v[152:155],v[28:31]\nds_read_b64_tr_b16 v[132:133],v226 offset:4096\nv_mfma_f32_16x16x32_bf16 v[28:31],a[60:63],v[156:159],v[28:31]\n"
"buffer_load_dwordx4 v235,s[40:43],s60 offen offset:3072 lds\nv_mfma_f32_16x16x32_bf16 v[24:27],a[44:47],v[156:159],0\nds_read_b64_tr_b16 v[134:135],v226 offset:4608\ns_add_u32 s60,s53,s58\n"
"v_mfma_f32_16x16x32_bf16 v[24:27],a[40:43],v[152:155],v[24:27]\nds_read_b64_tr_b16 v[136:137],v226 offset:8192\ns_add_u32 m0,s62,0\nv_mfma_f32_16x16x32_bf16 v[24:27],a[36:39],v[148:151],v[24:27]\n"
"ds_read_b64_tr_b16 v[138:139],v226 offset:8704\nv_mfma_f32_16x16x32_bf16 v[24:27],a[32:35],v[144:147],v[24:27]\nbuffer_load_dwordx4 v232,s[40:43],s60 offen offset:0 lds\nv_mfma_f32_16x16x32_bf16 v[20:23],a[16:19],v[144:147],0\n"
"ds_read_b64_tr_b16 v[140:141],v226 offset:12288\nv_mfma_f32_16x16x32_bf16 v[20:23],a[20:23],v[148:151],v[20:23]\nds_read_b64_tr_b16 v[142:143],v226 offset:12800\nv_mfma_f32_16x16x32_bf16 v[20:23],a[24:27],v[152:155],v[20:23]\n"
"s_cmp_lt_u32 s9,26\ns_cselect_b32 s67,s67,s6\ns_lshl_b32 s73,s10,2\nv_mfma_f32_16x16x32_bf16 v[20:23],a[28:31],v[156:159],v[20:23]\nbuffer_load_dwordx4 v233,s[40:43],s60 offen offset:1024 lds\n"
"v_mfma_f32_16x16x32_bf16 v[16:19],a[12:15],v[156:159],0\ns_add_u32 s73,s73,s5\ns_lshl_b32 s73,s73,15\ns_lshl_b32 s6,s13,14\nv_mfma_f32_16x16x32_bf16 v[16:19],a[8:11],v[152:155],v[16:19]\ns_add_u32 s73,s73,s6\n"
"s_mov_b32 s12,0x3e0293ee\nv_mfma_f32_16x16x32_bf16 v[16:19],a[4:7],v[148:151],v[16:19]\ns_waitcnt lgkmcnt(0)\ns_cmp_lt_u32 s9,13\ns_cselect_b32 s12,s12,0x3f800000\nv_mfma_f32_16x16x32_bf16 v[16:19],a[0:3],v[144:147],v[16:19]\n"
"buffer_load_dwordx4 v234,s[40:43],s60 offen offset:2048 lds\nv_mfma_f32_16x16x32_bf16 v[32:35],a[0:3],v[128:131],0\nds_read_b64_tr_b16 v[144:145],v227 offset:0\ns_and_b32 s14,s4,7\n"
"v_mfma_f32_16x16x32_bf16 v[32:35],a[4:7],v[132:135],v[32:35]\nds_read_b64_tr_b16 v[146:147],v227 offset:512\ns_lshl_b32 s14,s14,2\nv_mfma_f32_16x16x32_bf16 v[32:35],a[8:11],v[136:139],v[32:35]\n"
"ds_read_b64_tr_b16 v[148:149],v227 offset:4096\ns_lshr_b32 s6,s4,3\nv_mfma_f32_16x16x32_bf16 v[32:35],a[12:15],v[140:143],v[32:35]\nbuffer_load_dwordx4 v235,s[40:43],s60 offen offset:3072 lds\n"
"v_mfma_f32_16x16x32_bf16 v[36:39],a[28:31],v[140:143],0\nds_read_b64_tr_b16 v[150:151],v227 offset:4608\ns_add_u32 s60,s53,s59\nv_mfma_f32_16x16x32_bf16 v[36:39],a[24:27],v[136:139],v[36:39]\n"
"ds_read_b64_tr_b16 v[152:153],v227 offset:8192\ns_add_u32 m0,s62,16384\nv_mfma_f32_16x16x32_bf16 v[36:39],a[20:23],v[132:135],v[36:39]\nds_read_b64_tr_b16 v[154:155],v227 offset:8704\ns_and_b32 s6,s6,3\n"
"v_mfma_f32_16x16x32_bf16 v[36:39],a[16:19],v[128:131],v[36:39]\nbuffer_load_dwordx4 v232,s[40:43],s60 offen offset:0 lds\nv_mfma_f32_16x16x32_bf16 v[40:43],a[32:35],v[128:131],0\nds_read_b64_tr_b16 v[156:157],v227 offset:12288\n"
"s_waitcnt vmcnt(9)\nv_mfma_f32_16x16x32_bf16 v[40:43],a[36:39],v[132:135],v[40:43]\nds_read_b64_tr_b16 v[158:159],v227 offset:12800\ns_add_u32 s14,s14,s6\nv_mfma_f32_16x16x32_bf16 v[40:43],a[40:43],v[136:139],v[40:43]\n"
"ds_read_b128 a[64:67],v188 offset:16384\ns_lshl_b32 s14,s14,8\nv_mfma_f32_16x16x32_bf16 v[40:43],a[44:47],v[140:143],v[40:43]\nbuffer_load_dwordx4 v233,s[40:43],s60 offen offset:1024 lds\n"
"v_mfma_f32_16x16x32_bf16 v[44:47],a[60:63],v[140:143],0\nds_read_b128 a[68:71],v189 offset:16384\ns_lshr_b32 s16,s4,5\nv_mfma_f32_16x16x32_bf16 v[44:47],a[56:59],v[136:139],v[44:47]\nds_read_b128 a[72:75],v190 offset:16384\n"
"s_sub_u32 s16,15,s16\nv_mfma_f32_16x16x32_bf16 v[44:47],a[52:55],v[132:135],v[44:47]\ns_waitcnt vmcnt(14) lgkmcnt(3)\nds_read_b128 a[76:79],v191 offset:16384\nv_mfma_f32_16x16x32_bf16 v[44:47],a[48:51],v[128:131],v[44:47]\n"
"buffer_load_dwordx4 v234,s[40:43],s60 offen offset:2048 lds\nv_mfma_f32_16x16x32_bf16 v[60:63],a[48:51],v[144:147],0\ns_barrier\ns_lshl_b32 s16,s16,3\ns_and_b32 s6,s5,1\n"
"v_mfma_f32_16x16x32_bf16 v[60:63],a[52:55],v[148:151],v[60:63]\nds_read_b64_tr_b16 v[128:129],v224 offset:16384\ns_lshl_b32 s6,s6,2\nv_mfma_f32_16x16x32_bf16 v[60:63],a[56:59],v[152:155],v[60:63]\n"
"ds_read_b64_tr_b16 v[130:131],v224 offset:16896\ns_and_b32 s7,s5,2\nv_mfma_f32_16x16x32_bf16 v[60:63],a[60:63],v[156:159],v[60:63]\nbuffer_load_dwordx4 v235,s[40:43],s60 offen offset:3072 lds\n"
"v_mfma_f32_16x16x32_bf16 v[56:59],a[44:47],v[156:159],0\nds_read_b64_tr_b16 v[132:133],v224 offset:20480\ns_add_u32 s60,s49,s63\nv_mfma_f32_16x16x32_bf16 v[56:59],a[40:43],v[152:155],v[56:59]\n"
"ds_read_b64_tr_b16 v[134:135],v224 offset:20992\ns_add_u32 m0,s61,32768\nv_mfma_f32_16x16x32_bf16 v[56:59],a[36:39],v[148:151],v[56:59]\nds_read_b64_tr_b16 v[136:137],v224 offset:24576\ns_or_b32 s6,s6,s7\n"
"v_mfma_f32_16x16x32_bf16 v[56:59],a[32:35],v[144:147],v[56:59]\nbuffer_load_dwordx4 v228,s[36:39],s60 offen offset:0 lds\nv_mfma_f32_16x16x32_bf16 v[52:55],a[16:19],v[144:147],0\nds_read_b64_tr_b16 v[138:139],v224 offset:25088\n"
"s_waitcnt vmcnt(9)\nv_mfma_f32_16x16x32_bf16 v[52:55],a[20:23],v[148:151],v[52:55]\nds_read_b64_tr_b16 v[140:141],v224 offset:28672\ns_add_u32 s16,s16,s6\nv_mfma_f32_16x16x32_bf16 v[52:55],a[24:27],v[152:155],v[52:55]\n"
"ds_read_b64_tr_b16 v[142:143],v224 offset:29184\ns_lshr_b32 s17,s4,5\nv_mfma_f32_16x16x32_bf16 v[52:55],a[28:31],v[156:159],v[52:55]\nbuffer_load_dwordx4 v229,s[36:39],s60 offen offset:1024 lds\n"
"v_mfma_f32_16x16x32_bf16 v[48:51],a[12:15],v[156:159],0\nds_read_b128 a[80:83],v236 offset:49152\ns_lshl_b32 s17,s17,3\nv_mfma_f32_16x16x32_bf16 v[48:51],a[8:11],v[152:155],v[48:51]\nds_read_b128 a[84:87],v237 offset:49152\n"
"s_lshl_b32 s6,s5,1\nv_mfma_f32_16x16x32_bf16 v[48:51],a[4:7],v[148:151],v[48:51]\ns_waitcnt lgkmcnt(2)\nds_read_b128 a[88:91],v238 offset:49152\nv_mfma_f32_16x16x32_bf16 v[48:51],a[0:3],v[144:147],v[48:51]\n"
"buffer_load_dwordx4 v230,s[36:39],s60 offen offset:2048 lds\nv_mfma_f32_16x16x32_bf16 v[64:67],a[0:3],v[128:131],0\nds_read_b64_tr_b16 v[144:145],v225 offset:16384\ns_add_u32 s17,s17,s6\n"
"v_mfma_f32_16x16x32_bf16 v[64:67],a[4:7],v[132:135],v[64:67]\nds_read_b64_tr_b16 v[146:147],v225 offset:16896\ns_mul_i32 s89,s10,28\nv_mfma_f32_16x16x32_bf16 v[64:67],a[8:11],v[136:139],v[64:67]\n"
"ds_read_b64_tr_b16 v[148:149],v225 offset:20480\ns_add_u32 s89,s89,s9\nv_mfma_f32_16x16x32_bf16 v[64:67],a[12:15],v[140:143],v[64:67]\nbuffer_load_dwordx4 v231,s[36:39],s60 offen offset:3072 lds\n"
"v_mfma_f32_16x16x32_bf16 v[68:71],a[28:31],v[140:143],0\nds_read_b64_tr_b16 v[150:151],v225 offset:20992\ns_add_u32 s60,s54,s56\nv_mfma_f32_16x16x32_bf16 v[68:71],a[24:27],v[136:139],v[68:71]\n"
"ds_read_b64_tr_b16 v[152:153],v225 offset:24576\ns_add_u32 m0,s62,32768\nv_mfma_f32_16x16x32_bf16 v[68:71],a[20:23],v[132:135],v[68:71]\nds_read_b64_tr_b16 v[154:155],v225 offset:25088\ns_lshl_b32 s89,s89,2\n"
"v_mfma_f32_16x16x32_bf16 v[68:71],a[16:19],v[128:131],v[68:71]\nbuffer_load_dwordx4 v232,s[40:43],s60 offen offset:0 lds\nv_mfma_f32_16x16x32_bf16 v[72:75],a[32:35],v[128:131],0\nds_read_b64_tr_b16 v[156:157],v225 offset:28672\n"
"s_add_u32 s89,s89,s5\nv_mfma_f32_16x16x32_bf16 v[72:75],a[36:39],v[132:135],v[72:75]\nds_read_b64_tr_b16 v[158:159],v225 offset:29184\ns_and_b32 s89,s89,0x3ff\nv_mfma_f32_16x16x32_bf16 v[72:75],a[40:43],v[136:139],v[72:75]\n"
"ds_read_b128 a[92:95],v239 offset:49152\ns_lshr_b32 s83,0x249,s88\nv_mfma_f32_16x16x32_bf16 v[72:75],a[44:47],v[140:143],v[72:75]\nbuffer_load_dwordx4 v233,s[40:43],s60 offen offset:1024 lds\n"
"v_mfma_f32_16x16x32_bf16 v[76:79],a[60:63],v[140:143],0\ns_waitcnt vmcnt(10)\ns_and_b32 s83,s83,1\ns_lshl_b32 s6,s10,3\nv_mfma_f32_16x16x32_bf16 v[76:79],a[56:59],v[136:139],v[76:79]\nds_read_b128 a[96:99],v236 offset:0\n"
"s_lshl_b32 s7,s13,2\nv_mfma_f32_16x16x32_bf16 v[76:79],a[52:55],v[132:135],v[76:79]\ns_waitcnt lgkmcnt(2)\nds_read_b128 a[100:103],v237 offset:0\nv_mfma_f32_16x16x32_bf16 v[76:79],a[48:51],v[128:131],v[76:79]\n"
"buffer_load_dwordx4 v234,s[40:43],s60 offen offset:2048 lds\nv_mfma_f32_16x16x32_bf16 v[92:95],a[48:51],v[144:147],0\nds_read_b64_tr_b16 v[128:129],v226 offset:16384\ns_add_u32 s6,s6,s7\n"
"v_mfma_f32_16x16x32_bf16 v[92:95],a[52:55],v[148:151],v[92:95]\nds_read_b64_tr_b16 v[130:131],v226 offset:16896\ns_mul_i32 s7,s88,11\nv_mfma_f32_16x16x32_bf16 v[92:95],a[56:59],v[152:155],v[92:95]\n"
"ds_read_b64_tr_b16 v[132:133],v226 offset:20480\ns_lshr_b32 s7,s7,5\nv_mfma_f32_16x16x32_bf16 v[92:95],a[60:63],v[156:159],v[92:95]\nbuffer_load_dwordx4 v235,s[40:43],s60 offen offset:3072 lds\n"
"v_mfma_f32_16x16x32_bf16 v[88:91],a[44:47],v[156:159],0\nds_read_b64_tr_b16 v[134:135],v226 offset:20992\ns_add_u32 s60,s49,s64\nv_mfma_f32_16x16x32_bf16 v[88:91],a[40:43],v[152:155],v[88:91]\n"
"ds_read_b64_tr_b16 v[136:137],v226 offset:24576\ns_add_u32 m0,s61,49152\nv_mfma_f32_16x16x32_bf16 v[88:91],a[36:39],v[148:151],v[88:91]\nds_read_b64_tr_b16 v[138:139],v226 offset:25088\ns_add_u32 s6,s6,s7\n"
"v_mfma_f32_16x16x32_bf16 v[88:91],a[32:35],v[144:147],v[88:91]\nbuffer_load_dwordx4 v228,s[36:39],s60 offen offset:0 lds\nv_mfma_f32_16x16x32_bf16 v[84:87],a[16:19],v[144:147],0\nds_read_b64_tr_b16 v[140:141],v226 offset:28672\n"
"s_lshl_b32 s6,s6,2\nv_mfma_f32_16x16x32_bf16 v[84:87],a[20:23],v[148:151],v[84:87]\nds_read_b64_tr_b16 v[142:143],v226 offset:29184\ns_add_u32 s6,s6,s5\nv_mfma_f32_16x16x32_bf16 v[84:87],a[24:27],v[152:155],v[84:87]\n"
"ds_read_b128 a[104:107],v238 offset:0\ns_add_u32 s6,s6,0x380\nv_mfma_f32_16x16x32_bf16 v[84:87],a[28:31],v[156:159],v[84:87]\nbuffer_load_dwordx4 v229,s[36:39],s60 offen offset:1024 lds\n"
"v_mfma_f32_16x16x32_bf16 v[80:83],a[12:15],v[156:159],0\nds_read_b128 a[108:111],v239 offset:0\ns_waitcnt vmcnt(10)\nv_mfma_f32_16x16x32_bf16 v[80:83],a[8:11],v[152:155],v[80:83]\nds_read_b128 a[112:115],v236 offset:16384\n"
"v_mfma_f32_16x16x32_bf16 v[80:83],a[4:7],v[148:151],v[80:83]\ns_waitcnt lgkmcnt(3)\nds_read_b128 a[116:119],v237 offset:16384\nv_mfma_f32_16x16x32_bf16 v[80:83],a[0:3],v[144:147],v[80:83]\n"
"buffer_load_dwordx4 v230,s[36:39],s60 offen offset:2048 lds\nv_mfma_f32_16x16x32_bf16 v[96:99],a[0:3],v[128:131],0\nds_read_b64_tr_b16 v[144:145],v227 offset:16384\nv_mfma_f32_16x16x32_bf16 v[96:99],a[4:7],v[132:135],v[96:99]\n"
"ds_read_b64_tr_b16 v[146:147],v227 offset:16896\nv_mfma_f32_16x16x32_bf16 v[96:99],a[8:11],v[136:139],v[96:99]\nds_read_b64_tr_b16 v[148:149],v227 offset:20480\nv_mfma_f32_16x16x32_bf16 v[96:99],a[12:15],v[140:143],v[96:99]\n"
"buffer_load_dwordx4 v231,s[36:39],s60 offen offset:3072 lds\nv_mfma_f32_16x16x32_bf16 v[100:103],a[28:31],v[140:143],0\nds_read_b64_tr_b16 v[150:151],v227 offset:20992\ns_add_u32 s60,s54,s57\n"
"v_mfma_f32_16x16x32_bf16 v[100:103],a[24:27],v[136:139],v[100:103]\nds_read_b64_tr_b16 v[152:153],v227 offset:24576\ns_add_u32 m0,s62,65536\nv_mfma_f32_16x16x32_bf16 v[100:103],a[20:23],v[132:135],v[100:103]\n"
"ds_read_b64_tr_b16 v[154:155],v227 offset:25088\nv_mfma_f32_16x16x32_bf16 v[100:103],a[16:19],v[128:131],v[100:103]\nbuffer_load_dwordx4 v232,s[40:43],s60 offen offset:0 lds\n"
"v_mfma_f32_16x16x32_bf16 v[104:107],a[32:35],v[128:131],0\nds_read_b64_tr_b16 v[156:157],v227 offset:28672\nv_mfma_f32_16x16x32_bf16 v[104:107],a[36:39],v[132:135],v[104:107]\nds_read_b64_tr_b16 v[158:159],v227 offset:29184\n"
"v_mfma_f32_16x16x32_bf16 v[104:107],a[40:43],v[136:139],v[104:107]\nds_read_b128 a[120:123],v238 offset:16384\nv_mfma_f32_16x16x32_bf16 v[104:107],a[44:47],v[140:143],v[104:107]\n"
"buffer_load_dwordx4 v233,s[40:43],s60 offen offset:1024 lds\nv_mfma_f32_16x16x32_bf16 v[108:111],a[60:63],v[140:143],0\nds_read_b128 a[124:127],v239 offset:16384\n"
"v_mfma_f32_16x16x32_bf16 v[108:111],a[56:59],v[136:139],v[108:111]\ns_cmp_eq_u32 s83,1\ns_cselect_b32 s83,s6,0x7d0\ns_and_b32 s18,s89,7\nv_mfma_f32_16x16x32_bf16 v[108:111],a[52:55],v[132:135],v[108:111]\n"
"s_waitcnt vmcnt(10) lgkmcnt(2)\ns_lshr_b32 s18,s18,1\ns_mul_i32 s18,s18,0x90\nv_mfma_f32_16x16x32_bf16 v[108:111],a[48:51],v[128:131],v[108:111]\nbuffer_load_dwordx4 v234,s[40:43],s60 offen offset:2048 lds\n"
"v_mfma_f32_16x16x32_bf16 v[124:127],a[48:51],v[144:147],0\ns_barrier\ns_lshr_b32 s6,s89,3\ns_add_u32 s18,s18,s6\nv_mfma_f32_16x16x32_bf16 v[124:127],a[52:55],v[148:151],v[124:127]\nds_read_b64_tr_b16 v[128:129],v224 offset:32768\n"
"s_lshl_b32 s18,s18,13\nv_mfma_f32_16x16x32_bf16 v[124:127],a[56:59],v[152:155],v[124:127]\nds_read_b64_tr_b16 v[130:131],v224 offset:33280\ns_and_b32 s6,s89,1\nv_mfma_f32_16x16x32_bf16 v[124:127],a[60:63],v[156:159],v[124:127]\n"
"buffer_load_dwordx4 v235,s[40:43],s60 offen offset:3072 lds\nv_mfma_f32_16x16x32_bf16 v[120:123],a[44:47],v[156:159],0\nds_read_b64_tr_b16 v[132:133],v224 offset:36864\ns_add_u32 s60,s54,s58\n"
"v_mfma_f32_16x16x32_bf16 v[120:123],a[40:43],v[152:155],v[120:123]\nds_read_b64_tr_b16 v[134:135],v224 offset:37376\ns_add_u32 m0,s62,81920\nv_mfma_f32_16x16x32_bf16 v[120:123],a[36:39],v[148:151],v[120:123]\n"
"ds_read_b64_tr_b16 v[136:137],v224 offset:40960\ns_lshl_b32 s6,s6,12\nv_mfma_f32_16x16x32_bf16 v[120:123],a[32:35],v[144:147],v[120:123]\nbuffer_load_dwordx4 v232,s[40:43],s60 offen offset:0 lds\n"
"v_mfma_f32_16x16x32_bf16 v[116:119],a[16:19],v[144:147],0\nds_read_b64_tr_b16 v[138:139],v224 offset:41472\ns_add_u32 s18,s18,s6\nv_mfma_f32_16x16x32_bf16 v[116:119],a[20:23],v[148:151],v[116:119]\n"
"ds_read_b64_tr_b16 v[140:141],v224 offset:45056\ns_add_u32 s18,s18,0x2400000\nv_mfma_f32_16x16x32_bf16 v[116:119],a[24:27],v[152:155],v[116:119]\nds_read_b64_tr_b16 v[142:143],v224 offset:45568\ns_and_b32 s19,s83,7\n"
"v_mfma_f32_16x16x32_bf16 v[116:119],a[28:31],v[156:159],v[116:119]\nbuffer_load_dwordx4 v233,s[40:43],s60 offen offset:1024 lds\nv_mfma_f32_16x16x32_bf16 v[112:115],a[12:15],v[156:159],0\ns_lshr_b32 s19,s19,1\n"
"s_mul_i32 s19,s19,0x90\ns_lshr_b32 s6,s83,3\nv_mfma_f32_16x16x32_bf16 v[112:115],a[8:11],v[152:155],v[112:115]\ns_add_u32 s19,s19,s6\ns_lshl_b32 s19,s19,13\ns_and_b32 s6,s83,1\n"
"v_mfma_f32_16x16x32_bf16 v[112:115],a[4:7],v[148:151],v[112:115]\ns_waitcnt lgkmcnt(0)\ns_lshl_b32 s6,s6,12\ns_add_u32 s19,s19,s6\nv_mfma_f32_16x16x32_bf16 v[112:115],a[0:3],v[144:147],v[112:115]\n"
"buffer_load_dwordx4 v234,s[40:43],s60 offen offset:2048 lds\nv_mfma_f32_16x16x32_bf16 v[0:3],a[64:67],v[128:131],v[0:3]\nds_read_b64_tr_b16 v[144:145],v225 offset:32768\ns_add_u32 s19,s19,0x2400000\n"
"v_mfma_f32_16x16x32_bf16 v[0:3],a[68:71],v[132:135],v[0:3]\nds_read_b64_tr_b16 v[146:147],v225 offset:33280\nv_mfma_f32_16x16x32_bf16 v[0:3],a[72:75],v[136:139],v[0:3]\nds_read_b64_tr_b16 v[148:149],v225 offset:36864\n"
"v_mfma_f32_16x16x32_bf16 v[0:3],a[76:79],v[140:143],v[0:3]\nbuffer_load_dwordx4 v235,s[40:43],s60 offen offset:3072 lds\nv_mfma_f32_16x16x32_bf16 v[4:7],a[92:95],v[140:143],v[4:7]\nds_read_b64_tr_b16 v[150:151],v225 offset:37376\n"
"s_add_u32 s60,s54,s59\nv_mfma_f32_16x16x32_bf16 v[4:7],a[88:91],v[136:139],v[4:7]\nds_read_b64_tr_b16 v[152:153],v225 offset:40960\ns_add_u32 m0,s62,49152\nv_mfma_f32_16x16x32_bf16 v[4:7],a[84:87],v[132:135],v[4:7]\n"
"ds_read_b64_tr_b16 v[154:155],v225 offset:41472\ns_waitcnt vmcnt(12)\nv_mfma_f32_16x16x32_bf16 v[4:7],a[80:83],v[128:131],v[4:7]\nbuffer_load_dwordx4 v232,s[40:43],s60 offen offset:0 lds\n"
"v_mfma_f32_16x16x32_bf16 v[8:11],a[96:99],v[128:131],v[8:11]\nds_read_b64_tr_b16 v[156:157],v225 offset:45056\nv_mfma_f32_16x16x32_bf16 v[8:11],a[100:103],v[132:135],v[8:11]\nds_read_b64_tr_b16 v[158:159],v225 offset:45568\n"
"v_mfma_f32_16x16x32_bf16 v[8:11],a[104:107],v[136:139],v[8:11]\nds_read_b128 a[128:131],v236 offset:32768\nv_mfma_f32_16x16x32_bf16 v[8:11],a[108:111],v[140:143],v[8:11]\n"
"buffer_load_dwordx4 v233,s[40:43],s60 offen offset:1024 lds\nv_mfma_f32_16x16x32_bf16 v[12:15],a[124:127],v[140:143],v[12:15]\nds_read_b128 a[132:135],v237 offset:32768\n"
"v_mfma_f32_16x16x32_bf16 v[12:15],a[120:123],v[136:139],v[12:15]\nds_read_b128 a[136:139],v238 offset:32768\nv_mfma_f32_16x16x32_bf16 v[12:15],a[116:119],v[132:135],v[12:15]\ns_waitcnt lgkmcnt(3)\n"
"ds_read_b128 a[140:143],v239 offset:32768\nv_mfma_f32_16x16x32_bf16 v[12:15],a[112:115],v[128:131],v[12:15]\nbuffer_load_dwordx4 v234,s[40:43],s60 offen offset:2048 lds\n"
"v_mfma_f32_16x16x32_bf16 v[28:31],a[112:115],v[144:147],v[28:31]\nds_read_b64_tr_b16 v[128:129],v226 offset:32768\nv_mfma_f32_16x16x32_bf16 v[28:31],a[116:119],v[148:151],v[28:31]\nds_read_b64_tr_b16 v[130:131],v226 offset:33280\n"
"v_mfma_f32_16x16x32_bf16 v[28:31],a[120:123],v[152:155],v[28:31]\nds_read_b64_tr_b16 v[132:133],v226 offset:36864\nv_mfma_f32_16x16x32_bf16 v[28:31],a[124:127],v[156:159],v[28:31]\n"
"buffer_load_dwordx4 v235,s[40:43],s60 offen offset:3072 lds\nv_mfma_f32_16x16x32_bf16 v[24:27],a[108:111],v[156:159],v[24:27]\nds_read_b64_tr_b16 v[134:135],v226 offset:37376\ns_add_u32 s60,s50,s63\n"
"v_mfma_f32_16x16x32_bf16 v[24:27],a[104:107],v[152:155],v[24:27]\nds_read_b64_tr_b16 v[136:137],v226 offset:40960\ns_add_u32 m0,s61,0\nv_mfma_f32_16x16x32_bf16 v[24:27],a[100:103],v[148:151],v[24:27]\n"
"ds_read_b64_tr_b16 v[138:139],v226 offset:41472\nv_mfma_f32_16x16x32_bf16 v[24:27],a[96:99],v[144:147],v[24:27]\nbuffer_load_dwordx4 v228,s[36:39],s60 offen offset:0 lds\n"
"v_mfma_f32_16x16x32_bf16 v[20:23],a[80:83],v[144:147],v[20:23]\nds_read_b64_tr_b16 v[140:141],v226 offset:45056\nv_mfma_f32_16x16x32_bf16 v[20:23],a[84:87],v[148:151],v[20:23]\nds_read_b64_tr_b16 v[142:143],v226 offset:45568\n"
"v_mfma_f32_16x16x32_bf16 v[20:23],a[88:91],v[152:155],v[20:23]\ns_cmp_lt_u32 s83,0x480\ns_cselect_b32 s19,s19,0x40000000\nv_mfma_f32_16x16x32_bf16 v[20:23],a[92:95],v[156:159],v[20:23]\n"
"buffer_load_dwordx4 v229,s[36:39],s60 offen offset:1024 lds\nv_mfma_f32_16x16x32_bf16 v[16:19],a[76:79],v[156:159],v[16:19]\ns_cmp_lt_u32 s83,1024\ns_cselect_b64 s[80:81],s[90:91],s[80:81]\ns_lshr_b32 s6,s83,3\n"
"v_mfma_f32_16x16x32_bf16 v[16:19],a[72:75],v[152:155],v[16:19]\ns_cmp_lt_u32 s83,0x400\ns_cselect_b32 s7,0x7f,15\ns_and_b32 s6,s6,s7\nv_mfma_f32_16x16x32_bf16 v[16:19],a[68:71],v[148:151],v[16:19]\ns_waitcnt lgkmcnt(0)\n"
"s_lshl_b32 s6,s6,15\ns_and_b32 s7,s83,7\nv_mfma_f32_16x16x32_bf16 v[16:19],a[64:67],v[144:147],v[16:19]\nbuffer_load_dwordx4 v230,s[36:39],s60 offen offset:2048 lds\nv_mfma_f32_16x16x32_bf16 v[32:35],a[64:67],v[128:131],v[32:35]\n"
"ds_read_b64_tr_b16 v[144:145],v227 offset:32768\ns_lshl_b32 s7,s7,7\nv_mfma_f32_16x16x32_bf16 v[32:35],a[68:71],v[132:135],v[32:35]\nds_read_b64_tr_b16 v[146:147],v227 offset:33280\ns_add_u32 s6,s6,s7\n"
"v_mfma_f32_16x16x32_bf16 v[32:35],a[72:75],v[136:139],v[32:35]\nds_read_b64_tr_b16 v[148:149],v227 offset:36864\nv_mfma_f32_16x16x32_bf16 v[32:35],a[76:79],v[140:143],v[32:35]\n"
"buffer_load_dwordx4 v231,s[36:39],s60 offen offset:3072 lds\nv_mfma_f32_16x16x32_bf16 v[36:39],a[92:95],v[140:143],v[36:39]\nds_read_b64_tr_b16 v[150:151],v227 offset:37376\ns_add_u32 s60,s55,s56\n"
"v_mfma_f32_16x16x32_bf16 v[36:39],a[88:91],v[136:139],v[36:39]\nds_read_b64_tr_b16 v[152:153],v227 offset:40960\ns_add_u32 m0,s62,0\nv_mfma_f32_16x16x32_bf16 v[36:39],a[84:87],v[132:135],v[36:39]\n"
"ds_read_b64_tr_b16 v[154:155],v227 offset:41472\ns_waitcnt vmcnt(12)\nv_mfma_f32_16x16x32_bf16 v[36:39],a[80:83],v[128:131],v[36:39]\nbuffer_load_dwordx4 v232,s[40:43],s60 offen offset:0 lds\n"
"v_mfma_f32_16x16x32_bf16 v[40:43],a[96:99],v[128:131],v[40:43]\nds_read_b64_tr_b16 v[156:157],v227 offset:45056\nv_mfma_f32_16x16x32_bf16 v[40:43],a[100:103],v[132:135],v[40:43]\nds_read_b64_tr_b16 v[158:159],v227 offset:45568\n"
"v_mfma_f32_16x16x32_bf16 v[40:43],a[104:107],v[136:139],v[40:43]\nds_read_b128 a[144:147],v188 offset:0\nv_mfma_f32_16x16x32_bf16 v[40:43],a[108:111],v[140:143],v[40:43]\n"
"buffer_load_dwordx4 v233,s[40:43],s60 offen offset:1024 lds\nv_mfma_f32_16x16x32_bf16 v[44:47],a[124:127],v[140:143],v[44:47]\nds_read_b128 a[148:151],v189 offset:0\n"
"v_mfma_f32_16x16x32_bf16 v[44:47],a[120:123],v[136:139],v[44:47]\nds_read_b128 a[152:155],v190 offset:0\nv_mfma_f32_16x16x32_bf16 v[44:47],a[116:119],v[132:135],v[44:47]\ns_waitcnt vmcnt(18) lgkmcnt(3)\n"
"ds_read_b128 a[156:159],v191 offset:0\nv_mfma_f32_16x16x32_bf16 v[44:47],a[112:115],v[128:131],v[44:47]\nbuffer_load_dwordx4 v234,s[40:43],s60 offen offset:2048 lds\n"
"v_mfma_f32_16x16x32_bf16 v[60:63],a[112:115],v[144:147],v[60:63]\ns_barrier\ns_add_u32 s80,s80,s6\ns_addc_u32 s81,s81,0\nv_mfma_f32_16x16x32_bf16 v[60:63],a[116:119],v[148:151],v[60:63]\n"
"ds_read_b64_tr_b16 v[128:129],v224 offset:49152\ns_lshr_b32 s6,s89,3\nv_mfma_f32_16x16x32_bf16 v[60:63],a[120:123],v[152:155],v[60:63]\nds_read_b64_tr_b16 v[130:131],v224 offset:49664\ns_lshl_b32 s6,s6,15\n"
"v_mfma_f32_16x16x32_bf16 v[60:63],a[124:127],v[156:159],v[60:63]\nbuffer_load_dwordx4 v235,s[40:43],s60 offen offset:3072 lds\nv_mfma_f32_16x16x32_bf16 v[56:59],a[108:111],v[156:159],v[56:59]\n"
"ds_read_b64_tr_b16 v[132:133],v224 offset:53248\ns_add_u32 s60,s50,s64\nv_mfma_f32_16x16x32_bf16 v[56:59],a[104:107],v[152:155],v[56:59]\nds_read_b64_tr_b16 v[134:135],v224 offset:53760\ns_add_u32 m0,s61,16384\n"
"v_mfma_f32_16x16x32_bf16 v[56:59],a[100:103],v[148:151],v[56:59]\nds_read_b64_tr_b16 v[136:137],v224 offset:57344\ns_waitcnt vmcnt(12)\nv_mfma_f32_16x16x32_bf16 v[56:59],a[96:99],v[144:147],v[56:59]\n"
"buffer_load_dwordx4 v228,s[36:39],s60 offen offset:0 lds\nv_mfma_f32_16x16x32_bf16 v[52:55],a[80:83],v[144:147],v[52:55]\nds_read_b64_tr_b16 v[138:139],v224 offset:57856\ns_and_b32 s7,s89,7\n"
"v_mfma_f32_16x16x32_bf16 v[52:55],a[84:87],v[148:151],v[52:55]\nds_read_b64_tr_b16 v[140:141],v224 offset:61440\ns_lshl_b32 s7,s7,7\nv_mfma_f32_16x16x32_bf16 v[52:55],a[88:91],v[152:155],v[52:55]\n"
"ds_read_b64_tr_b16 v[142:143],v224 offset:61952\ns_add_u32 s6,s6,s7\nv_mfma_f32_16x16x32_bf16 v[52:55],a[92:95],v[156:159],v[52:55]\nbuffer_load_dwordx4 v229,s[36:39],s60 offen offset:1024 lds\n"
"v_mfma_f32_16x16x32_bf16 v[48:51],a[76:79],v[156:159],v[48:51]\nds_read_b128 a[160:163],v188 offset:16384\nv_mfma_f32_16x16x32_bf16 v[48:51],a[72:75],v[152:155],v[48:51]\nds_read_b128 a[164:167],v189 offset:16384\n"
"v_mfma_f32_16x16x32_bf16 v[48:51],a[68:71],v[148:151],v[48:51]\ns_waitcnt lgkmcnt(2)\nds_read_b128 a[168:171],v190 offset:16384\nv_mfma_f32_16x16x32_bf16 v[48:51],a[64:67],v[144:147],v[48:51]\n"
"buffer_load_dwordx4 v230,s[36:39],s60 offen offset:2048 lds\nv_mfma_f32_16x16x32_bf16 v[64:67],a[64:67],v[128:131],v[64:67]\nds_read_b64_tr_b16 v[144:145],v225 offset:49152\n"
"v_mfma_f32_16x16x32_bf16 v[64:67],a[68:71],v[132:135],v[64:67]\nds_read_b64_tr_b16 v[146:147],v225 offset:49664\nv_mfma_f32_16x16x32_bf16 v[64:67],a[72:75],v[136:139],v[64:67]\nds_read_b64_tr_b16 v[148:149],v225 offset:53248\n"
"v_mfma_f32_16x16x32_bf16 v[64:67],a[76:79],v[140:143],v[64:67]\nbuffer_load_dwordx4 v231,s[36:39],s60 offen offset:3072 lds\nv_mfma_f32_16x16x32_bf16 v[68:71],a[92:95],v[140:143],v[68:71]\n"
"ds_read_b64_tr_b16 v[150:151],v225 offset:53760\ns_add_u32 s60,s55,s57\nv_mfma_f32_16x16x32_bf16 v[68:71],a[88:91],v[136:139],v[68:71]\nds_read_b64_tr_b16 v[152:153],v225 offset:57344\ns_add_u32 m0,s62,16384\n"
"v_mfma_f32_16x16x32_bf16 v[68:71],a[84:87],v[132:135],v[68:71]\nds_read_b64_tr_b16 v[154:155],v225 offset:57856\nv_mfma_f32_16x16x32_bf16 v[68:71],a[80:83],v[128:131],v[68:71]\n"
"buffer_load_dwordx4 v232,s[40:43],s60 offen offset:0 lds\nv_mfma_f32_16x16x32_bf16 v[72:75],a[96:99],v[128:131],v[72:75]\nds_read_b64_tr_b16 v[156:157],v225 offset:61440\n"
"v_mfma_f32_16x16x32_bf16 v[72:75],a[100:103],v[132:135],v[72:75]\nds_read_b64_tr_b16 v[158:159],v225 offset:61952\nv_mfma_f32_16x16x32_bf16 v[72:75],a[104:107],v[136:139],v[72:75]\nds_read_b128 a[172:175],v191 offset:16384\n"
"s_waitcnt vmcnt(13)\nv_mfma_f32_16x16x32_bf16 v[72:75],a[108:111],v[140:143],v[72:75]\nbuffer_load_dwordx4 v233,s[40:43],s60 offen offset:1024 lds\nv_mfma_f32_16x16x32_bf16 v[76:79],a[124:127],v[140:143],v[76:79]\n"
"ds_read_b128 a[176:179],v236 offset:49152\nv_mfma_f32_16x16x32_bf16 v[76:79],a[120:123],v[136:139],v[76:79]\nds_read_b128 a[180:183],v237 offset:49152\nv_mfma_f32_16x16x32_bf16 v[76:79],a[116:119],v[132:135],v[76:79]\n"
"s_waitcnt lgkmcnt(3)\nds_read_b128 a[184:187],v238 offset:49152\nv_mfma_f32_16x16x32_bf16 v[76:79],a[112:115],v[128:131],v[76:79]\nbuffer_load_dwordx4 v234,s[40:43],s60 offen offset:2048 lds\n"
"v_mfma_f32_16x16x32_bf16 v[92:95],a[112:115],v[144:147],v[92:95]\nds_read_b64_tr_b16 v[128:129],v226 offset:49152\nv_mfma_f32_16x16x32_bf16 v[92:95],a[116:119],v[148:151],v[92:95]\nds_read_b64_tr_b16 v[130:131],v226 offset:49664\n"
"v_mfma_f32_16x16x32_bf16 v[92:95],a[120:123],v[152:155],v[92:95]\nds_read_b64_tr_b16 v[132:133],v226 offset:53248\nv_mfma_f32_16x16x32_bf16 v[92:95],a[124:127],v[156:159],v[92:95]\n"
"buffer_load_dwordx4 v235,s[40:43],s60 offen offset:3072 lds\nv_mfma_f32_16x16x32_bf16 v[88:91],a[108:111],v[156:159],v[88:91]\nds_read_b64_tr_b16 v[134:135],v226 offset:53760\ns_add_u32 s60,s55,s58\n"
"v_mfma_f32_16x16x32_bf16 v[88:91],a[104:107],v[152:155],v[88:91]\nds_read_b64_tr_b16 v[136:137],v226 offset:57344\ns_add_u32 m0,s62,32768\nv_mfma_f32_16x16x32_bf16 v[88:91],a[100:103],v[148:151],v[88:91]\n"
"ds_read_b64_tr_b16 v[138:139],v226 offset:57856\nv_mfma_f32_16x16x32_bf16 v[88:91],a[96:99],v[144:147],v[88:91]\nbuffer_load_dwordx4 v232,s[40:43],s60 offen offset:0 lds\n"
"v_mfma_f32_16x16x32_bf16 v[84:87],a[80:83],v[144:147],v[84:87]\nds_read_b64_tr_b16 v[140:141],v226 offset:61440\nv_mfma_f32_16x16x32_bf16 v[84:87],a[84:87],v[148:151],v[84:87]\nds_read_b64_tr_b16 v[142:143],v226 offset:61952\n"
"v_mfma_f32_16x16x32_bf16 v[84:87],a[88:91],v[152:155],v[84:87]\nds_read_b128 a[188:191],v239 offset:49152\nv_mfma_f32_16x16x32_bf16 v[84:87],a[92:95],v[156:159],v[84:87]\n"
"buffer_load_dwordx4 v233,s[40:43],s60 offen offset:1024 lds\nv_mfma_f32_16x16x32_bf16 v[80:83],a[76:79],v[156:159],v[80:83]\ns_add_u32 s90,s90,s6\ns_addc_u32 s91,s91,0\ns_lshl_b32 s6,s10,5\n"
"v_mfma_f32_16x16x32_bf16 v[80:83],a[72:75],v[152:155],v[80:83]\ns_and_b32 s7,s9,3\ns_lshl_b32 s7,s7,3\ns_add_u32 s6,s6,s7\nv_mfma_f32_16x16x32_bf16 v[80:83],a[68:71],v[148:151],v[80:83]\ns_waitcnt lgkmcnt(1)\ns_lshl_b32 s7,s5,1\n"
"s_add_u32 s6,s6,s7\nv_mfma_f32_16x16x32_bf16 v[80:83],a[64:67],v[144:147],v[80:83]\nbuffer_load_dwordx4 v234,s[40:43],s60 offen offset:2048 lds\nv_mfma_f32_16x16x32_bf16 v[96:99],a[64:67],v[128:131],v[96:99]\n"
"ds_read_b64_tr_b16 v[144:145],v227 offset:49152\ns_lshl_b32 s6,s6,10\nv_mfma_f32_16x16x32_bf16 v[96:99],a[68:71],v[132:135],v[96:99]\nds_read_b64_tr_b16 v[146:147],v227 offset:49664\ns_add_u32 s6,s6,0x4280000\n"
"v_mfma_f32_16x16x32_bf16 v[96:99],a[72:75],v[136:139],v[96:99]\nds_read_b64_tr_b16 v[148:149],v227 offset:53248\ns_lshl_b32 s7,s10,8\nv_mfma_f32_16x16x32_bf16 v[96:99],a[76:79],v[140:143],v[96:99]\n"
"buffer_load_dwordx4 v235,s[40:43],s60 offen offset:3072 lds\nv_mfma_f32_16x16x32_bf16 v[100:103],a[92:95],v[140:143],v[100:103]\nds_read_b64_tr_b16 v[150:151],v227 offset:53760\ns_add_u32 s60,s55,s59\n"
"v_mfma_f32_16x16x32_bf16 v[100:103],a[88:91],v[136:139],v[100:103]\nds_read_b64_tr_b16 v[152:153],v227 offset:57344\ns_add_u32 m0,s62,65536\nv_mfma_f32_16x16x32_bf16 v[100:103],a[84:87],v[132:135],v[100:103]\n"
"ds_read_b64_tr_b16 v[154:155],v227 offset:57856\ns_waitcnt vmcnt(12)\nv_mfma_f32_16x16x32_bf16 v[100:103],a[80:83],v[128:131],v[100:103]\nbuffer_load_dwordx4 v232,s[40:43],s60 offen offset:0 lds\n"
"v_mfma_f32_16x16x32_bf16 v[104:107],a[96:99],v[128:131],v[104:107]\nds_read_b64_tr_b16 v[156:157],v227 offset:61440\ns_lshl_b32 s84,s5,6\nv_mfma_f32_16x16x32_bf16 v[104:107],a[100:103],v[132:135],v[104:107]\n"
"ds_read_b64_tr_b16 v[158:159],v227 offset:61952\ns_add_u32 s7,s7,s84\nv_mfma_f32_16x16x32_bf16 v[104:107],a[104:107],v[136:139],v[104:107]\nds_read_b128 a[192:195],v236 offset:0\ns_and_b32 s84,s9,1\n"
"v_mfma_f32_16x16x32_bf16 v[104:107],a[108:111],v[140:143],v[104:107]\nbuffer_load_dwordx4 v233,s[40:43],s60 offen offset:1024 lds\nv_mfma_f32_16x16x32_bf16 v[108:111],a[124:127],v[140:143],v[108:111]\n"
"ds_read_b128 a[196:199],v237 offset:0\ns_lshl_b32 s84,s84,5\nv_mfma_f32_16x16x32_bf16 v[108:111],a[120:123],v[136:139],v[108:111]\nds_read_b128 a[200:203],v238 offset:0\ns_add_u32 s7,s7,s84\n"
"v_mfma_f32_16x16x32_bf16 v[108:111],a[116:119],v[132:135],v[108:111]\ns_waitcnt vmcnt(18) lgkmcnt(3)\nds_read_b128 a[204:207],v239 offset:0\nv_mfma_f32_16x16x32_bf16 v[108:111],a[112:115],v[128:131],v[108:111]\n"
"buffer_load_dwordx4 v234,s[40:43],s60 offen offset:2048 lds\nv_mfma_f32_16x16x32_bf16 v[124:127],a[112:115],v[144:147],v[124:127]\ns_barrier\ns_lshl_b32 s7,s7,2\ns_add_u32 s7,s7,0x42c8000\n"
"v_mfma_f32_16x16x32_bf16 v[124:127],a[116:119],v[148:151],v[124:127]\nds_read_b64_tr_b16 v[128:129],v224 offset:0\nv_mfma_f32_16x16x32_bf16 v[124:127],a[120:123],v[152:155],v[124:127]\n"
"ds_read_b64_tr_b16 v[130:131],v224 offset:512\nv_mfma_f32_16x16x32_bf16 v[124:127],a[124:127],v[156:159],v[124:127]\nbuffer_load_dwordx4 v235,s[40:43],s60 offen offset:3072 lds\n"
"v_mfma_f32_16x16x32_bf16 v[120:123],a[108:111],v[156:159],v[120:123]\nds_read_b64_tr_b16 v[132:133],v224 offset:4096\ns_add_u32 s60,s51,s63\nv_mfma_f32_16x16x32_bf16 v[120:123],a[104:107],v[152:155],v[120:123]\n"
"ds_read_b64_tr_b16 v[134:135],v224 offset:4608\ns_add_u32 m0,s61,32768\nv_mfma_f32_16x16x32_bf16 v[120:123],a[100:103],v[148:151],v[120:123]\nds_read_b64_tr_b16 v[136:137],v224 offset:8192\n"
"v_mfma_f32_16x16x32_bf16 v[120:123],a[96:99],v[144:147],v[120:123]\nbuffer_load_dwordx4 v228,s[36:39],s60 offen offset:0 lds\nv_mfma_f32_16x16x32_bf16 v[116:119],a[80:83],v[144:147],v[116:119]\n"
"ds_read_b64_tr_b16 v[138:139],v224 offset:8704\nv_mfma_f32_16x16x32_bf16 v[116:119],a[84:87],v[148:151],v[116:119]\nds_read_b64_tr_b16 v[140:141],v224 offset:12288\n"
"v_mfma_f32_16x16x32_bf16 v[116:119],a[88:91],v[152:155],v[116:119]\nds_read_b64_tr_b16 v[142:143],v224 offset:12800\nv_mfma_f32_16x16x32_bf16 v[116:119],a[92:95],v[156:159],v[116:119]\n"
"buffer_load_dwordx4 v229,s[36:39],s60 offen offset:1024 lds\nv_mfma_f32_16x16x32_bf16 v[112:115],a[76:79],v[156:159],v[112:115]\ns_cmp_ge_u32 s9,28\ns_cselect_b32 s6,s6,s7\n"
"v_mfma_f32_16x16x32_bf16 v[112:115],a[72:75],v[152:155],v[112:115]\ns_add_u32 s20,s26,s6\ns_addc_u32 s21,s27,0\ns_lshl_b32 s6,s10,2\nv_mfma_f32_16x16x32_bf16 v[112:115],a[68:71],v[148:151],v[112:115]\ns_waitcnt lgkmcnt(0)\n"
"s_and_b32 s7,s9,3\ns_add_u32 s6,s6,s7\nv_mfma_f32_16x16x32_bf16 v[112:115],a[64:67],v[144:147],v[112:115]\nbuffer_load_dwordx4 v230,s[36:39],s60 offen offset:2048 lds\nv_mfma_f32_16x16x32_bf16 v[0:3],a[128:131],v[128:131],v[0:3]\n"
"ds_read_b64_tr_b16 v[144:145],v225 offset:0\ns_lshl_b32 s6,s6,10\nv_mfma_f32_16x16x32_bf16 v[0:3],a[132:135],v[132:135],v[0:3]\nds_read_b64_tr_b16 v[146:147],v225 offset:512\ns_add_u32 s6,s6,0x42c0000\n"
"v_mfma_f32_16x16x32_bf16 v[0:3],a[136:139],v[136:139],v[0:3]\nds_read_b64_tr_b16 v[148:149],v225 offset:4096\nv_mfma_f32_16x16x32_bf16 v[0:3],a[140:143],v[140:143],v[0:3]\n"
"buffer_load_dwordx4 v231,s[36:39],s60 offen offset:3072 lds\nv_mfma_f32_16x16x32_bf16 v[4:7],a[156:159],v[140:143],v[4:7]\nds_read_b64_tr_b16 v[150:151],v225 offset:4608\ns_add_u32 s60,s51,s64\n"
"v_mfma_f32_16x16x32_bf16 v[4:7],a[152:155],v[136:139],v[4:7]\nds_read_b64_tr_b16 v[152:153],v225 offset:8192\ns_add_u32 m0,s61,49152\nv_mfma_f32_16x16x32_bf16 v[4:7],a[148:151],v[132:135],v[4:7]\n"
"ds_read_b64_tr_b16 v[154:155],v225 offset:8704\ns_waitcnt vmcnt(12)\nv_mfma_f32_16x16x32_bf16 v[4:7],a[144:147],v[128:131],v[4:7]\nbuffer_load_dwordx4 v228,s[36:39],s60 offen offset:0 lds\n"
"v_mfma_f32_16x16x32_bf16 v[8:11],a[160:163],v[128:131],v[8:11]\nds_read_b64_tr_b16 v[156:157],v225 offset:12288\nv_mfma_f32_16x16x32_bf16 v[8:11],a[164:167],v[132:135],v[8:11]\nds_read_b64_tr_b16 v[158:159],v225 offset:12800\n"
"v_mfma_f32_16x16x32_bf16 v[8:11],a[168:171],v[136:139],v[8:11]\nds_read_b128 a[208:211],v236 offset:16384\nv_mfma_f32_16x16x32_bf16 v[8:11],a[172:175],v[140:143],v[8:11]\n"
"buffer_load_dwordx4 v229,s[36:39],s60 offen offset:1024 lds\nv_mfma_f32_16x16x32_bf16 v[12:15],a[188:191],v[140:143],v[12:15]\nds_read_b128 a[212:215],v237 offset:16384\n"
"v_mfma_f32_16x16x32_bf16 v[12:15],a[184:187],v[136:139],v[12:15]\nds_read_b128 a[216:219],v238 offset:16384\nv_mfma_f32_16x16x32_bf16 v[12:15],a[180:183],v[132:135],v[12:15]\ns_waitcnt lgkmcnt(3)\n"
"ds_read_b128 a[220:223],v239 offset:16384\nv_mfma_f32_16x16x32_bf16 v[12:15],a[176:179],v[128:131],v[12:15]\nbuffer_load_dwordx4 v230,s[36:39],s60 offen offset:2048 lds\n"
"v_mfma_f32_16x16x32_bf16 v[28:31],a[176:179],v[144:147],v[28:31]\nds_read_b64_tr_b16 v[128:129],v226 offset:0\nv_mfma_f32_16x16x32_bf16 v[28:31],a[180:183],v[148:151],v[28:31]\nds_read_b64_tr_b16 v[130:131],v226 offset:512\n"
"v_mfma_f32_16x16x32_bf16 v[28:31],a[184:187],v[152:155],v[28:31]\nds_read_b64_tr_b16 v[132:133],v226 offset:4096\nv_mfma_f32_16x16x32_bf16 v[28:31],a[188:191],v[156:159],v[28:31]\n"
"buffer_load_dwordx4 v231,s[36:39],s60 offen offset:3072 lds\nv_mfma_f32_16x16x32_bf16 v[24:27],a[172:175],v[156:159],v[24:27]\nds_read_b64_tr_b16 v[134:135],v226 offset:4608\ns_waitcnt vmcnt(12)\n"
"v_mfma_f32_16x16x32_bf16 v[24:27],a[168:171],v[152:155],v[24:27]\nds_read_b64_tr_b16 v[136:137],v226 offset:8192\nv_mfma_f32_16x16x32_bf16 v[24:27],a[164:167],v[148:151],v[24:27]\nds_read_b64_tr_b16 v[138:139],v226 offset:8704\n"
"v_mfma_f32_16x16x32_bf16 v[24:27],a[160:163],v[144:147],v[24:27]\nbuffer_load_dword v235,v245,s[36:39],s65 offen offset:0\nv_mfma_f32_16x16x32_bf16 v[20:23],a[144:147],v[144:147],v[20:23]\n"
"ds_read_b64_tr_b16 v[140:141],v226 offset:12288\nv_mfma_f32_16x16x32_bf16 v[20:23],a[148:151],v[148:151],v[20:23]\nds_read_b64_tr_b16 v[142:143],v226 offset:12800\nv_mfma_f32_16x16x32_bf16 v[20:23],a[152:155],v[152:155],v[20:23]\n"
"ds_read_b128 a[224:227],v236 offset:32768\nv_mfma_f32_16x16x32_bf16 v[20:23],a[156:159],v[156:159],v[20:23]\nds_read_b128 a[228:231],v237 offset:32768\nv_mfma_f32_16x16x32_bf16 v[16:19],a[140:143],v[156:159],v[16:19]\n"
"ds_read_b128 a[232:235],v238 offset:32768\nv_mfma_f32_16x16x32_bf16 v[16:19],a[136:139],v[152:155],v[16:19]\nds_read_b128 a[236:239],v239 offset:32768\nv_mfma_f32_16x16x32_bf16 v[16:19],a[132:135],v[148:151],v[16:19]\n"
"s_waitcnt lgkmcnt(4)\ns_add_u32 s22,s26,s6\ns_addc_u32 s23,s27,0\nv_mfma_f32_16x16x32_bf16 v[16:19],a[128:131],v[144:147],v[16:19]\ns_lshl_b32 s82,s17,16\ns_add_u32 s82,s82,s14\n"
"v_mfma_f32_16x16x32_bf16 v[32:35],a[128:131],v[128:131],v[32:35]\nds_read_b64_tr_b16 v[144:145],v227 offset:0\nv_mfma_f32_16x16x32_bf16 v[32:35],a[132:135],v[132:135],v[32:35]\nds_read_b64_tr_b16 v[146:147],v227 offset:512\n"
"v_mfma_f32_16x16x32_bf16 v[32:35],a[136:139],v[136:139],v[32:35]\nds_read_b64_tr_b16 v[148:149],v227 offset:4096\nv_mfma_f32_16x16x32_bf16 v[32:35],a[140:143],v[140:143],v[32:35]\n"
"buffer_load_dword v235,v246,s[36:39],s65 offen offset:0\nv_mfma_f32_16x16x32_bf16 v[36:39],a[156:159],v[140:143],v[36:39]\nds_read_b64_tr_b16 v[150:151],v227 offset:4608\ns_waitcnt vmcnt(10)\n"
"v_mfma_f32_16x16x32_bf16 v[36:39],a[152:155],v[136:139],v[36:39]\nds_read_b64_tr_b16 v[152:153],v227 offset:8192\nv_mfma_f32_16x16x32_bf16 v[36:39],a[148:151],v[132:135],v[36:39]\nds_read_b64_tr_b16 v[154:155],v227 offset:8704\n"
"v_mfma_f32_16x16x32_bf16 v[36:39],a[144:147],v[128:131],v[36:39]\nds_read_b128 a[240:243],v188 offset:0\nv_mfma_f32_16x16x32_bf16 v[40:43],a[160:163],v[128:131],v[40:43]\nds_read_b64_tr_b16 v[156:157],v227 offset:12288\n"
"v_mfma_f32_16x16x32_bf16 v[40:43],a[164:167],v[132:135],v[40:43]\nds_read_b64_tr_b16 v[158:159],v227 offset:12800\nv_mfma_f32_16x16x32_bf16 v[40:43],a[168:171],v[136:139],v[40:43]\nds_read_b128 a[244:247],v189 offset:0\n"
"v_mfma_f32_16x16x32_bf16 v[40:43],a[172:175],v[140:143],v[40:43]\nds_read_b128 a[248:251],v190 offset:0\nv_mfma_f32_16x16x32_bf16 v[44:47],a[188:191],v[140:143],v[44:47]\n"
"buffer_load_dword v235,v245,s[36:39],s65 offen offset:1024\nv_mfma_f32_16x16x32_bf16 v[44:47],a[184:187],v[136:139],v[44:47]\nds_read_b128 a[252:255],v191 offset:0\n"
"v_mfma_f32_16x16x32_bf16 v[44:47],a[180:183],v[132:135],v[44:47]\ns_waitcnt vmcnt(23) lgkmcnt(3)\nv_lshrrev_b32 v238,7,v240\nv_lshlrev_b32 v238,13,v238\nv_mfma_f32_16x16x32_bf16 v[44:47],a[176:179],v[128:131],v[44:47]\n"
"v_and_b32 v239,64,v240\nv_lshl_add_u32 v238,v239,1,v238\nv_mfma_f32_16x16x32_bf16 v[60:63],a[176:179],v[144:147],v[60:63]\ns_barrier\nv_add_u32 v238,s82,v238\nv_mfma_f32_16x16x32_bf16 v[60:63],a[180:183],v[148:151],v[60:63]\n"
"ds_read_b64_tr_b16 v[128:129],v224 offset:16384\nv_mfma_f32_16x16x32_bf16 v[60:63],a[184:187],v[152:155],v[60:63]\nds_read_b64_tr_b16 v[130:131],v224 offset:16896\nv_mfma_f32_16x16x32_bf16 v[60:63],a[188:191],v[156:159],v[60:63]\n"
"v_mfma_f32_16x16x32_bf16 v[56:59],a[172:175],v[156:159],v[56:59]\nds_read_b64_tr_b16 v[132:133],v224 offset:20480\nv_mfma_f32_16x16x32_bf16 v[56:59],a[168:171],v[152:155],v[56:59]\nds_read_b64_tr_b16 v[134:135],v224 offset:20992\n"
"v_mfma_f32_16x16x32_bf16 v[56:59],a[164:167],v[148:151],v[56:59]\nds_read_b64_tr_b16 v[136:137],v224 offset:24576\nv_mfma_f32_16x16x32_bf16 v[56:59],a[160:163],v[144:147],v[56:59]\n"
"v_mfma_f32_16x16x32_bf16 v[52:55],a[144:147],v[144:147],v[52:55]\nds_read_b64_tr_b16 v[138:139],v224 offset:25088\nv_mfma_f32_16x16x32_bf16 v[52:55],a[148:151],v[148:151],v[52:55]\nds_read_b64_tr_b16 v[140:141],v224 offset:28672\n"
"v_mfma_f32_16x16x32_bf16 v[52:55],a[152:155],v[152:155],v[52:55]\nds_read_b64_tr_b16 v[142:143],v224 offset:29184\nv_mfma_f32_16x16x32_bf16 v[52:55],a[156:159],v[156:159],v[52:55]\n"
"buffer_load_dword v235,v246,s[36:39],s65 offen offset:1024\nv_mfma_f32_16x16x32_bf16 v[48:51],a[140:143],v[156:159],v[48:51]\ns_add_u32 s60,s48,s65\ns_add_u32 m0,s61,65536\n"
"v_mfma_f32_16x16x32_bf16 v[48:51],a[136:139],v[152:155],v[48:51]\nv_mfma_f32_16x16x32_bf16 v[48:51],a[132:135],v[148:151],v[48:51]\ns_waitcnt lgkmcnt(0)\nv_mfma_f32_16x16x32_bf16 v[48:51],a[128:131],v[144:147],v[48:51]\n"
"v_mfma_f32_16x16x32_bf16 v[64:67],a[128:131],v[128:131],v[64:67]\nds_read_b64_tr_b16 v[144:145],v225 offset:16384\nv_mfma_f32_16x16x32_bf16 v[64:67],a[132:135],v[132:135],v[64:67]\nds_read_b64_tr_b16 v[146:147],v225 offset:16896\n"
"v_mfma_f32_16x16x32_bf16 v[64:67],a[136:139],v[136:139],v[64:67]\nds_read_b64_tr_b16 v[148:149],v225 offset:20480\nv_mfma_f32_16x16x32_bf16 v[64:67],a[140:143],v[140:143],v[64:67]\n"
"v_mfma_f32_16x16x32_bf16 v[68:71],a[156:159],v[140:143],v[68:71]\nds_read_b64_tr_b16 v[150:151],v225 offset:20992\nv_mfma_f32_16x16x32_bf16 v[68:71],a[152:155],v[136:139],v[68:71]\nds_read_b64_tr_b16 v[152:153],v225 offset:24576\n"
"v_mfma_f32_16x16x32_bf16 v[68:71],a[148:151],v[132:135],v[68:71]\nds_read_b64_tr_b16 v[154:155],v225 offset:25088\nv_mfma_f32_16x16x32_bf16 v[68:71],a[144:147],v[128:131],v[68:71]\n"
"v_mfma_f32_16x16x32_bf16 v[72:75],a[160:163],v[128:131],v[72:75]\nds_read_b64_tr_b16 v[156:157],v225 offset:28672\nv_mfma_f32_16x16x32_bf16 v[72:75],a[164:167],v[132:135],v[72:75]\nds_read_b64_tr_b16 v[158:159],v225 offset:29184\n"
"v_mfma_f32_16x16x32_bf16 v[72:75],a[168:171],v[136:139],v[72:75]\nbuffer_load_dwordx4 v228,s[36:39],s60 offen offset:0 lds\nv_mfma_f32_16x16x32_bf16 v[72:75],a[172:175],v[140:143],v[72:75]\n"
"v_mfma_f32_16x16x32_bf16 v[76:79],a[188:191],v[140:143],v[76:79]\nv_mfma_f32_16x16x32_bf16 v[76:79],a[184:187],v[136:139],v[76:79]\nv_mfma_f32_16x16x32_bf16 v[76:79],a[180:183],v[132:135],v[76:79]\ns_waitcnt lgkmcnt(0)\n"
"v_mfma_f32_16x16x32_bf16 v[76:79],a[176:179],v[128:131],v[76:79]\nbuffer_load_dwordx4 v229,s[36:39],s60 offen offset:1024 lds\nv_mfma_f32_16x16x32_bf16 v[92:95],a[176:179],v[144:147],v[92:95]\n"
"ds_read_b64_tr_b16 v[128:129],v226 offset:16384\nv_mfma_f32_16x16x32_bf16 v[92:95],a[180:183],v[148:151],v[92:95]\nds_read_b64_tr_b16 v[130:131],v226 offset:16896\nv_mfma_f32_16x16x32_bf16 v[92:95],a[184:187],v[152:155],v[92:95]\n"
"ds_read_b64_tr_b16 v[132:133],v226 offset:20480\nv_mfma_f32_16x16x32_bf16 v[92:95],a[188:191],v[156:159],v[92:95]\nbuffer_load_dwordx4 v230,s[36:39],s60 offen offset:2048 lds\n"
"v_mfma_f32_16x16x32_bf16 v[88:91],a[172:175],v[156:159],v[88:91]\nds_read_b64_tr_b16 v[134:135],v226 offset:20992\nv_mfma_f32_16x16x32_bf16 v[88:91],a[168:171],v[152:155],v[88:91]\nds_read_b64_tr_b16 v[136:137],v226 offset:24576\n"
"v_mfma_f32_16x16x32_bf16 v[88:91],a[164:167],v[148:151],v[88:91]\nds_read_b64_tr_b16 v[138:139],v226 offset:25088\nv_mfma_f32_16x16x32_bf16 v[88:91],a[160:163],v[144:147],v[88:91]\n"
"buffer_load_dwordx4 v231,s[36:39],s60 offen offset:3072 lds\nv_mfma_f32_16x16x32_bf16 v[84:87],a[144:147],v[144:147],v[84:87]\nds_read_b64_tr_b16 v[140:141],v226 offset:28672\ns_add_u32 s60,s49,s65\n"
"v_mfma_f32_16x16x32_bf16 v[84:87],a[148:151],v[148:151],v[84:87]\nds_read_b64_tr_b16 v[142:143],v226 offset:29184\ns_add_u32 m0,s61,81920\nv_mfma_f32_16x16x32_bf16 v[84:87],a[152:155],v[152:155],v[84:87]\n"
"v_mfma_f32_16x16x32_bf16 v[84:87],a[156:159],v[156:159],v[84:87]\nbuffer_load_dwordx4 v228,s[36:39],s60 offen offset:0 lds\nv_mfma_f32_16x16x32_bf16 v[80:83],a[140:143],v[156:159],v[80:83]\n"
"v_mfma_f32_16x16x32_bf16 v[80:83],a[136:139],v[152:155],v[80:83]\nv_mfma_f32_16x16x32_bf16 v[80:83],a[132:135],v[148:151],v[80:83]\ns_waitcnt lgkmcnt(0)\nv_mfma_f32_16x16x32_bf16 v[80:83],a[128:131],v[144:147],v[80:83]\n"
"buffer_load_dwordx4 v229,s[36:39],s60 offen offset:1024 lds\nv_mfma_f32_16x16x32_bf16 v[96:99],a[128:131],v[128:131],v[96:99]\nds_read_b64_tr_b16 v[144:145],v227 offset:16384\n"
"v_mfma_f32_16x16x32_bf16 v[96:99],a[132:135],v[132:135],v[96:99]\nds_read_b64_tr_b16 v[146:147],v227 offset:16896\nv_mfma_f32_16x16x32_bf16 v[96:99],a[136:139],v[136:139],v[96:99]\nds_read_b64_tr_b16 v[148:149],v227 offset:20480\n"
"v_mfma_f32_16x16x32_bf16 v[96:99],a[140:143],v[140:143],v[96:99]\nbuffer_load_dwordx4 v230,s[36:39],s60 offen offset:2048 lds\nv_mfma_f32_16x16x32_bf16 v[100:103],a[156:159],v[140:143],v[100:103]\n"
"ds_read_b64_tr_b16 v[150:151],v227 offset:20992\nv_mfma_f32_16x16x32_bf16 v[100:103],a[152:155],v[136:139],v[100:103]\nds_read_b64_tr_b16 v[152:153],v227 offset:24576\n"
"v_mfma_f32_16x16x32_bf16 v[100:103],a[148:151],v[132:135],v[100:103]\nds_read_b64_tr_b16 v[154:155],v227 offset:25088\nv_mfma_f32_16x16x32_bf16 v[100:103],a[144:147],v[128:131],v[100:103]\n"
"buffer_load_dwordx4 v231,s[36:39],s60 offen offset:3072 lds\nv_mfma_f32_16x16x32_bf16 v[104:107],a[160:163],v[128:131],v[104:107]\nds_read_b64_tr_b16 v[156:157],v227 offset:28672\ns_add_u32 s60,s50,s65\n"
"v_mfma_f32_16x16x32_bf16 v[104:107],a[164:167],v[132:135],v[104:107]\nds_read_b64_tr_b16 v[158:159],v227 offset:29184\ns_add_u32 m0,s61,98304\nv_mfma_f32_16x16x32_bf16 v[104:107],a[168:171],v[136:139],v[104:107]\n"
"v_mfma_f32_16x16x32_bf16 v[104:107],a[172:175],v[140:143],v[104:107]\nbuffer_load_dwordx4 v228,s[36:39],s60 offen offset:0 lds\nv_mfma_f32_16x16x32_bf16 v[108:111],a[188:191],v[140:143],v[108:111]\n"
"v_mfma_f32_16x16x32_bf16 v[108:111],a[184:187],v[136:139],v[108:111]\nv_mfma_f32_16x16x32_bf16 v[108:111],a[180:183],v[132:135],v[108:111]\ns_waitcnt vmcnt(17) lgkmcnt(0)\n"
"v_mfma_f32_16x16x32_bf16 v[108:111],a[176:179],v[128:131],v[108:111]\nbuffer_load_dwordx4 v229,s[36:39],s60 offen offset:1024 lds\nv_mfma_f32_16x16x32_bf16 v[124:127],a[176:179],v[144:147],v[124:127]\ns_barrier\n"
"v_mfma_f32_16x16x32_bf16 v[124:127],a[180:183],v[148:151],v[124:127]\nds_read_b64_tr_b16 v[128:129],v224 offset:32768\nv_mfma_f32_16x16x32_bf16 v[124:127],a[184:187],v[152:155],v[124:127]\n"
"ds_read_b64_tr_b16 v[130:131],v224 offset:33280\nv_mfma_f32_16x16x32_bf16 v[124:127],a[188:191],v[156:159],v[124:127]\nbuffer_load_dwordx4 v230,s[36:39],s60 offen offset:2048 lds\n"
"v_mfma_f32_16x16x32_bf16 v[120:123],a[172:175],v[156:159],v[120:123]\nds_read_b64_tr_b16 v[132:133],v224 offset:36864\nv_mfma_f32_16x16x32_bf16 v[120:123],a[168:171],v[152:155],v[120:123]\n"
"ds_read_b64_tr_b16 v[134:135],v224 offset:37376\nv_mfma_f32_16x16x32_bf16 v[120:123],a[164:167],v[148:151],v[120:123]\nds_read_b64_tr_b16 v[136:137],v224 offset:40960\n"
"v_mfma_f32_16x16x32_bf16 v[120:123],a[160:163],v[144:147],v[120:123]\nbuffer_load_dwordx4 v231,s[36:39],s60 offen offset:3072 lds\nv_mfma_f32_16x16x32_bf16 v[116:119],a[144:147],v[144:147],v[116:119]\n"
"ds_read_b64_tr_b16 v[138:139],v224 offset:41472\ns_add_u32 s60,s51,s65\nv_mfma_f32_16x16x32_bf16 v[116:119],a[148:151],v[148:151],v[116:119]\nds_read_b64_tr_b16 v[140:141],v224 offset:45056\ns_add_u32 m0,s61,114688\n"
"v_mfma_f32_16x16x32_bf16 v[116:119],a[152:155],v[152:155],v[116:119]\nds_read_b64_tr_b16 v[142:143],v224 offset:45568\nv_mfma_f32_16x16x32_bf16 v[116:119],a[156:159],v[156:159],v[116:119]\n"
"buffer_load_dwordx4 v228,s[36:39],s60 offen offset:0 lds\nv_mfma_f32_16x16x32_bf16 v[112:115],a[140:143],v[156:159],v[112:115]\nv_mfma_f32_16x16x32_bf16 v[112:115],a[136:139],v[152:155],v[112:115]\n"
"v_mfma_f32_16x16x32_bf16 v[112:115],a[132:135],v[148:151],v[112:115]\ns_waitcnt lgkmcnt(0)\nv_mfma_f32_16x16x32_bf16 v[112:115],a[128:131],v[144:147],v[112:115]\nbuffer_load_dwordx4 v229,s[36:39],s60 offen offset:1024 lds\n"
"v_mfma_f32_16x16x32_bf16 v[0:3],a[192:195],v[128:131],v[0:3]\nds_read_b64_tr_b16 v[144:145],v225 offset:32768\nv_mfma_f32_16x16x32_bf16 v[0:3],a[196:199],v[132:135],v[0:3]\nds_read_b64_tr_b16 v[146:147],v225 offset:33280\n"
"v_mfma_f32_16x16x32_bf16 v[0:3],a[200:203],v[136:139],v[0:3]\nds_read_b64_tr_b16 v[148:149],v225 offset:36864\nv_mfma_f32_16x16x32_bf16 v[0:3],a[204:207],v[140:143],v[0:3]\n"
"buffer_load_dwordx4 v230,s[36:39],s60 offen offset:2048 lds\nv_mfma_f32_16x16x32_bf16 v[4:7],a[220:223],v[140:143],v[4:7]\nds_read_b64_tr_b16 v[150:151],v225 offset:37376\n"
"v_mfma_f32_16x16x32_bf16 v[4:7],a[216:219],v[136:139],v[4:7]\nds_read_b64_tr_b16 v[152:153],v225 offset:40960\nv_mfma_f32_16x16x32_bf16 v[4:7],a[212:215],v[132:135],v[4:7]\nds_read_b64_tr_b16 v[154:155],v225 offset:41472\n"
"v_mfma_f32_16x16x32_bf16 v[4:7],a[208:211],v[128:131],v[4:7]\nbuffer_load_dwordx4 v231,s[36:39],s60 offen offset:3072 lds\nv_mfma_f32_16x16x32_bf16 v[8:11],a[224:227],v[128:131],v[8:11]\n"
"ds_read_b64_tr_b16 v[156:157],v225 offset:45056\nv_mfma_f32_16x16x32_bf16 v[8:11],a[228:231],v[132:135],v[8:11]\nds_read_b64_tr_b16 v[158:159],v225 offset:45568\nv_mfma_f32_16x16x32_bf16 v[8:11],a[232:235],v[136:139],v[8:11]\n"
"v_mfma_f32_16x16x32_bf16 v[8:11],a[236:239],v[140:143],v[8:11]\nv_mfma_f32_16x16x32_bf16 v[12:15],a[252:255],v[140:143],v[12:15]\nv_mfma_f32_16x16x32_bf16 v[12:15],a[248:251],v[136:139],v[12:15]\n"
"v_mfma_f32_16x16x32_bf16 v[12:15],a[244:247],v[132:135],v[12:15]\ns_waitcnt lgkmcnt(0)\nv_mfma_f32_16x16x32_bf16 v[12:15],a[240:243],v[128:131],v[12:15]\nv_mfma_f32_16x16x32_bf16 v[28:31],a[240:243],v[144:147],v[28:31]\n"
"ds_read_b64_tr_b16 v[128:129],v226 offset:32768\nv_mfma_f32_16x16x32_bf16 v[28:31],a[244:247],v[148:151],v[28:31]\nds_read_b64_tr_b16 v[130:131],v226 offset:33280\nv_mfma_f32_16x16x32_bf16 v[28:31],a[248:251],v[152:155],v[28:31]\n"
"ds_read_b64_tr_b16 v[132:133],v226 offset:36864\nv_mfma_f32_16x16x32_bf16 v[28:31],a[252:255],v[156:159],v[28:31]\nglobal_load_dword v235,v238,s[34:35]\nv_mfma_f32_16x16x32_bf16 v[24:27],a[236:239],v[156:159],v[24:27]\n"
"ds_read_b64_tr_b16 v[134:135],v226 offset:37376\nv_xor_b32 v238,0x10000,v238\nv_mfma_f32_16x16x32_bf16 v[24:27],a[232:235],v[152:155],v[24:27]\nds_read_b64_tr_b16 v[136:137],v226 offset:40960\n"
"v_mfma_f32_16x16x32_bf16 v[24:27],a[228:231],v[148:151],v[24:27]\nds_read_b64_tr_b16 v[138:139],v226 offset:41472\nv_mfma_f32_16x16x32_bf16 v[24:27],a[224:227],v[144:147],v[24:27]\n"
"v_mfma_f32_16x16x32_bf16 v[20:23],a[208:211],v[144:147],v[20:23]\nds_read_b64_tr_b16 v[140:141],v226 offset:45056\nv_mfma_f32_16x16x32_bf16 v[20:23],a[212:215],v[148:151],v[20:23]\nds_read_b64_tr_b16 v[142:143],v226 offset:45568\n"
"v_mfma_f32_16x16x32_bf16 v[20:23],a[216:219],v[152:155],v[20:23]\nv_mfma_f32_16x16x32_bf16 v[20:23],a[220:223],v[156:159],v[20:23]\nv_mfma_f32_16x16x32_bf16 v[16:19],a[204:207],v[156:159],v[16:19]\n"
"v_mfma_f32_16x16x32_bf16 v[16:19],a[200:203],v[152:155],v[16:19]\nv_mfma_f32_16x16x32_bf16 v[16:19],a[196:199],v[148:151],v[16:19]\ns_waitcnt lgkmcnt(0)\nv_mfma_f32_16x16x32_bf16 v[16:19],a[192:195],v[144:147],v[16:19]\n"
"v_mfma_f32_16x16x32_bf16 v[32:35],a[192:195],v[128:131],v[32:35]\nds_read_b64_tr_b16 v[144:145],v227 offset:32768\nv_mfma_f32_16x16x32_bf16 v[32:35],a[196:199],v[132:135],v[32:35]\nds_read_b64_tr_b16 v[146:147],v227 offset:33280\n"
"v_mfma_f32_16x16x32_bf16 v[32:35],a[200:203],v[136:139],v[32:35]\nds_read_b64_tr_b16 v[148:149],v227 offset:36864\nv_mfma_f32_16x16x32_bf16 v[32:35],a[204:207],v[140:143],v[32:35]\nglobal_load_dword v235,v238,s[34:35]\n"
"v_mfma_f32_16x16x32_bf16 v[36:39],a[220:223],v[140:143],v[36:39]\nds_read_b64_tr_b16 v[150:151],v227 offset:37376\nv_mfma_f32_16x16x32_bf16 v[36:39],a[216:219],v[136:139],v[36:39]\nds_read_b64_tr_b16 v[152:153],v227 offset:40960\n"
"v_mfma_f32_16x16x32_bf16 v[36:39],a[212:215],v[132:135],v[36:39]\nds_read_b64_tr_b16 v[154:155],v227 offset:41472\nv_mfma_f32_16x16x32_bf16 v[36:39],a[208:211],v[128:131],v[36:39]\n"
"v_mfma_f32_16x16x32_bf16 v[40:43],a[224:227],v[128:131],v[40:43]\nds_read_b64_tr_b16 v[156:157],v227 offset:45056\nv_mfma_f32_16x16x32_bf16 v[40:43],a[228:231],v[132:135],v[40:43]\nds_read_b64_tr_b16 v[158:159],v227 offset:45568\n"
"v_mfma_f32_16x16x32_bf16 v[40:43],a[232:235],v[136:139],v[40:43]\nv_mfma_f32_16x16x32_bf16 v[40:43],a[236:239],v[140:143],v[40:43]\nv_mfma_f32_16x16x32_bf16 v[44:47],a[252:255],v[140:143],v[44:47]\n"
"v_mfma_f32_16x16x32_bf16 v[44:47],a[248:251],v[136:139],v[44:47]\nv_mfma_f32_16x16x32_bf16 v[44:47],a[244:247],v[132:135],v[44:47]\ns_waitcnt vmcnt(22) lgkmcnt(0)\nv_mfma_f32_16x16x32_bf16 v[44:47],a[240:243],v[128:131],v[44:47]\n"
"v_mfma_f32_16x16x32_bf16 v[60:63],a[240:243],v[144:147],v[60:63]\ns_barrier\nv_mfma_f32_16x16x32_bf16 v[60:63],a[244:247],v[148:151],v[60:63]\nds_read_b64_tr_b16 v[128:129],v224 offset:49152\n"
"v_mfma_f32_16x16x32_bf16 v[60:63],a[248:251],v[152:155],v[60:63]\nds_read_b64_tr_b16 v[130:131],v224 offset:49664\nv_mfma_f32_16x16x32_bf16 v[60:63],a[252:255],v[156:159],v[60:63]\n"
"v_mfma_f32_16x16x32_bf16 v[56:59],a[236:239],v[156:159],v[56:59]\nds_read_b64_tr_b16 v[132:133],v224 offset:53248\nv_mfma_f32_16x16x32_bf16 v[56:59],a[232:235],v[152:155],v[56:59]\nds_read_b64_tr_b16 v[134:135],v224 offset:53760\n"
"v_mfma_f32_16x16x32_bf16 v[56:59],a[228:231],v[148:151],v[56:59]\nds_read_b64_tr_b16 v[136:137],v224 offset:57344\nv_mfma_f32_16x16x32_bf16 v[56:59],a[224:227],v[144:147],v[56:59]\n"
"v_mfma_f32_16x16x32_bf16 v[52:55],a[208:211],v[144:147],v[52:55]\nds_read_b64_tr_b16 v[138:139],v224 offset:57856\nv_mfma_f32_16x16x32_bf16 v[52:55],a[212:215],v[148:151],v[52:55]\nds_read_b64_tr_b16 v[140:141],v224 offset:61440\n"
"v_mfma_f32_16x16x32_bf16 v[52:55],a[216:219],v[152:155],v[52:55]\nds_read_b64_tr_b16 v[142:143],v224 offset:61952\nv_mfma_f32_16x16x32_bf16 v[52:55],a[220:223],v[156:159],v[52:55]\n"
"v_mfma_f32_16x16x32_bf16 v[48:51],a[204:207],v[156:159],v[48:51]\nv_mfma_f32_16x16x32_bf16 v[48:51],a[200:203],v[152:155],v[48:51]\nv_mfma_f32_16x16x32_bf16 v[48:51],a[196:199],v[148:151],v[48:51]\ns_waitcnt lgkmcnt(0)\n"
"v_mfma_f32_16x16x32_bf16 v[48:51],a[192:195],v[144:147],v[48:51]\ns_nop 7\nv_cvt_pk_bf16_f32 v208,v32,v33\nv_cvt_pk_bf16_f32 v209,v34,v35\nv_cvt_pk_bf16_f32 v210,v36,v37\nv_cvt_pk_bf16_f32 v211,v38,v39\n"
"v_cvt_pk_bf16_f32 v212,v40,v41\nv_cvt_pk_bf16_f32 v213,v42,v43\nv_cvt_pk_bf16_f32 v214,v44,v45\nv_cvt_pk_bf16_f32 v215,v46,v47\nv_cvt_pk_bf16_f32 v216,v48,v49\nv_cvt_pk_bf16_f32 v217,v50,v51\nv_cvt_pk_bf16_f32 v218,v52,v53\n"
"v_cvt_pk_bf16_f32 v219,v54,v55\nv_cvt_pk_bf16_f32 v220,v56,v57\nv_cvt_pk_bf16_f32 v221,v58,v59\nv_cvt_pk_bf16_f32 v222,v60,v61\nv_cvt_pk_bf16_f32 v223,v62,v63\nv_max_f32 v32,0,v0\nv_mul_f32 v0,0x3fb8aa3b,v0\nv_max_f32 v33,0,v1\n"
"v_mul_f32 v1,0x3fb8aa3b,v1\nv_max_f32 v34,0,v2\nv_mul_f32 v2,0x3fb8aa3b,v2\nv_max_f32 v35,0,v3\nv_mul_f32 v3,0x3fb8aa3b,v3\nv_max_f32 v36,0,v4\nv_mul_f32 v4,0x3fb8aa3b,v4\nv_max_f32 v37,0,v5\nv_mul_f32 v5,0x3fb8aa3b,v5\n"
"v_max_f32 v38,0,v6\nv_mul_f32 v6,0x3fb8aa3b,v6\nv_max_f32 v39,0,v7\nv_mul_f32 v7,0x3fb8aa3b,v7\nv_max_f32 v40,0,v8\nv_mul_f32 v8,0x3fb8aa3b,v8\nv_max_f32 v41,0,v9\nv_mul_f32 v9,0x3fb8aa3b,v9\nv_max_f32 v42,0,v10\n"
"v_mul_f32 v10,0x3fb8aa3b,v10\nv_max_f32 v43,0,v11\nv_mul_f32 v11,0x3fb8aa3b,v11\nv_max_f32 v44,0,v12\nv_mul_f32 v12,0x3fb8aa3b,v12\nv_max_f32 v45,0,v13\nv_mul_f32 v13,0x3fb8aa3b,v13\nv_max_f32 v46,0,v14\n"
"v_mul_f32 v14,0x3fb8aa3b,v14\nv_max_f32 v47,0,v15\nv_mul_f32 v15,0x3fb8aa3b,v15\nv_max_f32 v63,0,v31\nv_mul_f32 v31,0x3fb8aa3b,v31\nv_max_f32 v62,0,v30\nv_mul_f32 v30,0x3fb8aa3b,v30\nv_max_f32 v61,0,v29\n"
"v_mul_f32 v29,0x3fb8aa3b,v29\nv_max_f32 v60,0,v28\nv_mul_f32 v28,0x3fb8aa3b,v28\nv_max_f32 v59,0,v27\nv_mul_f32 v27,0x3fb8aa3b,v27\nv_max_f32 v58,0,v26\nv_mul_f32 v26,0x3fb8aa3b,v26\nv_max_f32 v57,0,v25\n"
"v_mul_f32 v25,0x3fb8aa3b,v25\nv_max_f32 v56,0,v24\nv_mul_f32 v24,0x3fb8aa3b,v24\nv_max_f32 v55,0,v23\nv_mul_f32 v23,0x3fb8aa3b,v23\nv_max_f32 v54,0,v22\nv_mul_f32 v22,0x3fb8aa3b,v22\nv_max_f32 v53,0,v21\n"
"v_mul_f32 v21,0x3fb8aa3b,v21\nv_max_f32 v52,0,v20\nv_mul_f32 v20,0x3fb8aa3b,v20\nv_max_f32 v51,0,v19\nv_mul_f32 v19,0x3fb8aa3b,v19\nv_max_f32 v50,0,v18\nv_mul_f32 v18,0x3fb8aa3b,v18\nv_max_f32 v49,0,v17\n"
"v_mul_f32 v17,0x3fb8aa3b,v17\nv_max_f32 v48,0,v16\nv_mul_f32 v16,0x3fb8aa3b,v16\nv_exp_f32_e64 v0,v0 clamp\nv_add_f32 v32,0x358637bd,v32\nv_exp_f32_e64 v1,v1 clamp\nv_add_f32 v33,0x358637bd,v33\nv_add_f32 v32,v32,v0\n"
"v_exp_f32_e64 v2,v2 clamp\nv_add_f32 v34,0x358637bd,v34\nv_add_f32 v33,v33,v1\nv_exp_f32_e64 v3,v3 clamp\nv_add_f32 v35,0x358637bd,v35\nv_add_f32 v34,v34,v2\nv_exp_f32_e64 v4,v4 clamp\nv_add_f32 v36,0x358637bd,v36\n"
"v_add_f32 v35,v35,v3\nv_exp_f32_e64 v5,v5 clamp\nv_add_f32 v37,0x358637bd,v37\nv_add_f32 v36,v36,v4\nv_exp_f32_e64 v6,v6 clamp\nv_add_f32 v38,0x358637bd,v38\nv_add_f32 v37,v37,v5\nv_exp_f32_e64 v7,v7 clamp\n"
"v_add_f32 v39,0x358637bd,v39\nv_add_f32 v38,v38,v6\nv_exp_f32_e64 v8,v8 clamp\nv_add_f32 v40,0x358637bd,v40\nv_add_f32 v39,v39,v7\nv_exp_f32_e64 v9,v9 clamp\nv_add_f32 v41,0x358637bd,v41\nv_add_f32 v40,v40,v8\n"
"v_exp_f32_e64 v10,v10 clamp\nv_add_f32 v42,0x358637bd,v42\nv_add_f32 v41,v41,v9\nv_exp_f32_e64 v11,v11 clamp\nv_add_f32 v43,0x358637bd,v43\nv_add_f32 v42,v42,v10\nv_exp_f32_e64 v12,v12 clamp\nv_add_f32 v44,0x358637bd,v44\n"
"v_add_f32 v43,v43,v11\nv_exp_f32_e64 v13,v13 clamp\nv_add_f32 v45,0x358637bd,v45\nv_add_f32 v44,v44,v12\nv_exp_f32_e64 v14,v14 clamp\nv_add_f32 v46,0x358637bd,v46\nv_add_f32 v45,v45,v13\nv_exp_f32_e64 v15,v15 clamp\n"
"v_add_f32 v47,0x358637bd,v47\nv_add_f32 v46,v46,v14\nv_exp_f32_e64 v16,v16 clamp\nv_add_f32 v48,0x358637bd,v48\nv_add_f32 v47,v47,v15\nv_exp_f32_e64 v17,v17 clamp\nv_add_f32 v49,0x358637bd,v49\nv_add_f32 v48,v48,v16\n"
"v_exp_f32_e64 v18,v18 clamp\nv_add_f32 v50,0x358637bd,v50\nv_add_f32 v49,v49,v17\nv_exp_f32_e64 v19,v19 clamp\nv_add_f32 v51,0x358637bd,v51\nv_add_f32 v50,v50,v18\nv_exp_f32_e64 v20,v20 clamp\nv_add_f32 v52,0x358637bd,v52\n"
"v_add_f32 v51,v51,v19\nv_exp_f32_e64 v21,v21 clamp\nv_add_f32 v53,0x358637bd,v53\nv_add_f32 v52,v52,v20\nv_exp_f32_e64 v22,v22 clamp\nv_add_f32 v54,0x358637bd,v54\nv_add_f32 v53,v53,v21\nv_exp_f32_e64 v23,v23 clamp\n"
"v_add_f32 v55,0x358637bd,v55\nv_add_f32 v54,v54,v22\nv_exp_f32_e64 v24,v24 clamp\nv_add_f32 v56,0x358637bd,v56\nv_add_f32 v55,v55,v23\nv_exp_f32_e64 v25,v25 clamp\nv_add_f32 v57,0x358637bd,v57\nv_add_f32 v56,v56,v24\n"
"v_exp_f32_e64 v26,v26 clamp\nv_add_f32 v58,0x358637bd,v58\nv_add_f32 v57,v57,v25\nv_exp_f32_e64 v27,v27 clamp\nv_add_f32 v59,0x358637bd,v59\nv_add_f32 v58,v58,v26\nv_exp_f32_e64 v28,v28 clamp\nv_add_f32 v60,0x358637bd,v60\n"
"v_add_f32 v59,v59,v27\nv_exp_f32_e64 v29,v29 clamp\nv_add_f32 v61,0x358637bd,v61\nv_add_f32 v60,v60,v28\nv_exp_f32_e64 v30,v30 clamp\nv_add_f32 v62,0x358637bd,v62\nv_add_f32 v61,v61,v29\nv_exp_f32_e64 v31,v31 clamp\n"
"v_add_f32 v63,0x358637bd,v63\nv_add_f32 v62,v62,v30\nv_add_f32 v63,v63,v31\nv_cvt_pk_bf16_f32 v192,v32,v33\nv_cvt_pk_bf16_f32 v193,v34,v35\nv_cvt_pk_bf16_f32 v194,v36,v37\nv_cvt_pk_bf16_f32 v195,v38,v39\n"
"v_cvt_pk_bf16_f32 v196,v40,v41\nv_cvt_pk_bf16_f32 v197,v42,v43\nv_cvt_pk_bf16_f32 v198,v44,v45\nv_cvt_pk_bf16_f32 v199,v46,v47\nv_cvt_pk_bf16_f32 v200,v48,v49\nv_cvt_pk_bf16_f32 v201,v50,v51\nv_cvt_pk_bf16_f32 v202,v52,v53\n"
"v_cvt_pk_bf16_f32 v203,v54,v55\nv_cvt_pk_bf16_f32 v204,v56,v57\nv_cvt_pk_bf16_f32 v205,v58,v59\nv_cvt_pk_bf16_f32 v206,v60,v61\nv_cvt_pk_bf16_f32 v207,v62,v63\nv_cmp_eq_u32 vcc,s74,v241\ns_add_u32 s74,s74,1\nv_nop\n"
"v_cndmask_b32 v244,0,v243,vcc\nv_mov_b32 v245,v244\nv_mov_b32 v246,v244\nv_mov_b32 v247,v244\nv_mfma_f32_16x16x32_bf16 v[0:3],v[192:195],v[208:211],0\nv_mfma_f32_16x16x32_bf16 v[0:3],v[196:199],v[212:215],v[0:3]\n"
"v_mfma_f32_16x16x32_bf16 v[248:251],v[192:195],v[244:247],v[248:251]\nv_mfma_f32_16x16x32_bf16 v[248:251],v[196:199],v[244:247],v[248:251]\nv_cmp_eq_u32 vcc,s74,v241\ns_add_u32 s74,s74,1\nv_nop\nv_cndmask_b32 v244,0,v243,vcc\n"
"v_mov_b32 v245,v244\nv_mov_b32 v246,v244\nv_mov_b32 v247,v244\nv_mfma_f32_16x16x32_bf16 v[4:7],v[200:203],v[216:219],0\nv_mfma_f32_16x16x32_bf16 v[4:7],v[204:207],v[220:223],v[4:7]\n"
"v_mfma_f32_16x16x32_bf16 v[248:251],v[200:203],v[244:247],v[248:251]\nv_mfma_f32_16x16x32_bf16 v[248:251],v[204:207],v[244:247],v[248:251]\nv_nop\nv_nop\nv_nop\nv_nop\nv_nop\nv_nop\nds_write_b128 v242,v[0:3]\n"
"ds_write_b128 v242,v[4:7] offset:4096\nv_add_u32 v242,0x2000,v242\n.p2align 6\nLLl_%=:\nv_mfma_f32_16x16x32_bf16 v[64:67],a[192:195],v[128:131],v[64:67]\nds_read_b64_tr_b16 v[144:145],v225 offset:49152\n"
"v_mfma_f32_16x16x32_bf16 v[64:67],a[196:199],v[132:135],v[64:67]\nds_read_b64_tr_b16 v[146:147],v225 offset:49664\nv_xor_b32 v224,0x10000,v224\nv_mfma_f32_16x16x32_bf16 v[64:67],a[200:203],v[136:139],v[64:67]\n"
"ds_read_b64_tr_b16 v[148:149],v225 offset:53248\nv_mfma_f32_16x16x32_bf16 v[64:67],a[204:207],v[140:143],v[64:67]\ns_add_u32 s65,s65,s66\ns_cmp_gt_u32 s70,1\ns_cselect_b32 s75,s65,0x40000000\n"
"v_mfma_f32_16x16x32_bf16 v[68:71],a[220:223],v[140:143],v[68:71]\nds_read_b64_tr_b16 v[150:151],v225 offset:53760\ns_add_u32 s60,s48,s75\nv_mfma_f32_16x16x32_bf16 v[68:71],a[216:219],v[136:139],v[68:71]\n"
"ds_read_b64_tr_b16 v[152:153],v225 offset:57344\ns_add_u32 m0,s61,0\nv_mfma_f32_16x16x32_bf16 v[68:71],a[212:215],v[132:135],v[68:71]\nds_read_b64_tr_b16 v[154:155],v225 offset:57856\n"
"v_mfma_f32_16x16x32_bf16 v[68:71],a[208:211],v[128:131],v[68:71]\nbuffer_load_dwordx4 v228,s[36:39],s60 offen offset:0 lds\nv_mfma_f32_16x16x32_bf16 v[72:75],a[224:227],v[128:131],v[72:75]\n"
"ds_read_b64_tr_b16 v[156:157],v225 offset:61440\nv_mfma_f32_16x16x32_bf16 v[72:75],a[228:231],v[132:135],v[72:75]\nds_read_b64_tr_b16 v[158:159],v225 offset:61952\nv_mfma_f32_16x16x32_bf16 v[72:75],a[232:235],v[136:139],v[72:75]\n"
"v_mfma_f32_16x16x32_bf16 v[72:75],a[236:239],v[140:143],v[72:75]\nbuffer_load_dwordx4 v229,s[36:39],s60 offen offset:1024 lds\nv_mfma_f32_16x16x32_bf16 v[76:79],a[252:255],v[140:143],v[76:79]\n"
"v_mfma_f32_16x16x32_bf16 v[76:79],a[248:251],v[136:139],v[76:79]\nv_mfma_f32_16x16x32_bf16 v[76:79],a[244:247],v[132:135],v[76:79]\ns_waitcnt lgkmcnt(0)\nv_mfma_f32_16x16x32_bf16 v[76:79],a[240:243],v[128:131],v[76:79]\n"
"buffer_load_dwordx4 v230,s[36:39],s60 offen offset:2048 lds\nv_mfma_f32_16x16x32_bf16 v[92:95],a[240:243],v[144:147],v[92:95]\nds_read_b64_tr_b16 v[128:129],v226 offset:49152\n"
"v_mfma_f32_16x16x32_bf16 v[92:95],a[244:247],v[148:151],v[92:95]\nds_read_b64_tr_b16 v[130:131],v226 offset:49664\nv_xor_b32 v225,0x10000,v225\nv_mfma_f32_16x16x32_bf16 v[92:95],a[248:251],v[152:155],v[92:95]\n"
"ds_read_b64_tr_b16 v[132:133],v226 offset:53248\nv_mfma_f32_16x16x32_bf16 v[92:95],a[252:255],v[156:159],v[92:95]\nbuffer_load_dwordx4 v231,s[36:39],s60 offen offset:3072 lds\n"
"v_mfma_f32_16x16x32_bf16 v[88:91],a[236:239],v[156:159],v[88:91]\nds_read_b64_tr_b16 v[134:135],v226 offset:53760\ns_add_u32 s60,s49,s75\nv_mfma_f32_16x16x32_bf16 v[88:91],a[232:235],v[152:155],v[88:91]\n"
"ds_read_b64_tr_b16 v[136:137],v226 offset:57344\ns_add_u32 m0,s61,16384\nv_mfma_f32_16x16x32_bf16 v[88:91],a[228:231],v[148:151],v[88:91]\nds_read_b64_tr_b16 v[138:139],v226 offset:57856\nv_max_f32 v32,0,v64\n"
"v_mfma_f32_16x16x32_bf16 v[88:91],a[224:227],v[144:147],v[88:91]\nv_mul_f32 v0,0x3fb8aa3b,v64\nv_max_f32 v33,0,v65\nv_mfma_f32_16x16x32_bf16 v[84:87],a[208:211],v[144:147],v[84:87]\n"
"ds_read_b64_tr_b16 v[140:141],v226 offset:61440\nv_mul_f32 v1,0x3fb8aa3b,v65\nv_mfma_f32_16x16x32_bf16 v[84:87],a[212:215],v[148:151],v[84:87]\nds_read_b64_tr_b16 v[142:143],v226 offset:61952\nv_max_f32 v34,0,v66\n"
"v_mfma_f32_16x16x32_bf16 v[84:87],a[216:219],v[152:155],v[84:87]\nv_mul_f32 v2,0x3fb8aa3b,v66\nv_max_f32 v35,0,v67\nv_mfma_f32_16x16x32_bf16 v[84:87],a[220:223],v[156:159],v[84:87]\nv_mul_f32 v3,0x3fb8aa3b,v67\n"
"v_max_f32 v36,0,v68\nv_mfma_f32_16x16x32_bf16 v[80:83],a[204:207],v[156:159],v[80:83]\nv_mul_f32 v4,0x3fb8aa3b,v68\nv_max_f32 v37,0,v69\nv_mfma_f32_16x16x32_bf16 v[80:83],a[200:203],v[152:155],v[80:83]\n"
"v_mul_f32 v5,0x3fb8aa3b,v69\nv_max_f32 v38,0,v70\nv_mfma_f32_16x16x32_bf16 v[80:83],a[196:199],v[148:151],v[80:83]\ns_waitcnt lgkmcnt(0)\nv_mul_f32 v6,0x3fb8aa3b,v70\nv_max_f32 v39,0,v71\n"
"v_mfma_f32_16x16x32_bf16 v[80:83],a[192:195],v[144:147],v[80:83]\nv_mul_f32 v7,0x3fb8aa3b,v71\nv_max_f32 v40,0,v72\nv_mfma_f32_16x16x32_bf16 v[96:99],a[192:195],v[128:131],v[96:99]\n"
"ds_read_b64_tr_b16 v[144:145],v227 offset:49152\nv_mul_f32 v8,0x3fb8aa3b,v72\nv_mfma_f32_16x16x32_bf16 v[96:99],a[196:199],v[132:135],v[96:99]\nds_read_b64_tr_b16 v[146:147],v227 offset:49664\nv_xor_b32 v226,0x10000,v226\n"
"v_mfma_f32_16x16x32_bf16 v[96:99],a[200:203],v[136:139],v[96:99]\nds_read_b64_tr_b16 v[148:149],v227 offset:53248\nv_max_f32 v41,0,v73\nv_mfma_f32_16x16x32_bf16 v[96:99],a[204:207],v[140:143],v[96:99]\n"
"v_mul_f32 v9,0x3fb8aa3b,v73\nv_max_f32 v42,0,v74\nv_mfma_f32_16x16x32_bf16 v[100:103],a[220:223],v[140:143],v[100:103]\nds_read_b64_tr_b16 v[150:151],v227 offset:53760\nv_mul_f32 v10,0x3fb8aa3b,v74\n"
"v_mfma_f32_16x16x32_bf16 v[100:103],a[216:219],v[136:139],v[100:103]\nds_read_b64_tr_b16 v[152:153],v227 offset:57344\nv_max_f32 v43,0,v75\nv_mfma_f32_16x16x32_bf16 v[100:103],a[212:215],v[132:135],v[100:103]\n"
"ds_read_b64_tr_b16 v[154:155],v227 offset:57856\nv_mul_f32 v11,0x3fb8aa3b,v75\nv_mfma_f32_16x16x32_bf16 v[100:103],a[208:211],v[128:131],v[100:103]\nv_max_f32 v44,0,v76\nv_mul_f32 v12,0x3fb8aa3b,v76\n"
"v_mfma_f32_16x16x32_bf16 v[104:107],a[224:227],v[128:131],v[104:107]\nds_read_b64_tr_b16 v[156:157],v227 offset:61440\nv_max_f32 v45,0,v77\nv_mfma_f32_16x16x32_bf16 v[104:107],a[228:231],v[132:135],v[104:107]\n"
"ds_read_b64_tr_b16 v[158:159],v227 offset:61952\nv_mul_f32 v13,0x3fb8aa3b,v77\nv_mfma_f32_16x16x32_bf16 v[104:107],a[232:235],v[136:139],v[104:107]\nv_max_f32 v46,0,v78\nv_mul_f32 v14,0x3fb8aa3b,v78\n"
"v_mfma_f32_16x16x32_bf16 v[104:107],a[236:239],v[140:143],v[104:107]\nv_max_f32 v47,0,v79\nv_mul_f32 v15,0x3fb8aa3b,v79\nv_mfma_f32_16x16x32_bf16 v[108:111],a[252:255],v[140:143],v[108:111]\nv_max_f32 v63,0,v95\n"
"v_mul_f32 v31,0x3fb8aa3b,v95\nv_mfma_f32_16x16x32_bf16 v[108:111],a[248:251],v[136:139],v[108:111]\nv_max_f32 v62,0,v94\nv_mul_f32 v30,0x3fb8aa3b,v94\nv_mfma_f32_16x16x32_bf16 v[108:111],a[244:247],v[132:135],v[108:111]\n"
"s_waitcnt vmcnt(16) lgkmcnt(0)\nv_max_f32 v61,0,v93\nv_mul_f32 v29,0x3fb8aa3b,v93\nv_mfma_f32_16x16x32_bf16 v[108:111],a[240:243],v[128:131],v[108:111]\nv_max_f32 v60,0,v92\nv_mul_f32 v28,0x3fb8aa3b,v92\n"
"v_mfma_f32_16x16x32_bf16 v[124:127],a[240:243],v[144:147],v[124:127]\ns_barrier\nv_max_f32 v59,0,v91\nv_mul_f32 v27,0x3fb8aa3b,v91\nv_mfma_f32_16x16x32_bf16 v[124:127],a[244:247],v[148:151],v[124:127]\n"
"ds_read_b64_tr_b16 v[128:129],v224 offset:0\nv_xor_b32 v227,0x10000,v227\nv_mfma_f32_16x16x32_bf16 v[124:127],a[248:251],v[152:155],v[124:127]\nds_read_b64_tr_b16 v[130:131],v224 offset:512\nv_max_f32 v58,0,v90\n"
"v_mfma_f32_16x16x32_bf16 v[124:127],a[252:255],v[156:159],v[124:127]\nv_mul_f32 v26,0x3fb8aa3b,v90\nv_max_f32 v57,0,v89\nv_mfma_f32_16x16x32_bf16 v[120:123],a[236:239],v[156:159],v[120:123]\n"
"ds_read_b64_tr_b16 v[132:133],v224 offset:4096\nv_mul_f32 v25,0x3fb8aa3b,v89\nv_mfma_f32_16x16x32_bf16 v[120:123],a[232:235],v[152:155],v[120:123]\nds_read_b64_tr_b16 v[134:135],v224 offset:4608\nv_max_f32 v56,0,v88\n"
"v_mfma_f32_16x16x32_bf16 v[120:123],a[228:231],v[148:151],v[120:123]\nds_read_b64_tr_b16 v[136:137],v224 offset:8192\nv_mul_f32 v24,0x3fb8aa3b,v88\nv_mfma_f32_16x16x32_bf16 v[120:123],a[224:227],v[144:147],v[120:123]\n"
"v_max_f32 v55,0,v87\nv_mul_f32 v23,0x3fb8aa3b,v87\nv_mfma_f32_16x16x32_bf16 v[116:119],a[208:211],v[144:147],v[116:119]\nds_read_b64_tr_b16 v[138:139],v224 offset:8704\nv_max_f32 v54,0,v86\n"
"v_mfma_f32_16x16x32_bf16 v[116:119],a[212:215],v[148:151],v[116:119]\nds_read_b64_tr_b16 v[140:141],v224 offset:12288\nv_mul_f32 v22,0x3fb8aa3b,v86\nv_mfma_f32_16x16x32_bf16 v[116:119],a[216:219],v[152:155],v[116:119]\n"
"ds_read_b64_tr_b16 v[142:143],v224 offset:12800\nv_max_f32 v53,0,v85\nv_mfma_f32_16x16x32_bf16 v[116:119],a[220:223],v[156:159],v[116:119]\nv_mul_f32 v21,0x3fb8aa3b,v85\nv_max_f32 v52,0,v84\n"
"v_mfma_f32_16x16x32_bf16 v[112:115],a[204:207],v[156:159],v[112:115]\nv_mul_f32 v20,0x3fb8aa3b,v84\nv_max_f32 v51,0,v83\nv_mfma_f32_16x16x32_bf16 v[112:115],a[200:203],v[152:155],v[112:115]\nv_mul_f32 v19,0x3fb8aa3b,v83\n"
"v_max_f32 v50,0,v82\nv_mfma_f32_16x16x32_bf16 v[112:115],a[196:199],v[148:151],v[112:115]\ns_waitcnt lgkmcnt(0)\nv_mul_f32 v18,0x3fb8aa3b,v82\nv_max_f32 v49,0,v81\n"
"v_mfma_f32_16x16x32_bf16 v[112:115],a[192:195],v[144:147],v[112:115]\nv_mul_f32 v17,0x3fb8aa3b,v81\nv_max_f32 v48,0,v80\nv_mfma_f32_16x16x32_bf16 v[64:67],a[0:3],v[128:131],0\nds_read_b64_tr_b16 v[144:145],v225 offset:0\n"
"v_mul_f32 v16,0x3fb8aa3b,v80\nv_mfma_f32_16x16x32_bf16 v[64:67],a[4:7],v[132:135],v[64:67]\nds_read_b64_tr_b16 v[146:147],v225 offset:512\nv_cvt_pk_bf16_f32 v176,v96,v97\n"
"v_mfma_f32_16x16x32_bf16 v[64:67],a[8:11],v[136:139],v[64:67]\nds_read_b64_tr_b16 v[148:149],v225 offset:4096\nv_cvt_pk_bf16_f32 v177,v98,v99\nv_mfma_f32_16x16x32_bf16 v[64:67],a[12:15],v[140:143],v[64:67]\n"
"buffer_load_dwordx4 v228,s[36:39],s60 offen offset:0 lds\nv_mfma_f32_16x16x32_bf16 v[68:71],a[28:31],v[140:143],0\nds_read_b64_tr_b16 v[150:151],v225 offset:4608\nv_cvt_pk_bf16_f32 v178,v100,v101\n"
"v_mfma_f32_16x16x32_bf16 v[68:71],a[24:27],v[136:139],v[68:71]\nds_read_b64_tr_b16 v[152:153],v225 offset:8192\nv_cvt_pk_bf16_f32 v179,v102,v103\nv_mfma_f32_16x16x32_bf16 v[68:71],a[20:23],v[132:135],v[68:71]\n"
"ds_read_b64_tr_b16 v[154:155],v225 offset:8704\nv_cvt_pk_bf16_f32 v180,v104,v105\nv_mfma_f32_16x16x32_bf16 v[68:71],a[16:19],v[128:131],v[68:71]\nv_cvt_pk_bf16_f32 v181,v106,v107\nv_cvt_pk_bf16_f32 v182,v108,v109\n"
"v_mfma_f32_16x16x32_bf16 v[72:75],a[32:35],v[128:131],0\nds_read_b64_tr_b16 v[156:157],v225 offset:12288\nv_cvt_pk_bf16_f32 v183,v110,v111\nv_mfma_f32_16x16x32_bf16 v[72:75],a[36:39],v[132:135],v[72:75]\n"
"ds_read_b64_tr_b16 v[158:159],v225 offset:12800\nv_cvt_pk_bf16_f32 v190,v124,v125\nv_mfma_f32_16x16x32_bf16 v[72:75],a[40:43],v[136:139],v[72:75]\nv_cvt_pk_bf16_f32 v191,v126,v127\nv_cvt_pk_bf16_f32 v188,v120,v121\n"
"v_mfma_f32_16x16x32_bf16 v[72:75],a[44:47],v[140:143],v[72:75]\nv_cvt_pk_bf16_f32 v189,v122,v123\nv_cvt_pk_bf16_f32 v186,v116,v117\nv_mfma_f32_16x16x32_bf16 v[76:79],a[60:63],v[140:143],0\nv_cvt_pk_bf16_f32 v187,v118,v119\n"
"v_cvt_pk_bf16_f32 v184,v112,v113\nv_mfma_f32_16x16x32_bf16 v[76:79],a[56:59],v[136:139],v[76:79]\nv_cvt_pk_bf16_f32 v185,v114,v115\nv_mfma_f32_16x16x32_bf16 v[76:79],a[52:55],v[132:135],v[76:79]\ns_waitcnt lgkmcnt(0)\n"
"v_exp_f32_e64 v0,v0 clamp\nv_mfma_f32_16x16x32_bf16 v[76:79],a[48:51],v[128:131],v[76:79]\nv_add_f32 v32,0x358637bd,v32\nv_mfma_f32_16x16x32_bf16 v[92:95],a[48:51],v[144:147],0\nds_read_b64_tr_b16 v[128:129],v226 offset:0\n"
"v_mfma_f32_16x16x32_bf16 v[92:95],a[52:55],v[148:151],v[92:95]\nds_read_b64_tr_b16 v[130:131],v226 offset:512\nv_mfma_f32_16x16x32_bf16 v[92:95],a[56:59],v[152:155],v[92:95]\nds_read_b64_tr_b16 v[132:133],v226 offset:4096\n"
"v_mfma_f32_16x16x32_bf16 v[92:95],a[60:63],v[156:159],v[92:95]\nbuffer_load_dwordx4 v229,s[36:39],s60 offen offset:1024 lds\nv_mfma_f32_16x16x32_bf16 v[88:91],a[44:47],v[156:159],0\nds_read_b64_tr_b16 v[134:135],v226 offset:4608\n"
"v_mfma_f32_16x16x32_bf16 v[88:91],a[40:43],v[152:155],v[88:91]\nds_read_b64_tr_b16 v[136:137],v226 offset:8192\nv_mfma_f32_16x16x32_bf16 v[88:91],a[36:39],v[148:151],v[88:91]\nds_read_b64_tr_b16 v[138:139],v226 offset:8704\n"
"v_mfma_f32_16x16x32_bf16 v[88:91],a[32:35],v[144:147],v[88:91]\nv_exp_f32_e64 v1,v1 clamp\nv_mfma_f32_16x16x32_bf16 v[84:87],a[16:19],v[144:147],0\nds_read_b64_tr_b16 v[140:141],v226 offset:12288\nv_add_f32 v33,0x358637bd,v33\n"
"v_mfma_f32_16x16x32_bf16 v[84:87],a[20:23],v[148:151],v[84:87]\nds_read_b64_tr_b16 v[142:143],v226 offset:12800\nv_add_f32 v32,v32,v0\nv_mfma_f32_16x16x32_bf16 v[84:87],a[24:27],v[152:155],v[84:87]\nv_exp_f32_e64 v2,v2 clamp\n"
"v_mfma_f32_16x16x32_bf16 v[84:87],a[28:31],v[156:159],v[84:87]\nv_add_f32 v34,0x358637bd,v34\nv_add_f32 v33,v33,v1\nv_mfma_f32_16x16x32_bf16 v[80:83],a[12:15],v[156:159],0\nv_exp_f32_e64 v3,v3 clamp\n"
"v_mfma_f32_16x16x32_bf16 v[80:83],a[8:11],v[152:155],v[80:83]\nv_add_f32 v35,0x358637bd,v35\nv_add_f32 v34,v34,v2\nv_mfma_f32_16x16x32_bf16 v[80:83],a[4:7],v[148:151],v[80:83]\ns_waitcnt lgkmcnt(0)\nv_exp_f32_e64 v4,v4 clamp\n"
"v_mfma_f32_16x16x32_bf16 v[80:83],a[0:3],v[144:147],v[80:83]\nv_add_f32 v36,0x358637bd,v36\nv_add_f32 v35,v35,v3\nv_mfma_f32_16x16x32_bf16 v[96:99],a[0:3],v[128:131],0\nds_read_b64_tr_b16 v[144:145],v227 offset:0\n"
"v_mfma_f32_16x16x32_bf16 v[96:99],a[4:7],v[132:135],v[96:99]\nds_read_b64_tr_b16 v[146:147],v227 offset:512\nv_mfma_f32_16x16x32_bf16 v[96:99],a[8:11],v[136:139],v[96:99]\nds_read_b64_tr_b16 v[148:149],v227 offset:4096\n"
"v_mfma_f32_16x16x32_bf16 v[96:99],a[12:15],v[140:143],v[96:99]\nbuffer_load_dwordx4 v230,s[36:39],s60 offen offset:2048 lds\nv_mfma_f32_16x16x32_bf16 v[100:103],a[28:31],v[140:143],0\n"
"ds_read_b64_tr_b16 v[150:151],v227 offset:4608\nv_mfma_f32_16x16x32_bf16 v[100:103],a[24:27],v[136:139],v[100:103]\nds_read_b64_tr_b16 v[152:153],v227 offset:8192\n"
"v_mfma_f32_16x16x32_bf16 v[100:103],a[20:23],v[132:135],v[100:103]\nds_read_b64_tr_b16 v[154:155],v227 offset:8704\nv_mfma_f32_16x16x32_bf16 v[100:103],a[16:19],v[128:131],v[100:103]\nv_exp_f32_e64 v5,v5 clamp\n"
"v_mfma_f32_16x16x32_bf16 v[104:107],a[32:35],v[128:131],0\nds_read_b64_tr_b16 v[156:157],v227 offset:12288\nv_add_f32 v37,0x358637bd,v37\nv_mfma_f32_16x16x32_bf16 v[104:107],a[36:39],v[132:135],v[104:107]\n"
"ds_read_b64_tr_b16 v[158:159],v227 offset:12800\nv_add_f32 v36,v36,v4\nv_mfma_f32_16x16x32_bf16 v[104:107],a[40:43],v[136:139],v[104:107]\nv_exp_f32_e64 v6,v6 clamp\n"
"v_mfma_f32_16x16x32_bf16 v[104:107],a[44:47],v[140:143],v[104:107]\nv_add_f32 v38,0x358637bd,v38\nv_add_f32 v37,v37,v5\nv_mfma_f32_16x16x32_bf16 v[108:111],a[60:63],v[140:143],0\nv_exp_f32_e64 v7,v7 clamp\n"
"v_mfma_f32_16x16x32_bf16 v[108:111],a[56:59],v[136:139],v[108:111]\nv_add_f32 v39,0x358637bd,v39\nv_add_f32 v38,v38,v6\nv_mfma_f32_16x16x32_bf16 v[108:111],a[52:55],v[132:135],v[108:111]\ns_waitcnt vmcnt(15) lgkmcnt(0)\n"
"v_exp_f32_e64 v8,v8 clamp\nv_mfma_f32_16x16x32_bf16 v[108:111],a[48:51],v[128:131],v[108:111]\nv_add_f32 v40,0x358637bd,v40\nv_add_f32 v39,v39,v7\nv_mfma_f32_16x16x32_bf16 v[124:127],a[48:51],v[144:147],0\ns_barrier\n"
"v_exp_f32_e64 v9,v9 clamp\nv_mfma_f32_16x16x32_bf16 v[124:127],a[52:55],v[148:151],v[124:127]\nds_read_b64_tr_b16 v[128:129],v224 offset:16384\nv_add_f32 v41,0x358637bd,v41\n"
"v_mfma_f32_16x16x32_bf16 v[124:127],a[56:59],v[152:155],v[124:127]\nds_read_b64_tr_b16 v[130:131],v224 offset:16896\nv_add_f32 v40,v40,v8\nv_mfma_f32_16x16x32_bf16 v[124:127],a[60:63],v[156:159],v[124:127]\n"
"buffer_load_dwordx4 v231,s[36:39],s60 offen offset:3072 lds\nv_mfma_f32_16x16x32_bf16 v[120:123],a[44:47],v[156:159],0\nds_read_b64_tr_b16 v[132:133],v224 offset:20480\ns_add_u32 s60,s50,s75\n"
"v_mfma_f32_16x16x32_bf16 v[120:123],a[40:43],v[152:155],v[120:123]\nds_read_b64_tr_b16 v[134:135],v224 offset:20992\ns_add_u32 m0,s61,32768\nv_mfma_f32_16x16x32_bf16 v[120:123],a[36:39],v[148:151],v[120:123]\n"
"ds_read_b64_tr_b16 v[136:137],v224 offset:24576\nv_mfma_f32_16x16x32_bf16 v[120:123],a[32:35],v[144:147],v[120:123]\nv_exp_f32_e64 v10,v10 clamp\nv_mfma_f32_16x16x32_bf16 v[116:119],a[16:19],v[144:147],0\n"
"ds_read_b64_tr_b16 v[138:139],v224 offset:25088\nv_add_f32 v42,0x358637bd,v42\nv_mfma_f32_16x16x32_bf16 v[116:119],a[20:23],v[148:151],v[116:119]\nds_read_b64_tr_b16 v[140:141],v224 offset:28672\nv_add_f32 v41,v41,v9\n"
"v_mfma_f32_16x16x32_bf16 v[116:119],a[24:27],v[152:155],v[116:119]\nds_read_b64_tr_b16 v[142:143],v224 offset:29184\nv_mfma_f32_16x16x32_bf16 v[116:119],a[28:31],v[156:159],v[116:119]\nv_exp_f32_e64 v11,v11 clamp\n"
"v_mfma_f32_16x16x32_bf16 v[112:115],a[12:15],v[156:159],0\nv_add_f32 v43,0x358637bd,v43\nv_add_f32 v42,v42,v10\nv_mfma_f32_16x16x32_bf16 v[112:115],a[8:11],v[152:155],v[112:115]\nv_exp_f32_e64 v12,v12 clamp\n"
"v_mfma_f32_16x16x32_bf16 v[112:115],a[4:7],v[148:151],v[112:115]\ns_waitcnt lgkmcnt(0)\nv_add_f32 v44,0x358637bd,v44\nv_add_f32 v43,v43,v11\nv_mfma_f32_16x16x32_bf16 v[112:115],a[0:3],v[144:147],v[112:115]\n"
"v_exp_f32_e64 v13,v13 clamp\nv_mfma_f32_16x16x32_bf16 v[64:67],a[64:67],v[128:131],v[64:67]\nds_read_b64_tr_b16 v[144:145],v225 offset:16384\nv_add_f32 v45,0x358637bd,v45\n"
"v_mfma_f32_16x16x32_bf16 v[64:67],a[68:71],v[132:135],v[64:67]\nds_read_b64_tr_b16 v[146:147],v225 offset:16896\nv_add_f32 v44,v44,v12\nv_mfma_f32_16x16x32_bf16 v[64:67],a[72:75],v[136:139],v[64:67]\n"
"ds_read_b64_tr_b16 v[148:149],v225 offset:20480\nv_mfma_f32_16x16x32_bf16 v[64:67],a[76:79],v[140:143],v[64:67]\nbuffer_load_dwordx4 v228,s[36:39],s60 offen offset:0 lds\n"
"v_mfma_f32_16x16x32_bf16 v[68:71],a[92:95],v[140:143],v[68:71]\nds_read_b64_tr_b16 v[150:151],v225 offset:20992\nv_mfma_f32_16x16x32_bf16 v[68:71],a[88:91],v[136:139],v[68:71]\nds_read_b64_tr_b16 v[152:153],v225 offset:24576\n"
"v_mfma_f32_16x16x32_bf16 v[68:71],a[84:87],v[132:135],v[68:71]\nds_read_b64_tr_b16 v[154:155],v225 offset:25088\nv_mfma_f32_16x16x32_bf16 v[68:71],a[80:83],v[128:131],v[68:71]\nv_exp_f32_e64 v14,v14 clamp\n"
"v_mfma_f32_16x16x32_bf16 v[72:75],a[96:99],v[128:131],v[72:75]\nds_read_b64_tr_b16 v[156:157],v225 offset:28672\nv_add_f32 v46,0x358637bd,v46\nv_mfma_f32_16x16x32_bf16 v[72:75],a[100:103],v[132:135],v[72:75]\n"
"ds_read_b64_tr_b16 v[158:159],v225 offset:29184\nv_add_f32 v45,v45,v13\nv_mfma_f32_16x16x32_bf16 v[72:75],a[104:107],v[136:139],v[72:75]\nv_exp_f32_e64 v15,v15 clamp\n"
"v_mfma_f32_16x16x32_bf16 v[72:75],a[108:111],v[140:143],v[72:75]\nv_add_f32 v47,0x358637bd,v47\nv_add_f32 v46,v46,v14\nv_mfma_f32_16x16x32_bf16 v[76:79],a[124:127],v[140:143],v[76:79]\nv_exp_f32_e64 v16,v16 clamp\n"
"v_mfma_f32_16x16x32_bf16 v[76:79],a[120:123],v[136:139],v[76:79]\nv_add_f32 v48,0x358637bd,v48\nv_add_f32 v47,v47,v15\nv_mfma_f32_16x16x32_bf16 v[76:79],a[116:119],v[132:135],v[76:79]\ns_waitcnt lgkmcnt(0)\n"
"v_exp_f32_e64 v17,v17 clamp\nv_mfma_f32_16x16x32_bf16 v[76:79],a[112:115],v[128:131],v[76:79]\nv_add_f32 v49,0x358637bd,v49\nv_add_f32 v48,v48,v16\nv_mfma_f32_16x16x32_bf16 v[92:95],a[112:115],v[144:147],v[92:95]\n"
"ds_read_b64_tr_b16 v[128:129],v226 offset:16384\nv_mfma_f32_16x16x32_bf16 v[92:95],a[116:119],v[148:151],v[92:95]\nds_read_b64_tr_b16 v[130:131],v226 offset:16896\nv_mfma_f32_16x16x32_bf16 v[92:95],a[120:123],v[152:155],v[92:95]\n"
"ds_read_b64_tr_b16 v[132:133],v226 offset:20480\nv_mfma_f32_16x16x32_bf16 v[92:95],a[124:127],v[156:159],v[92:95]\nbuffer_load_dwordx4 v229,s[36:39],s60 offen offset:1024 lds\n"
"v_mfma_f32_16x16x32_bf16 v[88:91],a[108:111],v[156:159],v[88:91]\nds_read_b64_tr_b16 v[134:135],v226 offset:20992\nv_mfma_f32_16x16x32_bf16 v[88:91],a[104:107],v[152:155],v[88:91]\nds_read_b64_tr_b16 v[136:137],v226 offset:24576\n"
"v_mfma_f32_16x16x32_bf16 v[88:91],a[100:103],v[148:151],v[88:91]\nds_read_b64_tr_b16 v[138:139],v226 offset:25088\nv_mfma_f32_16x16x32_bf16 v[88:91],a[96:99],v[144:147],v[88:91]\nv_exp_f32_e64 v18,v18 clamp\n"
"v_mfma_f32_16x16x32_bf16 v[84:87],a[80:83],v[144:147],v[84:87]\nds_read_b64_tr_b16 v[140:141],v226 offset:28672\nv_add_f32 v50,0x358637bd,v50\nv_mfma_f32_16x16x32_bf16 v[84:87],a[84:87],v[148:151],v[84:87]\n"
"ds_read_b64_tr_b16 v[142:143],v226 offset:29184\nv_add_f32 v49,v49,v17\nv_mfma_f32_16x16x32_bf16 v[84:87],a[88:91],v[152:155],v[84:87]\nv_exp_f32_e64 v19,v19 clamp\nv_mfma_f32_16x16x32_bf16 v[84:87],a[92:95],v[156:159],v[84:87]\n"
"v_add_f32 v51,0x358637bd,v51\nv_add_f32 v50,v50,v18\nv_mfma_f32_16x16x32_bf16 v[80:83],a[76:79],v[156:159],v[80:83]\nv_exp_f32_e64 v20,v20 clamp\nv_mfma_f32_16x16x32_bf16 v[80:83],a[72:75],v[152:155],v[80:83]\n"
"v_add_f32 v52,0x358637bd,v52\nv_add_f32 v51,v51,v19\nv_mfma_f32_16x16x32_bf16 v[80:83],a[68:71],v[148:151],v[80:83]\ns_waitcnt lgkmcnt(0)\nv_exp_f32_e64 v21,v21 clamp\n"
"v_mfma_f32_16x16x32_bf16 v[80:83],a[64:67],v[144:147],v[80:83]\nv_add_f32 v53,0x358637bd,v53\nv_add_f32 v52,v52,v20\nv_mfma_f32_16x16x32_bf16 v[96:99],a[64:67],v[128:131],v[96:99]\nds_read_b64_tr_b16 v[144:145],v227 offset:16384\n"
"v_mfma_f32_16x16x32_bf16 v[96:99],a[68:71],v[132:135],v[96:99]\nds_read_b64_tr_b16 v[146:147],v227 offset:16896\nv_mfma_f32_16x16x32_bf16 v[96:99],a[72:75],v[136:139],v[96:99]\nds_read_b64_tr_b16 v[148:149],v227 offset:20480\n"
"v_mfma_f32_16x16x32_bf16 v[96:99],a[76:79],v[140:143],v[96:99]\nbuffer_load_dwordx4 v230,s[36:39],s60 offen offset:2048 lds\nv_mfma_f32_16x16x32_bf16 v[100:103],a[92:95],v[140:143],v[100:103]\n"
"ds_read_b64_tr_b16 v[150:151],v227 offset:20992\nv_mfma_f32_16x16x32_bf16 v[100:103],a[88:91],v[136:139],v[100:103]\nds_read_b64_tr_b16 v[152:153],v227 offset:24576\n"
"v_mfma_f32_16x16x32_bf16 v[100:103],a[84:87],v[132:135],v[100:103]\nds_read_b64_tr_b16 v[154:155],v227 offset:25088\nv_mfma_f32_16x16x32_bf16 v[100:103],a[80:83],v[128:131],v[100:103]\nv_exp_f32_e64 v22,v22 clamp\n"
"v_mfma_f32_16x16x32_bf16 v[104:107],a[96:99],v[128:131],v[104:107]\nds_read_b64_tr_b16 v[156:157],v227 offset:28672\nv_add_f32 v54,0x358637bd,v54\nv_mfma_f32_16x16x32_bf16 v[104:107],a[100:103],v[132:135],v[104:107]\n"
"ds_read_b64_tr_b16 v[158:159],v227 offset:29184\nv_add_f32 v53,v53,v21\nv_mfma_f32_16x16x32_bf16 v[104:107],a[104:107],v[136:139],v[104:107]\nv_exp_f32_e64 v23,v23 clamp\n"
"v_mfma_f32_16x16x32_bf16 v[104:107],a[108:111],v[140:143],v[104:107]\nv_add_f32 v55,0x358637bd,v55\nv_add_f32 v54,v54,v22\nv_mfma_f32_16x16x32_bf16 v[108:111],a[124:127],v[140:143],v[108:111]\nv_exp_f32_e64 v24,v24 clamp\n"
"v_mfma_f32_16x16x32_bf16 v[108:111],a[120:123],v[136:139],v[108:111]\nv_add_f32 v56,0x358637bd,v56\nv_add_f32 v55,v55,v23\nv_mfma_f32_16x16x32_bf16 v[108:111],a[116:119],v[132:135],v[108:111]\ns_waitcnt vmcnt(15) lgkmcnt(0)\n"
"v_exp_f32_e64 v25,v25 clamp\nv_mfma_f32_16x16x32_bf16 v[108:111],a[112:115],v[128:131],v[108:111]\nv_add_f32 v57,0x358637bd,v57\nv_add_f32 v56,v56,v24\nv_mfma_f32_16x16x32_bf16 v[124:127],a[112:115],v[144:147],v[124:127]\n"
"s_barrier\nv_exp_f32_e64 v26,v26 clamp\nv_mfma_f32_16x16x32_bf16 v[124:127],a[116:119],v[148:151],v[124:127]\nds_read_b64_tr_b16 v[128:129],v224 offset:32768\nv_add_f32 v58,0x358637bd,v58\n"
"v_mfma_f32_16x16x32_bf16 v[124:127],a[120:123],v[152:155],v[124:127]\nds_read_b64_tr_b16 v[130:131],v224 offset:33280\nv_add_f32 v57,v57,v25\nv_mfma_f32_16x16x32_bf16 v[124:127],a[124:127],v[156:159],v[124:127]\n"
"buffer_load_dwordx4 v231,s[36:39],s60 offen offset:3072 lds\nv_mfma_f32_16x16x32_bf16 v[120:123],a[108:111],v[156:159],v[120:123]\nds_read_b64_tr_b16 v[132:133],v224 offset:36864\ns_add_u32 s60,s51,s75\n"
"v_mfma_f32_16x16x32_bf16 v[120:123],a[104:107],v[152:155],v[120:123]\nds_read_b64_tr_b16 v[134:135],v224 offset:37376\ns_add_u32 m0,s61,49152\nv_mfma_f32_16x16x32_bf16 v[120:123],a[100:103],v[148:151],v[120:123]\n"
"ds_read_b64_tr_b16 v[136:137],v224 offset:40960\nv_mfma_f32_16x16x32_bf16 v[120:123],a[96:99],v[144:147],v[120:123]\nv_exp_f32_e64 v27,v27 clamp\nv_mfma_f32_16x16x32_bf16 v[116:119],a[80:83],v[144:147],v[116:119]\n"
"ds_read_b64_tr_b16 v[138:139],v224 offset:41472\nv_add_f32 v59,0x358637bd,v59\nv_mfma_f32_16x16x32_bf16 v[116:119],a[84:87],v[148:151],v[116:119]\nds_read_b64_tr_b16 v[140:141],v224 offset:45056\nv_add_f32 v58,v58,v26\n"
"v_mfma_f32_16x16x32_bf16 v[116:119],a[88:91],v[152:155],v[116:119]\nds_read_b64_tr_b16 v[142:143],v224 offset:45568\nv_mfma_f32_16x16x32_bf16 v[116:119],a[92:95],v[156:159],v[116:119]\nv_exp_f32_e64 v28,v28 clamp\n"
"v_mfma_f32_16x16x32_bf16 v[112:115],a[76:79],v[156:159],v[112:115]\nv_add_f32 v60,0x358637bd,v60\nv_add_f32 v59,v59,v27\nv_mfma_f32_16x16x32_bf16 v[112:115],a[72:75],v[152:155],v[112:115]\nv_exp_f32_e64 v29,v29 clamp\n"
"v_mfma_f32_16x16x32_bf16 v[112:115],a[68:71],v[148:151],v[112:115]\ns_waitcnt lgkmcnt(0)\nv_add_f32 v61,0x358637bd,v61\nv_add_f32 v60,v60,v28\nv_mfma_f32_16x16x32_bf16 v[112:115],a[64:67],v[144:147],v[112:115]\n"
"v_exp_f32_e64 v30,v30 clamp\nv_mfma_f32_16x16x32_bf16 v[64:67],a[128:131],v[128:131],v[64:67]\nds_read_b64_tr_b16 v[144:145],v225 offset:32768\nv_add_f32 v62,0x358637bd,v62\n"
"v_mfma_f32_16x16x32_bf16 v[64:67],a[132:135],v[132:135],v[64:67]\nds_read_b64_tr_b16 v[146:147],v225 offset:33280\nv_add_f32 v61,v61,v29\nv_mfma_f32_16x16x32_bf16 v[64:67],a[136:139],v[136:139],v[64:67]\n"
"ds_read_b64_tr_b16 v[148:149],v225 offset:36864\nv_mfma_f32_16x16x32_bf16 v[64:67],a[140:143],v[140:143],v[64:67]\nbuffer_load_dwordx4 v228,s[36:39],s60 offen offset:0 lds\n"
"v_mfma_f32_16x16x32_bf16 v[68:71],a[156:159],v[140:143],v[68:71]\nds_read_b64_tr_b16 v[150:151],v225 offset:37376\nv_mfma_f32_16x16x32_bf16 v[68:71],a[152:155],v[136:139],v[68:71]\nds_read_b64_tr_b16 v[152:153],v225 offset:40960\n"
"v_mfma_f32_16x16x32_bf16 v[68:71],a[148:151],v[132:135],v[68:71]\nds_read_b64_tr_b16 v[154:155],v225 offset:41472\nv_mfma_f32_16x16x32_bf16 v[68:71],a[144:147],v[128:131],v[68:71]\nv_exp_f32_e64 v31,v31 clamp\n"
"v_mfma_f32_16x16x32_bf16 v[72:75],a[160:163],v[128:131],v[72:75]\nds_read_b64_tr_b16 v[156:157],v225 offset:45056\nv_add_f32 v63,0x358637bd,v63\nv_mfma_f32_16x16x32_bf16 v[72:75],a[164:167],v[132:135],v[72:75]\n"
"ds_read_b64_tr_b16 v[158:159],v225 offset:45568\nv_add_f32 v62,v62,v30\nv_mfma_f32_16x16x32_bf16 v[72:75],a[168:171],v[136:139],v[72:75]\nv_add_f32 v63,v63,v31\nv_cvt_pk_bf16_f32 v160,v32,v33\n"
"v_mfma_f32_16x16x32_bf16 v[72:75],a[172:175],v[140:143],v[72:75]\nv_cvt_pk_bf16_f32 v161,v34,v35\nv_cvt_pk_bf16_f32 v162,v36,v37\nv_mfma_f32_16x16x32_bf16 v[76:79],a[188:191],v[140:143],v[76:79]\nv_cvt_pk_bf16_f32 v163,v38,v39\n"
"v_cvt_pk_bf16_f32 v164,v40,v41\nv_mfma_f32_16x16x32_bf16 v[76:79],a[184:187],v[136:139],v[76:79]\nv_cvt_pk_bf16_f32 v165,v42,v43\nv_cvt_pk_bf16_f32 v166,v44,v45\nv_mfma_f32_16x16x32_bf16 v[76:79],a[180:183],v[132:135],v[76:79]\n"
"s_waitcnt lgkmcnt(0)\nv_cvt_pk_bf16_f32 v167,v46,v47\nv_cvt_pk_bf16_f32 v168,v48,v49\nv_mfma_f32_16x16x32_bf16 v[76:79],a[176:179],v[128:131],v[76:79]\nv_cvt_pk_bf16_f32 v169,v50,v51\nv_cvt_pk_bf16_f32 v170,v52,v53\n"
"v_mfma_f32_16x16x32_bf16 v[92:95],a[176:179],v[144:147],v[92:95]\nds_read_b64_tr_b16 v[128:129],v226 offset:32768\nv_cvt_pk_bf16_f32 v171,v54,v55\nv_mfma_f32_16x16x32_bf16 v[92:95],a[180:183],v[148:151],v[92:95]\n"
"ds_read_b64_tr_b16 v[130:131],v226 offset:33280\nv_cvt_pk_bf16_f32 v172,v56,v57\nv_mfma_f32_16x16x32_bf16 v[92:95],a[184:187],v[152:155],v[92:95]\nds_read_b64_tr_b16 v[132:133],v226 offset:36864\nv_cvt_pk_bf16_f32 v173,v58,v59\n"
"v_mfma_f32_16x16x32_bf16 v[92:95],a[188:191],v[156:159],v[92:95]\nbuffer_load_dwordx4 v229,s[36:39],s60 offen offset:1024 lds\nv_mfma_f32_16x16x32_bf16 v[88:91],a[172:175],v[156:159],v[88:91]\n"
"ds_read_b64_tr_b16 v[134:135],v226 offset:37376\nv_cvt_pk_bf16_f32 v174,v60,v61\nv_mfma_f32_16x16x32_bf16 v[88:91],a[168:171],v[152:155],v[88:91]\nds_read_b64_tr_b16 v[136:137],v226 offset:40960\nv_cvt_pk_bf16_f32 v175,v62,v63\n"
"v_mfma_f32_16x16x32_bf16 v[88:91],a[164:167],v[148:151],v[88:91]\nds_read_b64_tr_b16 v[138:139],v226 offset:41472\nv_cmp_eq_u32 vcc,s74,v241\nv_mfma_f32_16x16x32_bf16 v[88:91],a[160:163],v[144:147],v[88:91]\ns_add_u32 s74,s74,1\n"
"v_nop\nv_mfma_f32_16x16x32_bf16 v[84:87],a[144:147],v[144:147],v[84:87]\nds_read_b64_tr_b16 v[140:141],v226 offset:45056\nv_cndmask_b32 v244,0,v243,vcc\nv_mfma_f32_16x16x32_bf16 v[84:87],a[148:151],v[148:151],v[84:87]\n"
"ds_read_b64_tr_b16 v[142:143],v226 offset:45568\nv_mov_b32 v245,v244\nv_mfma_f32_16x16x32_bf16 v[84:87],a[152:155],v[152:155],v[84:87]\nv_mov_b32 v246,v244\nv_mov_b32 v247,v244\n"
"v_mfma_f32_16x16x32_bf16 v[84:87],a[156:159],v[156:159],v[84:87]\nv_mfma_f32_16x16x32_bf16 v[0:3],v[160:163],v[176:179],0\nv_mfma_f32_16x16x32_bf16 v[0:3],v[164:167],v[180:183],v[0:3]\n"
"v_mfma_f32_16x16x32_bf16 v[80:83],a[140:143],v[156:159],v[80:83]\nv_mfma_f32_16x16x32_bf16 v[248:251],v[160:163],v[244:247],v[248:251]\nv_mfma_f32_16x16x32_bf16 v[248:251],v[164:167],v[244:247],v[248:251]\n"
"v_mfma_f32_16x16x32_bf16 v[80:83],a[136:139],v[152:155],v[80:83]\nv_cmp_eq_u32 vcc,s74,v241\ns_add_u32 s74,s74,1\nv_mfma_f32_16x16x32_bf16 v[80:83],a[132:135],v[148:151],v[80:83]\ns_waitcnt lgkmcnt(0)\nv_nop\n"
"v_cndmask_b32 v244,0,v243,vcc\nv_mfma_f32_16x16x32_bf16 v[80:83],a[128:131],v[144:147],v[80:83]\nv_mov_b32 v245,v244\nv_mov_b32 v246,v244\nv_mfma_f32_16x16x32_bf16 v[96:99],a[128:131],v[128:131],v[96:99]\n"
"ds_read_b64_tr_b16 v[144:145],v227 offset:32768\nv_mov_b32 v247,v244\nv_mfma_f32_16x16x32_bf16 v[96:99],a[132:135],v[132:135],v[96:99]\nds_read_b64_tr_b16 v[146:147],v227 offset:33280\n"
"v_mfma_f32_16x16x32_bf16 v[4:7],v[168:171],v[184:187],0\nv_mfma_f32_16x16x32_bf16 v[96:99],a[136:139],v[136:139],v[96:99]\nds_read_b64_tr_b16 v[148:149],v227 offset:36864\n"
"v_mfma_f32_16x16x32_bf16 v[4:7],v[172:175],v[188:191],v[4:7]\nv_mfma_f32_16x16x32_bf16 v[96:99],a[140:143],v[140:143],v[96:99]\nbuffer_load_dwordx4 v230,s[36:39],s60 offen offset:2048 lds\n"
"v_mfma_f32_16x16x32_bf16 v[100:103],a[156:159],v[140:143],v[100:103]\nds_read_b64_tr_b16 v[150:151],v227 offset:37376\nv_mfma_f32_16x16x32_bf16 v[248:251],v[168:171],v[244:247],v[248:251]\n"
"v_mfma_f32_16x16x32_bf16 v[100:103],a[152:155],v[136:139],v[100:103]\nds_read_b64_tr_b16 v[152:153],v227 offset:40960\nv_mfma_f32_16x16x32_bf16 v[248:251],v[172:175],v[244:247],v[248:251]\n"
"v_mfma_f32_16x16x32_bf16 v[100:103],a[148:151],v[132:135],v[100:103]\nds_read_b64_tr_b16 v[154:155],v227 offset:41472\nv_nop\nv_mfma_f32_16x16x32_bf16 v[100:103],a[144:147],v[128:131],v[100:103]\nv_nop\nv_nop\n"
"v_mfma_f32_16x16x32_bf16 v[104:107],a[160:163],v[128:131],v[104:107]\nds_read_b64_tr_b16 v[156:157],v227 offset:45056\nv_nop\nv_mfma_f32_16x16x32_bf16 v[104:107],a[164:167],v[132:135],v[104:107]\n"
"ds_read_b64_tr_b16 v[158:159],v227 offset:45568\nv_nop\nv_mfma_f32_16x16x32_bf16 v[104:107],a[168:171],v[136:139],v[104:107]\nv_nop\nv_mfma_f32_16x16x32_bf16 v[104:107],a[172:175],v[140:143],v[104:107]\nds_write_b128 v242,v[0:3]\n"
"v_mfma_f32_16x16x32_bf16 v[108:111],a[188:191],v[140:143],v[108:111]\nds_write_b128 v242,v[4:7] offset:4096\nv_add_u32 v242,0x2000,v242\nv_mfma_f32_16x16x32_bf16 v[108:111],a[184:187],v[136:139],v[108:111]\n"
"v_mfma_f32_16x16x32_bf16 v[108:111],a[180:183],v[132:135],v[108:111]\ns_waitcnt vmcnt(15) lgkmcnt(2)\nv_mfma_f32_16x16x32_bf16 v[108:111],a[176:179],v[128:131],v[108:111]\n"
"v_mfma_f32_16x16x32_bf16 v[124:127],a[176:179],v[144:147],v[124:127]\ns_barrier\nv_mfma_f32_16x16x32_bf16 v[124:127],a[180:183],v[148:151],v[124:127]\nds_read_b64_tr_b16 v[128:129],v224 offset:49152\n"
"v_mfma_f32_16x16x32_bf16 v[124:127],a[184:187],v[152:155],v[124:127]\nds_read_b64_tr_b16 v[130:131],v224 offset:49664\nv_mfma_f32_16x16x32_bf16 v[124:127],a[188:191],v[156:159],v[124:127]\n"
"buffer_load_dwordx4 v231,s[36:39],s60 offen offset:3072 lds\nv_mfma_f32_16x16x32_bf16 v[120:123],a[172:175],v[156:159],v[120:123]\nds_read_b64_tr_b16 v[132:133],v224 offset:53248\ns_xor_b32 s61,s61,0x10000\n"
"v_mfma_f32_16x16x32_bf16 v[120:123],a[168:171],v[152:155],v[120:123]\nds_read_b64_tr_b16 v[134:135],v224 offset:53760\nv_mfma_f32_16x16x32_bf16 v[120:123],a[164:167],v[148:151],v[120:123]\n"
"ds_read_b64_tr_b16 v[136:137],v224 offset:57344\nv_mfma_f32_16x16x32_bf16 v[120:123],a[160:163],v[144:147],v[120:123]\nv_mfma_f32_16x16x32_bf16 v[116:119],a[144:147],v[144:147],v[116:119]\n"
"ds_read_b64_tr_b16 v[138:139],v224 offset:57856\nv_mfma_f32_16x16x32_bf16 v[116:119],a[148:151],v[148:151],v[116:119]\nds_read_b64_tr_b16 v[140:141],v224 offset:61440\n"
"v_mfma_f32_16x16x32_bf16 v[116:119],a[152:155],v[152:155],v[116:119]\nds_read_b64_tr_b16 v[142:143],v224 offset:61952\nv_mfma_f32_16x16x32_bf16 v[116:119],a[156:159],v[156:159],v[116:119]\n"
"v_mfma_f32_16x16x32_bf16 v[112:115],a[140:143],v[156:159],v[112:115]\nv_mfma_f32_16x16x32_bf16 v[112:115],a[136:139],v[152:155],v[112:115]\nv_mfma_f32_16x16x32_bf16 v[112:115],a[132:135],v[148:151],v[112:115]\n"
"s_waitcnt lgkmcnt(0)\nv_mfma_f32_16x16x32_bf16 v[112:115],a[128:131],v[144:147],v[112:115]\ns_sub_u32 s70,s70,1\ns_cmp_lg_u32 s70,0\ns_cbranch_scc1 LLl_%=\nv_lshrrev_b32 v229,8,v240\nv_bfe_u32 v230,v240,4,4\n"
"v_xor_b32 v230,v230,v229\nv_lshlrev_b32 v230,4,v230\nv_lshl_add_u32 v232,v229,10,v230\nv_xor_b32 v233,64,v232\nv_add_u32 v233,3072,v233\nv_xor_b32 v234,128,v232\nv_add_u32 v234,6144,v234\nv_xor_b32 v235,192,v232\n"
"v_add_u32 v235,9216,v235\ns_mov_b64 s[40:41],s[30:31]\ns_mov_b32 s42,0x200000\ns_and_b32 s41,s41,0xffff\ns_mov_b32 s82,0\ns_lshl_b32 s56,s16,14\ns_add_u32 s57,s56,0x4000\ns_lshl_b32 s58,s17,14\ns_add_u32 s59,s58,0x4000\n"
"s_lshl_b32 s62,s5,12\ns_add_u32 s62,s62,0x10000\nv_mfma_f32_16x16x32_bf16 v[64:67],a[192:195],v[128:131],v[64:67]\nds_read_b64_tr_b16 v[144:145],v225 offset:49152\ns_add_u32 s60,s82,s56\n"
"v_mfma_f32_16x16x32_bf16 v[64:67],a[196:199],v[132:135],v[64:67]\nds_read_b64_tr_b16 v[146:147],v225 offset:49664\ns_add_u32 m0,s62,0\nv_mfma_f32_16x16x32_bf16 v[64:67],a[200:203],v[136:139],v[64:67]\n"
"ds_read_b64_tr_b16 v[148:149],v225 offset:53248\nv_mfma_f32_16x16x32_bf16 v[64:67],a[204:207],v[140:143],v[64:67]\nbuffer_load_dwordx4 v232,s[40:43],s60 offen offset:0 lds\n"
"v_mfma_f32_16x16x32_bf16 v[68:71],a[220:223],v[140:143],v[68:71]\nds_read_b64_tr_b16 v[150:151],v225 offset:53760\nv_mfma_f32_16x16x32_bf16 v[68:71],a[216:219],v[136:139],v[68:71]\nds_read_b64_tr_b16 v[152:153],v225 offset:57344\n"
"v_mfma_f32_16x16x32_bf16 v[68:71],a[212:215],v[132:135],v[68:71]\nds_read_b64_tr_b16 v[154:155],v225 offset:57856\nv_mfma_f32_16x16x32_bf16 v[68:71],a[208:211],v[128:131],v[68:71]\n"
"buffer_load_dwordx4 v233,s[40:43],s60 offen offset:1024 lds\nv_mfma_f32_16x16x32_bf16 v[72:75],a[224:227],v[128:131],v[72:75]\nds_read_b64_tr_b16 v[156:157],v225 offset:61440\n"
"v_mfma_f32_16x16x32_bf16 v[72:75],a[228:231],v[132:135],v[72:75]\nds_read_b64_tr_b16 v[158:159],v225 offset:61952\nv_mfma_f32_16x16x32_bf16 v[72:75],a[232:235],v[136:139],v[72:75]\n"
"v_mfma_f32_16x16x32_bf16 v[72:75],a[236:239],v[140:143],v[72:75]\nbuffer_load_dwordx4 v234,s[40:43],s60 offen offset:2048 lds\nv_mfma_f32_16x16x32_bf16 v[76:79],a[252:255],v[140:143],v[76:79]\n"
"v_mfma_f32_16x16x32_bf16 v[76:79],a[248:251],v[136:139],v[76:79]\nv_mfma_f32_16x16x32_bf16 v[76:79],a[244:247],v[132:135],v[76:79]\ns_waitcnt lgkmcnt(0)\nv_mfma_f32_16x16x32_bf16 v[76:79],a[240:243],v[128:131],v[76:79]\n"
"buffer_load_dwordx4 v235,s[40:43],s60 offen offset:3072 lds\nv_mfma_f32_16x16x32_bf16 v[92:95],a[240:243],v[144:147],v[92:95]\nds_read_b64_tr_b16 v[128:129],v226 offset:49152\ns_add_u32 s60,s82,s57\n"
"v_mfma_f32_16x16x32_bf16 v[92:95],a[244:247],v[148:151],v[92:95]\nds_read_b64_tr_b16 v[130:131],v226 offset:49664\ns_add_u32 m0,s62,16384\nv_mfma_f32_16x16x32_bf16 v[92:95],a[248:251],v[152:155],v[92:95]\n"
"ds_read_b64_tr_b16 v[132:133],v226 offset:53248\nv_mfma_f32_16x16x32_bf16 v[92:95],a[252:255],v[156:159],v[92:95]\nbuffer_load_dwordx4 v232,s[40:43],s60 offen offset:0 lds\n"
"v_mfma_f32_16x16x32_bf16 v[88:91],a[236:239],v[156:159],v[88:91]\nds_read_b64_tr_b16 v[134:135],v226 offset:53760\nv_max_f32 v32,0,v64\nv_mfma_f32_16x16x32_bf16 v[88:91],a[232:235],v[152:155],v[88:91]\n"
"ds_read_b64_tr_b16 v[136:137],v226 offset:57344\nv_mul_f32 v0,0x3fb8aa3b,v64\nv_mfma_f32_16x16x32_bf16 v[88:91],a[228:231],v[148:151],v[88:91]\nds_read_b64_tr_b16 v[138:139],v226 offset:57856\nv_max_f32 v33,0,v65\n"
"v_mfma_f32_16x16x32_bf16 v[88:91],a[224:227],v[144:147],v[88:91]\nbuffer_load_dwordx4 v233,s[40:43],s60 offen offset:1024 lds\nv_mfma_f32_16x16x32_bf16 v[84:87],a[208:211],v[144:147],v[84:87]\n"
"ds_read_b64_tr_b16 v[140:141],v226 offset:61440\nv_mul_f32 v1,0x3fb8aa3b,v65\nv_mfma_f32_16x16x32_bf16 v[84:87],a[212:215],v[148:151],v[84:87]\nds_read_b64_tr_b16 v[142:143],v226 offset:61952\nv_max_f32 v34,0,v66\n"
"v_mfma_f32_16x16x32_bf16 v[84:87],a[216:219],v[152:155],v[84:87]\nv_mul_f32 v2,0x3fb8aa3b,v66\nv_max_f32 v35,0,v67\nv_mfma_f32_16x16x32_bf16 v[84:87],a[220:223],v[156:159],v[84:87]\n"
"buffer_load_dwordx4 v234,s[40:43],s60 offen offset:2048 lds\nv_mfma_f32_16x16x32_bf16 v[80:83],a[204:207],v[156:159],v[80:83]\nv_mul_f32 v3,0x3fb8aa3b,v67\nv_max_f32 v36,0,v68\n"
"v_mfma_f32_16x16x32_bf16 v[80:83],a[200:203],v[152:155],v[80:83]\nv_mul_f32 v4,0x3fb8aa3b,v68\nv_max_f32 v37,0,v69\nv_mfma_f32_16x16x32_bf16 v[80:83],a[196:199],v[148:151],v[80:83]\ns_waitcnt lgkmcnt(0)\n"
"v_mul_f32 v5,0x3fb8aa3b,v69\nv_max_f32 v38,0,v70\nv_mfma_f32_16x16x32_bf16 v[80:83],a[192:195],v[144:147],v[80:83]\nbuffer_load_dwordx4 v235,s[40:43],s60 offen offset:3072 lds\n"
"v_mfma_f32_16x16x32_bf16 v[96:99],a[192:195],v[128:131],v[96:99]\nds_read_b64_tr_b16 v[144:145],v227 offset:49152\ns_add_u32 s60,s82,s58\nv_mfma_f32_16x16x32_bf16 v[96:99],a[196:199],v[132:135],v[96:99]\n"
"ds_read_b64_tr_b16 v[146:147],v227 offset:49664\ns_add_u32 m0,s62,32768\nv_mfma_f32_16x16x32_bf16 v[96:99],a[200:203],v[136:139],v[96:99]\nds_read_b64_tr_b16 v[148:149],v227 offset:53248\nv_mul_f32 v6,0x3fb8aa3b,v70\n"
"v_mfma_f32_16x16x32_bf16 v[96:99],a[204:207],v[140:143],v[96:99]\nbuffer_load_dwordx4 v232,s[40:43],s60 offen offset:0 lds\nv_mfma_f32_16x16x32_bf16 v[100:103],a[220:223],v[140:143],v[100:103]\n"
"ds_read_b64_tr_b16 v[150:151],v227 offset:53760\nv_max_f32 v39,0,v71\nv_mfma_f32_16x16x32_bf16 v[100:103],a[216:219],v[136:139],v[100:103]\nds_read_b64_tr_b16 v[152:153],v227 offset:57344\nv_mul_f32 v7,0x3fb8aa3b,v71\n"
"v_mfma_f32_16x16x32_bf16 v[100:103],a[212:215],v[132:135],v[100:103]\nds_read_b64_tr_b16 v[154:155],v227 offset:57856\nv_max_f32 v40,0,v72\nv_mfma_f32_16x16x32_bf16 v[100:103],a[208:211],v[128:131],v[100:103]\n"
"buffer_load_dwordx4 v233,s[40:43],s60 offen offset:1024 lds\nv_mfma_f32_16x16x32_bf16 v[104:107],a[224:227],v[128:131],v[104:107]\nds_read_b64_tr_b16 v[156:157],v227 offset:61440\nv_mul_f32 v8,0x3fb8aa3b,v72\n"
"v_mfma_f32_16x16x32_bf16 v[104:107],a[228:231],v[132:135],v[104:107]\nds_read_b64_tr_b16 v[158:159],v227 offset:61952\nv_max_f32 v41,0,v73\nv_mfma_f32_16x16x32_bf16 v[104:107],a[232:235],v[136:139],v[104:107]\n"
"v_mul_f32 v9,0x3fb8aa3b,v73\nv_max_f32 v42,0,v74\nv_mfma_f32_16x16x32_bf16 v[104:107],a[236:239],v[140:143],v[104:107]\nbuffer_load_dwordx4 v234,s[40:43],s60 offen offset:2048 lds\n"
"v_mfma_f32_16x16x32_bf16 v[108:111],a[252:255],v[140:143],v[108:111]\nv_mul_f32 v10,0x3fb8aa3b,v74\nv_max_f32 v43,0,v75\nv_mfma_f32_16x16x32_bf16 v[108:111],a[248:251],v[136:139],v[108:111]\nv_mul_f32 v11,0x3fb8aa3b,v75\n"
"v_max_f32 v44,0,v76\nv_mfma_f32_16x16x32_bf16 v[108:111],a[244:247],v[132:135],v[108:111]\ns_waitcnt lgkmcnt(0)\nv_mul_f32 v12,0x3fb8aa3b,v76\nv_max_f32 v45,0,v77\n"
"v_mfma_f32_16x16x32_bf16 v[108:111],a[240:243],v[128:131],v[108:111]\nbuffer_load_dwordx4 v235,s[40:43],s60 offen offset:3072 lds\nv_mfma_f32_16x16x32_bf16 v[124:127],a[240:243],v[144:147],v[124:127]\n"
"v_mul_f32 v13,0x3fb8aa3b,v77\nv_max_f32 v46,0,v78\nv_mfma_f32_16x16x32_bf16 v[124:127],a[244:247],v[148:151],v[124:127]\nv_mul_f32 v14,0x3fb8aa3b,v78\nv_max_f32 v47,0,v79\n"
"v_mfma_f32_16x16x32_bf16 v[124:127],a[248:251],v[152:155],v[124:127]\nv_mul_f32 v15,0x3fb8aa3b,v79\nv_max_f32 v63,0,v95\nv_mfma_f32_16x16x32_bf16 v[124:127],a[252:255],v[156:159],v[124:127]\nv_mul_f32 v31,0x3fb8aa3b,v95\n"
"v_max_f32 v62,0,v94\nv_mfma_f32_16x16x32_bf16 v[120:123],a[236:239],v[156:159],v[120:123]\nv_mul_f32 v30,0x3fb8aa3b,v94\nv_max_f32 v61,0,v93\nv_mfma_f32_16x16x32_bf16 v[120:123],a[232:235],v[152:155],v[120:123]\n"
"v_mul_f32 v29,0x3fb8aa3b,v93\nv_max_f32 v60,0,v92\nv_mfma_f32_16x16x32_bf16 v[120:123],a[228:231],v[148:151],v[120:123]\nv_mul_f32 v28,0x3fb8aa3b,v92\nv_max_f32 v59,0,v91\n"
"v_mfma_f32_16x16x32_bf16 v[120:123],a[224:227],v[144:147],v[120:123]\nv_mul_f32 v27,0x3fb8aa3b,v91\nv_max_f32 v58,0,v90\nv_mfma_f32_16x16x32_bf16 v[116:119],a[208:211],v[144:147],v[116:119]\nv_mul_f32 v26,0x3fb8aa3b,v90\n"
"v_max_f32 v57,0,v89\nv_mfma_f32_16x16x32_bf16 v[116:119],a[212:215],v[148:151],v[116:119]\nv_mul_f32 v25,0x3fb8aa3b,v89\nv_max_f32 v56,0,v88\nv_mfma_f32_16x16x32_bf16 v[116:119],a[216:219],v[152:155],v[116:119]\n"
"v_mul_f32 v24,0x3fb8aa3b,v88\nv_max_f32 v55,0,v87\nv_mfma_f32_16x16x32_bf16 v[116:119],a[220:223],v[156:159],v[116:119]\nv_mul_f32 v23,0x3fb8aa3b,v87\nv_max_f32 v54,0,v86\n"
"v_mfma_f32_16x16x32_bf16 v[112:115],a[204:207],v[156:159],v[112:115]\nv_mul_f32 v22,0x3fb8aa3b,v86\nv_max_f32 v53,0,v85\nv_mfma_f32_16x16x32_bf16 v[112:115],a[200:203],v[152:155],v[112:115]\nv_mul_f32 v21,0x3fb8aa3b,v85\n"
"v_max_f32 v52,0,v84\nv_mfma_f32_16x16x32_bf16 v[112:115],a[196:199],v[148:151],v[112:115]\nv_mul_f32 v20,0x3fb8aa3b,v84\nv_max_f32 v51,0,v83\nv_mfma_f32_16x16x32_bf16 v[112:115],a[192:195],v[144:147],v[112:115]\n"
"v_mul_f32 v19,0x3fb8aa3b,v83\nv_max_f32 v50,0,v82\ns_nop 7\nv_mul_f32 v18,0x3fb8aa3b,v82\nv_max_f32 v49,0,v81\nv_mul_f32 v17,0x3fb8aa3b,v81\nv_max_f32 v48,0,v80\nv_mul_f32 v16,0x3fb8aa3b,v80\nv_cvt_pk_bf16_f32 v176,v96,v97\n"
"v_cvt_pk_bf16_f32 v177,v98,v99\nv_cvt_pk_bf16_f32 v178,v100,v101\nv_cvt_pk_bf16_f32 v179,v102,v103\nv_cvt_pk_bf16_f32 v180,v104,v105\nv_cvt_pk_bf16_f32 v181,v106,v107\nv_cvt_pk_bf16_f32 v182,v108,v109\n"
"v_cvt_pk_bf16_f32 v183,v110,v111\nv_cvt_pk_bf16_f32 v190,v124,v125\nv_cvt_pk_bf16_f32 v191,v126,v127\nv_cvt_pk_bf16_f32 v188,v120,v121\nv_cvt_pk_bf16_f32 v189,v122,v123\nv_cvt_pk_bf16_f32 v186,v116,v117\n"
"v_cvt_pk_bf16_f32 v187,v118,v119\nv_cvt_pk_bf16_f32 v184,v112,v113\nv_cvt_pk_bf16_f32 v185,v114,v115\nv_exp_f32_e64 v0,v0 clamp\nv_add_f32 v32,0x358637bd,v32\nv_exp_f32_e64 v1,v1 clamp\nv_add_f32 v33,0x358637bd,v33\n"
"v_add_f32 v32,v32,v0\nv_exp_f32_e64 v2,v2 clamp\nv_add_f32 v34,0x358637bd,v34\nv_add_f32 v33,v33,v1\nv_exp_f32_e64 v3,v3 clamp\nv_add_f32 v35,0x358637bd,v35\nv_add_f32 v34,v34,v2\nv_exp_f32_e64 v4,v4 clamp\n"
"v_add_f32 v36,0x358637bd,v36\nv_add_f32 v35,v35,v3\nv_exp_f32_e64 v5,v5 clamp\nv_add_f32 v37,0x358637bd,v37\nv_add_f32 v36,v36,v4\nv_exp_f32_e64 v6,v6 clamp\nv_add_f32 v38,0x358637bd,v38\nv_add_f32 v37,v37,v5\n"
"v_exp_f32_e64 v7,v7 clamp\nv_add_f32 v39,0x358637bd,v39\nv_add_f32 v38,v38,v6\nv_exp_f32_e64 v8,v8 clamp\nv_add_f32 v40,0x358637bd,v40\nv_add_f32 v39,v39,v7\nv_exp_f32_e64 v9,v9 clamp\nv_add_f32 v41,0x358637bd,v41\n"
"v_add_f32 v40,v40,v8\nv_exp_f32_e64 v10,v10 clamp\nv_add_f32 v42,0x358637bd,v42\nv_add_f32 v41,v41,v9\nv_exp_f32_e64 v11,v11 clamp\nv_add_f32 v43,0x358637bd,v43\nv_add_f32 v42,v42,v10\nv_exp_f32_e64 v12,v12 clamp\n"
"v_add_f32 v44,0x358637bd,v44\nv_add_f32 v43,v43,v11\nv_exp_f32_e64 v13,v13 clamp\nv_add_f32 v45,0x358637bd,v45\nv_add_f32 v44,v44,v12\nv_exp_f32_e64 v14,v14 clamp\nv_add_f32 v46,0x358637bd,v46\nv_add_f32 v45,v45,v13\n"
"v_exp_f32_e64 v15,v15 clamp\nv_add_f32 v47,0x358637bd,v47\nv_add_f32 v46,v46,v14\nv_exp_f32_e64 v16,v16 clamp\nv_add_f32 v48,0x358637bd,v48\nv_add_f32 v47,v47,v15\nv_exp_f32_e64 v17,v17 clamp\nv_add_f32 v49,0x358637bd,v49\n"
"v_add_f32 v48,v48,v16\nv_exp_f32_e64 v18,v18 clamp\nv_add_f32 v50,0x358637bd,v50\nv_add_f32 v49,v49,v17\nv_exp_f32_e64 v19,v19 clamp\nv_add_f32 v51,0x358637bd,v51\nv_add_f32 v50,v50,v18\nv_exp_f32_e64 v20,v20 clamp\n"
"v_add_f32 v52,0x358637bd,v52\nv_add_f32 v51,v51,v19\nv_exp_f32_e64 v21,v21 clamp\nv_add_f32 v53,0x358637bd,v53\nv_add_f32 v52,v52,v20\nv_exp_f32_e64 v22,v22 clamp\nv_add_f32 v54,0x358637bd,v54\nv_add_f32 v53,v53,v21\n"
"v_exp_f32_e64 v23,v23 clamp\nv_add_f32 v55,0x358637bd,v55\nv_add_f32 v54,v54,v22\nv_exp_f32_e64 v24,v24 clamp\nv_add_f32 v56,0x358637bd,v56\nv_add_f32 v55,v55,v23\nv_exp_f32_e64 v25,v25 clamp\nv_add_f32 v57,0x358637bd,v57\n"
"v_add_f32 v56,v56,v24\nv_exp_f32_e64 v26,v26 clamp\nv_add_f32 v58,0x358637bd,v58\nv_add_f32 v57,v57,v25\nv_exp_f32_e64 v27,v27 clamp\nv_add_f32 v59,0x358637bd,v59\nv_add_f32 v58,v58,v26\nv_exp_f32_e64 v28,v28 clamp\n"
"v_add_f32 v60,0x358637bd,v60\nv_add_f32 v59,v59,v27\nv_exp_f32_e64 v29,v29 clamp\nv_add_f32 v61,0x358637bd,v61\nv_add_f32 v60,v60,v28\nv_exp_f32_e64 v30,v30 clamp\nv_add_f32 v62,0x358637bd,v62\nv_add_f32 v61,v61,v29\n"
"v_exp_f32_e64 v31,v31 clamp\nv_add_f32 v63,0x358637bd,v63\nv_add_f32 v62,v62,v30\nv_add_f32 v63,v63,v31\nv_cvt_pk_bf16_f32 v160,v32,v33\nv_cvt_pk_bf16_f32 v161,v34,v35\nv_cvt_pk_bf16_f32 v162,v36,v37\n"
"v_cvt_pk_bf16_f32 v163,v38,v39\nv_cvt_pk_bf16_f32 v164,v40,v41\nv_cvt_pk_bf16_f32 v165,v42,v43\nv_cvt_pk_bf16_f32 v166,v44,v45\nv_cvt_pk_bf16_f32 v167,v46,v47\nv_cvt_pk_bf16_f32 v168,v48,v49\nv_cvt_pk_bf16_f32 v169,v50,v51\n"
"v_cvt_pk_bf16_f32 v170,v52,v53\nv_cvt_pk_bf16_f32 v171,v54,v55\nv_cvt_pk_bf16_f32 v172,v56,v57\nv_cvt_pk_bf16_f32 v173,v58,v59\nv_cvt_pk_bf16_f32 v174,v60,v61\nv_cvt_pk_bf16_f32 v175,v62,v63\nv_cmp_eq_u32 vcc,s74,v241\n"
"s_add_u32 s74,s74,1\nv_nop\nv_cndmask_b32 v244,0,v243,vcc\nv_mov_b32 v245,v244\nv_mov_b32 v246,v244\nv_mov_b32 v247,v244\nv_mfma_f32_16x16x32_bf16 v[0:3],v[160:163],v[176:179],0\n"
"v_mfma_f32_16x16x32_bf16 v[0:3],v[164:167],v[180:183],v[0:3]\nv_mfma_f32_16x16x32_bf16 v[248:251],v[160:163],v[244:247],v[248:251]\nv_mfma_f32_16x16x32_bf16 v[248:251],v[164:167],v[244:247],v[248:251]\nv_cmp_eq_u32 vcc,s74,v241\n"
"s_add_u32 s74,s74,1\nv_nop\nv_cndmask_b32 v244,0,v243,vcc\nv_mov_b32 v245,v244\nv_mov_b32 v246,v244\nv_mov_b32 v247,v244\nv_mfma_f32_16x16x32_bf16 v[4:7],v[168:171],v[184:187],0\n"
"v_mfma_f32_16x16x32_bf16 v[4:7],v[172:175],v[188:191],v[4:7]\nv_mfma_f32_16x16x32_bf16 v[248:251],v[168:171],v[244:247],v[248:251]\nv_mfma_f32_16x16x32_bf16 v[248:251],v[172:175],v[244:247],v[248:251]\nv_nop\nv_nop\nv_nop\nv_nop\n"
"v_nop\nv_nop\nds_write_b128 v242,v[0:3]\nds_write_b128 v242,v[4:7] offset:4096\nv_add_u32 v242,0x2000,v242\ns_waitcnt vmcnt(12)\ns_waitcnt lgkmcnt(0)\ns_barrier\ns_lshl_b32 s71,s5,10\nv_add_u32 v246,0x1c000,v240\n"
"v_add_u32 v244,s71,v246\nds_write_b128 v244,v[248:251]\ns_lshl_b32 s71,s5,13\ns_add_u32 s71,s71,0x20000\nv_add_u32 v245,s71,v240\nds_read_b128 v[0:3],v245 offset:0\nds_read_b128 v[4:7],v245 offset:1024\n"
"ds_read_b128 v[8:11],v245 offset:2048\nds_read_b128 v[12:15],v245 offset:3072\nds_read_b128 v[16:19],v245 offset:4096\nds_read_b128 v[20:23],v245 offset:5120\nds_read_b128 v[24:27],v245 offset:6144\n"
"ds_read_b128 v[28:31],v245 offset:7168\ns_waitcnt lgkmcnt(0)\ns_barrier\nds_read_b128 v[32:35],v246 offset:0\nds_read_b128 v[36:39],v246 offset:1024\nds_read_b128 v[40:43],v246 offset:2048\nds_read_b128 v[44:47],v246 offset:3072\n"
"s_add_u32 s60,s82,s59\ns_add_u32 m0,s62,65536\ns_nop 0\nbuffer_load_dwordx4 v232,s[40:43],s60 offen offset:0 lds\nbuffer_load_dwordx4 v233,s[40:43],s60 offen offset:1024 lds\n"
"buffer_load_dwordx4 v234,s[40:43],s60 offen offset:2048 lds\nbuffer_load_dwordx4 v235,s[40:43],s60 offen offset:3072 lds\nv_pk_add_f32 v[0:1],v[0:1],v[4:5]\nv_pk_add_f32 v[2:3],v[2:3],v[6:7]\nv_pk_add_f32 v[8:9],v[8:9],v[12:13]\n"
"v_pk_add_f32 v[10:11],v[10:11],v[14:15]\nv_pk_add_f32 v[0:1],v[0:1],v[8:9]\nv_pk_add_f32 v[2:3],v[2:3],v[10:11]\nv_pk_add_f32 v[16:17],v[16:17],v[20:21]\nv_pk_add_f32 v[18:19],v[18:19],v[22:23]\n"
"v_pk_add_f32 v[24:25],v[24:25],v[28:29]\nv_pk_add_f32 v[26:27],v[26:27],v[30:31]\nv_pk_add_f32 v[16:17],v[16:17],v[24:25]\nv_pk_add_f32 v[18:19],v[18:19],v[26:27]\nglobal_store_dwordx4 v240,v[0:3],s[20:21] sc1\n"
"global_store_dwordx4 v240,v[16:19],s[20:21] offset:1024 sc1\ns_waitcnt lgkmcnt(0)\ns_cmp_lg_u32 s5,0\ns_cbranch_scc1 LEND_%=\nv_pk_add_f32 v[32:33],v[32:33],v[36:37]\nv_pk_add_f32 v[34:35],v[34:35],v[38:39]\n"
"v_pk_add_f32 v[40:41],v[40:41],v[44:45]\nv_pk_add_f32 v[42:43],v[42:43],v[46:47]\nv_pk_add_f32 v[32:33],v[32:33],v[40:41]\nv_pk_add_f32 v[34:35],v[34:35],v[42:43]\nglobal_store_dwordx4 v240,v[32:35],s[22:23] sc1\nLEND_%=:\n"
".Lgm_k2:\ns_barrier\ns_mov_b64 s[44:45],s[26:27]\ns_add_u32 s60,s44,70033408\ns_addc_u32 s61,s45,0\ns_mov_b64 s[72:73],s[30:31]\ns_and_b32 s73,s73,0xffff\ns_mov_b32 s74,0x200000\ns_mov_b32 s75,0x20000\ns_mov_b32 s54,s5\n"
"s_lshl_b32 s89,s54,12\ns_add_u32 s90,s89,0x10000\ns_lshl_b32 s84,s16,14\ns_add_u32 s85,s84,0x4000\ns_mov_b32 s81,256\ns_mov_b32 s51,0x2000\nv_mbcnt_lo_u32_b32 v248,-1,0\nv_mbcnt_hi_u32_b32 v248,-1,v248\ns_cmp_lg_u32 s15,0\n"
"s_cbranch_scc1 LHW_%=\ns_mov_b64 s[40:41],s[34:35]\ns_and_b32 s41,s41,0xffff\ns_mov_b32 s42,0x400000\ns_mov_b32 s43,0x20000\ns_mov_b32 s91,s14\ns_add_u32 s56,s91,128\ns_mov_b32 s76,0\ns_mov_b32 s77,1048576\ns_mov_b32 s78,2097152\n"
"s_mov_b32 s79,3145728\nv_lshrrev_b32 v249,3,v248\ns_lshl_b32 s55,s54,5\nv_add_u32 v249,s55,v249\nv_mul_lo_u32 v249,v249,s51\nv_lshrrev_b32 v250,3,v248\nv_and_b32 v250,2,v250\nv_and_b32 v251,7,v248\nv_xor_b32 v250,v250,v251\n"
"v_lshrrev_b32 v251,2,v250\nv_mul_lo_u32 v251,v251,0\nv_lshl_add_u32 v251,v250,4,v251\nv_add_u32 v228,v249,v251\nv_xor_b32 v250,4,v250\nv_lshrrev_b32 v251,2,v250\nv_mul_lo_u32 v251,v251,0\nv_lshl_add_u32 v251,v250,4,v251\n"
"v_add_u32 v229,v249,v251\ns_lshl_b32 s55,s51,3\ns_sub_u32 s55,s55,1024\nv_add_u32 v229,s55,v229\ns_lshl_b32 s55,s55,1\nv_add_u32 v230,s55,v228\nv_add_u32 v231,s55,v229\ns_add_u32 s88,s76,s91\ns_add_u32 m0,s89,0\ns_nop 0\n"
"buffer_load_dwordx4 v228,s[40:43],s88 offen offset:0 lds\nbuffer_load_dwordx4 v229,s[40:43],s88 offen offset:1024 lds\nbuffer_load_dwordx4 v230,s[40:43],s88 offen offset:2048 lds\n"
"buffer_load_dwordx4 v231,s[40:43],s88 offen offset:3072 lds\ns_add_u32 s88,s76,s56\ns_add_u32 m0,s89,16384\ns_nop 0\nbuffer_load_dwordx4 v228,s[40:43],s88 offen offset:0 lds\n"
"buffer_load_dwordx4 v229,s[40:43],s88 offen offset:1024 lds\nbuffer_load_dwordx4 v230,s[40:43],s88 offen offset:2048 lds\nbuffer_load_dwordx4 v231,s[40:43],s88 offen offset:3072 lds\ns_add_u32 s88,s77,s91\ns_add_u32 m0,s89,32768\n"
"s_nop 0\nbuffer_load_dwordx4 v228,s[40:43],s88 offen offset:0 lds\nbuffer_load_dwordx4 v229,s[40:43],s88 offen offset:1024 lds\nbuffer_load_dwordx4 v230,s[40:43],s88 offen offset:2048 lds\n"
"buffer_load_dwordx4 v231,s[40:43],s88 offen offset:3072 lds\ns_add_u32 s88,s77,s56\ns_add_u32 m0,s89,49152\ns_nop 0\nbuffer_load_dwordx4 v228,s[40:43],s88 offen offset:0 lds\n"
"buffer_load_dwordx4 v229,s[40:43],s88 offen offset:1024 lds\nbuffer_load_dwordx4 v230,s[40:43],s88 offen offset:2048 lds\nbuffer_load_dwordx4 v231,s[40:43],s88 offen offset:3072 lds\nLHW_%=:\nv_mov_b32 v8,0\n"
"global_load_dwordx2 v[10:11],v8,s[60:61] offset:2048 sc1\nv_lshrrev_b32 v249,4,v248\nv_and_b32 v250,15,v248\nv_xor_b32 v250,v250,v249\nv_lshlrev_b32 v250,4,v250\nv_lshl_add_u32 v232,v249,10,v250\nv_xor_b32 v233,64,v232\n"
"v_add_u32 v233,3072,v233\nv_xor_b32 v234,128,v232\nv_add_u32 v234,6144,v234\nv_xor_b32 v235,192,v232\nv_add_u32 v235,9216,v235\ns_add_u32 s88,s81,s84\ns_add_u32 m0,s90,81920\ns_nop 0\n"
"buffer_load_dwordx4 v232,s[72:75],s88 offen offset:0 lds\nbuffer_load_dwordx4 v233,s[72:75],s88 offen offset:1024 lds\nbuffer_load_dwordx4 v234,s[72:75],s88 offen offset:2048 lds\n"
"buffer_load_dwordx4 v235,s[72:75],s88 offen offset:3072 lds\ns_add_u32 s88,s81,s85\ns_add_u32 m0,s90,49152\ns_nop 0\nbuffer_load_dwordx4 v232,s[72:75],s88 offen offset:0 lds\n"
"buffer_load_dwordx4 v233,s[72:75],s88 offen offset:1024 lds\nbuffer_load_dwordx4 v234,s[72:75],s88 offen offset:2048 lds\nbuffer_load_dwordx4 v235,s[72:75],s88 offen offset:3072 lds\nv_and_b32 v249,15,v248\n"
"v_lshl_add_u32 v249,v249,8,v250\nv_add_u32 v236,s90,v249\nv_xor_b32 v237,64,v236\nv_xor_b32 v238,128,v236\nv_xor_b32 v239,192,v236\nv_add_u32 v60,0x10000,v236\nv_add_u32 v61,0x10000,v237\nv_add_u32 v62,0x10000,v238\n"
"v_add_u32 v63,0x10000,v239\nv_lshrrev_b32 v249,4,v248\nv_lshlrev_b32 v249,10,v249\nv_bfe_u32 v250,v248,2,2\nv_lshl_add_u32 v249,v250,7,v249\nv_and_b32 v250,3,v248\nv_lshl_add_u32 v249,v250,3,v249\nv_bfe_u32 v250,v248,3,2\n"
"v_lshl_add_u32 v224,v250,5,v249\nv_xor_b32 v225,32,v224\nv_xor_b32 v226,64,v224\nv_xor_b32 v227,96,v224\ns_mov_b64 s[40:41],s[34:35]\ns_and_b32 s41,s41,0xffff\ns_mov_b32 s42,0x400000\ns_mov_b32 s43,0x20000\ns_mov_b32 s91,s14\n"
"s_add_u32 s56,s91,128\ns_mov_b32 s76,0\ns_mov_b32 s77,1048576\ns_mov_b32 s78,2097152\ns_mov_b32 s79,3145728\nv_lshrrev_b32 v249,3,v248\ns_lshl_b32 s55,s54,5\nv_add_u32 v249,s55,v249\nv_mul_lo_u32 v249,v249,s51\n"
"v_lshrrev_b32 v250,3,v248\nv_and_b32 v250,2,v250\nv_and_b32 v251,7,v248\nv_xor_b32 v250,v250,v251\nv_lshrrev_b32 v251,2,v250\nv_mul_lo_u32 v251,v251,0\nv_lshl_add_u32 v251,v250,4,v251\nv_add_u32 v228,v249,v251\n"
"v_xor_b32 v250,4,v250\nv_lshrrev_b32 v251,2,v250\nv_mul_lo_u32 v251,v251,0\nv_lshl_add_u32 v251,v250,4,v251\nv_add_u32 v229,v249,v251\ns_lshl_b32 s55,s51,3\ns_sub_u32 s55,s55,1024\nv_add_u32 v229,s55,v229\ns_lshl_b32 s55,s55,1\n"
"v_add_u32 v230,s55,v228\nv_add_u32 v231,s55,v229\ns_lshl_b32 s23,s4,2\ns_add_u32 s23,s23,s5\ns_lshr_b32 s24,s4,5\ns_lshl_b32 s24,s24,1\ns_add_u32 s24,s24,2\ns_lshl_b32 s55,s14,12\ns_add_u32 s36,s44,s55\ns_addc_u32 s37,s45,0\n"
"s_and_b32 s37,s37,0xffff\ns_mov_b32 s38,0x100000\ns_mov_b32 s39,0x20000\ns_and_b32 s45,s45,0xffff\ns_mov_b32 s46,70033408\ns_mov_b32 s47,0x20000\ns_mov_b32 s53,s24\ns_mov_b32 s57,s16\ns_mov_b32 s58,s17\ns_lshl_b32 s59,s14,4\n"
"s_add_u32 s59,s59,50855936\ns_mov_b32 s63,0xff800000\ns_lshl_b32 s86,s17,14\ns_add_u32 s87,s86,0x4000\ns_mov_b32 s80,0\ns_mov_b32 s82,512\ns_mov_b32 s83,768\nv_lshlrev_b32 v247,4,v248\nv_mov_b32 v246,v247\ns_waitcnt vmcnt(8)\n"
"s_barrier\nds_read_b64_tr_b16 v[192:193],v224 offset:0\nds_read_b64_tr_b16 v[194:195],v224 offset:512\nds_read_b64_tr_b16 v[196:197],v224 offset:4096\nds_read_b64_tr_b16 v[198:199],v224 offset:4608\n"
"ds_read_b64_tr_b16 v[200:201],v224 offset:8192\nds_read_b64_tr_b16 v[202:203],v224 offset:8704\nds_read_b64_tr_b16 v[204:205],v224 offset:12288\nds_read_b64_tr_b16 v[206:207],v224 offset:12800\nv_readfirstlane_b32 s98,v10\n"
"v_readfirstlane_b32 s99,v11\ns_lshl_b32 s55,s23,1\nv_mov_b32 v8,s55\ns_add_u32 s98,s98,1\ns_addc_u32 s99,s99,0\nv_mov_b32 v10,s98\nv_mov_b32 v11,s99\ns_cmp_lg_u32 s54,0\ns_cbranch_scc1 LNP_%=\ns_mov_b64 exec,1\n"
"global_store_dwordx2 v8,v[10:11],s[60:61] sc1\ns_mov_b64 exec,-1\nLNP_%=:\nv_lshlrev_b32 v8,1,v247\ns_waitcnt vmcnt(21)\nds_read_b128 a[0:3],v236 offset:0\nds_read_b128 a[4:7],v237 offset:0\nds_read_b128 a[8:11],v238 offset:0\n"
"ds_read_b128 a[12:15],v239 offset:0\ns_waitcnt vmcnt(17)\nds_read_b128 a[16:19],v236 offset:16384\nds_read_b128 a[20:23],v237 offset:16384\nds_read_b128 a[24:27],v238 offset:16384\nds_read_b128 a[28:31],v239 offset:16384\n"
"s_waitcnt vmcnt(13)\nds_read_b128 a[32:35],v236 offset:32768\nds_read_b128 a[36:39],v237 offset:32768\nds_read_b128 a[40:43],v238 offset:32768\nds_read_b128 a[44:47],v239 offset:32768\ns_waitcnt vmcnt(9)\n"
"ds_read_b128 a[48:51],v60 offset:0\nds_read_b128 a[52:55],v61 offset:0\nds_read_b128 a[56:59],v62 offset:0\nds_read_b128 a[60:63],v63 offset:0\ns_waitcnt lgkmcnt(0)\nv_mfma_f32_16x16x32_bf16 v[64:67],v[192:195],a[0:3],0\n"
"ds_read_b64_tr_b16 v[208:209],v225 offset:0\ns_add_u32 s88,s81,s86\nv_mfma_f32_16x16x32_bf16 v[64:67],v[196:199],a[4:7],v[64:67]\nds_read_b64_tr_b16 v[210:211],v225 offset:512\ns_add_u32 m0,s90,0\n"
"v_mfma_f32_16x16x32_bf16 v[64:67],v[200:203],a[8:11],v[64:67]\nds_read_b64_tr_b16 v[212:213],v225 offset:4096\ns_lshr_b32 s12,s14,9\nv_mfma_f32_16x16x32_bf16 v[64:67],v[204:207],a[12:15],v[64:67]\n"
"buffer_load_dwordx4 v232,s[72:75],s88 offen offset:0 lds\nv_mfma_f32_16x16x32_bf16 v[68:71],v[204:207],a[28:31],0\nds_read_b64_tr_b16 v[214:215],v225 offset:4608\ns_lshr_b32 s13,s4,3\n"
"v_mfma_f32_16x16x32_bf16 v[68:71],v[200:203],a[24:27],v[68:71]\nds_read_b64_tr_b16 v[216:217],v225 offset:8192\ns_and_b32 s13,s13,3\nv_mfma_f32_16x16x32_bf16 v[68:71],v[196:199],a[20:23],v[68:71]\n"
"ds_read_b64_tr_b16 v[218:219],v225 offset:8704\ns_and_b32 s13,s13,1\nv_mfma_f32_16x16x32_bf16 v[68:71],v[192:195],a[16:19],v[68:71]\nbuffer_load_dwordx4 v233,s[72:75],s88 offen offset:1024 lds\n"
"v_mfma_f32_16x16x32_bf16 v[72:75],v[192:195],a[32:35],0\nds_read_b64_tr_b16 v[220:221],v225 offset:12288\nv_and_b32 v249,15,v248\nv_mfma_f32_16x16x32_bf16 v[72:75],v[196:199],a[36:39],v[72:75]\n"
"ds_read_b64_tr_b16 v[222:223],v225 offset:12800\nv_lshrrev_b32 v250,4,v248\nv_mfma_f32_16x16x32_bf16 v[72:75],v[200:203],a[40:43],v[72:75]\ns_cmp_eq_u32 s13,0\ns_cselect_b32 s13,s17,s16\ns_lshl_b32 s8,s5,6\n"
"v_mfma_f32_16x16x32_bf16 v[72:75],v[204:207],a[44:47],v[72:75]\nbuffer_load_dwordx4 v234,s[72:75],s88 offen offset:2048 lds\nv_mfma_f32_16x16x32_bf16 v[76:79],v[204:207],a[60:63],0\ns_lshl_b32 s6,s12,1\ns_add_u32 s8,s8,s6\n"
"s_lshl_b32 s8,s8,10\nv_mfma_f32_16x16x32_bf16 v[76:79],v[200:203],a[56:59],v[76:79]\ns_add_u32 s8,s8,0x4280000\ns_lshl_b32 s9,s5,3\ns_lshr_b32 s6,s12,2\nv_mfma_f32_16x16x32_bf16 v[76:79],v[196:199],a[52:55],v[76:79]\n"
"s_waitcnt lgkmcnt(0)\ns_add_u32 s9,s9,s6\ns_lshl_b32 s9,s9,10\nv_mfma_f32_16x16x32_bf16 v[76:79],v[192:195],a[48:51],v[76:79]\nbuffer_load_dwordx4 v235,s[72:75],s88 offen offset:3072 lds\n"
"v_mfma_f32_16x16x32_bf16 v[92:95],v[208:211],a[48:51],0\nds_read_b64_tr_b16 v[192:193],v226 offset:0\ns_add_u32 s88,s81,s87\nv_mfma_f32_16x16x32_bf16 v[92:95],v[212:215],a[52:55],v[92:95]\n"
"ds_read_b64_tr_b16 v[194:195],v226 offset:512\ns_add_u32 m0,s90,16384\nv_mfma_f32_16x16x32_bf16 v[92:95],v[216:219],a[56:59],v[92:95]\nds_read_b64_tr_b16 v[196:197],v226 offset:4096\ns_add_u32 s9,s9,0x42c0000\n"
"v_mfma_f32_16x16x32_bf16 v[92:95],v[220:223],a[60:63],v[92:95]\nbuffer_load_dwordx4 v232,s[72:75],s88 offen offset:0 lds\nv_mfma_f32_16x16x32_bf16 v[88:91],v[220:223],a[44:47],0\nds_read_b64_tr_b16 v[198:199],v226 offset:4608\n"
"s_lshr_b32 s10,s12,2\nv_mfma_f32_16x16x32_bf16 v[88:91],v[216:219],a[40:43],v[88:91]\nds_read_b64_tr_b16 v[200:201],v226 offset:8192\ns_add_u32 s10,s10,32\nv_mfma_f32_16x16x32_bf16 v[88:91],v[212:215],a[36:39],v[88:91]\n"
"ds_read_b64_tr_b16 v[202:203],v226 offset:8704\ns_lshl_b32 s10,s10,5\nv_mfma_f32_16x16x32_bf16 v[88:91],v[208:211],a[32:35],v[88:91]\nbuffer_load_dwordx4 v233,s[72:75],s88 offen offset:1024 lds\n"
"v_mfma_f32_16x16x32_bf16 v[84:87],v[208:211],a[16:19],0\nds_read_b64_tr_b16 v[204:205],v226 offset:12288\ns_lshr_b32 s6,s13,2\nv_mfma_f32_16x16x32_bf16 v[84:87],v[212:215],a[20:23],v[84:87]\n"
"ds_read_b64_tr_b16 v[206:207],v226 offset:12800\ns_add_u32 s10,s10,s6\nv_mfma_f32_16x16x32_bf16 v[84:87],v[216:219],a[24:27],v[84:87]\ns_lshl_b32 s10,s10,15\ns_and_b32 s6,s13,3\ns_lshl_b32 s6,s6,2\n"
"v_mfma_f32_16x16x32_bf16 v[84:87],v[220:223],a[28:31],v[84:87]\nbuffer_load_dwordx4 v234,s[72:75],s88 offen offset:2048 lds\nv_mfma_f32_16x16x32_bf16 v[80:83],v[220:223],a[12:15],0\ns_and_b32 s7,s12,3\ns_add_u32 s6,s6,s7\n"
"s_lshl_b32 s6,s6,10\nv_mfma_f32_16x16x32_bf16 v[80:83],v[216:219],a[8:11],v[80:83]\ns_add_u32 s10,s10,s6\ns_add_u32 s10,s10,0\ns_lshl_b32 s11,s13,6\nv_mfma_f32_16x16x32_bf16 v[80:83],v[212:215],a[4:7],v[80:83]\n"
"s_waitcnt lgkmcnt(0)\ns_add_u32 s11,s11,0x42c8000\ns_mul_i32 s13,s13,0x90\nv_mfma_f32_16x16x32_bf16 v[80:83],v[208:211],a[0:3],v[80:83]\nbuffer_load_dwordx4 v235,s[72:75],s88 offen offset:3072 lds\n"
"v_mfma_f32_16x16x32_bf16 v[96:99],v[192:195],a[0:3],0\nds_read_b64_tr_b16 v[208:209],v227 offset:0\ns_add_u32 s88,s82,s84\nv_mfma_f32_16x16x32_bf16 v[96:99],v[196:199],a[4:7],v[96:99]\n"
"ds_read_b64_tr_b16 v[210:211],v227 offset:512\ns_add_u32 m0,s90,32768\nv_mfma_f32_16x16x32_bf16 v[96:99],v[200:203],a[8:11],v[96:99]\nds_read_b64_tr_b16 v[212:213],v227 offset:4096\ns_add_u32 s13,s13,s12\n"
"v_mfma_f32_16x16x32_bf16 v[96:99],v[204:207],a[12:15],v[96:99]\nbuffer_load_dwordx4 v232,s[72:75],s88 offen offset:0 lds\nv_mfma_f32_16x16x32_bf16 v[100:103],v[204:207],a[28:31],0\nds_read_b64_tr_b16 v[214:215],v227 offset:4608\n"
"s_add_u32 s13,s13,0x80\nv_mfma_f32_16x16x32_bf16 v[100:103],v[200:203],a[24:27],v[100:103]\nds_read_b64_tr_b16 v[216:217],v227 offset:8192\ns_lshl_b32 s13,s13,10\n"
"v_mfma_f32_16x16x32_bf16 v[100:103],v[196:199],a[20:23],v[100:103]\nds_read_b64_tr_b16 v[218:219],v227 offset:8704\ns_add_u32 s13,s13,0x3080000\nv_mfma_f32_16x16x32_bf16 v[100:103],v[192:195],a[16:19],v[100:103]\n"
"buffer_load_dwordx4 v233,s[72:75],s88 offen offset:1024 lds\nv_mfma_f32_16x16x32_bf16 v[104:107],v[192:195],a[32:35],0\nds_read_b64_tr_b16 v[220:221],v227 offset:12288\ns_waitcnt vmcnt(14)\n"
"v_mfma_f32_16x16x32_bf16 v[104:107],v[196:199],a[36:39],v[104:107]\nds_read_b64_tr_b16 v[222:223],v227 offset:12800\ns_and_b32 s12,s12,3\nv_mfma_f32_16x16x32_bf16 v[104:107],v[200:203],a[40:43],v[104:107]\n"
"ds_read_b128 a[64:67],v60 offset:16384\ns_lshl_b32 s12,s12,6\nv_mfma_f32_16x16x32_bf16 v[104:107],v[204:207],a[44:47],v[104:107]\nbuffer_load_dwordx4 v234,s[72:75],s88 offen offset:2048 lds\n"
"v_mfma_f32_16x16x32_bf16 v[108:111],v[204:207],a[60:63],0\nds_read_b128 a[68:71],v61 offset:16384\ns_and_b32 s24,s4,0xf8\nv_mfma_f32_16x16x32_bf16 v[108:111],v[200:203],a[56:59],v[108:111]\nds_read_b128 a[72:75],v62 offset:16384\n"
"s_and_b32 s6,s4,7\nv_mfma_f32_16x16x32_bf16 v[108:111],v[196:199],a[52:55],v[108:111]\ns_waitcnt vmcnt(44) lgkmcnt(3)\nds_read_b128 a[76:79],v63 offset:16384\nv_mfma_f32_16x16x32_bf16 v[108:111],v[192:195],a[48:51],v[108:111]\n"
"buffer_load_dwordx4 v235,s[72:75],s88 offen offset:3072 lds\nv_mfma_f32_16x16x32_bf16 v[124:127],v[208:211],a[48:51],0\ns_barrier\ns_add_u32 s88,s78,s91\ns_waitcnt vmcnt(12)\n"
"v_mfma_f32_16x16x32_bf16 v[124:127],v[212:215],a[52:55],v[124:127]\nds_read_b64_tr_b16 v[192:193],v224 offset:16384\ns_add_u32 m0,s89,0\nv_mfma_f32_16x16x32_bf16 v[124:127],v[216:219],a[56:59],v[124:127]\n"
"ds_read_b64_tr_b16 v[194:195],v224 offset:16896\ns_lshl_b32 s6,s6,2\nv_mfma_f32_16x16x32_bf16 v[124:127],v[220:223],a[60:63],v[124:127]\nbuffer_load_dwordx4 v228,s[40:43],s88 offen offset:0 lds\n"
"v_mfma_f32_16x16x32_bf16 v[120:123],v[220:223],a[44:47],0\nds_read_b64_tr_b16 v[196:197],v224 offset:20480\ns_lshr_b32 s6,0x76543210,s6\nv_mfma_f32_16x16x32_bf16 v[120:123],v[216:219],a[40:43],v[120:123]\n"
"ds_read_b64_tr_b16 v[198:199],v224 offset:20992\ns_and_b32 s6,s6,7\nv_mfma_f32_16x16x32_bf16 v[120:123],v[212:215],a[36:39],v[120:123]\nds_read_b64_tr_b16 v[200:201],v224 offset:24576\ns_or_b32 s24,s24,s6\n"
"v_mfma_f32_16x16x32_bf16 v[120:123],v[208:211],a[32:35],v[120:123]\nbuffer_load_dwordx4 v229,s[40:43],s88 offen offset:1024 lds\nv_mfma_f32_16x16x32_bf16 v[116:119],v[208:211],a[16:19],0\n"
"ds_read_b64_tr_b16 v[202:203],v224 offset:25088\ns_lshr_b32 s19,s4,5\nv_mfma_f32_16x16x32_bf16 v[116:119],v[212:215],a[20:23],v[116:119]\nds_read_b64_tr_b16 v[204:205],v224 offset:28672\ns_lshl_b32 s19,s19,2\n"
"v_mfma_f32_16x16x32_bf16 v[116:119],v[216:219],a[24:27],v[116:119]\nds_read_b64_tr_b16 v[206:207],v224 offset:29184\ns_add_u32 s19,s19,s5\nv_mfma_f32_16x16x32_bf16 v[116:119],v[220:223],a[28:31],v[116:119]\n"
"buffer_load_dwordx4 v230,s[40:43],s88 offen offset:2048 lds\nv_mfma_f32_16x16x32_bf16 v[112:115],v[220:223],a[12:15],0\nds_read_b128 a[80:83],v236 offset:49152\ns_lshl_b32 s19,s19,8\n"
"v_mfma_f32_16x16x32_bf16 v[112:115],v[216:219],a[8:11],v[112:115]\nds_read_b128 a[84:87],v237 offset:49152\ns_and_b32 s6,s4,7\nv_mfma_f32_16x16x32_bf16 v[112:115],v[212:215],a[4:7],v[112:115]\ns_waitcnt lgkmcnt(2)\n"
"ds_read_b128 a[88:91],v238 offset:49152\nv_mfma_f32_16x16x32_bf16 v[112:115],v[208:211],a[0:3],v[112:115]\nbuffer_load_dwordx4 v231,s[40:43],s88 offen offset:3072 lds\nv_mfma_f32_16x16x32_bf16 v[128:131],v[192:195],a[0:3],0\n"
"ds_read_b64_tr_b16 v[208:209],v225 offset:16384\ns_add_u32 s88,s82,s85\nv_mfma_f32_16x16x32_bf16 v[128:131],v[196:199],a[4:7],v[128:131]\nds_read_b64_tr_b16 v[210:211],v225 offset:16896\ns_add_u32 m0,s90,65536\n"
"v_mfma_f32_16x16x32_bf16 v[128:131],v[200:203],a[8:11],v[128:131]\nds_read_b64_tr_b16 v[212:213],v225 offset:20480\ns_lshl_b32 s6,s6,2\nv_mfma_f32_16x16x32_bf16 v[128:131],v[204:207],a[12:15],v[128:131]\n"
"buffer_load_dwordx4 v232,s[72:75],s88 offen offset:0 lds\nv_mfma_f32_16x16x32_bf16 v[132:135],v[204:207],a[28:31],0\nds_read_b64_tr_b16 v[214:215],v225 offset:20992\ns_lshr_b32 s7,s4,3\n"
"v_mfma_f32_16x16x32_bf16 v[132:135],v[200:203],a[24:27],v[132:135]\nds_read_b64_tr_b16 v[216:217],v225 offset:24576\ns_and_b32 s7,s7,3\nv_mfma_f32_16x16x32_bf16 v[132:135],v[196:199],a[20:23],v[132:135]\n"
"ds_read_b64_tr_b16 v[218:219],v225 offset:25088\ns_add_u32 s6,s6,s7\nv_mfma_f32_16x16x32_bf16 v[132:135],v[192:195],a[16:19],v[132:135]\nbuffer_load_dwordx4 v233,s[72:75],s88 offen offset:1024 lds\n"
"v_mfma_f32_16x16x32_bf16 v[136:139],v[192:195],a[32:35],0\nds_read_b64_tr_b16 v[220:221],v225 offset:28672\ns_lshl_b32 s6,s6,3\nv_mfma_f32_16x16x32_bf16 v[136:139],v[196:199],a[36:39],v[136:139]\n"
"ds_read_b64_tr_b16 v[222:223],v225 offset:29184\ns_add_u32 s19,s19,s6\nv_mfma_f32_16x16x32_bf16 v[136:139],v[200:203],a[40:43],v[136:139]\nds_read_b128 a[92:95],v239 offset:49152\ns_add_u32 s19,s19,0x1000\n"
"v_mfma_f32_16x16x32_bf16 v[136:139],v[204:207],a[44:47],v[136:139]\nbuffer_load_dwordx4 v234,s[72:75],s88 offen offset:2048 lds\nv_mfma_f32_16x16x32_bf16 v[140:143],v[204:207],a[60:63],0\ns_waitcnt vmcnt(15)\ns_lshr_b32 s18,s4,5\n"
"s_lshl_b32 s18,s18,1\nv_mfma_f32_16x16x32_bf16 v[140:143],v[200:203],a[56:59],v[140:143]\nds_read_b128 a[96:99],v236 offset:0\ns_and_b32 s6,s5,1\nv_mfma_f32_16x16x32_bf16 v[140:143],v[196:199],a[52:55],v[140:143]\n"
"s_waitcnt lgkmcnt(2)\nds_read_b128 a[100:103],v237 offset:0\nv_mfma_f32_16x16x32_bf16 v[140:143],v[192:195],a[48:51],v[140:143]\nbuffer_load_dwordx4 v235,s[72:75],s88 offen offset:3072 lds\n"
"v_mfma_f32_16x16x32_bf16 v[156:159],v[208:211],a[48:51],0\nds_read_b64_tr_b16 v[192:193],v226 offset:16384\ns_add_u32 s88,s82,s86\nv_mfma_f32_16x16x32_bf16 v[156:159],v[212:215],a[52:55],v[156:159]\n"
"ds_read_b64_tr_b16 v[194:195],v226 offset:16896\ns_add_u32 m0,s90,81920\nv_mfma_f32_16x16x32_bf16 v[156:159],v[216:219],a[56:59],v[156:159]\nds_read_b64_tr_b16 v[196:197],v226 offset:20480\ns_add_u32 s18,s18,s6\n"
"v_mfma_f32_16x16x32_bf16 v[156:159],v[220:223],a[60:63],v[156:159]\nbuffer_load_dwordx4 v232,s[72:75],s88 offen offset:0 lds\nv_mfma_f32_16x16x32_bf16 v[152:155],v[220:223],a[44:47],0\n"
"ds_read_b64_tr_b16 v[198:199],v226 offset:20992\ns_lshl_b32 s18,s18,9\nv_mfma_f32_16x16x32_bf16 v[152:155],v[216:219],a[40:43],v[152:155]\nds_read_b64_tr_b16 v[200:201],v226 offset:24576\ns_and_b32 s6,s4,7\n"
"v_mfma_f32_16x16x32_bf16 v[152:155],v[212:215],a[36:39],v[152:155]\nds_read_b64_tr_b16 v[202:203],v226 offset:25088\ns_lshl_b32 s6,s6,2\nv_mfma_f32_16x16x32_bf16 v[152:155],v[208:211],a[32:35],v[152:155]\n"
"buffer_load_dwordx4 v233,s[72:75],s88 offen offset:1024 lds\nv_mfma_f32_16x16x32_bf16 v[148:151],v[208:211],a[16:19],0\nds_read_b64_tr_b16 v[204:205],v226 offset:28672\ns_lshr_b32 s7,s4,3\n"
"v_mfma_f32_16x16x32_bf16 v[148:151],v[212:215],a[20:23],v[148:151]\nds_read_b64_tr_b16 v[206:207],v226 offset:29184\ns_and_b32 s7,s7,3\nv_mfma_f32_16x16x32_bf16 v[148:151],v[216:219],a[24:27],v[148:151]\n"
"ds_read_b128 a[104:107],v238 offset:0\ns_add_u32 s6,s6,s7\nv_mfma_f32_16x16x32_bf16 v[148:151],v[220:223],a[28:31],v[148:151]\nbuffer_load_dwordx4 v234,s[72:75],s88 offen offset:2048 lds\n"
"v_mfma_f32_16x16x32_bf16 v[144:147],v[220:223],a[12:15],0\nds_read_b128 a[108:111],v239 offset:0\ns_lshl_b32 s6,s6,4\nv_mfma_f32_16x16x32_bf16 v[144:147],v[216:219],a[8:11],v[144:147]\ns_waitcnt vmcnt(15)\ns_add_u32 s18,s18,s6\n"
"s_lshr_b32 s6,s5,1\nv_mfma_f32_16x16x32_bf16 v[144:147],v[212:215],a[4:7],v[144:147]\ns_waitcnt lgkmcnt(2)\nds_read_b128 a[112:115],v236 offset:16384\nv_mfma_f32_16x16x32_bf16 v[144:147],v[208:211],a[0:3],v[144:147]\n"
"buffer_load_dwordx4 v235,s[72:75],s88 offen offset:3072 lds\nv_mfma_f32_16x16x32_bf16 v[160:163],v[192:195],a[0:3],0\nds_read_b64_tr_b16 v[208:209],v227 offset:16384\ns_add_u32 s88,s82,s87\n"
"v_mfma_f32_16x16x32_bf16 v[160:163],v[196:199],a[4:7],v[160:163]\nds_read_b64_tr_b16 v[210:211],v227 offset:16896\ns_add_u32 m0,s90,49152\nv_mfma_f32_16x16x32_bf16 v[160:163],v[200:203],a[8:11],v[160:163]\n"
"ds_read_b64_tr_b16 v[212:213],v227 offset:20480\ns_lshl_b32 s6,s6,3\nv_mfma_f32_16x16x32_bf16 v[160:163],v[204:207],a[12:15],v[160:163]\nbuffer_load_dwordx4 v232,s[72:75],s88 offen offset:0 lds\n"
"v_mfma_f32_16x16x32_bf16 v[164:167],v[204:207],a[28:31],0\nds_read_b64_tr_b16 v[214:215],v227 offset:20992\ns_add_u32 s18,s18,s6\nv_mfma_f32_16x16x32_bf16 v[164:167],v[200:203],a[24:27],v[164:167]\n"
"ds_read_b64_tr_b16 v[216:217],v227 offset:24576\ns_add_u32 s18,s18,0x3000\nv_mfma_f32_16x16x32_bf16 v[164:167],v[196:199],a[20:23],v[164:167]\nds_read_b64_tr_b16 v[218:219],v227 offset:25088\ns_lshr_b32 s20,s4,3\n"
"v_mfma_f32_16x16x32_bf16 v[164:167],v[192:195],a[16:19],v[164:167]\nbuffer_load_dwordx4 v233,s[72:75],s88 offen offset:1024 lds\nv_mfma_f32_16x16x32_bf16 v[168:171],v[192:195],a[32:35],0\n"
"ds_read_b64_tr_b16 v[220:221],v227 offset:28672\ns_and_b32 s20,s20,3\nv_mfma_f32_16x16x32_bf16 v[168:171],v[196:199],a[36:39],v[168:171]\nds_read_b64_tr_b16 v[222:223],v227 offset:29184\ns_mul_i32 s20,s20,0x90\n"
"v_mfma_f32_16x16x32_bf16 v[168:171],v[200:203],a[40:43],v[168:171]\nds_read_b128 a[116:119],v237 offset:16384\ns_lshr_b32 s6,s24,1\nv_mfma_f32_16x16x32_bf16 v[168:171],v[204:207],a[44:47],v[168:171]\n"
"buffer_load_dwordx4 v234,s[72:75],s88 offen offset:2048 lds\nv_mfma_f32_16x16x32_bf16 v[172:175],v[204:207],a[60:63],0\nds_read_b128 a[120:123],v238 offset:16384\ns_and_b32 s6,s6,3\n"
"v_mfma_f32_16x16x32_bf16 v[172:175],v[200:203],a[56:59],v[172:175]\nds_read_b128 a[124:127],v239 offset:16384\ns_mul_i32 s6,s6,36\nv_mfma_f32_16x16x32_bf16 v[172:175],v[196:199],a[52:55],v[172:175]\n"
"s_waitcnt vmcnt(56) lgkmcnt(3)\ns_waitcnt vmcnt(15)\ns_add_u32 s20,s20,s6\nv_mfma_f32_16x16x32_bf16 v[172:175],v[192:195],a[48:51],v[172:175]\nbuffer_load_dwordx4 v235,s[72:75],s88 offen offset:3072 lds\n"
"v_mfma_f32_16x16x32_bf16 v[188:191],v[208:211],a[48:51],0\ns_barrier\ns_add_u32 s88,s78,s56\ns_lshl_b32 s20,s20,3\nv_mfma_f32_16x16x32_bf16 v[188:191],v[212:215],a[52:55],v[188:191]\n"
"ds_read_b64_tr_b16 v[192:193],v224 offset:32768\ns_add_u32 m0,s89,16384\nv_mfma_f32_16x16x32_bf16 v[188:191],v[216:219],a[56:59],v[188:191]\nds_read_b64_tr_b16 v[194:195],v224 offset:33280\ns_lshl_b32 s6,s5,1\n"
"v_mfma_f32_16x16x32_bf16 v[188:191],v[220:223],a[60:63],v[188:191]\nbuffer_load_dwordx4 v228,s[40:43],s88 offen offset:0 lds\nv_mfma_f32_16x16x32_bf16 v[184:187],v[220:223],a[44:47],0\n"
"ds_read_b64_tr_b16 v[196:197],v224 offset:36864\ns_add_u32 s20,s20,s6\nv_mfma_f32_16x16x32_bf16 v[184:187],v[216:219],a[40:43],v[184:187]\nds_read_b64_tr_b16 v[198:199],v224 offset:37376\ns_lshl_b32 s20,s20,10\n"
"v_mfma_f32_16x16x32_bf16 v[184:187],v[212:215],a[36:39],v[184:187]\nds_read_b64_tr_b16 v[200:201],v224 offset:40960\ns_add_u32 s20,s20,0x2400000\nv_mfma_f32_16x16x32_bf16 v[184:187],v[208:211],a[32:35],v[184:187]\n"
"buffer_load_dwordx4 v229,s[40:43],s88 offen offset:1024 lds\nv_mfma_f32_16x16x32_bf16 v[180:183],v[208:211],a[16:19],0\nds_read_b64_tr_b16 v[202:203],v224 offset:41472\ns_lshr_b32 s21,s4,5\n"
"v_mfma_f32_16x16x32_bf16 v[180:183],v[212:215],a[20:23],v[180:183]\nds_read_b64_tr_b16 v[204:205],v224 offset:45056\ns_lshl_b32 s21,s21,1\nv_mfma_f32_16x16x32_bf16 v[180:183],v[216:219],a[24:27],v[180:183]\n"
"ds_read_b64_tr_b16 v[206:207],v224 offset:45568\ns_and_b32 s6,s24,1\nv_mfma_f32_16x16x32_bf16 v[180:183],v[220:223],a[28:31],v[180:183]\nbuffer_load_dwordx4 v230,s[40:43],s88 offen offset:2048 lds\n"
"v_mfma_f32_16x16x32_bf16 v[176:179],v[220:223],a[12:15],0\nds_read_b128 a[128:131],v236 offset:32768\ns_add_u32 s21,s21,s6\nv_mfma_f32_16x16x32_bf16 v[176:179],v[216:219],a[8:11],v[176:179]\n"
"ds_read_b128 a[132:135],v237 offset:32768\ns_mul_i32 s21,s21,3\nv_mfma_f32_16x16x32_bf16 v[176:179],v[212:215],a[4:7],v[176:179]\ns_waitcnt lgkmcnt(2)\nds_read_b128 a[136:139],v238 offset:32768\n"
"v_mfma_f32_16x16x32_bf16 v[176:179],v[208:211],a[0:3],v[176:179]\nbuffer_load_dwordx4 v231,s[40:43],s88 offen offset:3072 lds\nv_mfma_f32_16x16x32_bf16 v[64:67],v[192:195],a[64:67],v[64:67]\n"
"ds_read_b64_tr_b16 v[208:209],v225 offset:32768\ns_add_u32 s88,s83,s84\nv_mfma_f32_16x16x32_bf16 v[64:67],v[196:199],a[68:71],v[64:67]\nds_read_b64_tr_b16 v[210:211],v225 offset:33280\ns_add_u32 m0,s90,0\n"
"v_mfma_f32_16x16x32_bf16 v[64:67],v[200:203],a[72:75],v[64:67]\nds_read_b64_tr_b16 v[212:213],v225 offset:36864\ns_lshr_b32 s21,s21,1\nv_mfma_f32_16x16x32_bf16 v[64:67],v[204:207],a[76:79],v[64:67]\n"
"buffer_load_dwordx4 v232,s[72:75],s88 offen offset:0 lds\nv_mfma_f32_16x16x32_bf16 v[68:71],v[204:207],a[92:95],v[68:71]\nds_read_b64_tr_b16 v[214:215],v225 offset:37376\ns_lshl_b32 s21,s21,8\n"
"v_mfma_f32_16x16x32_bf16 v[68:71],v[200:203],a[88:91],v[68:71]\nds_read_b64_tr_b16 v[216:217],v225 offset:40960\ns_add_u32 s21,s21,0x1000\nv_mfma_f32_16x16x32_bf16 v[68:71],v[196:199],a[84:87],v[68:71]\n"
"ds_read_b64_tr_b16 v[218:219],v225 offset:41472\ns_lshr_b32 s22,s4,5\nv_mfma_f32_16x16x32_bf16 v[68:71],v[192:195],a[80:83],v[68:71]\nbuffer_load_dwordx4 v233,s[72:75],s88 offen offset:1024 lds\n"
"v_mfma_f32_16x16x32_bf16 v[72:75],v[192:195],a[96:99],v[72:75]\nds_read_b64_tr_b16 v[220:221],v225 offset:45056\ns_lshl_b32 s22,s22,1\nv_mfma_f32_16x16x32_bf16 v[72:75],v[196:199],a[100:103],v[72:75]\n"
"ds_read_b64_tr_b16 v[222:223],v225 offset:45568\ns_and_b32 s6,s24,1\nv_mfma_f32_16x16x32_bf16 v[72:75],v[200:203],a[104:107],v[72:75]\nds_read_b128 a[140:143],v239 offset:32768\ns_add_u32 s22,s22,s6\n"
"v_mfma_f32_16x16x32_bf16 v[72:75],v[204:207],a[108:111],v[72:75]\nbuffer_load_dwordx4 v234,s[72:75],s88 offen offset:2048 lds\nv_mfma_f32_16x16x32_bf16 v[76:79],v[204:207],a[124:127],v[76:79]\ns_waitcnt vmcnt(15)\n"
"s_mul_i32 s22,s22,0x1b0\ns_lshr_b32 s6,s24,1\nv_mfma_f32_16x16x32_bf16 v[76:79],v[200:203],a[120:123],v[76:79]\nds_read_b128 a[144:147],v60 offset:0\ns_and_b32 s6,s6,3\n"
"v_mfma_f32_16x16x32_bf16 v[76:79],v[196:199],a[116:119],v[76:79]\ns_waitcnt lgkmcnt(2)\nds_read_b128 a[148:151],v61 offset:0\nv_mfma_f32_16x16x32_bf16 v[76:79],v[192:195],a[112:115],v[76:79]\n"
"buffer_load_dwordx4 v235,s[72:75],s88 offen offset:3072 lds\nv_mfma_f32_16x16x32_bf16 v[92:95],v[208:211],a[112:115],v[92:95]\nds_read_b64_tr_b16 v[192:193],v226 offset:32768\ns_add_u32 s88,s83,s85\n"
"v_mfma_f32_16x16x32_bf16 v[92:95],v[212:215],a[116:119],v[92:95]\nds_read_b64_tr_b16 v[194:195],v226 offset:33280\ns_add_u32 m0,s90,16384\nv_mfma_f32_16x16x32_bf16 v[92:95],v[216:219],a[120:123],v[92:95]\n"
"ds_read_b64_tr_b16 v[196:197],v226 offset:36864\ns_mul_i32 s6,s6,36\nv_mfma_f32_16x16x32_bf16 v[92:95],v[220:223],a[124:127],v[92:95]\nbuffer_load_dwordx4 v232,s[72:75],s88 offen offset:0 lds\n"
"v_mfma_f32_16x16x32_bf16 v[88:91],v[220:223],a[108:111],v[88:91]\nds_read_b64_tr_b16 v[198:199],v226 offset:37376\ns_add_u32 s22,s22,s6\nv_mfma_f32_16x16x32_bf16 v[88:91],v[216:219],a[104:107],v[88:91]\n"
"ds_read_b64_tr_b16 v[200:201],v226 offset:40960\ns_add_u32 s22,s22,s5\nv_mfma_f32_16x16x32_bf16 v[88:91],v[212:215],a[100:103],v[88:91]\nds_read_b64_tr_b16 v[202:203],v226 offset:41472\ns_lshl_b32 s22,s22,10\n"
"v_mfma_f32_16x16x32_bf16 v[88:91],v[208:211],a[96:99],v[88:91]\nbuffer_load_dwordx4 v233,s[72:75],s88 offen offset:1024 lds\nv_mfma_f32_16x16x32_bf16 v[84:87],v[208:211],a[80:83],v[84:87]\n"
"ds_read_b64_tr_b16 v[204:205],v226 offset:45056\ns_add_u32 s22,s22,0x3080000\nv_mfma_f32_16x16x32_bf16 v[84:87],v[212:215],a[84:87],v[84:87]\nds_read_b64_tr_b16 v[206:207],v226 offset:45568\ns_lshr_b32 s15,s4,5\n"
"v_mfma_f32_16x16x32_bf16 v[84:87],v[216:219],a[88:91],v[84:87]\nds_read_b128 a[152:155],v62 offset:0\ns_lshl_b32 s15,s15,1\nv_mfma_f32_16x16x32_bf16 v[84:87],v[220:223],a[92:95],v[84:87]\n"
"buffer_load_dwordx4 v234,s[72:75],s88 offen offset:2048 lds\nv_mfma_f32_16x16x32_bf16 v[80:83],v[220:223],a[76:79],v[80:83]\nds_read_b128 a[156:159],v63 offset:0\ns_and_b32 s6,s24,1\n"
"v_mfma_f32_16x16x32_bf16 v[80:83],v[216:219],a[72:75],v[80:83]\ns_waitcnt vmcnt(15)\ns_add_u32 s15,s15,s6\ns_lshl_b32 s15,s15,2\nv_mfma_f32_16x16x32_bf16 v[80:83],v[212:215],a[68:71],v[80:83]\ns_waitcnt lgkmcnt(2)\n"
"ds_read_b128 a[160:163],v60 offset:16384\nv_mfma_f32_16x16x32_bf16 v[80:83],v[208:211],a[64:67],v[80:83]\nbuffer_load_dwordx4 v235,s[72:75],s88 offen offset:3072 lds\n"
"v_mfma_f32_16x16x32_bf16 v[96:99],v[192:195],a[64:67],v[96:99]\nds_read_b64_tr_b16 v[208:209],v227 offset:32768\ns_add_u32 s88,s83,s86\nv_mfma_f32_16x16x32_bf16 v[96:99],v[196:199],a[68:71],v[96:99]\n"
"ds_read_b64_tr_b16 v[210:211],v227 offset:33280\ns_add_u32 m0,s90,32768\nv_mfma_f32_16x16x32_bf16 v[96:99],v[200:203],a[72:75],v[96:99]\nds_read_b64_tr_b16 v[212:213],v227 offset:36864\ns_lshr_b32 s6,s4,3\n"
"v_mfma_f32_16x16x32_bf16 v[96:99],v[204:207],a[76:79],v[96:99]\nbuffer_load_dwordx4 v232,s[72:75],s88 offen offset:0 lds\nv_mfma_f32_16x16x32_bf16 v[100:103],v[204:207],a[92:95],v[100:103]\n"
"ds_read_b64_tr_b16 v[214:215],v227 offset:37376\ns_and_b32 s6,s6,3\nv_mfma_f32_16x16x32_bf16 v[100:103],v[200:203],a[88:91],v[100:103]\nds_read_b64_tr_b16 v[216:217],v227 offset:40960\ns_add_u32 s15,s15,s6\n"
"v_mfma_f32_16x16x32_bf16 v[100:103],v[196:199],a[84:87],v[100:103]\nds_read_b64_tr_b16 v[218:219],v227 offset:41472\ns_lshl_b32 s15,s15,17\nv_mfma_f32_16x16x32_bf16 v[100:103],v[192:195],a[80:83],v[100:103]\n"
"buffer_load_dwordx4 v233,s[72:75],s88 offen offset:1024 lds\nv_mfma_f32_16x16x32_bf16 v[104:107],v[192:195],a[96:99],v[104:107]\nds_read_b64_tr_b16 v[220:221],v227 offset:45056\ns_lshr_b32 s6,s24,1\n"
"v_mfma_f32_16x16x32_bf16 v[104:107],v[196:199],a[100:103],v[104:107]\nds_read_b64_tr_b16 v[222:223],v227 offset:45568\ns_and_b32 s6,s6,3\nv_mfma_f32_16x16x32_bf16 v[104:107],v[200:203],a[104:107],v[104:107]\n"
"ds_read_b128 a[164:167],v61 offset:16384\ns_add_u32 s6,s6,4\nv_mfma_f32_16x16x32_bf16 v[104:107],v[204:207],a[108:111],v[104:107]\nbuffer_load_dwordx4 v234,s[72:75],s88 offen offset:2048 lds\n"
"v_mfma_f32_16x16x32_bf16 v[108:111],v[204:207],a[124:127],v[108:111]\nds_read_b128 a[168:171],v62 offset:16384\ns_lshl_b32 s6,s6,14\nv_mfma_f32_16x16x32_bf16 v[108:111],v[200:203],a[120:123],v[108:111]\n"
"ds_read_b128 a[172:175],v63 offset:16384\ns_add_u32 s15,s15,s6\nv_mfma_f32_16x16x32_bf16 v[108:111],v[196:199],a[116:119],v[108:111]\ns_waitcnt vmcnt(63) lgkmcnt(3)\ns_waitcnt vmcnt(15)\ns_lshl_b32 s6,s5,10\n"
"v_mfma_f32_16x16x32_bf16 v[108:111],v[192:195],a[112:115],v[108:111]\nbuffer_load_dwordx4 v235,s[72:75],s88 offen offset:3072 lds\nv_mfma_f32_16x16x32_bf16 v[124:127],v[208:211],a[112:115],v[124:127]\ns_barrier\n"
"s_add_u32 s88,s79,s91\ns_add_u32 s15,s15,s6\nv_mfma_f32_16x16x32_bf16 v[124:127],v[212:215],a[116:119],v[124:127]\nds_read_b64_tr_b16 v[192:193],v224 offset:49152\ns_add_u32 m0,s89,32768\n"
"v_mfma_f32_16x16x32_bf16 v[124:127],v[216:219],a[120:123],v[124:127]\nds_read_b64_tr_b16 v[194:195],v224 offset:49664\ns_add_u32 s15,s15,0x2880000\nv_mfma_f32_16x16x32_bf16 v[124:127],v[220:223],a[124:127],v[124:127]\n"
"buffer_load_dwordx4 v228,s[40:43],s88 offen offset:0 lds\nv_mfma_f32_16x16x32_bf16 v[120:123],v[220:223],a[108:111],v[120:123]\nds_read_b64_tr_b16 v[196:197],v224 offset:53248\nv_lshlrev_b32 v250,2,v250\n"
"v_mfma_f32_16x16x32_bf16 v[120:123],v[216:219],a[104:107],v[120:123]\nds_read_b64_tr_b16 v[198:199],v224 offset:53760\nv_sub_u32 v249,v249,v250\nv_mfma_f32_16x16x32_bf16 v[120:123],v[212:215],a[100:103],v[120:123]\n"
"ds_read_b64_tr_b16 v[200:201],v224 offset:57344\nv_mov_b32 v250,s63\nv_mfma_f32_16x16x32_bf16 v[120:123],v[208:211],a[96:99],v[120:123]\nbuffer_load_dwordx4 v229,s[40:43],s88 offen offset:1024 lds\n"
"v_mfma_f32_16x16x32_bf16 v[116:119],v[208:211],a[80:83],v[116:119]\nds_read_b64_tr_b16 v[202:203],v224 offset:57856\nv_mov_b32 v251,0x7f800000\nv_mfma_f32_16x16x32_bf16 v[116:119],v[212:215],a[84:87],v[116:119]\n"
"ds_read_b64_tr_b16 v[204:205],v224 offset:61440\nv_cmp_le_i32 s[64:65],0,v249\nv_mfma_f32_16x16x32_bf16 v[116:119],v[216:219],a[88:91],v[116:119]\nds_read_b64_tr_b16 v[206:207],v224 offset:61952\nv_cmp_le_i32 s[66:67],1,v249\n"
"v_mfma_f32_16x16x32_bf16 v[116:119],v[220:223],a[92:95],v[116:119]\nbuffer_load_dwordx4 v230,s[40:43],s88 offen offset:2048 lds\nv_mfma_f32_16x16x32_bf16 v[112:115],v[220:223],a[76:79],v[112:115]\n"
"ds_read_b128 a[176:179],v236 offset:49152\nv_cmp_le_i32 s[68:69],2,v249\nv_mfma_f32_16x16x32_bf16 v[112:115],v[216:219],a[72:75],v[112:115]\nds_read_b128 a[180:183],v237 offset:49152\nv_cmp_le_i32 s[70:71],3,v249\n"
"v_mfma_f32_16x16x32_bf16 v[112:115],v[212:215],a[68:71],v[112:115]\ns_waitcnt lgkmcnt(2)\nds_read_b128 a[184:187],v238 offset:49152\nv_mfma_f32_16x16x32_bf16 v[112:115],v[208:211],a[64:67],v[112:115]\n"
"buffer_load_dwordx4 v231,s[40:43],s88 offen offset:3072 lds\nv_mfma_f32_16x16x32_bf16 v[128:131],v[192:195],a[64:67],v[128:131]\nds_read_b64_tr_b16 v[208:209],v225 offset:49152\ns_add_u32 s88,s83,s87\n"
"v_mfma_f32_16x16x32_bf16 v[128:131],v[196:199],a[68:71],v[128:131]\nds_read_b64_tr_b16 v[210:211],v225 offset:49664\ns_add_u32 m0,s90,65536\nv_mfma_f32_16x16x32_bf16 v[128:131],v[200:203],a[72:75],v[128:131]\n"
"ds_read_b64_tr_b16 v[212:213],v225 offset:53248\nv_nop\nv_mfma_f32_16x16x32_bf16 v[128:131],v[204:207],a[76:79],v[128:131]\nbuffer_load_dwordx4 v232,s[72:75],s88 offen offset:0 lds\n"
"v_mfma_f32_16x16x32_bf16 v[132:135],v[204:207],a[92:95],v[132:135]\nds_read_b64_tr_b16 v[214:215],v225 offset:53760\nv_nop\nv_mfma_f32_16x16x32_bf16 v[132:135],v[200:203],a[88:91],v[132:135]\n"
"ds_read_b64_tr_b16 v[216:217],v225 offset:57344\nv_cndmask_b32 v242,v250,v251,s[64:65]\nv_mfma_f32_16x16x32_bf16 v[132:135],v[196:199],a[84:87],v[132:135]\nds_read_b64_tr_b16 v[218:219],v225 offset:57856\n"
"v_cndmask_b32 v243,v250,v251,s[66:67]\nv_mfma_f32_16x16x32_bf16 v[132:135],v[192:195],a[80:83],v[132:135]\nbuffer_load_dwordx4 v233,s[72:75],s88 offen offset:1024 lds\n"
"v_mfma_f32_16x16x32_bf16 v[136:139],v[192:195],a[96:99],v[136:139]\nds_read_b64_tr_b16 v[220:221],v225 offset:61440\nv_cndmask_b32 v244,v250,v251,s[68:69]\nv_mfma_f32_16x16x32_bf16 v[136:139],v[196:199],a[100:103],v[136:139]\n"
"ds_read_b64_tr_b16 v[222:223],v225 offset:61952\nv_cndmask_b32 v245,v250,v251,s[70:71]\nv_mfma_f32_16x16x32_bf16 v[136:139],v[200:203],a[104:107],v[136:139]\nds_read_b128 a[188:191],v239 offset:49152\ns_waitcnt vmcnt(14)\n"
"v_mfma_f32_16x16x32_bf16 v[136:139],v[204:207],a[108:111],v[136:139]\nbuffer_load_dwordx4 v234,s[72:75],s88 offen offset:2048 lds\nv_mfma_f32_16x16x32_bf16 v[140:143],v[204:207],a[124:127],v[140:143]\n"
"ds_read_b128 a[192:195],v236 offset:0\nv_and_b32 v248,0xf0,v247\nv_mfma_f32_16x16x32_bf16 v[140:143],v[200:203],a[120:123],v[140:143]\nds_read_b128 a[196:199],v237 offset:0\nv_lshrrev_b32 v248,2,v248\n"
"v_mfma_f32_16x16x32_bf16 v[140:143],v[196:199],a[116:119],v[140:143]\ns_waitcnt lgkmcnt(3)\nds_read_b128 a[200:203],v238 offset:0\nv_mfma_f32_16x16x32_bf16 v[140:143],v[192:195],a[112:115],v[140:143]\n"
"buffer_load_dwordx4 v235,s[72:75],s88 offen offset:3072 lds\nv_mfma_f32_16x16x32_bf16 v[156:159],v[208:211],a[112:115],v[156:159]\nds_read_b64_tr_b16 v[192:193],v226 offset:49152\ns_add_u32 s88,s79,s56\n"
"v_mfma_f32_16x16x32_bf16 v[156:159],v[212:215],a[116:119],v[156:159]\nds_read_b64_tr_b16 v[194:195],v226 offset:49664\ns_add_u32 m0,s89,49152\nv_mfma_f32_16x16x32_bf16 v[156:159],v[216:219],a[120:123],v[156:159]\n"
"ds_read_b64_tr_b16 v[196:197],v226 offset:53248\nv_accvgpr_write_b32 a0,0\nv_mfma_f32_16x16x32_bf16 v[156:159],v[220:223],a[124:127],v[156:159]\nds_read_b128 a[204:207],v239 offset:0\ns_waitcnt vmcnt(12)\n"
"v_mfma_f32_16x16x32_bf16 v[152:155],v[220:223],a[108:111],v[152:155]\nds_read_b64_tr_b16 v[198:199],v226 offset:53760\nv_accvgpr_write_b32 a1,0\nv_mfma_f32_16x16x32_bf16 v[152:155],v[216:219],a[104:107],v[152:155]\n"
"ds_read_b64_tr_b16 v[200:201],v226 offset:57344\nv_accvgpr_write_b32 a2,0\nv_mfma_f32_16x16x32_bf16 v[152:155],v[212:215],a[100:103],v[152:155]\nds_read_b64_tr_b16 v[202:203],v226 offset:57856\nv_accvgpr_write_b32 a3,0\n"
"v_mfma_f32_16x16x32_bf16 v[152:155],v[208:211],a[96:99],v[152:155]\nds_read_b128 a[208:211],v236 offset:16384\nv_accvgpr_write_b32 a4,0\nv_mfma_f32_16x16x32_bf16 v[148:151],v[208:211],a[80:83],v[148:151]\n"
"ds_read_b64_tr_b16 v[204:205],v226 offset:61440\nv_accvgpr_write_b32 a5,0\nv_mfma_f32_16x16x32_bf16 v[148:151],v[212:215],a[84:87],v[148:151]\nds_read_b64_tr_b16 v[206:207],v226 offset:61952\nv_accvgpr_write_b32 a6,0\n"
"v_mfma_f32_16x16x32_bf16 v[148:151],v[216:219],a[88:91],v[148:151]\nds_read_b128 a[212:215],v237 offset:16384\nv_accvgpr_write_b32 a7,0\nv_mfma_f32_16x16x32_bf16 v[148:151],v[220:223],a[92:95],v[148:151]\n"
"ds_read_b128 a[216:219],v238 offset:16384\nv_accvgpr_write_b32 a8,0\nv_mfma_f32_16x16x32_bf16 v[144:147],v[220:223],a[76:79],v[144:147]\nds_read_b128 a[220:223],v239 offset:16384\nv_accvgpr_write_b32 a9,0\n"
"v_mfma_f32_16x16x32_bf16 v[144:147],v[216:219],a[72:75],v[144:147]\nv_accvgpr_write_b32 a10,0\nv_accvgpr_write_b32 a11,0\nv_mfma_f32_16x16x32_bf16 v[144:147],v[212:215],a[68:71],v[144:147]\ns_waitcnt lgkmcnt(3)\n"
"v_accvgpr_write_b32 a12,0\nv_accvgpr_write_b32 a13,0\nv_mfma_f32_16x16x32_bf16 v[144:147],v[208:211],a[64:67],v[144:147]\ns_waitcnt vmcnt(8)\nds_read_b128 a[224:227],v236 offset:32768\n"
"v_mfma_f32_16x16x32_bf16 v[160:163],v[192:195],a[64:67],v[160:163]\nds_read_b64_tr_b16 v[208:209],v227 offset:49152\nv_accvgpr_write_b32 a14,0\nv_mfma_f32_16x16x32_bf16 v[160:163],v[196:199],a[68:71],v[160:163]\n"
"ds_read_b64_tr_b16 v[210:211],v227 offset:49664\nv_accvgpr_write_b32 a15,0\nv_mfma_f32_16x16x32_bf16 v[160:163],v[200:203],a[72:75],v[160:163]\nds_read_b64_tr_b16 v[212:213],v227 offset:53248\nv_accvgpr_write_b32 a16,0\n"
"v_mfma_f32_16x16x32_bf16 v[160:163],v[204:207],a[76:79],v[160:163]\nds_read_b128 a[228:231],v237 offset:32768\nv_accvgpr_write_b32 a17,0\nv_mfma_f32_16x16x32_bf16 v[164:167],v[204:207],a[92:95],v[164:167]\n"
"ds_read_b64_tr_b16 v[214:215],v227 offset:53760\nv_accvgpr_write_b32 a18,0\nv_mfma_f32_16x16x32_bf16 v[164:167],v[200:203],a[88:91],v[164:167]\nds_read_b64_tr_b16 v[216:217],v227 offset:57344\nv_accvgpr_write_b32 a19,0\n"
"v_mfma_f32_16x16x32_bf16 v[164:167],v[196:199],a[84:87],v[164:167]\nds_read_b64_tr_b16 v[218:219],v227 offset:57856\nv_accvgpr_write_b32 a20,0\nv_mfma_f32_16x16x32_bf16 v[164:167],v[192:195],a[80:83],v[164:167]\n"
"ds_read_b128 a[232:235],v238 offset:32768\nv_accvgpr_write_b32 a21,0\nv_mfma_f32_16x16x32_bf16 v[168:171],v[192:195],a[96:99],v[168:171]\nds_read_b64_tr_b16 v[220:221],v227 offset:61440\nv_accvgpr_write_b32 a22,0\n"
"v_mfma_f32_16x16x32_bf16 v[168:171],v[196:199],a[100:103],v[168:171]\nds_read_b64_tr_b16 v[222:223],v227 offset:61952\nv_accvgpr_write_b32 a23,0\nv_mfma_f32_16x16x32_bf16 v[168:171],v[200:203],a[104:107],v[168:171]\n"
"ds_read_b128 a[236:239],v239 offset:32768\nv_accvgpr_write_b32 a24,0\nv_mfma_f32_16x16x32_bf16 v[168:171],v[204:207],a[108:111],v[168:171]\nv_accvgpr_write_b32 a25,0\nv_accvgpr_write_b32 a26,0\n"
"v_mfma_f32_16x16x32_bf16 v[172:175],v[204:207],a[124:127],v[172:175]\nv_accvgpr_write_b32 a27,0\nv_accvgpr_write_b32 a28,0\nv_mfma_f32_16x16x32_bf16 v[172:175],v[200:203],a[120:123],v[172:175]\nv_accvgpr_write_b32 a29,0\n"
"v_accvgpr_write_b32 a30,0\nv_mfma_f32_16x16x32_bf16 v[172:175],v[196:199],a[116:119],v[172:175]\ns_waitcnt vmcnt(36) lgkmcnt(1)\nv_accvgpr_write_b32 a31,0\nv_accvgpr_write_b32 a32,0\n"
"v_mfma_f32_16x16x32_bf16 v[172:175],v[192:195],a[112:115],v[172:175]\nv_accvgpr_write_b32 a33,0\nv_accvgpr_write_b32 a34,0\nv_mfma_f32_16x16x32_bf16 v[188:191],v[208:211],a[112:115],v[188:191]\ns_barrier\n"
"v_accvgpr_write_b32 a35,0\nv_accvgpr_write_b32 a36,0\nv_mfma_f32_16x16x32_bf16 v[188:191],v[212:215],a[116:119],v[188:191]\nds_read_b64_tr_b16 v[192:193],v224 offset:0\nv_accvgpr_write_b32 a37,0\n"
"v_mfma_f32_16x16x32_bf16 v[188:191],v[216:219],a[120:123],v[188:191]\nds_read_b64_tr_b16 v[194:195],v224 offset:512\nv_accvgpr_write_b32 a38,0\nv_mfma_f32_16x16x32_bf16 v[188:191],v[220:223],a[124:127],v[188:191]\n"
"buffer_load_dwordx4 v228,s[40:43],s88 offen offset:0 lds\nv_mfma_f32_16x16x32_bf16 v[184:187],v[220:223],a[108:111],v[184:187]\nds_read_b64_tr_b16 v[196:197],v224 offset:4096\nv_accvgpr_write_b32 a39,0\n"
"v_mfma_f32_16x16x32_bf16 v[184:187],v[216:219],a[104:107],v[184:187]\nds_read_b64_tr_b16 v[198:199],v224 offset:4608\nv_accvgpr_write_b32 a40,0\nv_mfma_f32_16x16x32_bf16 v[184:187],v[212:215],a[100:103],v[184:187]\n"
"ds_read_b64_tr_b16 v[200:201],v224 offset:8192\nv_accvgpr_write_b32 a41,0\nv_mfma_f32_16x16x32_bf16 v[184:187],v[208:211],a[96:99],v[184:187]\nbuffer_load_dwordx4 v229,s[40:43],s88 offen offset:1024 lds\n"
"v_mfma_f32_16x16x32_bf16 v[180:183],v[208:211],a[80:83],v[180:183]\nds_read_b64_tr_b16 v[202:203],v224 offset:8704\nv_accvgpr_write_b32 a42,0\nv_mfma_f32_16x16x32_bf16 v[180:183],v[212:215],a[84:87],v[180:183]\n"
"ds_read_b64_tr_b16 v[204:205],v224 offset:12288\nv_accvgpr_write_b32 a43,0\nv_mfma_f32_16x16x32_bf16 v[180:183],v[216:219],a[88:91],v[180:183]\nds_read_b64_tr_b16 v[206:207],v224 offset:12800\nv_accvgpr_write_b32 a44,0\n"
"v_mfma_f32_16x16x32_bf16 v[180:183],v[220:223],a[92:95],v[180:183]\nbuffer_load_dwordx4 v230,s[40:43],s88 offen offset:2048 lds\nv_mfma_f32_16x16x32_bf16 v[176:179],v[220:223],a[76:79],v[176:179]\nv_accvgpr_write_b32 a45,0\n"
"v_accvgpr_write_b32 a46,0\nv_mfma_f32_16x16x32_bf16 v[176:179],v[216:219],a[72:75],v[176:179]\nv_accvgpr_write_b32 a47,0\nv_accvgpr_write_b32 a48,0\nv_mfma_f32_16x16x32_bf16 v[176:179],v[212:215],a[68:71],v[176:179]\n"
"s_waitcnt lgkmcnt(0)\nv_accvgpr_write_b32 a49,0\nv_accvgpr_write_b32 a50,0\nv_mfma_f32_16x16x32_bf16 v[176:179],v[208:211],a[64:67],v[176:179]\nbuffer_load_dwordx4 v231,s[40:43],s88 offen offset:3072 lds\n"
"v_mfma_f32_16x16x32_bf16 v[64:67],v[192:195],a[128:131],v[64:67]\nds_read_b64_tr_b16 v[208:209],v225 offset:0\ns_lshl_b32 s49,s54,13\nv_mfma_f32_16x16x32_bf16 v[64:67],v[196:199],a[132:135],v[64:67]\n"
"ds_read_b64_tr_b16 v[210:211],v225 offset:512\ns_mov_b32 m0,s49\nv_mfma_f32_16x16x32_bf16 v[64:67],v[200:203],a[136:139],v[64:67]\nds_read_b64_tr_b16 v[212:213],v225 offset:4096\ns_add_u32 s55,s49,0x1000\n"
"v_mfma_f32_16x16x32_bf16 v[64:67],v[204:207],a[140:143],v[64:67]\ns_waitcnt vmcnt(4)\nds_read_b128 a[240:243],v60 offset:0\nv_mfma_f32_16x16x32_bf16 v[68:71],v[204:207],a[156:159],v[68:71]\n"
"ds_read_b64_tr_b16 v[214:215],v225 offset:4608\nv_accvgpr_write_b32 a51,0\nv_mfma_f32_16x16x32_bf16 v[68:71],v[200:203],a[152:155],v[68:71]\nds_read_b64_tr_b16 v[216:217],v225 offset:8192\nv_accvgpr_write_b32 a52,0\n"
"v_mfma_f32_16x16x32_bf16 v[68:71],v[196:199],a[148:151],v[68:71]\nds_read_b64_tr_b16 v[218:219],v225 offset:8704\nv_accvgpr_write_b32 a53,0\nv_mfma_f32_16x16x32_bf16 v[68:71],v[192:195],a[144:147],v[68:71]\n"
"ds_read_b128 a[244:247],v61 offset:0\nv_accvgpr_write_b32 a54,0\nv_mfma_f32_16x16x32_bf16 v[72:75],v[192:195],a[160:163],v[72:75]\nds_read_b64_tr_b16 v[220:221],v225 offset:12288\nv_accvgpr_write_b32 a55,0\n"
"v_mfma_f32_16x16x32_bf16 v[72:75],v[196:199],a[164:167],v[72:75]\nds_read_b64_tr_b16 v[222:223],v225 offset:12800\nv_accvgpr_write_b32 a56,0\nv_mfma_f32_16x16x32_bf16 v[72:75],v[200:203],a[168:171],v[72:75]\n"
"ds_read_b128 a[248:251],v62 offset:0\nv_accvgpr_write_b32 a57,0\nv_mfma_f32_16x16x32_bf16 v[72:75],v[204:207],a[172:175],v[72:75]\nds_read_b128 a[252:255],v63 offset:0\nv_accvgpr_write_b32 a58,0\n"
"v_mfma_f32_16x16x32_bf16 v[76:79],v[204:207],a[188:191],v[76:79]\nv_accvgpr_write_b32 a59,0\nv_accvgpr_write_b32 a60,0\nv_mfma_f32_16x16x32_bf16 v[76:79],v[200:203],a[184:187],v[76:79]\nv_accvgpr_write_b32 a61,0\n"
"v_accvgpr_write_b32 a62,0\nv_mfma_f32_16x16x32_bf16 v[76:79],v[196:199],a[180:183],v[76:79]\ns_waitcnt lgkmcnt(2)\nv_accvgpr_write_b32 a63,0\nv_accvgpr_write_b32 a64,0\n"
"v_mfma_f32_16x16x32_bf16 v[76:79],v[192:195],a[176:179],v[76:79]\nv_accvgpr_write_b32 a65,0\nv_accvgpr_write_b32 a66,0\nv_mfma_f32_16x16x32_bf16 v[92:95],v[208:211],a[176:179],v[92:95]\n"
"ds_read_b64_tr_b16 v[192:193],v226 offset:0\nv_accvgpr_write_b32 a67,0\nv_mfma_f32_16x16x32_bf16 v[92:95],v[212:215],a[180:183],v[92:95]\nds_read_b64_tr_b16 v[194:195],v226 offset:512\nv_accvgpr_write_b32 a68,0\n"
"v_mfma_f32_16x16x32_bf16 v[92:95],v[216:219],a[184:187],v[92:95]\nds_read_b64_tr_b16 v[196:197],v226 offset:4096\nv_accvgpr_write_b32 a69,0\nv_mfma_f32_16x16x32_bf16 v[92:95],v[220:223],a[188:191],v[92:95]\n"
"global_load_dwordx4 v[0:3],v8,s[60:61] offset:0 sc1\nv_mfma_f32_16x16x32_bf16 v[88:91],v[220:223],a[172:175],v[88:91]\nds_read_b64_tr_b16 v[198:199],v226 offset:4608\nv_accvgpr_write_b32 a70,0\n"
"v_mfma_f32_16x16x32_bf16 v[88:91],v[216:219],a[168:171],v[88:91]\nds_read_b64_tr_b16 v[200:201],v226 offset:8192\nv_accvgpr_write_b32 a71,0\nv_mfma_f32_16x16x32_bf16 v[88:91],v[212:215],a[164:167],v[88:91]\n"
"ds_read_b64_tr_b16 v[202:203],v226 offset:8704\nv_accvgpr_write_b32 a72,0\nv_mfma_f32_16x16x32_bf16 v[88:91],v[208:211],a[160:163],v[88:91]\nglobal_load_dwordx4 v[4:7],v8,s[60:61] offset:16 sc1\n"
"v_mfma_f32_16x16x32_bf16 v[84:87],v[208:211],a[144:147],v[84:87]\nds_read_b64_tr_b16 v[204:205],v226 offset:12288\nv_accvgpr_write_b32 a73,0\nv_mfma_f32_16x16x32_bf16 v[84:87],v[212:215],a[148:151],v[84:87]\n"
"ds_read_b64_tr_b16 v[206:207],v226 offset:12800\nv_accvgpr_write_b32 a74,0\nv_mfma_f32_16x16x32_bf16 v[84:87],v[216:219],a[152:155],v[84:87]\nv_accvgpr_write_b32 a75,0\nv_accvgpr_write_b32 a76,0\n"
"v_mfma_f32_16x16x32_bf16 v[84:87],v[220:223],a[156:159],v[84:87]\nv_accvgpr_write_b32 a77,0\nv_accvgpr_write_b32 a78,0\nv_mfma_f32_16x16x32_bf16 v[80:83],v[220:223],a[140:143],v[80:83]\nv_accvgpr_write_b32 a79,0\n"
"v_accvgpr_write_b32 a80,0\nv_mfma_f32_16x16x32_bf16 v[80:83],v[216:219],a[136:139],v[80:83]\nv_accvgpr_write_b32 a81,0\nv_accvgpr_write_b32 a82,0\nv_mfma_f32_16x16x32_bf16 v[80:83],v[212:215],a[132:135],v[80:83]\n"
"s_waitcnt lgkmcnt(0)\nv_accvgpr_write_b32 a83,0\nv_accvgpr_write_b32 a84,0\nv_mfma_f32_16x16x32_bf16 v[80:83],v[208:211],a[128:131],v[80:83]\nv_accvgpr_write_b32 a85,0\nv_accvgpr_write_b32 a86,0\n"
"v_mfma_f32_16x16x32_bf16 v[96:99],v[192:195],a[128:131],v[96:99]\nds_read_b64_tr_b16 v[208:209],v227 offset:0\nv_accvgpr_write_b32 a87,0\nv_mfma_f32_16x16x32_bf16 v[96:99],v[196:199],a[132:135],v[96:99]\n"
"ds_read_b64_tr_b16 v[210:211],v227 offset:512\nv_accvgpr_write_b32 a88,0\nv_mfma_f32_16x16x32_bf16 v[96:99],v[200:203],a[136:139],v[96:99]\nds_read_b64_tr_b16 v[212:213],v227 offset:4096\nv_accvgpr_write_b32 a89,0\n"
"v_mfma_f32_16x16x32_bf16 v[96:99],v[204:207],a[140:143],v[96:99]\nv_accvgpr_write_b32 a90,0\nv_accvgpr_write_b32 a91,0\nv_mfma_f32_16x16x32_bf16 v[100:103],v[204:207],a[156:159],v[100:103]\n"
"ds_read_b64_tr_b16 v[214:215],v227 offset:4608\nv_accvgpr_write_b32 a92,0\nv_mfma_f32_16x16x32_bf16 v[100:103],v[200:203],a[152:155],v[100:103]\nds_read_b64_tr_b16 v[216:217],v227 offset:8192\nv_accvgpr_write_b32 a93,0\n"
"v_mfma_f32_16x16x32_bf16 v[100:103],v[196:199],a[148:151],v[100:103]\nds_read_b64_tr_b16 v[218:219],v227 offset:8704\nv_accvgpr_write_b32 a94,0\nv_mfma_f32_16x16x32_bf16 v[100:103],v[192:195],a[144:147],v[100:103]\n"
"v_accvgpr_write_b32 a95,0\nv_accvgpr_write_b32 a96,0\nv_mfma_f32_16x16x32_bf16 v[104:107],v[192:195],a[160:163],v[104:107]\nds_read_b64_tr_b16 v[220:221],v227 offset:12288\nv_accvgpr_write_b32 a97,0\n"
"v_mfma_f32_16x16x32_bf16 v[104:107],v[196:199],a[164:167],v[104:107]\nds_read_b64_tr_b16 v[222:223],v227 offset:12800\nv_accvgpr_write_b32 a98,0\nv_mfma_f32_16x16x32_bf16 v[104:107],v[200:203],a[168:171],v[104:107]\n"
"v_accvgpr_write_b32 a99,0\nv_accvgpr_write_b32 a100,0\nv_mfma_f32_16x16x32_bf16 v[104:107],v[204:207],a[172:175],v[104:107]\nv_accvgpr_write_b32 a101,0\nv_accvgpr_write_b32 a102,0\n"
"v_mfma_f32_16x16x32_bf16 v[108:111],v[204:207],a[188:191],v[108:111]\nv_accvgpr_write_b32 a103,0\nv_accvgpr_write_b32 a104,0\nv_mfma_f32_16x16x32_bf16 v[108:111],v[200:203],a[184:187],v[108:111]\nv_accvgpr_write_b32 a105,0\n"
"v_accvgpr_write_b32 a106,0\nv_mfma_f32_16x16x32_bf16 v[108:111],v[196:199],a[180:183],v[108:111]\ns_waitcnt vmcnt(26) lgkmcnt(0)\nv_accvgpr_write_b32 a107,0\nv_accvgpr_write_b32 a108,0\n"
"v_mfma_f32_16x16x32_bf16 v[108:111],v[192:195],a[176:179],v[108:111]\nv_accvgpr_write_b32 a109,0\nv_accvgpr_write_b32 a110,0\nv_mfma_f32_16x16x32_bf16 v[124:127],v[208:211],a[176:179],v[124:127]\ns_barrier\n"
"v_accvgpr_write_b32 a111,0\nv_accvgpr_write_b32 a112,0\nv_mfma_f32_16x16x32_bf16 v[124:127],v[212:215],a[180:183],v[124:127]\nds_read_b64_tr_b16 v[192:193],v224 offset:16384\nv_accvgpr_write_b32 a113,0\n"
"v_mfma_f32_16x16x32_bf16 v[124:127],v[216:219],a[184:187],v[124:127]\nds_read_b64_tr_b16 v[194:195],v224 offset:16896\nv_accvgpr_write_b32 a114,0\nv_mfma_f32_16x16x32_bf16 v[124:127],v[220:223],a[188:191],v[124:127]\n"
"v_accvgpr_write_b32 a115,0\nv_accvgpr_write_b32 a116,0\nv_mfma_f32_16x16x32_bf16 v[120:123],v[220:223],a[172:175],v[120:123]\nds_read_b64_tr_b16 v[196:197],v224 offset:20480\nv_accvgpr_write_b32 a117,0\n"
"v_mfma_f32_16x16x32_bf16 v[120:123],v[216:219],a[168:171],v[120:123]\nds_read_b64_tr_b16 v[198:199],v224 offset:20992\nv_accvgpr_write_b32 a118,0\nv_mfma_f32_16x16x32_bf16 v[120:123],v[212:215],a[164:167],v[120:123]\n"
"ds_read_b64_tr_b16 v[200:201],v224 offset:24576\nv_accvgpr_write_b32 a119,0\nv_mfma_f32_16x16x32_bf16 v[120:123],v[208:211],a[160:163],v[120:123]\nv_accvgpr_write_b32 a120,0\nv_accvgpr_write_b32 a121,0\n"
"v_mfma_f32_16x16x32_bf16 v[116:119],v[208:211],a[144:147],v[116:119]\nds_read_b64_tr_b16 v[202:203],v224 offset:25088\nv_accvgpr_write_b32 a122,0\nv_mfma_f32_16x16x32_bf16 v[116:119],v[212:215],a[148:151],v[116:119]\n"
"ds_read_b64_tr_b16 v[204:205],v224 offset:28672\nv_accvgpr_write_b32 a123,0\nv_mfma_f32_16x16x32_bf16 v[116:119],v[216:219],a[152:155],v[116:119]\nds_read_b64_tr_b16 v[206:207],v224 offset:29184\nv_accvgpr_write_b32 a124,0\n"
"v_mfma_f32_16x16x32_bf16 v[116:119],v[220:223],a[156:159],v[116:119]\nv_accvgpr_write_b32 a125,0\nv_accvgpr_write_b32 a126,0\nv_mfma_f32_16x16x32_bf16 v[112:115],v[220:223],a[140:143],v[112:115]\nv_accvgpr_write_b32 a127,0\n"
"v_mfma_f32_16x16x32_bf16 v[112:115],v[216:219],a[136:139],v[112:115]\nv_mfma_f32_16x16x32_bf16 v[112:115],v[212:215],a[132:135],v[112:115]\ns_waitcnt lgkmcnt(0)\n"
"v_mfma_f32_16x16x32_bf16 v[112:115],v[208:211],a[128:131],v[112:115]\ns_mov_b32 s68,0x40000\ns_waitcnt vmcnt(0)\ns_branch LPCA_%=\nLPLA_%=:\nglobal_load_dwordx4 v[0:3],v8,s[60:61] offset:0 sc1\n"
"global_load_dwordx4 v[4:7],v8,s[60:61] offset:16 sc1\ns_waitcnt vmcnt(0)\nLPCA_%=:\nv_cmp_ne_u64 vcc,s[98:99],v[0:1]\nv_cmp_ne_u64 s[64:65],s[98:99],v[2:3]\ns_or_b64 vcc,vcc,s[64:65]\nv_cmp_ne_u64 s[64:65],s[98:99],v[4:5]\n"
"s_or_b64 vcc,vcc,s[64:65]\nv_cmp_ne_u64 s[64:65],s[98:99],v[6:7]\ns_or_b64 vcc,vcc,s[64:65]\ns_cmp_eq_u64 vcc,0\ns_cbranch_scc1 LPDA_%=\ns_sleep 2\ns_sub_u32 s68,s68,1\ns_cmp_lg_u32 s68,0\ns_cbranch_scc1 LPLA_%=\nLPDA_%=:\n"
"s_cmp_lg_u32 s23,0\ns_cbranch_scc1 LNB_%=\nv_mov_b32 v8,0\ns_mov_b64 exec,1\nglobal_store_dwordx2 v8,v[10:11],s[60:61] offset:2048 sc1\ns_mov_b64 exec,-1\nLNB_%=:\n"
"v_mfma_f32_16x16x32_bf16 v[128:131],v[192:195],a[128:131],v[128:131]\nds_read_b64_tr_b16 v[208:209],v225 offset:16384\nv_mfma_f32_16x16x32_bf16 v[128:131],v[196:199],a[132:135],v[128:131]\n"
"ds_read_b64_tr_b16 v[210:211],v225 offset:16896\nv_mfma_f32_16x16x32_bf16 v[128:131],v[200:203],a[136:139],v[128:131]\nds_read_b64_tr_b16 v[212:213],v225 offset:20480\n"
"v_mfma_f32_16x16x32_bf16 v[128:131],v[204:207],a[140:143],v[128:131]\ns_lshl_b32 s70,s54,12\ns_add_u32 s70,s70,147456\nv_add_u32 v249,s70,v247\nv_mfma_f32_16x16x32_bf16 v[132:135],v[204:207],a[156:159],v[132:135]\n"
"ds_read_b64_tr_b16 v[214:215],v225 offset:20992\nv_and_b32 v251,0x300,v247\nv_mfma_f32_16x16x32_bf16 v[132:135],v[200:203],a[152:155],v[132:135]\nds_read_b64_tr_b16 v[216:217],v225 offset:24576\nv_add_u32 v251,s12,v251\n"
"v_mfma_f32_16x16x32_bf16 v[132:135],v[196:199],a[148:151],v[132:135]\nds_read_b64_tr_b16 v[218:219],v225 offset:25088\nv_add_u32 v251,147456,v251\nv_mfma_f32_16x16x32_bf16 v[132:135],v[192:195],a[144:147],v[132:135]\n"
"buffer_load_dwordx4 v[0:3],v247,s[44:47],s8 offen sc1\nv_mfma_f32_16x16x32_bf16 v[136:139],v[192:195],a[160:163],v[136:139]\nds_read_b64_tr_b16 v[220:221],v225 offset:28672\n"
"v_mfma_f32_16x16x32_bf16 v[136:139],v[196:199],a[164:167],v[136:139]\nds_read_b64_tr_b16 v[222:223],v225 offset:29184\nv_mfma_f32_16x16x32_bf16 v[136:139],v[200:203],a[168:171],v[136:139]\n"
"v_mfma_f32_16x16x32_bf16 v[136:139],v[204:207],a[172:175],v[136:139]\nbuffer_load_dwordx4 v[4:7],v247,s[44:47],s8 offen offset:1024 sc1\nv_mfma_f32_16x16x32_bf16 v[140:143],v[204:207],a[188:191],v[140:143]\n"
"v_mfma_f32_16x16x32_bf16 v[140:143],v[200:203],a[184:187],v[140:143]\nv_mfma_f32_16x16x32_bf16 v[140:143],v[196:199],a[180:183],v[140:143]\ns_waitcnt lgkmcnt(0)\n"
"v_mfma_f32_16x16x32_bf16 v[140:143],v[192:195],a[176:179],v[140:143]\nbuffer_load_dwordx4 v[8:11],v247,s[44:47],s9 offen sc1\nv_mfma_f32_16x16x32_bf16 v[156:159],v[208:211],a[176:179],v[156:159]\n"
"ds_read_b64_tr_b16 v[192:193],v226 offset:16384\nv_mfma_f32_16x16x32_bf16 v[156:159],v[212:215],a[180:183],v[156:159]\nds_read_b64_tr_b16 v[194:195],v226 offset:16896\n"
"v_mfma_f32_16x16x32_bf16 v[156:159],v[216:219],a[184:187],v[156:159]\nds_read_b64_tr_b16 v[196:197],v226 offset:20480\nv_mfma_f32_16x16x32_bf16 v[156:159],v[220:223],a[188:191],v[156:159]\ns_add_u32 s70,s8,0x8000\n"
"s_add_u32 s71,s9,0x1000\nv_mfma_f32_16x16x32_bf16 v[152:155],v[220:223],a[172:175],v[152:155]\nds_read_b64_tr_b16 v[198:199],v226 offset:20992\nv_mfma_f32_16x16x32_bf16 v[152:155],v[216:219],a[168:171],v[152:155]\n"
"ds_read_b64_tr_b16 v[200:201],v226 offset:24576\nv_mfma_f32_16x16x32_bf16 v[152:155],v[212:215],a[164:167],v[152:155]\nds_read_b64_tr_b16 v[202:203],v226 offset:25088\n"
"v_mfma_f32_16x16x32_bf16 v[152:155],v[208:211],a[160:163],v[152:155]\nbuffer_load_dwordx4 v[12:15],v247,s[44:47],s70 offen sc1\nv_mfma_f32_16x16x32_bf16 v[148:151],v[208:211],a[144:147],v[148:151]\n"
"ds_read_b64_tr_b16 v[204:205],v226 offset:28672\nv_mfma_f32_16x16x32_bf16 v[148:151],v[212:215],a[148:151],v[148:151]\nds_read_b64_tr_b16 v[206:207],v226 offset:29184\n"
"v_mfma_f32_16x16x32_bf16 v[148:151],v[216:219],a[152:155],v[148:151]\nv_mfma_f32_16x16x32_bf16 v[148:151],v[220:223],a[156:159],v[148:151]\nbuffer_load_dwordx4 v[16:19],v247,s[44:47],s70 offen offset:1024 sc1\n"
"v_mfma_f32_16x16x32_bf16 v[144:147],v[220:223],a[140:143],v[144:147]\nv_mfma_f32_16x16x32_bf16 v[144:147],v[216:219],a[136:139],v[144:147]\nv_mfma_f32_16x16x32_bf16 v[144:147],v[212:215],a[132:135],v[144:147]\n"
"s_waitcnt lgkmcnt(0)\nv_mfma_f32_16x16x32_bf16 v[144:147],v[208:211],a[128:131],v[144:147]\nbuffer_load_dwordx4 v[20:23],v247,s[44:47],s71 offen sc1\nv_mfma_f32_16x16x32_bf16 v[160:163],v[192:195],a[128:131],v[160:163]\n"
"ds_read_b64_tr_b16 v[208:209],v227 offset:16384\ns_add_u32 s70,s10,0x1000\nv_mfma_f32_16x16x32_bf16 v[160:163],v[196:199],a[132:135],v[160:163]\nds_read_b64_tr_b16 v[210:211],v227 offset:16896\n"
"v_mfma_f32_16x16x32_bf16 v[160:163],v[200:203],a[136:139],v[160:163]\nds_read_b64_tr_b16 v[212:213],v227 offset:20480\nv_mfma_f32_16x16x32_bf16 v[160:163],v[204:207],a[140:143],v[160:163]\n"
"buffer_load_dwordx4 v[24:27],v247,s[44:47],s10 offen sc1\nv_mfma_f32_16x16x32_bf16 v[164:167],v[204:207],a[156:159],v[164:167]\nds_read_b64_tr_b16 v[214:215],v227 offset:20992\n"
"v_mfma_f32_16x16x32_bf16 v[164:167],v[200:203],a[152:155],v[164:167]\nds_read_b64_tr_b16 v[216:217],v227 offset:24576\nv_mfma_f32_16x16x32_bf16 v[164:167],v[196:199],a[148:151],v[164:167]\n"
"ds_read_b64_tr_b16 v[218:219],v227 offset:25088\nv_mfma_f32_16x16x32_bf16 v[164:167],v[192:195],a[144:147],v[164:167]\nbuffer_load_dwordx4 v[28:31],v247,s[44:47],s70 offen sc1\n"
"v_mfma_f32_16x16x32_bf16 v[168:171],v[192:195],a[160:163],v[168:171]\nds_read_b64_tr_b16 v[220:221],v227 offset:28672\nv_mfma_f32_16x16x32_bf16 v[168:171],v[196:199],a[164:167],v[168:171]\n"
"ds_read_b64_tr_b16 v[222:223],v227 offset:29184\nv_mfma_f32_16x16x32_bf16 v[168:171],v[200:203],a[168:171],v[168:171]\nv_mfma_f32_16x16x32_bf16 v[168:171],v[204:207],a[172:175],v[168:171]\n"
"buffer_load_dword v240,v248,s[44:47],s11 offen sc1\nv_mfma_f32_16x16x32_bf16 v[172:175],v[204:207],a[188:191],v[172:175]\nv_mfma_f32_16x16x32_bf16 v[172:175],v[200:203],a[184:187],v[172:175]\n"
"v_mfma_f32_16x16x32_bf16 v[172:175],v[196:199],a[180:183],v[172:175]\ns_waitcnt vmcnt(19) lgkmcnt(0)\nv_mfma_f32_16x16x32_bf16 v[172:175],v[192:195],a[176:179],v[172:175]\n"
"buffer_load_dword v241,v248,s[44:47],s11 offen offset:64 sc1\nv_mfma_f32_16x16x32_bf16 v[188:191],v[208:211],a[176:179],v[188:191]\ns_barrier\nv_mfma_f32_16x16x32_bf16 v[188:191],v[212:215],a[180:183],v[188:191]\n"
"ds_read_b64_tr_b16 v[192:193],v224 offset:32768\nv_mfma_f32_16x16x32_bf16 v[188:191],v[216:219],a[184:187],v[188:191]\nds_read_b64_tr_b16 v[194:195],v224 offset:33280\n"
"v_mfma_f32_16x16x32_bf16 v[188:191],v[220:223],a[188:191],v[188:191]\nbuffer_load_dwordx4 v247,s[36:39],s49 offen offset:0 sc1 lds\nv_mfma_f32_16x16x32_bf16 v[184:187],v[220:223],a[172:175],v[184:187]\n"
"ds_read_b64_tr_b16 v[196:197],v224 offset:36864\nv_mfma_f32_16x16x32_bf16 v[184:187],v[216:219],a[168:171],v[184:187]\nds_read_b64_tr_b16 v[198:199],v224 offset:37376\n"
"v_mfma_f32_16x16x32_bf16 v[184:187],v[212:215],a[164:167],v[184:187]\nds_read_b64_tr_b16 v[200:201],v224 offset:40960\nv_mfma_f32_16x16x32_bf16 v[184:187],v[208:211],a[160:163],v[184:187]\n"
"buffer_load_dwordx4 v247,s[36:39],s49 offen offset:1024 sc1 lds\nv_mfma_f32_16x16x32_bf16 v[180:183],v[208:211],a[144:147],v[180:183]\nds_read_b64_tr_b16 v[202:203],v224 offset:41472\n"
"v_mfma_f32_16x16x32_bf16 v[180:183],v[212:215],a[148:151],v[180:183]\nds_read_b64_tr_b16 v[204:205],v224 offset:45056\nv_mfma_f32_16x16x32_bf16 v[180:183],v[216:219],a[152:155],v[180:183]\n"
"ds_read_b64_tr_b16 v[206:207],v224 offset:45568\nv_mfma_f32_16x16x32_bf16 v[180:183],v[220:223],a[156:159],v[180:183]\nbuffer_load_dwordx4 v247,s[36:39],s49 offen offset:2048 sc1 lds\n"
"v_mfma_f32_16x16x32_bf16 v[176:179],v[220:223],a[140:143],v[176:179]\nv_mfma_f32_16x16x32_bf16 v[176:179],v[216:219],a[136:139],v[176:179]\nv_mfma_f32_16x16x32_bf16 v[176:179],v[212:215],a[132:135],v[176:179]\n"
"s_waitcnt lgkmcnt(0)\nv_mfma_f32_16x16x32_bf16 v[176:179],v[208:211],a[128:131],v[176:179]\nbuffer_load_dwordx4 v247,s[36:39],s49 offen offset:3072 sc1 lds\nv_mfma_f32_16x16x32_bf16 v[64:67],v[192:195],a[192:195],v[64:67]\n"
"ds_read_b64_tr_b16 v[208:209],v225 offset:32768\ns_add_u32 m0,m0,0x1000\nv_mfma_f32_16x16x32_bf16 v[64:67],v[196:199],a[196:199],v[64:67]\nds_read_b64_tr_b16 v[210:211],v225 offset:33280\n"
"v_mfma_f32_16x16x32_bf16 v[64:67],v[200:203],a[200:203],v[64:67]\nds_read_b64_tr_b16 v[212:213],v225 offset:36864\nv_mfma_f32_16x16x32_bf16 v[64:67],v[204:207],a[204:207],v[64:67]\n"
"buffer_load_dwordx4 v247,s[36:39],s55 offen offset:0 sc1 lds\nv_mfma_f32_16x16x32_bf16 v[68:71],v[204:207],a[220:223],v[68:71]\nds_read_b64_tr_b16 v[214:215],v225 offset:37376\n"
"v_mfma_f32_16x16x32_bf16 v[68:71],v[200:203],a[216:219],v[68:71]\nds_read_b64_tr_b16 v[216:217],v225 offset:40960\nv_mfma_f32_16x16x32_bf16 v[68:71],v[196:199],a[212:215],v[68:71]\nds_read_b64_tr_b16 v[218:219],v225 offset:41472\n"
"v_mfma_f32_16x16x32_bf16 v[68:71],v[192:195],a[208:211],v[68:71]\nbuffer_load_dwordx4 v247,s[36:39],s55 offen offset:1024 sc1 lds\nv_mfma_f32_16x16x32_bf16 v[72:75],v[192:195],a[224:227],v[72:75]\n"
"ds_read_b64_tr_b16 v[220:221],v225 offset:45056\nv_mfma_f32_16x16x32_bf16 v[72:75],v[196:199],a[228:231],v[72:75]\nds_read_b64_tr_b16 v[222:223],v225 offset:45568\nv_mfma_f32_16x16x32_bf16 v[72:75],v[200:203],a[232:235],v[72:75]\n"
"v_mfma_f32_16x16x32_bf16 v[72:75],v[204:207],a[236:239],v[72:75]\nbuffer_load_dwordx4 v247,s[36:39],s55 offen offset:2048 sc1 lds\nv_mfma_f32_16x16x32_bf16 v[76:79],v[204:207],a[252:255],v[76:79]\n"
"v_mfma_f32_16x16x32_bf16 v[76:79],v[200:203],a[248:251],v[76:79]\nv_mfma_f32_16x16x32_bf16 v[76:79],v[196:199],a[244:247],v[76:79]\ns_waitcnt lgkmcnt(0)\nv_mfma_f32_16x16x32_bf16 v[76:79],v[192:195],a[240:243],v[76:79]\n"
"buffer_load_dwordx4 v247,s[36:39],s55 offen offset:3072 sc1 lds\nv_mfma_f32_16x16x32_bf16 v[92:95],v[208:211],a[240:243],v[92:95]\nds_read_b64_tr_b16 v[192:193],v226 offset:32768\n"
"v_mfma_f32_16x16x32_bf16 v[92:95],v[212:215],a[244:247],v[92:95]\nds_read_b64_tr_b16 v[194:195],v226 offset:33280\nv_mfma_f32_16x16x32_bf16 v[92:95],v[216:219],a[248:251],v[92:95]\nds_read_b64_tr_b16 v[196:197],v226 offset:36864\n"
"v_mfma_f32_16x16x32_bf16 v[92:95],v[220:223],a[252:255],v[92:95]\ns_cmp_eq_u32 s54,3\ns_cselect_b32 s55,0,0x8000\ns_add_u32 s48,s49,s55\nv_mfma_f32_16x16x32_bf16 v[88:91],v[220:223],a[236:239],v[88:91]\n"
"ds_read_b64_tr_b16 v[198:199],v226 offset:37376\ns_add_u32 s50,s49,0x10000\nv_mfma_f32_16x16x32_bf16 v[88:91],v[216:219],a[232:235],v[88:91]\nds_read_b64_tr_b16 v[200:201],v226 offset:40960\ns_mov_b32 m0,s50\n"
"v_mfma_f32_16x16x32_bf16 v[88:91],v[212:215],a[228:231],v[88:91]\nds_read_b64_tr_b16 v[202:203],v226 offset:41472\ns_add_u32 s55,s48,0x1000\nv_mfma_f32_16x16x32_bf16 v[88:91],v[208:211],a[224:227],v[88:91]\ns_waitcnt vmcnt(8)\n"
"s_add_u32 s70,s13,147456\nv_add_f32 v0,v0,v12\nv_mfma_f32_16x16x32_bf16 v[84:87],v[208:211],a[208:211],v[84:87]\nds_read_b64_tr_b16 v[204:205],v226 offset:45056\nv_add_f32 v1,v1,v13\n"
"v_mfma_f32_16x16x32_bf16 v[84:87],v[212:215],a[212:215],v[84:87]\nds_read_b64_tr_b16 v[206:207],v226 offset:45568\nv_add_f32 v2,v2,v14\nv_mfma_f32_16x16x32_bf16 v[84:87],v[216:219],a[216:219],v[84:87]\n"
"buffer_load_dwordx4 v247,s[36:39],s48 offen offset:0 sc1 lds\nv_mfma_f32_16x16x32_bf16 v[84:87],v[220:223],a[220:223],v[84:87]\nv_add_f32 v3,v3,v15\nv_add_f32 v4,v4,v16\n"
"v_mfma_f32_16x16x32_bf16 v[80:83],v[220:223],a[204:207],v[80:83]\nv_add_f32 v5,v5,v17\nv_add_f32 v6,v6,v18\nv_mfma_f32_16x16x32_bf16 v[80:83],v[216:219],a[200:203],v[80:83]\nv_add_f32 v7,v7,v19\nv_add_f32 v8,v8,v20\n"
"v_mfma_f32_16x16x32_bf16 v[80:83],v[212:215],a[196:199],v[80:83]\ns_waitcnt lgkmcnt(0)\nv_add_f32 v9,v9,v21\nv_add_f32 v10,v10,v22\nv_mfma_f32_16x16x32_bf16 v[80:83],v[208:211],a[192:195],v[80:83]\n"
"buffer_load_dwordx4 v247,s[36:39],s48 offen offset:1024 sc1 lds\nv_mfma_f32_16x16x32_bf16 v[96:99],v[192:195],a[192:195],v[96:99]\nds_read_b64_tr_b16 v[208:209],v227 offset:32768\nv_add_f32 v11,v11,v23\n"
"v_mfma_f32_16x16x32_bf16 v[96:99],v[196:199],a[196:199],v[96:99]\nds_read_b64_tr_b16 v[210:211],v227 offset:33280\nv_lshlrev_b32 v32,16,v24\nv_mfma_f32_16x16x32_bf16 v[96:99],v[200:203],a[200:203],v[96:99]\n"
"ds_read_b64_tr_b16 v[212:213],v227 offset:36864\nv_and_b32 v33,0xffff0000,v24\nv_mfma_f32_16x16x32_bf16 v[96:99],v[204:207],a[204:207],v[96:99]\nds_write_b128 v249,v[0:3] offset:0\nv_lshlrev_b32 v34,16,v25\n"
"v_mfma_f32_16x16x32_bf16 v[100:103],v[204:207],a[220:223],v[100:103]\nds_read_b64_tr_b16 v[214:215],v227 offset:37376\nv_and_b32 v35,0xffff0000,v25\nv_mfma_f32_16x16x32_bf16 v[100:103],v[200:203],a[216:219],v[100:103]\n"
"ds_read_b64_tr_b16 v[216:217],v227 offset:40960\nv_lshlrev_b32 v36,16,v26\nv_mfma_f32_16x16x32_bf16 v[100:103],v[196:199],a[212:215],v[100:103]\nds_read_b64_tr_b16 v[218:219],v227 offset:41472\nv_and_b32 v37,0xffff0000,v26\n"
"v_mfma_f32_16x16x32_bf16 v[100:103],v[192:195],a[208:211],v[100:103]\nds_write_b128 v249,v[4:7] offset:1024\nv_lshlrev_b32 v38,16,v27\nv_mfma_f32_16x16x32_bf16 v[104:107],v[192:195],a[224:227],v[104:107]\n"
"ds_read_b64_tr_b16 v[220:221],v227 offset:45056\nv_and_b32 v39,0xffff0000,v27\nv_mfma_f32_16x16x32_bf16 v[104:107],v[196:199],a[228:231],v[104:107]\nds_read_b64_tr_b16 v[222:223],v227 offset:45568\nv_lshlrev_b32 v40,16,v28\n"
"v_mfma_f32_16x16x32_bf16 v[104:107],v[200:203],a[232:235],v[104:107]\nds_write_b128 v249,v[8:11] offset:2048\nv_and_b32 v41,0xffff0000,v28\nv_mfma_f32_16x16x32_bf16 v[104:107],v[204:207],a[236:239],v[104:107]\n"
"buffer_load_dwordx4 v247,s[36:39],s48 offen offset:2048 sc1 lds\nv_mfma_f32_16x16x32_bf16 v[108:111],v[204:207],a[252:255],v[108:111]\nv_lshlrev_b32 v42,16,v29\nv_and_b32 v43,0xffff0000,v29\n"
"v_mfma_f32_16x16x32_bf16 v[108:111],v[200:203],a[248:251],v[108:111]\nv_lshlrev_b32 v44,16,v30\nv_and_b32 v45,0xffff0000,v30\nv_mfma_f32_16x16x32_bf16 v[108:111],v[196:199],a[244:247],v[108:111]\ns_waitcnt vmcnt(23) lgkmcnt(1)\n"
"v_lshlrev_b32 v46,16,v31\nv_and_b32 v47,0xffff0000,v31\nv_mfma_f32_16x16x32_bf16 v[108:111],v[192:195],a[240:243],v[108:111]\nbuffer_load_dwordx4 v247,s[36:39],s48 offen offset:3072 sc1 lds\n"
"v_mfma_f32_16x16x32_bf16 v[124:127],v[208:211],a[240:243],v[124:127]\ns_barrier\ns_add_u32 m0,m0,0x1000\nv_mul_f32 v48,0x3fb8aa3b,v32\nv_mfma_f32_16x16x32_bf16 v[124:127],v[212:215],a[244:247],v[124:127]\n"
"ds_read_b64_tr_b16 v[192:193],v224 offset:49152\nv_mul_f32 v49,0x3fb8aa3b,v33\nv_mfma_f32_16x16x32_bf16 v[124:127],v[216:219],a[248:251],v[124:127]\nds_read_b64_tr_b16 v[194:195],v224 offset:49664\nv_mul_f32 v50,0x3fb8aa3b,v34\n"
"v_mfma_f32_16x16x32_bf16 v[124:127],v[220:223],a[252:255],v[124:127]\nbuffer_load_dwordx4 v247,s[36:39],s55 offen offset:0 sc1 lds\nv_mfma_f32_16x16x32_bf16 v[120:123],v[220:223],a[236:239],v[120:123]\n"
"ds_read_b64_tr_b16 v[196:197],v224 offset:53248\nv_mul_f32 v51,0x3fb8aa3b,v35\nv_mfma_f32_16x16x32_bf16 v[120:123],v[216:219],a[232:235],v[120:123]\nds_read_b64_tr_b16 v[198:199],v224 offset:53760\nv_mul_f32 v52,0x3fb8aa3b,v36\n"
"v_mfma_f32_16x16x32_bf16 v[120:123],v[212:215],a[228:231],v[120:123]\nds_read_b64_tr_b16 v[200:201],v224 offset:57344\nv_mul_f32 v53,0x3fb8aa3b,v37\nv_mfma_f32_16x16x32_bf16 v[120:123],v[208:211],a[224:227],v[120:123]\n"
"buffer_load_dwordx4 v247,s[36:39],s55 offen offset:1024 sc1 lds\nv_mfma_f32_16x16x32_bf16 v[116:119],v[208:211],a[208:211],v[116:119]\nds_read_b64_tr_b16 v[202:203],v224 offset:57856\nv_mul_f32 v54,0x3fb8aa3b,v38\n"
"v_mfma_f32_16x16x32_bf16 v[116:119],v[212:215],a[212:215],v[116:119]\nds_read_b64_tr_b16 v[204:205],v224 offset:61440\nv_mul_f32 v55,0x3fb8aa3b,v39\nv_mfma_f32_16x16x32_bf16 v[116:119],v[216:219],a[216:219],v[116:119]\n"
"ds_read_b64_tr_b16 v[206:207],v224 offset:61952\nv_mul_f32 v56,0x3fb8aa3b,v40\nv_mfma_f32_16x16x32_bf16 v[116:119],v[220:223],a[220:223],v[116:119]\nbuffer_load_dwordx4 v247,s[36:39],s55 offen offset:2048 sc1 lds\n"
"v_mfma_f32_16x16x32_bf16 v[112:115],v[220:223],a[204:207],v[112:115]\nv_mul_f32 v57,0x3fb8aa3b,v41\nv_mul_f32 v58,0x3fb8aa3b,v42\nv_mfma_f32_16x16x32_bf16 v[112:115],v[216:219],a[200:203],v[112:115]\nv_mul_f32 v59,0x3fb8aa3b,v43\n"
"v_mul_f32 v60,0x3fb8aa3b,v44\nv_mfma_f32_16x16x32_bf16 v[112:115],v[212:215],a[196:199],v[112:115]\ns_waitcnt lgkmcnt(0)\nv_mul_f32 v61,0x3fb8aa3b,v45\nv_mul_f32 v62,0x3fb8aa3b,v46\n"
"v_mfma_f32_16x16x32_bf16 v[112:115],v[208:211],a[192:195],v[112:115]\nbuffer_load_dwordx4 v247,s[36:39],s55 offen offset:3072 sc1 lds\nv_mfma_f32_16x16x32_bf16 v[128:131],v[192:195],a[192:195],v[128:131]\n"
"ds_read_b64_tr_b16 v[208:209],v225 offset:49152\nv_mul_f32 v63,0x3fb8aa3b,v47\nv_mfma_f32_16x16x32_bf16 v[128:131],v[196:199],a[196:199],v[128:131]\nds_read_b64_tr_b16 v[210:211],v225 offset:49664\n"
"v_mfma_f32_16x16x32_bf16 v[128:131],v[200:203],a[200:203],v[128:131]\nds_read_b64_tr_b16 v[212:213],v225 offset:53248\nv_mfma_f32_16x16x32_bf16 v[128:131],v[204:207],a[204:207],v[128:131]\ns_bitcmp1_b32 s54,0\n"
"s_cselect_b32 s51,s51,0x12000\nv_mfma_f32_16x16x32_bf16 v[132:135],v[204:207],a[220:223],v[132:135]\nds_read_b64_tr_b16 v[214:215],v225 offset:53760\nv_mfma_f32_16x16x32_bf16 v[132:135],v[200:203],a[216:219],v[132:135]\n"
"ds_read_b64_tr_b16 v[216:217],v225 offset:57344\nv_mfma_f32_16x16x32_bf16 v[132:135],v[196:199],a[212:215],v[132:135]\nds_read_b64_tr_b16 v[218:219],v225 offset:57856\n"
"v_mfma_f32_16x16x32_bf16 v[132:135],v[192:195],a[208:211],v[132:135]\nv_exp_f32_e64 v48,v48 clamp\nv_mfma_f32_16x16x32_bf16 v[136:139],v[192:195],a[224:227],v[136:139]\nds_read_b64_tr_b16 v[220:221],v225 offset:61440\n"
"v_max_f32 v32,0,v32\nv_mfma_f32_16x16x32_bf16 v[136:139],v[196:199],a[228:231],v[136:139]\nds_read_b64_tr_b16 v[222:223],v225 offset:61952\nv_add_f32 v32,0x358637bd,v32\n"
"v_mfma_f32_16x16x32_bf16 v[136:139],v[200:203],a[232:235],v[136:139]\nv_exp_f32_e64 v49,v49 clamp\nv_mfma_f32_16x16x32_bf16 v[136:139],v[204:207],a[236:239],v[136:139]\nv_max_f32 v33,0,v33\nv_add_f32 v33,0x358637bd,v33\n"
"v_mfma_f32_16x16x32_bf16 v[140:143],v[204:207],a[252:255],v[140:143]\nv_exp_f32_e64 v50,v50 clamp\nv_mfma_f32_16x16x32_bf16 v[140:143],v[200:203],a[248:251],v[140:143]\nv_max_f32 v34,0,v34\nv_add_f32 v34,0x358637bd,v34\n"
"v_mfma_f32_16x16x32_bf16 v[140:143],v[196:199],a[244:247],v[140:143]\ns_waitcnt lgkmcnt(0)\nv_exp_f32_e64 v51,v51 clamp\nv_mfma_f32_16x16x32_bf16 v[140:143],v[192:195],a[240:243],v[140:143]\nv_max_f32 v35,0,v35\n"
"v_add_f32 v35,0x358637bd,v35\nv_mfma_f32_16x16x32_bf16 v[156:159],v[208:211],a[240:243],v[156:159]\nds_read_b64_tr_b16 v[192:193],v226 offset:49152\nv_mfma_f32_16x16x32_bf16 v[156:159],v[212:215],a[244:247],v[156:159]\n"
"ds_read_b64_tr_b16 v[194:195],v226 offset:49664\nv_mfma_f32_16x16x32_bf16 v[156:159],v[216:219],a[248:251],v[156:159]\nds_read_b64_tr_b16 v[196:197],v226 offset:53248\n"
"v_mfma_f32_16x16x32_bf16 v[156:159],v[220:223],a[252:255],v[156:159]\nv_exp_f32_e64 v52,v52 clamp\nv_mfma_f32_16x16x32_bf16 v[152:155],v[220:223],a[236:239],v[152:155]\nds_read_b64_tr_b16 v[198:199],v226 offset:53760\n"
"v_max_f32 v36,0,v36\nv_mfma_f32_16x16x32_bf16 v[152:155],v[216:219],a[232:235],v[152:155]\nds_read_b64_tr_b16 v[200:201],v226 offset:57344\nv_add_f32 v36,0x358637bd,v36\n"
"v_mfma_f32_16x16x32_bf16 v[152:155],v[212:215],a[228:231],v[152:155]\nds_read_b64_tr_b16 v[202:203],v226 offset:57856\nv_mfma_f32_16x16x32_bf16 v[152:155],v[208:211],a[224:227],v[152:155]\nv_exp_f32_e64 v53,v53 clamp\n"
"v_mfma_f32_16x16x32_bf16 v[148:151],v[208:211],a[208:211],v[148:151]\nds_read_b64_tr_b16 v[204:205],v226 offset:61440\nv_max_f32 v37,0,v37\nv_mfma_f32_16x16x32_bf16 v[148:151],v[212:215],a[212:215],v[148:151]\n"
"ds_read_b64_tr_b16 v[206:207],v226 offset:61952\nv_add_f32 v37,0x358637bd,v37\nv_mfma_f32_16x16x32_bf16 v[148:151],v[216:219],a[216:219],v[148:151]\nv_exp_f32_e64 v54,v54 clamp\n"
"v_mfma_f32_16x16x32_bf16 v[148:151],v[220:223],a[220:223],v[148:151]\nv_max_f32 v38,0,v38\nv_add_f32 v38,0x358637bd,v38\nv_mfma_f32_16x16x32_bf16 v[144:147],v[220:223],a[204:207],v[144:147]\nv_exp_f32_e64 v55,v55 clamp\n"
"v_mfma_f32_16x16x32_bf16 v[144:147],v[216:219],a[200:203],v[144:147]\nv_max_f32 v39,0,v39\nv_add_f32 v39,0x358637bd,v39\nv_mfma_f32_16x16x32_bf16 v[144:147],v[212:215],a[196:199],v[144:147]\ns_waitcnt lgkmcnt(0)\n"
"v_exp_f32_e64 v56,v56 clamp\nv_mfma_f32_16x16x32_bf16 v[144:147],v[208:211],a[192:195],v[144:147]\nv_max_f32 v40,0,v40\nv_add_f32 v40,0x358637bd,v40\nv_mfma_f32_16x16x32_bf16 v[160:163],v[192:195],a[192:195],v[160:163]\n"
"ds_read_b64_tr_b16 v[208:209],v227 offset:49152\nv_mfma_f32_16x16x32_bf16 v[160:163],v[196:199],a[196:199],v[160:163]\nds_read_b64_tr_b16 v[210:211],v227 offset:49664\n"
"v_mfma_f32_16x16x32_bf16 v[160:163],v[200:203],a[200:203],v[160:163]\nds_read_b64_tr_b16 v[212:213],v227 offset:53248\nv_mfma_f32_16x16x32_bf16 v[160:163],v[204:207],a[204:207],v[160:163]\nv_exp_f32_e64 v57,v57 clamp\n"
"v_mfma_f32_16x16x32_bf16 v[164:167],v[204:207],a[220:223],v[164:167]\nds_read_b64_tr_b16 v[214:215],v227 offset:53760\nv_max_f32 v41,0,v41\nv_mfma_f32_16x16x32_bf16 v[164:167],v[200:203],a[216:219],v[164:167]\n"
"ds_read_b64_tr_b16 v[216:217],v227 offset:57344\nv_add_f32 v41,0x358637bd,v41\nv_mfma_f32_16x16x32_bf16 v[164:167],v[196:199],a[212:215],v[164:167]\nds_read_b64_tr_b16 v[218:219],v227 offset:57856\n"
"v_mfma_f32_16x16x32_bf16 v[164:167],v[192:195],a[208:211],v[164:167]\nv_exp_f32_e64 v58,v58 clamp\nv_mfma_f32_16x16x32_bf16 v[168:171],v[192:195],a[224:227],v[168:171]\nds_read_b64_tr_b16 v[220:221],v227 offset:61440\n"
"v_max_f32 v42,0,v42\nv_mfma_f32_16x16x32_bf16 v[168:171],v[196:199],a[228:231],v[168:171]\nds_read_b64_tr_b16 v[222:223],v227 offset:61952\nv_add_f32 v42,0x358637bd,v42\n"
"v_mfma_f32_16x16x32_bf16 v[168:171],v[200:203],a[232:235],v[168:171]\nv_exp_f32_e64 v59,v59 clamp\nv_mfma_f32_16x16x32_bf16 v[168:171],v[204:207],a[236:239],v[168:171]\nv_max_f32 v43,0,v43\nv_add_f32 v43,0x358637bd,v43\n"
"v_mfma_f32_16x16x32_bf16 v[172:175],v[204:207],a[252:255],v[172:175]\nv_exp_f32_e64 v60,v60 clamp\nv_mfma_f32_16x16x32_bf16 v[172:175],v[200:203],a[248:251],v[172:175]\nv_max_f32 v44,0,v44\nv_add_f32 v44,0x358637bd,v44\n"
"v_mfma_f32_16x16x32_bf16 v[172:175],v[196:199],a[244:247],v[172:175]\ns_waitcnt lgkmcnt(0)\nv_exp_f32_e64 v61,v61 clamp\nv_mfma_f32_16x16x32_bf16 v[172:175],v[192:195],a[240:243],v[172:175]\nv_max_f32 v45,0,v45\n"
"v_add_f32 v45,0x358637bd,v45\nv_mfma_f32_16x16x32_bf16 v[188:191],v[208:211],a[240:243],v[188:191]\nv_exp_f32_e64 v62,v62 clamp\nv_mfma_f32_16x16x32_bf16 v[188:191],v[212:215],a[244:247],v[188:191]\nv_max_f32 v46,0,v46\n"
"v_add_f32 v46,0x358637bd,v46\nv_mfma_f32_16x16x32_bf16 v[188:191],v[216:219],a[248:251],v[188:191]\nv_exp_f32_e64 v63,v63 clamp\nv_mfma_f32_16x16x32_bf16 v[188:191],v[220:223],a[252:255],v[188:191]\nv_max_f32 v47,0,v47\n"
"v_add_f32 v47,0x358637bd,v47\nv_mfma_f32_16x16x32_bf16 v[184:187],v[220:223],a[236:239],v[184:187]\nv_add_f32 v32,v32,v48\nv_add_f32 v33,v33,v49\nv_mfma_f32_16x16x32_bf16 v[184:187],v[216:219],a[232:235],v[184:187]\n"
"v_add_f32 v34,v34,v50\nv_add_f32 v35,v35,v51\nv_mfma_f32_16x16x32_bf16 v[184:187],v[212:215],a[228:231],v[184:187]\nv_add_f32 v36,v36,v52\nv_add_f32 v37,v37,v53\n"
"v_mfma_f32_16x16x32_bf16 v[184:187],v[208:211],a[224:227],v[184:187]\nv_add_f32 v38,v38,v54\nv_add_f32 v39,v39,v55\nv_mfma_f32_16x16x32_bf16 v[180:183],v[208:211],a[208:211],v[180:183]\nv_add_f32 v40,v40,v56\n"
"v_add_f32 v41,v41,v57\nv_mfma_f32_16x16x32_bf16 v[180:183],v[212:215],a[212:215],v[180:183]\nv_add_f32 v42,v42,v58\nv_add_f32 v43,v43,v59\nv_mfma_f32_16x16x32_bf16 v[180:183],v[216:219],a[216:219],v[180:183]\n"
"v_add_f32 v44,v44,v60\nv_add_f32 v45,v45,v61\nv_mfma_f32_16x16x32_bf16 v[180:183],v[220:223],a[220:223],v[180:183]\nv_add_f32 v46,v46,v62\nv_add_f32 v47,v47,v63\n"
"v_mfma_f32_16x16x32_bf16 v[176:179],v[220:223],a[204:207],v[176:179]\nv_cvt_pk_bf16_f32 v232,v32,v33\nv_cvt_pk_bf16_f32 v233,v34,v35\nv_mfma_f32_16x16x32_bf16 v[176:179],v[216:219],a[200:203],v[176:179]\n"
"v_cvt_pk_bf16_f32 v234,v36,v37\nv_cvt_pk_bf16_f32 v235,v38,v39\nv_mfma_f32_16x16x32_bf16 v[176:179],v[212:215],a[196:199],v[176:179]\nv_cvt_pk_bf16_f32 v236,v40,v41\nv_cvt_pk_bf16_f32 v237,v42,v43\n"
"v_mfma_f32_16x16x32_bf16 v[176:179],v[208:211],a[192:195],v[176:179]\nv_cvt_pk_bf16_f32 v238,v44,v45\nv_cvt_pk_bf16_f32 v239,v46,v47\ns_lshl_b32 s55,s54,13\ns_add_u32 s55,s55,0x18000\nv_add_u32 v250,s55,v247\n"
"v_and_b32 v228,0x33f,v247\ns_nop 3\nv_cvt_pk_bf16_f32 v0,v64,v65\nv_cvt_pk_bf16_f32 v1,v66,v67\nv_cvt_pk_bf16_f32 v16,v68,v69\nv_cvt_pk_bf16_f32 v17,v70,v71\nv_cvt_pk_bf16_f32 v2,v80,v81\nv_cvt_pk_bf16_f32 v3,v82,v83\n"
"v_cvt_pk_bf16_f32 v18,v84,v85\nv_cvt_pk_bf16_f32 v19,v86,v87\nv_cvt_pk_bf16_f32 v4,v96,v97\nv_cvt_pk_bf16_f32 v5,v98,v99\nv_cvt_pk_bf16_f32 v20,v100,v101\nv_cvt_pk_bf16_f32 v21,v102,v103\nv_cvt_pk_bf16_f32 v6,v112,v113\n"
"v_cvt_pk_bf16_f32 v7,v114,v115\nv_cvt_pk_bf16_f32 v22,v116,v117\nv_cvt_pk_bf16_f32 v23,v118,v119\nv_cvt_pk_bf16_f32 v8,v128,v129\nv_cvt_pk_bf16_f32 v9,v130,v131\nv_cvt_pk_bf16_f32 v24,v132,v133\nv_cvt_pk_bf16_f32 v25,v134,v135\n"
"v_cvt_pk_bf16_f32 v10,v144,v145\nv_cvt_pk_bf16_f32 v11,v146,v147\nv_cvt_pk_bf16_f32 v26,v148,v149\nv_cvt_pk_bf16_f32 v27,v150,v151\nv_cvt_pk_bf16_f32 v12,v160,v161\nv_cvt_pk_bf16_f32 v13,v162,v163\n"
"v_cvt_pk_bf16_f32 v28,v164,v165\nv_cvt_pk_bf16_f32 v29,v166,v167\nv_cvt_pk_bf16_f32 v14,v176,v177\nv_cvt_pk_bf16_f32 v15,v178,v179\nv_cvt_pk_bf16_f32 v30,v180,v181\nv_cvt_pk_bf16_f32 v31,v182,v183\nv_mov_b32 v249,v250\n"
"v_xor_b32 v250,0x4000,v250\nds_write_b128 v249,v[0:3] offset:0\nv_cvt_pk_bf16_f32 v32,v72,v73\nv_cvt_pk_bf16_f32 v33,v74,v75\nv_cvt_pk_bf16_f32 v48,v76,v77\nv_cvt_pk_bf16_f32 v49,v78,v79\nds_write_b128 v249,v[4:7] offset:1024\n"
"v_cvt_pk_bf16_f32 v34,v88,v89\nv_cvt_pk_bf16_f32 v35,v90,v91\nv_cvt_pk_bf16_f32 v50,v92,v93\nv_cvt_pk_bf16_f32 v51,v94,v95\nds_write_b128 v249,v[8:11] offset:2048\nv_cvt_pk_bf16_f32 v36,v104,v105\nv_cvt_pk_bf16_f32 v37,v106,v107\n"
"v_cvt_pk_bf16_f32 v52,v108,v109\nv_cvt_pk_bf16_f32 v53,v110,v111\nds_write_b128 v249,v[12:15] offset:3072\nv_cvt_pk_bf16_f32 v38,v120,v121\nv_cvt_pk_bf16_f32 v39,v122,v123\nv_cvt_pk_bf16_f32 v54,v124,v125\n"
"v_cvt_pk_bf16_f32 v55,v126,v127\nds_write_b128 v249,v[16:19] offset:4096\nv_cvt_pk_bf16_f32 v40,v136,v137\nv_cvt_pk_bf16_f32 v41,v138,v139\nv_cvt_pk_bf16_f32 v56,v140,v141\nv_cvt_pk_bf16_f32 v57,v142,v143\n"
"ds_write_b128 v249,v[20:23] offset:5120\nv_cvt_pk_bf16_f32 v42,v152,v153\nv_cvt_pk_bf16_f32 v43,v154,v155\nv_cvt_pk_bf16_f32 v58,v156,v157\nv_cvt_pk_bf16_f32 v59,v158,v159\nds_write_b128 v249,v[24:27] offset:6144\n"
"v_cvt_pk_bf16_f32 v44,v168,v169\nv_cvt_pk_bf16_f32 v45,v170,v171\nv_cvt_pk_bf16_f32 v60,v172,v173\nv_cvt_pk_bf16_f32 v61,v174,v175\nds_write_b128 v249,v[28:31] offset:7168\nv_cvt_pk_bf16_f32 v46,v184,v185\n"
"v_cvt_pk_bf16_f32 v47,v186,v187\nv_cvt_pk_bf16_f32 v62,v188,v189\nv_cvt_pk_bf16_f32 v63,v190,v191\ns_waitcnt vmcnt(8) lgkmcnt(0)\ns_barrier\nds_read_b128 v[192:195],v228 offset:0\nds_read_b128 v[196:199],v228 offset:1024\n"
"ds_read_b128 v[200:203],v228 offset:2048\nds_read_b128 v[204:207],v228 offset:3072\nds_read_b128 a[128:131],v249 offset:0\nds_read_b128 a[132:135],v249 offset:1024\nds_read_b128 a[136:139],v249 offset:2048\n"
"ds_read_b128 a[140:143],v249 offset:3072\nds_read_b128 a[144:147],v249 offset:4096\nds_read_b128 a[148:151],v249 offset:5120\nds_read_b128 a[152:155],v249 offset:6144\nds_read_b128 a[156:159],v249 offset:7168\n"
"v_add_u32 v248,147456,v247\ns_waitcnt lgkmcnt(8)\nv_mfma_f32_16x16x32_bf16 v[128:131],v[192:195],v[32:35],0\nds_read_b128 a[160:163],v250 offset:0\nv_mov_b32 v95,0\n"
"v_mfma_f32_16x16x32_bf16 v[128:131],v[196:199],v[36:39],v[128:131]\nds_read_b128 a[164:167],v250 offset:1024\nv_mov_b32 v94,0\nv_mfma_f32_16x16x32_bf16 v[128:131],v[200:203],v[40:43],v[128:131]\n"
"ds_read_b128 a[168:171],v250 offset:2048\nv_mov_b32 v93,0\nv_mfma_f32_16x16x32_bf16 v[128:131],v[204:207],v[44:47],v[128:131]\nds_read_b128 a[172:175],v250 offset:3072\nv_mov_b32 v92,0\n"
"v_mfma_f32_16x16x32_bf16 v[132:135],v[192:195],v[48:51],0\nds_read_b128 a[176:179],v250 offset:4096\nv_mov_b32 v91,0\nv_mfma_f32_16x16x32_bf16 v[132:135],v[196:199],v[52:55],v[132:135]\nds_read_b128 a[180:183],v250 offset:5120\n"
"v_mov_b32 v90,0\nv_mfma_f32_16x16x32_bf16 v[132:135],v[200:203],v[56:59],v[132:135]\nds_read_b128 a[184:187],v250 offset:6144\nv_mov_b32 v89,0\nv_mfma_f32_16x16x32_bf16 v[132:135],v[204:207],v[60:63],v[132:135]\n"
"ds_read_b128 a[188:191],v250 offset:7168\nv_mov_b32 v88,0\ns_waitcnt lgkmcnt(8)\nv_mfma_f32_16x16x32_bf16 v[136:139],v[192:195],a[128:131],0\nds_read_b128 v[96:99],v248 offset:0\nv_mov_b32 v87,0\n"
"v_mfma_f32_16x16x32_bf16 v[136:139],v[196:199],a[132:135],v[136:139]\nds_read_b128 v[100:103],v248 offset:1024\nv_mov_b32 v86,0\nv_mfma_f32_16x16x32_bf16 v[136:139],v[200:203],a[136:139],v[136:139]\n"
"ds_read_b128 v[104:107],v251 offset:2048\nv_mov_b32 v85,0\nv_mfma_f32_16x16x32_bf16 v[136:139],v[204:207],a[140:143],v[136:139]\nds_read_b128 v[108:111],v251 offset:2080\nv_mov_b32 v84,0\n"
"v_mfma_f32_16x16x32_bf16 v[140:143],v[192:195],a[144:147],0\nds_read_b128 v[112:115],v248 offset:4096\nv_mov_b32 v83,0\nv_mfma_f32_16x16x32_bf16 v[140:143],v[196:199],a[148:151],v[140:143]\n"
"ds_read_b128 v[116:119],v248 offset:5120\nv_mov_b32 v82,0\nv_mfma_f32_16x16x32_bf16 v[140:143],v[200:203],a[152:155],v[140:143]\nds_read_b128 v[120:123],v251 offset:6144\nv_mov_b32 v81,0\n"
"v_mfma_f32_16x16x32_bf16 v[140:143],v[204:207],a[156:159],v[140:143]\nds_read_b128 v[124:127],v251 offset:6176\nv_mov_b32 v80,0\ns_waitcnt lgkmcnt(8)\nv_mfma_f32_16x16x32_bf16 v[144:147],v[192:195],a[160:163],0\n"
"ds_read_b128 v[160:163],v248 offset:8192\nv_mov_b32 v79,0\nv_mfma_f32_16x16x32_bf16 v[144:147],v[196:199],a[164:167],v[144:147]\nds_read_b128 v[164:167],v248 offset:9216\nv_mov_b32 v78,0\n"
"v_mfma_f32_16x16x32_bf16 v[144:147],v[200:203],a[168:171],v[144:147]\nds_read_b128 v[168:171],v251 offset:10240\nv_mov_b32 v77,0\nv_mfma_f32_16x16x32_bf16 v[144:147],v[204:207],a[172:175],v[144:147]\n"
"ds_read_b128 v[172:175],v251 offset:10272\nv_mov_b32 v76,0\nv_mfma_f32_16x16x32_bf16 v[148:151],v[192:195],a[176:179],0\nds_read_b128 v[176:179],v248 offset:12288\nv_mov_b32 v75,0\n"
"v_mfma_f32_16x16x32_bf16 v[148:151],v[196:199],a[180:183],v[148:151]\nds_read_b128 v[180:183],v248 offset:13312\nv_mov_b32 v74,0\nv_mfma_f32_16x16x32_bf16 v[148:151],v[200:203],a[184:187],v[148:151]\n"
"ds_read_b128 v[184:187],v251 offset:14336\nv_mov_b32 v73,0\nv_mfma_f32_16x16x32_bf16 v[148:151],v[204:207],a[188:191],v[148:151]\nds_read_b128 v[188:191],v251 offset:14368\nv_mov_b32 v72,0\nds_read_b128 a[224:227],v246 offset:0\n"
"ds_read_b128 a[228:231],v246 offset:1024\nds_read_b128 a[232:235],v246 offset:2048\nds_read_b128 a[236:239],v246 offset:3072\ns_waitcnt lgkmcnt(4)\nv_pk_add_f32 v[96:97],v[96:97],v[112:113]\n"
"v_pk_add_f32 v[160:161],v[160:161],v[176:177]\nv_pk_add_f32 v[98:99],v[98:99],v[114:115]\nv_pk_add_f32 v[162:163],v[162:163],v[178:179]\nv_pk_add_f32 v[100:101],v[100:101],v[116:117]\n"
"v_pk_add_f32 v[164:165],v[164:165],v[180:181]\nv_pk_add_f32 v[102:103],v[102:103],v[118:119]\nv_pk_add_f32 v[166:167],v[166:167],v[182:183]\nv_pk_add_f32 v[104:105],v[104:105],v[120:121]\n"
"v_pk_add_f32 v[168:169],v[168:169],v[184:185]\nv_pk_add_f32 v[106:107],v[106:107],v[122:123]\nv_pk_add_f32 v[170:171],v[170:171],v[186:187]\nv_pk_add_f32 v[108:109],v[108:109],v[124:125]\n"
"v_pk_add_f32 v[172:173],v[172:173],v[188:189]\nv_pk_add_f32 v[110:111],v[110:111],v[126:127]\nv_pk_add_f32 v[174:175],v[174:175],v[190:191]\nv_pk_add_f32 v[96:97],v[96:97],v[160:161]\nv_pk_add_f32 v[98:99],v[98:99],v[162:163]\n"
"v_pk_add_f32 v[100:101],v[100:101],v[164:165]\nv_pk_add_f32 v[102:103],v[102:103],v[166:167]\nv_pk_add_f32 v[104:105],v[104:105],v[168:169]\nv_pk_add_f32 v[106:107],v[106:107],v[170:171]\n"
"v_pk_add_f32 v[108:109],v[108:109],v[172:173]\nv_pk_add_f32 v[110:111],v[110:111],v[174:175]\nv_cvt_pk_bf16_f32 v112,v96,v97\nv_cvt_pk_bf16_f32 v113,v98,v99\nv_mov_b32 v114,0\nv_mov_b32 v115,0\nv_cvt_pk_bf16_f32 v116,v100,v101\n"
"v_cvt_pk_bf16_f32 v117,v102,v103\nv_cvt_pk_bf16_f32 v118,v104,v105\nv_cvt_pk_bf16_f32 v119,v106,v107\nv_mov_b32 v120,0\nv_mov_b32 v121,0\nv_cvt_pk_bf16_f32 v122,v108,v109\nv_cvt_pk_bf16_f32 v123,v110,v111\ns_nop 1\n"
"v_mfma_f32_16x16x32_bf16 v[168:171],v[118:121],v[232:235],0\nds_read_b128 a[240:243],v246 offset:4096\nv_max3_f32 v128,v128,v129,v130\nv_mfma_f32_16x16x32_bf16 v[172:175],v[120:123],v[232:235],0\n"
"ds_read_b128 a[244:247],v246 offset:5120\nv_max3_f32 v132,v132,v133,v134\nv_mfma_f32_16x16x32_bf16 v[160:163],v[112:115],v[232:235],0\nds_read_b128 a[248:251],v246 offset:6144\nv_max3_f32 v136,v136,v137,v138\n"
"v_mfma_f32_16x16x32_bf16 v[164:167],v[114:117],v[232:235],0\nds_read_b128 a[252:255],v246 offset:7168\nv_max3_f32 v140,v140,v141,v142\nv_mfma_f32_16x16x32_bf16 v[184:187],v[118:121],v[236:239],0\n"
"ds_read_b128 a[192:195],v246 offset:8192\nv_max3_f32 v144,v144,v145,v146\nv_mfma_f32_16x16x32_bf16 v[188:191],v[120:123],v[236:239],0\nds_read_b128 a[196:199],v246 offset:9216\nv_max3_f32 v148,v148,v149,v150\n"
"v_mfma_f32_16x16x32_bf16 v[176:179],v[112:115],v[236:239],0\nds_read_b128 a[200:203],v246 offset:10240\nv_mfma_f32_16x16x32_bf16 v[180:183],v[114:117],v[236:239],0\nds_read_b128 a[204:207],v246 offset:11264\n"
"ds_read_b128 a[208:211],v246 offset:12288\nds_read_b128 a[212:215],v246 offset:13312\nds_read_b128 a[216:219],v246 offset:14336\nds_read_b128 a[220:223],v246 offset:15360\nv_min_f32_e64 v129,-v128,-v131\n"
"v_min_f32_e64 v133,-v132,-v135\nv_min_f32_e64 v224,-v136,-v139\nv_min_f32_e64 v228,-v140,-v143\nv_min_f32_e64 v145,-v144,-v147\nv_min_f32_e64 v149,-v148,-v151\nv_add_f32 v168,0x358637bd,v168\nv_add_f32 v172,0x358637bd,v172\n"
"v_add_f32 v184,0x358637bd,v184\nv_add_f32 v188,0x358637bd,v188\nv_rcp_f32 v168,v168\nv_rcp_f32 v172,v172\nv_rcp_f32 v184,v184\nv_rcp_f32 v188,v188\ns_nop 0\nv_mul_f32 v169,v168,v240\nv_mul_f32 v168,v168,v240\n"
"v_mul_f32 v173,v172,v240\nv_mul_f32 v172,v172,v240\nv_mul_f32 v185,v184,v241\nv_mul_f32 v184,v184,v241\nv_mul_f32 v189,v188,v241\nv_mul_f32 v188,v188,v241\nv_pk_mul_f32 v[160:161],v[160:161],v[168:169]\n"
"v_pk_mul_f32 v[162:163],v[162:163],v[168:169]\nv_pk_mul_f32 v[164:165],v[164:165],v[172:173]\nv_pk_mul_f32 v[166:167],v[166:167],v[172:173]\nv_cvt_pk_bf16_f32 v160,v160,v161\nv_cvt_pk_bf16_f32 v161,v162,v163\n"
"v_cvt_pk_bf16_f32 v162,v164,v165\nv_cvt_pk_bf16_f32 v163,v166,v167\nbuffer_store_dwordx4 v[160:163],v247,s[44:47],s13 offen sc1\nv_pk_mul_f32 v[176:177],v[176:177],v[184:185]\nv_pk_mul_f32 v[178:179],v[178:179],v[184:185]\n"
"v_pk_mul_f32 v[180:181],v[180:181],v[188:189]\nv_pk_mul_f32 v[182:183],v[182:183],v[188:189]\nv_cvt_pk_bf16_f32 v176,v176,v177\nv_cvt_pk_bf16_f32 v177,v178,v179\nv_cvt_pk_bf16_f32 v178,v180,v181\nv_cvt_pk_bf16_f32 v179,v182,v183\n"
"buffer_store_dwordx4 v[176:179],v247,s[44:47],s70 offen sc1\nv_mov_b32 v248,-1\nv_mov_b32 v249,-1\nv_mov_b32 v250,-1\nv_mov_b32 v251,-1\nv_mov_b32 v238,v129\nv_mov_b32 v239,v133\nv_mov_b32 v240,v145\nv_mov_b32 v241,v149\n"
"v_mov_b32 v225,v224\nv_mov_b32 v226,v224\nv_mov_b32 v227,v224\nv_mov_b32 v229,v228\nv_mov_b32 v230,v228\nv_mov_b32 v231,v228\nv_mov_b32 v192,v238\nv_mov_b32 v193,v238\nv_mov_b32 v194,v238\nv_mov_b32 v195,v238\n"
"v_mov_b32 v196,v239\nv_mov_b32 v197,v239\nv_mov_b32 v198,v239\nv_mov_b32 v199,v239\nv_mov_b32 v200,v238\nv_mov_b32 v201,v238\nv_mov_b32 v202,v238\nv_mov_b32 v203,v238\nv_mov_b32 v204,v239\nv_mov_b32 v205,v239\n"
"v_mov_b32 v206,v239\nv_mov_b32 v207,v239\nv_mov_b32 v208,v238\nv_mov_b32 v209,v238\nv_mov_b32 v210,v238\nv_mov_b32 v211,v238\nv_mov_b32 v212,v239\nv_mov_b32 v213,v239\nv_mov_b32 v214,v239\nv_mov_b32 v215,v239\n"
"v_mov_b32 v216,v238\nv_mov_b32 v217,v238\nv_mov_b32 v218,v238\nv_mov_b32 v219,v238\nv_mov_b32 v220,v239\nv_mov_b32 v221,v239\nv_mov_b32 v222,v239\nv_mov_b32 v223,v239\ns_cmp_lg_u32 s53,2\ns_cbranch_scc1 LP1_%=\n"
"s_cmp_lg_u32 s54,0\ns_cbranch_scc1 LW1_0_%=\nv_min_f32 v192,v238,v242\nv_min_f32 v193,v238,v243\nv_min_f32 v194,v238,v244\nv_min_f32 v195,v238,v245\nv_mov_b32 v200,s63\nv_mov_b32 v201,s63\nv_mov_b32 v202,s63\nv_mov_b32 v203,s63\n"
"v_min_f32 v204,v239,v242\nv_min_f32 v205,v239,v243\nv_min_f32 v206,v239,v244\nv_min_f32 v207,v239,v245\nv_mov_b32 v208,s63\nv_mov_b32 v209,s63\nv_mov_b32 v210,s63\nv_mov_b32 v211,s63\nv_mov_b32 v212,s63\nv_mov_b32 v213,s63\n"
"v_mov_b32 v214,s63\nv_mov_b32 v215,s63\nv_mov_b32 v216,s63\nv_mov_b32 v217,s63\nv_mov_b32 v218,s63\nv_mov_b32 v219,s63\nv_mov_b32 v220,s63\nv_mov_b32 v221,s63\nv_mov_b32 v222,s63\nv_mov_b32 v223,s63\nLW1_0_%=:\n"
"s_cmp_lg_u32 s54,1\ns_cbranch_scc1 LW1_1_%=\nv_min_f32 v208,v238,v242\nv_min_f32 v209,v238,v243\nv_min_f32 v210,v238,v244\nv_min_f32 v211,v238,v245\nv_mov_b32 v216,s63\nv_mov_b32 v217,s63\nv_mov_b32 v218,s63\nv_mov_b32 v219,s63\n"
"v_min_f32 v220,v239,v242\nv_min_f32 v221,v239,v243\nv_min_f32 v222,v239,v244\nv_min_f32 v223,v239,v245\nLW1_1_%=:\nLP1_%=:\ns_waitcnt lgkmcnt(0)\ns_nop 1\nv_mfma_f32_16x16x32_bf16 v[96:99],a[224:227],v[32:35],v[192:195]\n"
"v_mov_b32 v237,0\nv_mov_b32 v236,0\nv_mov_b32 v235,0\nv_mfma_f32_16x16x32_bf16 v[96:99],a[228:231],v[36:39],v[96:99]\nbuffer_store_dwordx4 v[248:251],v247,s[44:47],s15 offen sc1\nv_mov_b32 v234,0\nv_mov_b32 v233,0\n"
"v_mov_b32 v232,0\nv_mfma_f32_16x16x32_bf16 v[96:99],a[232:235],v[40:43],v[96:99]\nv_mov_b32 v71,0\nv_mov_b32 v70,0\nv_mov_b32 v69,0\nv_mfma_f32_16x16x32_bf16 v[96:99],a[236:239],v[44:47],v[96:99]\nv_mov_b32 v68,0\n"
"v_mov_b32 v67,0\nv_mov_b32 v66,0\nv_mfma_f32_16x16x32_bf16 v[100:103],a[252:255],v[44:47],v[200:203]\nv_mov_b32 v65,0\nv_mov_b32 v64,0\nv_mov_b32 v31,0\nv_mfma_f32_16x16x32_bf16 v[100:103],a[248:251],v[40:43],v[100:103]\n"
"s_add_u32 s15,s15,4096\nv_mov_b32 v30,0\nv_mov_b32 v29,0\nv_mov_b32 v28,0\nv_mfma_f32_16x16x32_bf16 v[100:103],a[244:247],v[36:39],v[100:103]\nv_mov_b32 v27,0\nv_mov_b32 v26,0\nv_mov_b32 v25,0\n"
"v_mfma_f32_16x16x32_bf16 v[100:103],a[240:243],v[32:35],v[100:103]\nv_mov_b32 v24,0\nv_mov_b32 v23,0\nv_mov_b32 v22,0\nv_mfma_f32_16x16x32_bf16 v[108:111],a[240:243],v[48:51],v[204:207]\nv_mov_b32 v21,0\nv_mov_b32 v20,0\n"
"v_mov_b32 v19,0\nv_mfma_f32_16x16x32_bf16 v[108:111],a[244:247],v[52:55],v[108:111]\nbuffer_store_dwordx4 v[248:251],v247,s[44:47],s15 offen sc1\nv_mov_b32 v18,0\nv_mov_b32 v17,0\nv_mov_b32 v16,0\n"
"v_mfma_f32_16x16x32_bf16 v[108:111],a[248:251],v[56:59],v[108:111]\nv_mov_b32 v15,0\nv_mov_b32 v14,0\nv_mov_b32 v13,0\nv_mfma_f32_16x16x32_bf16 v[108:111],a[252:255],v[60:63],v[108:111]\nv_mov_b32 v12,0\nv_mov_b32 v11,0\n"
"v_mov_b32 v10,0\nv_mfma_f32_16x16x32_bf16 v[104:107],a[236:239],v[60:63],v[196:199]\nv_mov_b32 v9,0\nv_mov_b32 v8,0\nv_mov_b32 v7,0\nv_mfma_f32_16x16x32_bf16 v[104:107],a[232:235],v[56:59],v[104:107]\ns_add_u32 s15,s15,4096\n"
"v_mov_b32 v6,0\nv_mov_b32 v5,0\nv_mov_b32 v4,0\nv_mfma_f32_16x16x32_bf16 v[104:107],a[228:231],v[52:55],v[104:107]\nv_mov_b32 v3,0\nv_mov_b32 v2,0\nv_mov_b32 v1,0\n"
"v_mfma_f32_16x16x32_bf16 v[104:107],a[224:227],v[48:51],v[104:107]\nv_mov_b32 v0,0\nv_mfma_f32_16x16x32_bf16 v[112:115],a[224:227],a[128:131],v[224:227]\nv_exp_f32 v96,v96\n"
"v_mfma_f32_16x16x32_bf16 v[112:115],a[228:231],a[132:135],v[112:115]\nbuffer_store_dwordx4 v[248:251],v247,s[44:47],s15 offen sc1\nv_add_f32 v234,v234,v96\nv_mfma_f32_16x16x32_bf16 v[112:115],a[232:235],a[136:139],v[112:115]\n"
"v_exp_f32 v97,v97\nv_mfma_f32_16x16x32_bf16 v[112:115],a[236:239],a[140:143],v[112:115]\nv_add_f32 v234,v234,v97\nv_cvt_pk_bf16_f32 v160,v96,v97\nv_mfma_f32_16x16x32_bf16 v[116:119],a[252:255],a[140:143],v[224:227]\n"
"v_exp_f32 v98,v98\nv_mfma_f32_16x16x32_bf16 v[116:119],a[248:251],a[136:139],v[116:119]\ns_add_u32 s15,s15,4096\nv_add_f32 v234,v234,v98\nv_mfma_f32_16x16x32_bf16 v[116:119],a[244:247],a[132:135],v[116:119]\nv_exp_f32 v99,v99\n"
"v_mfma_f32_16x16x32_bf16 v[116:119],a[240:243],a[128:131],v[116:119]\nv_add_f32 v234,v234,v99\nv_cvt_pk_bf16_f32 v161,v98,v99\nv_mfma_f32_16x16x32_bf16 v[124:127],a[240:243],a[144:147],v[228:231]\nv_exp_f32 v100,v100\n"
"v_mfma_f32_16x16x32_bf16 v[124:127],a[244:247],a[148:151],v[124:127]\nbuffer_store_dwordx4 v[248:251],v247,s[44:47],s15 offen sc1\nv_add_f32 v234,v234,v100\nv_mfma_f32_16x16x32_bf16 v[124:127],a[248:251],a[152:155],v[124:127]\n"
"v_exp_f32 v101,v101\nv_mfma_f32_16x16x32_bf16 v[124:127],a[252:255],a[156:159],v[124:127]\nv_add_f32 v234,v234,v101\nv_cvt_pk_bf16_f32 v162,v100,v101\nv_mfma_f32_16x16x32_bf16 v[120:123],a[236:239],a[156:159],v[228:231]\n"
"v_exp_f32 v102,v102\nv_mfma_f32_16x16x32_bf16 v[120:123],a[232:235],a[152:155],v[120:123]\ns_add_u32 s15,s15,4096\nv_add_f32 v234,v234,v102\nv_mfma_f32_16x16x32_bf16 v[120:123],a[228:231],a[148:151],v[120:123]\n"
"v_exp_f32 v103,v103\nv_mfma_f32_16x16x32_bf16 v[120:123],a[224:227],a[144:147],v[120:123]\nv_add_f32 v234,v234,v103\nv_cvt_pk_bf16_f32 v163,v102,v103\ns_nop 7\nv_exp_f32 v104,v104\nv_exp_f32 v105,v105\nv_add_f32 v235,v235,v104\n"
"v_exp_f32 v106,v106\nv_add_f32 v235,v235,v105\nv_cvt_pk_bf16_f32 v164,v104,v105\nv_exp_f32 v107,v107\nv_add_f32 v235,v235,v106\nv_exp_f32 v108,v108\nv_add_f32 v235,v235,v107\nv_cvt_pk_bf16_f32 v165,v106,v107\nv_exp_f32 v109,v109\n"
"v_add_f32 v235,v235,v108\nv_exp_f32 v110,v110\nv_add_f32 v235,v235,v109\nv_cvt_pk_bf16_f32 v166,v108,v109\nv_exp_f32 v111,v111\nv_add_f32 v235,v235,v110\nv_exp_f32 v112,v112\nv_add_f32 v235,v235,v111\n"
"v_cvt_pk_bf16_f32 v167,v110,v111\nv_exp_f32 v113,v113\nv_add_f32 v232,v232,v112\nv_exp_f32 v114,v114\nv_add_f32 v232,v232,v113\nv_cvt_pk_bf16_f32 v168,v112,v113\nv_exp_f32 v115,v115\nv_add_f32 v232,v232,v114\nv_exp_f32 v116,v116\n"
"v_add_f32 v232,v232,v115\nv_cvt_pk_bf16_f32 v169,v114,v115\nv_exp_f32 v117,v117\nv_add_f32 v232,v232,v116\nv_exp_f32 v118,v118\nv_add_f32 v232,v232,v117\nv_cvt_pk_bf16_f32 v170,v116,v117\nv_exp_f32 v119,v119\n"
"v_add_f32 v232,v232,v118\ns_nop 0\nv_add_f32 v232,v232,v119\nv_cvt_pk_bf16_f32 v171,v118,v119\nv_mfma_f32_16x16x32_bf16 v[128:131],a[192:195],v[32:35],v[208:211]\nv_exp_f32 v120,v120\n"
"v_mfma_f32_16x16x32_bf16 v[128:131],a[196:199],v[36:39],v[128:131]\nds_read_b128 a[224:227],v246 offset:16384\nv_add_f32 v233,v233,v120\nv_mfma_f32_16x16x32_bf16 v[128:131],a[200:203],v[40:43],v[128:131]\nv_exp_f32 v121,v121\n"
"v_mfma_f32_16x16x32_bf16 v[128:131],a[204:207],v[44:47],v[128:131]\nv_add_f32 v233,v233,v121\nv_cvt_pk_bf16_f32 v172,v120,v121\nv_mfma_f32_16x16x32_bf16 v[132:135],a[220:223],v[44:47],v[216:219]\nv_exp_f32 v122,v122\n"
"v_mfma_f32_16x16x32_bf16 v[132:135],a[216:219],v[40:43],v[132:135]\nds_read_b128 a[228:231],v246 offset:17408\nv_add_f32 v233,v233,v122\nv_mfma_f32_16x16x32_bf16 v[132:135],a[212:215],v[36:39],v[132:135]\nv_exp_f32 v123,v123\n"
"v_mfma_f32_16x16x32_bf16 v[132:135],a[208:211],v[32:35],v[132:135]\nv_add_f32 v233,v233,v123\nv_cvt_pk_bf16_f32 v173,v122,v123\nv_mfma_f32_16x16x32_bf16 v[140:143],a[208:211],v[48:51],v[220:223]\nv_exp_f32 v124,v124\n"
"v_mfma_f32_16x16x32_bf16 v[140:143],a[212:215],v[52:55],v[140:143]\nds_read_b128 a[232:235],v246 offset:18432\nv_add_f32 v233,v233,v124\nv_mfma_f32_16x16x32_bf16 v[140:143],a[216:219],v[56:59],v[140:143]\nv_exp_f32 v125,v125\n"
"v_mfma_f32_16x16x32_bf16 v[140:143],a[220:223],v[60:63],v[140:143]\nv_add_f32 v233,v233,v125\nv_cvt_pk_bf16_f32 v174,v124,v125\nv_mfma_f32_16x16x32_bf16 v[136:139],a[204:207],v[60:63],v[212:215]\nv_exp_f32 v126,v126\n"
"v_mfma_f32_16x16x32_bf16 v[136:139],a[200:203],v[56:59],v[136:139]\nds_read_b128 a[236:239],v246 offset:19456\nv_add_f32 v233,v233,v126\nv_mfma_f32_16x16x32_bf16 v[136:139],a[196:199],v[52:55],v[136:139]\nv_exp_f32 v127,v127\n"
"v_mfma_f32_16x16x32_bf16 v[136:139],a[192:195],v[48:51],v[136:139]\nv_add_f32 v233,v233,v127\nv_cvt_pk_bf16_f32 v175,v126,v127\nv_mfma_f32_16x16x32_bf16 v[144:147],a[192:195],a[128:131],v[224:227]\nv_exp_f32 v128,v128\n"
"v_mfma_f32_16x16x32_bf16 v[144:147],a[196:199],a[132:135],v[144:147]\nds_read_b128 a[240:243],v246 offset:20480\nv_add_f32 v234,v234,v128\nv_mfma_f32_16x16x32_bf16 v[144:147],a[200:203],a[136:139],v[144:147]\nv_exp_f32 v129,v129\n"
"v_mfma_f32_16x16x32_bf16 v[144:147],a[204:207],a[140:143],v[144:147]\nv_add_f32 v234,v234,v129\nv_cvt_pk_bf16_f32 v176,v128,v129\nv_mfma_f32_16x16x32_bf16 v[148:151],a[220:223],a[140:143],v[224:227]\nv_exp_f32 v130,v130\n"
"v_mfma_f32_16x16x32_bf16 v[148:151],a[216:219],a[136:139],v[148:151]\nds_read_b128 a[244:247],v246 offset:21504\nv_add_f32 v234,v234,v130\nv_mfma_f32_16x16x32_bf16 v[148:151],a[212:215],a[132:135],v[148:151]\nv_exp_f32 v131,v131\n"
"v_mfma_f32_16x16x32_bf16 v[148:151],a[208:211],a[128:131],v[148:151]\nv_add_f32 v234,v234,v131\nv_cvt_pk_bf16_f32 v177,v130,v131\nv_mfma_f32_16x16x32_bf16 v[156:159],a[208:211],a[144:147],v[228:231]\nv_exp_f32 v132,v132\n"
"v_mfma_f32_16x16x32_bf16 v[156:159],a[212:215],a[148:151],v[156:159]\nds_read_b128 a[248:251],v246 offset:22528\nv_add_f32 v234,v234,v132\nv_mfma_f32_16x16x32_bf16 v[156:159],a[216:219],a[152:155],v[156:159]\nv_exp_f32 v133,v133\n"
"v_mfma_f32_16x16x32_bf16 v[156:159],a[220:223],a[156:159],v[156:159]\nds_read_b128 a[252:255],v246 offset:23552\nv_add_f32 v234,v234,v133\nv_cvt_pk_bf16_f32 v178,v132,v133\n"
"v_mfma_f32_16x16x32_bf16 v[152:155],a[204:207],a[156:159],v[228:231]\nv_exp_f32 v134,v134\nv_mfma_f32_16x16x32_bf16 v[152:155],a[200:203],a[152:155],v[152:155]\nv_add_f32 v234,v234,v134\n"
"v_mfma_f32_16x16x32_bf16 v[152:155],a[196:199],a[148:151],v[152:155]\nv_exp_f32 v135,v135\nv_mfma_f32_16x16x32_bf16 v[152:155],a[192:195],a[144:147],v[152:155]\ns_waitcnt vmcnt(0) lgkmcnt(0)\nv_add_f32 v234,v234,v135\n"
"v_cvt_pk_bf16_f32 v179,v134,v135\ns_sub_u32 s52,s53,2\ns_cmp_eq_u32 s52,0\ns_cbranch_scc1 LA1_%=\n.p2align 6\nLL1_%=:\nv_mfma_f32_16x16x32_bf16 a[0:3],a[224:227],v[160:163],a[0:3]\ns_barrier\nv_exp_f32 v136,v136\n"
"v_xor_b32 v246,0x10000,v246\nv_mfma_f32_16x16x32_bf16 a[4:7],a[224:227],v[164:167],a[4:7]\nds_read_b128 a[192:195],v246 offset:0\nv_add_f32 v235,v235,v136\nv_mfma_f32_16x16x32_bf16 v[0:3],a[224:227],v[168:171],v[0:3]\n"
"s_xor_b32 s50,s50,0x10000\nv_exp_f32 v137,v137\nv_mfma_f32_16x16x32_bf16 v[4:7],a[224:227],v[172:175],v[4:7]\ns_add_u32 s48,s48,0x8000\nv_add_f32 v235,v235,v137\nv_cvt_pk_bf16_f32 v180,v136,v137\n"
"v_mfma_f32_16x16x32_bf16 v[12:15],a[228:231],v[172:175],v[12:15]\ns_mov_b32 m0,s50\nv_exp_f32 v138,v138\nv_mfma_f32_16x16x32_bf16 v[8:11],a[228:231],v[168:171],v[8:11]\nds_read_b128 a[196:199],v246 offset:1024\n"
"v_add_f32 v235,v235,v138\nv_mfma_f32_16x16x32_bf16 a[12:15],a[228:231],v[164:167],a[12:15]\ns_add_u32 s55,s48,0x1000\nv_exp_f32 v139,v139\nv_mfma_f32_16x16x32_bf16 a[8:11],a[228:231],v[160:163],a[8:11]\nv_add_f32 v235,v235,v139\n"
"v_cvt_pk_bf16_f32 v181,v138,v139\nv_mfma_f32_16x16x32_bf16 a[16:19],a[232:235],v[160:163],a[16:19]\nv_exp_f32 v140,v140\nv_mfma_f32_16x16x32_bf16 a[20:23],a[232:235],v[164:167],a[20:23]\nds_read_b128 a[200:203],v246 offset:2048\n"
"v_add_f32 v235,v235,v140\nv_mfma_f32_16x16x32_bf16 v[16:19],a[232:235],v[168:171],v[16:19]\nbuffer_load_dwordx4 v247,s[36:39],s48 offen offset:0 sc1 lds\nv_exp_f32 v141,v141\n"
"v_mfma_f32_16x16x32_bf16 v[20:23],a[232:235],v[172:175],v[20:23]\nv_add_f32 v235,v235,v141\nv_cvt_pk_bf16_f32 v182,v140,v141\nv_mfma_f32_16x16x32_bf16 v[28:31],a[236:239],v[172:175],v[28:31]\nv_exp_f32 v142,v142\n"
"v_mfma_f32_16x16x32_bf16 v[24:27],a[236:239],v[168:171],v[24:27]\nds_read_b128 a[204:207],v246 offset:3072\nv_add_f32 v235,v235,v142\nv_mfma_f32_16x16x32_bf16 a[28:31],a[236:239],v[164:167],a[28:31]\n"
"buffer_load_dwordx4 v247,s[36:39],s48 offen offset:1024 sc1 lds\nv_exp_f32 v143,v143\nv_mfma_f32_16x16x32_bf16 a[24:27],a[236:239],v[160:163],a[24:27]\nv_add_f32 v235,v235,v143\nv_cvt_pk_bf16_f32 v183,v142,v143\n"
"v_mfma_f32_16x16x32_bf16 a[32:35],a[240:243],v[160:163],a[32:35]\nv_exp_f32 v144,v144\nv_mfma_f32_16x16x32_bf16 a[36:39],a[240:243],v[164:167],a[36:39]\nds_read_b128 a[220:223],v246 offset:7168\nv_add_f32 v232,v232,v144\n"
"v_mfma_f32_16x16x32_bf16 v[64:67],a[240:243],v[168:171],v[64:67]\nbuffer_load_dwordx4 v247,s[36:39],s48 offen offset:2048 sc1 lds\nv_exp_f32 v145,v145\nv_mfma_f32_16x16x32_bf16 v[68:71],a[240:243],v[172:175],v[68:71]\n"
"v_add_f32 v232,v232,v145\nv_cvt_pk_bf16_f32 v184,v144,v145\nv_mfma_f32_16x16x32_bf16 v[76:79],a[244:247],v[172:175],v[76:79]\nv_exp_f32 v146,v146\nv_mfma_f32_16x16x32_bf16 v[72:75],a[244:247],v[168:171],v[72:75]\n"
"ds_read_b128 a[216:219],v246 offset:6144\nv_add_f32 v232,v232,v146\nv_mfma_f32_16x16x32_bf16 a[44:47],a[244:247],v[164:167],a[44:47]\nbuffer_load_dwordx4 v247,s[36:39],s48 offen offset:3072 sc1 lds\nv_exp_f32 v147,v147\n"
"v_mfma_f32_16x16x32_bf16 a[40:43],a[244:247],v[160:163],a[40:43]\ns_add_u32 m0,m0,0x1000\nv_add_f32 v232,v232,v147\nv_cvt_pk_bf16_f32 v185,v146,v147\nv_mfma_f32_16x16x32_bf16 a[48:51],a[248:251],v[160:163],a[48:51]\n"
"v_exp_f32 v148,v148\nv_mfma_f32_16x16x32_bf16 a[52:55],a[248:251],v[164:167],a[52:55]\nds_read_b128 a[212:215],v246 offset:5120\nv_add_f32 v232,v232,v148\nv_mfma_f32_16x16x32_bf16 v[80:83],a[248:251],v[168:171],v[80:83]\n"
"buffer_load_dwordx4 v247,s[36:39],s55 offen offset:0 sc1 lds\nv_exp_f32 v149,v149\nv_mfma_f32_16x16x32_bf16 v[84:87],a[248:251],v[172:175],v[84:87]\nv_add_f32 v232,v232,v149\nv_cvt_pk_bf16_f32 v186,v148,v149\n"
"v_mfma_f32_16x16x32_bf16 v[92:95],a[252:255],v[172:175],v[92:95]\nv_exp_f32 v150,v150\nv_mfma_f32_16x16x32_bf16 v[88:91],a[252:255],v[168:171],v[88:91]\nds_read_b128 a[208:211],v246 offset:4096\nv_add_f32 v232,v232,v150\n"
"v_mfma_f32_16x16x32_bf16 a[60:63],a[252:255],v[164:167],a[60:63]\nbuffer_load_dwordx4 v247,s[36:39],s55 offen offset:1024 sc1 lds\nv_exp_f32 v151,v151\nv_mfma_f32_16x16x32_bf16 a[56:59],a[252:255],v[160:163],a[56:59]\n"
"s_waitcnt lgkmcnt(1)\nv_add_f32 v232,v232,v151\nv_cvt_pk_bf16_f32 v187,v150,v151\nv_mfma_f32_16x16x32_bf16 v[96:99],a[192:195],v[32:35],v[192:195]\nv_exp_f32 v152,v152\n"
"v_mfma_f32_16x16x32_bf16 v[96:99],a[196:199],v[36:39],v[96:99]\nds_read_b128 a[224:227],v246 offset:24576\nv_add_f32 v233,v233,v152\nv_mfma_f32_16x16x32_bf16 v[96:99],a[200:203],v[40:43],v[96:99]\n"
"buffer_load_dwordx4 v247,s[36:39],s55 offen offset:2048 sc1 lds\nv_exp_f32 v153,v153\nv_mfma_f32_16x16x32_bf16 v[96:99],a[204:207],v[44:47],v[96:99]\nv_add_f32 v233,v233,v153\nv_cvt_pk_bf16_f32 v188,v152,v153\n"
"v_mfma_f32_16x16x32_bf16 v[100:103],a[220:223],v[44:47],v[200:203]\ns_waitcnt lgkmcnt(1)\nv_exp_f32 v154,v154\nv_mfma_f32_16x16x32_bf16 v[100:103],a[216:219],v[40:43],v[100:103]\nds_read_b128 a[228:231],v246 offset:25600\n"
"v_add_f32 v233,v233,v154\nv_mfma_f32_16x16x32_bf16 v[100:103],a[212:215],v[36:39],v[100:103]\nbuffer_load_dwordx4 v247,s[36:39],s55 offen offset:3072 sc1 lds\nv_exp_f32 v155,v155\n"
"v_mfma_f32_16x16x32_bf16 v[100:103],a[208:211],v[32:35],v[100:103]\nv_add_f32 v233,v233,v155\nv_cvt_pk_bf16_f32 v189,v154,v155\nv_mfma_f32_16x16x32_bf16 v[108:111],a[208:211],v[48:51],v[204:207]\nv_exp_f32 v156,v156\n"
"v_mfma_f32_16x16x32_bf16 v[108:111],a[212:215],v[52:55],v[108:111]\nds_read_b128 a[232:235],v246 offset:26624\nv_add_f32 v233,v233,v156\nv_mfma_f32_16x16x32_bf16 v[108:111],a[216:219],v[56:59],v[108:111]\nv_exp_f32 v157,v157\n"
"v_mfma_f32_16x16x32_bf16 v[108:111],a[220:223],v[60:63],v[108:111]\nv_add_f32 v233,v233,v157\nv_cvt_pk_bf16_f32 v190,v156,v157\nv_mfma_f32_16x16x32_bf16 v[104:107],a[204:207],v[60:63],v[196:199]\nv_exp_f32 v158,v158\n"
"v_mfma_f32_16x16x32_bf16 v[104:107],a[200:203],v[56:59],v[104:107]\nds_read_b128 a[236:239],v246 offset:27648\nv_add_f32 v233,v233,v158\nv_mfma_f32_16x16x32_bf16 v[104:107],a[196:199],v[52:55],v[104:107]\nv_exp_f32 v159,v159\n"
"v_mfma_f32_16x16x32_bf16 v[104:107],a[192:195],v[48:51],v[104:107]\nv_add_f32 v233,v233,v159\nv_cvt_pk_bf16_f32 v191,v158,v159\nv_mfma_f32_16x16x32_bf16 v[112:115],a[192:195],a[128:131],v[224:227]\nv_exp_f32 v96,v96\n"
"v_mfma_f32_16x16x32_bf16 v[112:115],a[196:199],a[132:135],v[112:115]\nds_read_b128 a[240:243],v246 offset:28672\nv_add_f32 v234,v234,v96\nv_mfma_f32_16x16x32_bf16 v[112:115],a[200:203],a[136:139],v[112:115]\nv_exp_f32 v97,v97\n"
"v_mfma_f32_16x16x32_bf16 v[112:115],a[204:207],a[140:143],v[112:115]\nv_add_f32 v234,v234,v97\nv_cvt_pk_bf16_f32 v160,v96,v97\nv_mfma_f32_16x16x32_bf16 v[116:119],a[220:223],a[140:143],v[224:227]\nv_exp_f32 v98,v98\n"
"v_mfma_f32_16x16x32_bf16 v[116:119],a[216:219],a[136:139],v[116:119]\nds_read_b128 a[244:247],v246 offset:29696\nv_add_f32 v234,v234,v98\nv_mfma_f32_16x16x32_bf16 v[116:119],a[212:215],a[132:135],v[116:119]\nv_exp_f32 v99,v99\n"
"v_mfma_f32_16x16x32_bf16 v[116:119],a[208:211],a[128:131],v[116:119]\nv_add_f32 v234,v234,v99\nv_cvt_pk_bf16_f32 v161,v98,v99\nv_mfma_f32_16x16x32_bf16 v[124:127],a[208:211],a[144:147],v[228:231]\nv_exp_f32 v100,v100\n"
"v_mfma_f32_16x16x32_bf16 v[124:127],a[212:215],a[148:151],v[124:127]\nds_read_b128 a[248:251],v246 offset:30720\nv_add_f32 v234,v234,v100\nv_mfma_f32_16x16x32_bf16 v[124:127],a[216:219],a[152:155],v[124:127]\nv_exp_f32 v101,v101\n"
"v_mfma_f32_16x16x32_bf16 v[124:127],a[220:223],a[156:159],v[124:127]\nv_add_f32 v234,v234,v101\nv_cvt_pk_bf16_f32 v162,v100,v101\nv_mfma_f32_16x16x32_bf16 v[120:123],a[204:207],a[156:159],v[228:231]\nv_exp_f32 v102,v102\n"
"v_mfma_f32_16x16x32_bf16 v[120:123],a[200:203],a[152:155],v[120:123]\nds_read_b128 a[252:255],v246 offset:31744\nv_add_f32 v234,v234,v102\nv_mfma_f32_16x16x32_bf16 v[120:123],a[196:199],a[148:151],v[120:123]\nv_exp_f32 v103,v103\n"
"v_mfma_f32_16x16x32_bf16 v[120:123],a[192:195],a[144:147],v[120:123]\ns_waitcnt lgkmcnt(1)\nv_add_f32 v234,v234,v103\nv_cvt_pk_bf16_f32 v163,v102,v103\nv_mfma_f32_16x16x32_bf16 a[0:3],a[224:227],v[176:179],a[0:3]\n"
"v_exp_f32 v104,v104\nv_mfma_f32_16x16x32_bf16 a[4:7],a[224:227],v[180:183],a[4:7]\nds_read_b128 a[192:195],v246 offset:8192\nv_add_f32 v235,v235,v104\nv_mfma_f32_16x16x32_bf16 v[0:3],a[224:227],v[184:187],v[0:3]\n"
"v_exp_f32 v105,v105\nv_mfma_f32_16x16x32_bf16 v[4:7],a[224:227],v[188:191],v[4:7]\nv_add_f32 v235,v235,v105\nv_cvt_pk_bf16_f32 v164,v104,v105\nv_mfma_f32_16x16x32_bf16 v[12:15],a[228:231],v[188:191],v[12:15]\n"
"s_waitcnt lgkmcnt(1)\nv_exp_f32 v106,v106\nv_mfma_f32_16x16x32_bf16 v[8:11],a[228:231],v[184:187],v[8:11]\nds_read_b128 a[196:199],v246 offset:9216\nv_add_f32 v235,v235,v106\n"
"v_mfma_f32_16x16x32_bf16 a[12:15],a[228:231],v[180:183],a[12:15]\nv_exp_f32 v107,v107\nv_mfma_f32_16x16x32_bf16 a[8:11],a[228:231],v[176:179],a[8:11]\nv_add_f32 v235,v235,v107\nv_cvt_pk_bf16_f32 v165,v106,v107\n"
"v_mfma_f32_16x16x32_bf16 a[16:19],a[232:235],v[176:179],a[16:19]\nv_exp_f32 v108,v108\nv_mfma_f32_16x16x32_bf16 a[20:23],a[232:235],v[180:183],a[20:23]\nds_read_b128 a[200:203],v246 offset:10240\nv_add_f32 v235,v235,v108\n"
"v_mfma_f32_16x16x32_bf16 v[16:19],a[232:235],v[184:187],v[16:19]\nv_exp_f32 v109,v109\nv_mfma_f32_16x16x32_bf16 v[20:23],a[232:235],v[188:191],v[20:23]\nv_add_f32 v235,v235,v109\nv_cvt_pk_bf16_f32 v166,v108,v109\n"
"v_mfma_f32_16x16x32_bf16 v[28:31],a[236:239],v[188:191],v[28:31]\nv_exp_f32 v110,v110\nv_mfma_f32_16x16x32_bf16 v[24:27],a[236:239],v[184:187],v[24:27]\nds_read_b128 a[204:207],v246 offset:11264\nv_add_f32 v235,v235,v110\n"
"v_mfma_f32_16x16x32_bf16 a[28:31],a[236:239],v[180:183],a[28:31]\nv_exp_f32 v111,v111\nv_mfma_f32_16x16x32_bf16 a[24:27],a[236:239],v[176:179],a[24:27]\nv_add_f32 v235,v235,v111\nv_cvt_pk_bf16_f32 v167,v110,v111\n"
"v_mfma_f32_16x16x32_bf16 a[32:35],a[240:243],v[176:179],a[32:35]\nv_exp_f32 v112,v112\nv_mfma_f32_16x16x32_bf16 a[36:39],a[240:243],v[180:183],a[36:39]\nds_read_b128 a[220:223],v246 offset:15360\nv_add_f32 v232,v232,v112\n"
"v_mfma_f32_16x16x32_bf16 v[64:67],a[240:243],v[184:187],v[64:67]\nv_exp_f32 v113,v113\nv_mfma_f32_16x16x32_bf16 v[68:71],a[240:243],v[188:191],v[68:71]\nv_add_f32 v232,v232,v113\nv_cvt_pk_bf16_f32 v168,v112,v113\n"
"v_mfma_f32_16x16x32_bf16 v[76:79],a[244:247],v[188:191],v[76:79]\nv_exp_f32 v114,v114\nv_mfma_f32_16x16x32_bf16 v[72:75],a[244:247],v[184:187],v[72:75]\nds_read_b128 a[216:219],v246 offset:14336\nv_add_f32 v232,v232,v114\n"
"v_mfma_f32_16x16x32_bf16 a[44:47],a[244:247],v[180:183],a[44:47]\nv_exp_f32 v115,v115\nv_mfma_f32_16x16x32_bf16 a[40:43],a[244:247],v[176:179],a[40:43]\nv_add_f32 v232,v232,v115\nv_cvt_pk_bf16_f32 v169,v114,v115\n"
"v_mfma_f32_16x16x32_bf16 a[48:51],a[248:251],v[176:179],a[48:51]\nv_exp_f32 v116,v116\nv_mfma_f32_16x16x32_bf16 a[52:55],a[248:251],v[180:183],a[52:55]\nds_read_b128 a[212:215],v246 offset:13312\nv_add_f32 v232,v232,v116\n"
"v_mfma_f32_16x16x32_bf16 v[80:83],a[248:251],v[184:187],v[80:83]\nv_exp_f32 v117,v117\nv_mfma_f32_16x16x32_bf16 v[84:87],a[248:251],v[188:191],v[84:87]\nv_add_f32 v232,v232,v117\nv_cvt_pk_bf16_f32 v170,v116,v117\n"
"v_mfma_f32_16x16x32_bf16 v[92:95],a[252:255],v[188:191],v[92:95]\nv_exp_f32 v118,v118\nv_mfma_f32_16x16x32_bf16 v[88:91],a[252:255],v[184:187],v[88:91]\nds_read_b128 a[208:211],v246 offset:12288\nv_add_f32 v232,v232,v118\n"
"v_mfma_f32_16x16x32_bf16 a[60:63],a[252:255],v[180:183],a[60:63]\nv_exp_f32 v119,v119\nv_mfma_f32_16x16x32_bf16 a[56:59],a[252:255],v[176:179],a[56:59]\ns_waitcnt lgkmcnt(1)\nv_add_f32 v232,v232,v119\n"
"v_cvt_pk_bf16_f32 v171,v118,v119\nv_mfma_f32_16x16x32_bf16 v[128:131],a[192:195],v[32:35],v[208:211]\nv_exp_f32 v120,v120\nv_mfma_f32_16x16x32_bf16 v[128:131],a[196:199],v[36:39],v[128:131]\n"
"ds_read_b128 a[224:227],v246 offset:16384\nv_add_f32 v233,v233,v120\nv_mfma_f32_16x16x32_bf16 v[128:131],a[200:203],v[40:43],v[128:131]\nv_exp_f32 v121,v121\nv_mfma_f32_16x16x32_bf16 v[128:131],a[204:207],v[44:47],v[128:131]\n"
"v_add_f32 v233,v233,v121\nv_cvt_pk_bf16_f32 v172,v120,v121\nv_mfma_f32_16x16x32_bf16 v[132:135],a[220:223],v[44:47],v[216:219]\ns_waitcnt lgkmcnt(1)\nv_exp_f32 v122,v122\n"
"v_mfma_f32_16x16x32_bf16 v[132:135],a[216:219],v[40:43],v[132:135]\nds_read_b128 a[228:231],v246 offset:17408\nv_add_f32 v233,v233,v122\nv_mfma_f32_16x16x32_bf16 v[132:135],a[212:215],v[36:39],v[132:135]\nv_exp_f32 v123,v123\n"
"v_mfma_f32_16x16x32_bf16 v[132:135],a[208:211],v[32:35],v[132:135]\nv_add_f32 v233,v233,v123\nv_cvt_pk_bf16_f32 v173,v122,v123\nv_mfma_f32_16x16x32_bf16 v[140:143],a[208:211],v[48:51],v[220:223]\nv_exp_f32 v124,v124\n"
"v_mfma_f32_16x16x32_bf16 v[140:143],a[212:215],v[52:55],v[140:143]\nds_read_b128 a[232:235],v246 offset:18432\nv_add_f32 v233,v233,v124\nv_mfma_f32_16x16x32_bf16 v[140:143],a[216:219],v[56:59],v[140:143]\nv_exp_f32 v125,v125\n"
"v_mfma_f32_16x16x32_bf16 v[140:143],a[220:223],v[60:63],v[140:143]\nv_add_f32 v233,v233,v125\nv_cvt_pk_bf16_f32 v174,v124,v125\nv_mfma_f32_16x16x32_bf16 v[136:139],a[204:207],v[60:63],v[212:215]\nv_exp_f32 v126,v126\n"
"v_mfma_f32_16x16x32_bf16 v[136:139],a[200:203],v[56:59],v[136:139]\nds_read_b128 a[236:239],v246 offset:19456\nv_add_f32 v233,v233,v126\nv_mfma_f32_16x16x32_bf16 v[136:139],a[196:199],v[52:55],v[136:139]\nv_exp_f32 v127,v127\n"
"v_mfma_f32_16x16x32_bf16 v[136:139],a[192:195],v[48:51],v[136:139]\nv_add_f32 v233,v233,v127\nv_cvt_pk_bf16_f32 v175,v126,v127\nv_mfma_f32_16x16x32_bf16 v[144:147],a[192:195],a[128:131],v[224:227]\nv_exp_f32 v128,v128\n"
"v_mfma_f32_16x16x32_bf16 v[144:147],a[196:199],a[132:135],v[144:147]\nds_read_b128 a[240:243],v246 offset:20480\nv_add_f32 v234,v234,v128\nv_mfma_f32_16x16x32_bf16 v[144:147],a[200:203],a[136:139],v[144:147]\nv_exp_f32 v129,v129\n"
"v_mfma_f32_16x16x32_bf16 v[144:147],a[204:207],a[140:143],v[144:147]\nv_add_f32 v234,v234,v129\nv_cvt_pk_bf16_f32 v176,v128,v129\nv_mfma_f32_16x16x32_bf16 v[148:151],a[220:223],a[140:143],v[224:227]\nv_exp_f32 v130,v130\n"
"v_mfma_f32_16x16x32_bf16 v[148:151],a[216:219],a[136:139],v[148:151]\nds_read_b128 a[244:247],v246 offset:21504\nv_add_f32 v234,v234,v130\nv_mfma_f32_16x16x32_bf16 v[148:151],a[212:215],a[132:135],v[148:151]\nv_exp_f32 v131,v131\n"
"v_mfma_f32_16x16x32_bf16 v[148:151],a[208:211],a[128:131],v[148:151]\nv_add_f32 v234,v234,v131\nv_cvt_pk_bf16_f32 v177,v130,v131\nv_mfma_f32_16x16x32_bf16 v[156:159],a[208:211],a[144:147],v[228:231]\ns_sub_u32 s52,s52,1\n"
"v_exp_f32 v132,v132\nv_mfma_f32_16x16x32_bf16 v[156:159],a[212:215],a[148:151],v[156:159]\nds_read_b128 a[248:251],v246 offset:22528\nv_add_f32 v234,v234,v132\nv_mfma_f32_16x16x32_bf16 v[156:159],a[216:219],a[152:155],v[156:159]\n"
"s_cmp_gt_u32 s52,1\nv_exp_f32 v133,v133\nv_mfma_f32_16x16x32_bf16 v[156:159],a[220:223],a[156:159],v[156:159]\nds_read_b128 a[252:255],v246 offset:23552\nv_add_f32 v234,v234,v133\nv_cvt_pk_bf16_f32 v178,v132,v133\n"
"v_mfma_f32_16x16x32_bf16 v[152:155],a[204:207],a[156:159],v[228:231]\nv_exp_f32 v134,v134\nv_mfma_f32_16x16x32_bf16 v[152:155],a[200:203],a[152:155],v[152:155]\nv_add_f32 v234,v234,v134\n"
"v_mfma_f32_16x16x32_bf16 v[152:155],a[196:199],a[148:151],v[152:155]\nv_exp_f32 v135,v135\nv_mfma_f32_16x16x32_bf16 v[152:155],a[192:195],a[144:147],v[152:155]\ns_waitcnt vmcnt(0) lgkmcnt(0)\nv_add_f32 v234,v234,v135\n"
"v_cvt_pk_bf16_f32 v179,v134,v135\ns_cbranch_scc1 LL1_%=\ns_cmp_eq_u32 s52,0\ns_cbranch_scc1 LA1_%=\ns_cmp_lg_u32 s54,0\ns_cbranch_scc1 LW2_0_%=\nv_min_f32 v192,v238,v242\nv_min_f32 v193,v238,v243\nv_min_f32 v194,v238,v244\n"
"v_min_f32 v195,v238,v245\nv_mov_b32 v200,s63\nv_mov_b32 v201,s63\nv_mov_b32 v202,s63\nv_mov_b32 v203,s63\nv_min_f32 v204,v239,v242\nv_min_f32 v205,v239,v243\nv_min_f32 v206,v239,v244\nv_min_f32 v207,v239,v245\nv_mov_b32 v208,s63\n"
"v_mov_b32 v209,s63\nv_mov_b32 v210,s63\nv_mov_b32 v211,s63\nv_mov_b32 v212,s63\nv_mov_b32 v213,s63\nv_mov_b32 v214,s63\nv_mov_b32 v215,s63\nv_mov_b32 v216,s63\nv_mov_b32 v217,s63\nv_mov_b32 v218,s63\nv_mov_b32 v219,s63\n"
"v_mov_b32 v220,s63\nv_mov_b32 v221,s63\nv_mov_b32 v222,s63\nv_mov_b32 v223,s63\nLW2_0_%=:\ns_cmp_lg_u32 s54,1\ns_cbranch_scc1 LW2_1_%=\nv_min_f32 v208,v238,v242\nv_min_f32 v209,v238,v243\nv_min_f32 v210,v238,v244\n"
"v_min_f32 v211,v238,v245\nv_mov_b32 v216,s63\nv_mov_b32 v217,s63\nv_mov_b32 v218,s63\nv_mov_b32 v219,s63\nv_min_f32 v220,v239,v242\nv_min_f32 v221,v239,v243\nv_min_f32 v222,v239,v244\nv_min_f32 v223,v239,v245\nLW2_1_%=:\n"
"s_branch LL1_%=\nLA1_%=:\ns_cmp_lg_u32 s54,0\ns_cbranch_scc1 LW3_0_%=\nv_mov_b32 v192,s63\nv_mov_b32 v193,s63\nv_mov_b32 v194,s63\nv_mov_b32 v195,s63\nv_mov_b32 v196,s63\nv_mov_b32 v197,s63\nv_mov_b32 v198,s63\n"
"v_mov_b32 v199,s63\nv_mov_b32 v204,s63\nv_mov_b32 v205,s63\nv_mov_b32 v206,s63\nv_mov_b32 v207,s63\nLW3_0_%=:\ns_cmp_lg_u32 s54,1\ns_cbranch_scc1 LW3_1_%=\nv_mov_b32 v192,s63\nv_mov_b32 v193,s63\nv_mov_b32 v194,s63\n"
"v_mov_b32 v195,s63\nv_mov_b32 v196,s63\nv_mov_b32 v197,s63\nv_mov_b32 v198,s63\nv_mov_b32 v199,s63\nv_mov_b32 v200,s63\nv_mov_b32 v201,s63\nv_mov_b32 v202,s63\nv_mov_b32 v203,s63\nv_mov_b32 v204,s63\nv_mov_b32 v205,s63\n"
"v_mov_b32 v206,s63\nv_mov_b32 v207,s63\nv_mov_b32 v208,s63\nv_mov_b32 v209,s63\nv_mov_b32 v210,s63\nv_mov_b32 v211,s63\nv_mov_b32 v212,s63\nv_mov_b32 v213,s63\nv_mov_b32 v214,s63\nv_mov_b32 v215,s63\nv_mov_b32 v220,s63\n"
"v_mov_b32 v221,s63\nv_mov_b32 v222,s63\nv_mov_b32 v223,s63\nLW3_1_%=:\ns_cmp_lg_u32 s54,2\ns_cbranch_scc1 LW3_2_%=\nv_min_f32 v192,v238,v242\nv_min_f32 v193,v238,v243\nv_min_f32 v194,v238,v244\nv_min_f32 v195,v238,v245\n"
"v_mov_b32 v200,s63\nv_mov_b32 v201,s63\nv_mov_b32 v202,s63\nv_mov_b32 v203,s63\nv_min_f32 v204,v239,v242\nv_min_f32 v205,v239,v243\nv_min_f32 v206,v239,v244\nv_min_f32 v207,v239,v245\nv_mov_b32 v208,s63\nv_mov_b32 v209,s63\n"
"v_mov_b32 v210,s63\nv_mov_b32 v211,s63\nv_mov_b32 v212,s63\nv_mov_b32 v213,s63\nv_mov_b32 v214,s63\nv_mov_b32 v215,s63\nv_mov_b32 v216,s63\nv_mov_b32 v217,s63\nv_mov_b32 v218,s63\nv_mov_b32 v219,s63\nv_mov_b32 v220,s63\n"
"v_mov_b32 v221,s63\nv_mov_b32 v222,s63\nv_mov_b32 v223,s63\nLW3_2_%=:\ns_cmp_lg_u32 s54,3\ns_cbranch_scc1 LW3_3_%=\nv_min_f32 v208,v238,v242\nv_min_f32 v209,v238,v243\nv_min_f32 v210,v238,v244\nv_min_f32 v211,v238,v245\n"
"v_mov_b32 v216,s63\nv_mov_b32 v217,s63\nv_mov_b32 v218,s63\nv_mov_b32 v219,s63\nv_min_f32 v220,v239,v242\nv_min_f32 v221,v239,v243\nv_min_f32 v222,v239,v244\nv_min_f32 v223,v239,v245\nLW3_3_%=:\ns_lshr_b32 s55,s54,1\n"
"s_add_u32 s55,s55,s53\ns_lshl_b32 s55,s55,15\ns_and_b32 s56,s54,1\ns_lshl_b32 s56,s56,14\ns_add_u32 s48,s55,s56\ns_add_u32 s49,s55,0x2000\ns_lshl_b32 s55,s53,15\ns_sub_u32 s55,s55,0x2000\ns_bitcmp1_b32 s54,0\n"
"s_cselect_b32 s49,s55,s49\ns_lshl_b32 s55,s54,13\ns_add_u32 s50,s50,s55\nv_mfma_f32_16x16x32_bf16 a[0:3],a[224:227],v[160:163],a[0:3]\ns_barrier\nv_exp_f32 v136,v136\nv_xor_b32 v246,0x10000,v246\n"
"v_mfma_f32_16x16x32_bf16 a[4:7],a[224:227],v[164:167],a[4:7]\nds_read_b128 a[192:195],v246 offset:0\nv_add_f32 v235,v235,v136\nv_mfma_f32_16x16x32_bf16 v[0:3],a[224:227],v[168:171],v[0:3]\ns_xor_b32 s50,s50,0x10000\n"
"v_exp_f32 v137,v137\nv_mfma_f32_16x16x32_bf16 v[4:7],a[224:227],v[172:175],v[4:7]\ns_mov_b32 m0,s50\nv_add_f32 v235,v235,v137\nv_cvt_pk_bf16_f32 v180,v136,v137\nv_mfma_f32_16x16x32_bf16 v[12:15],a[228:231],v[172:175],v[12:15]\n"
"s_add_u32 s55,s48,0x1000\nv_exp_f32 v138,v138\nv_mfma_f32_16x16x32_bf16 v[8:11],a[228:231],v[168:171],v[8:11]\nds_read_b128 a[196:199],v246 offset:1024\nv_add_f32 v235,v235,v138\n"
"v_mfma_f32_16x16x32_bf16 a[12:15],a[228:231],v[164:167],a[12:15]\nbuffer_load_dwordx4 v247,s[36:39],s48 offen offset:0 sc1 lds\nv_exp_f32 v139,v139\nv_mfma_f32_16x16x32_bf16 a[8:11],a[228:231],v[160:163],a[8:11]\n"
"v_add_f32 v235,v235,v139\nv_cvt_pk_bf16_f32 v181,v138,v139\nv_mfma_f32_16x16x32_bf16 a[16:19],a[232:235],v[160:163],a[16:19]\nv_exp_f32 v140,v140\nv_mfma_f32_16x16x32_bf16 a[20:23],a[232:235],v[164:167],a[20:23]\n"
"ds_read_b128 a[200:203],v246 offset:2048\nv_add_f32 v235,v235,v140\nv_mfma_f32_16x16x32_bf16 v[16:19],a[232:235],v[168:171],v[16:19]\nbuffer_load_dwordx4 v247,s[36:39],s48 offen offset:1024 sc1 lds\nv_exp_f32 v141,v141\n"
"v_mfma_f32_16x16x32_bf16 v[20:23],a[232:235],v[172:175],v[20:23]\nv_add_f32 v235,v235,v141\nv_cvt_pk_bf16_f32 v182,v140,v141\nv_mfma_f32_16x16x32_bf16 v[28:31],a[236:239],v[172:175],v[28:31]\nv_exp_f32 v142,v142\n"
"v_mfma_f32_16x16x32_bf16 v[24:27],a[236:239],v[168:171],v[24:27]\nds_read_b128 a[204:207],v246 offset:3072\nv_add_f32 v235,v235,v142\nv_mfma_f32_16x16x32_bf16 a[28:31],a[236:239],v[164:167],a[28:31]\n"
"buffer_load_dwordx4 v247,s[36:39],s48 offen offset:2048 sc1 lds\nv_exp_f32 v143,v143\nv_mfma_f32_16x16x32_bf16 a[24:27],a[236:239],v[160:163],a[24:27]\nv_add_f32 v235,v235,v143\nv_cvt_pk_bf16_f32 v183,v142,v143\n"
"v_mfma_f32_16x16x32_bf16 a[32:35],a[240:243],v[160:163],a[32:35]\nv_exp_f32 v144,v144\nv_mfma_f32_16x16x32_bf16 a[36:39],a[240:243],v[164:167],a[36:39]\nds_read_b128 a[220:223],v246 offset:7168\nv_add_f32 v232,v232,v144\n"
"v_mfma_f32_16x16x32_bf16 v[64:67],a[240:243],v[168:171],v[64:67]\nbuffer_load_dwordx4 v247,s[36:39],s48 offen offset:3072 sc1 lds\nv_exp_f32 v145,v145\nv_mfma_f32_16x16x32_bf16 v[68:71],a[240:243],v[172:175],v[68:71]\n"
"s_add_u32 m0,m0,0x1000\nv_add_f32 v232,v232,v145\nv_cvt_pk_bf16_f32 v184,v144,v145\nv_mfma_f32_16x16x32_bf16 v[76:79],a[244:247],v[172:175],v[76:79]\nv_exp_f32 v146,v146\n"
"v_mfma_f32_16x16x32_bf16 v[72:75],a[244:247],v[168:171],v[72:75]\nds_read_b128 a[216:219],v246 offset:6144\nv_add_f32 v232,v232,v146\nv_mfma_f32_16x16x32_bf16 a[44:47],a[244:247],v[164:167],a[44:47]\n"
"buffer_load_dwordx4 v247,s[36:39],s55 offen offset:0 sc1 lds\nv_exp_f32 v147,v147\nv_mfma_f32_16x16x32_bf16 a[40:43],a[244:247],v[160:163],a[40:43]\nv_add_f32 v232,v232,v147\nv_cvt_pk_bf16_f32 v185,v146,v147\n"
"v_mfma_f32_16x16x32_bf16 a[48:51],a[248:251],v[160:163],a[48:51]\nv_exp_f32 v148,v148\nv_mfma_f32_16x16x32_bf16 a[52:55],a[248:251],v[164:167],a[52:55]\nds_read_b128 a[212:215],v246 offset:5120\nv_add_f32 v232,v232,v148\n"
"v_mfma_f32_16x16x32_bf16 v[80:83],a[248:251],v[168:171],v[80:83]\nbuffer_load_dwordx4 v247,s[36:39],s55 offen offset:1024 sc1 lds\nv_exp_f32 v149,v149\nv_mfma_f32_16x16x32_bf16 v[84:87],a[248:251],v[172:175],v[84:87]\n"
"v_add_f32 v232,v232,v149\nv_cvt_pk_bf16_f32 v186,v148,v149\nv_mfma_f32_16x16x32_bf16 v[92:95],a[252:255],v[172:175],v[92:95]\nv_exp_f32 v150,v150\nv_mfma_f32_16x16x32_bf16 v[88:91],a[252:255],v[168:171],v[88:91]\n"
"ds_read_b128 a[208:211],v246 offset:4096\nv_add_f32 v232,v232,v150\nv_mfma_f32_16x16x32_bf16 a[60:63],a[252:255],v[164:167],a[60:63]\nbuffer_load_dwordx4 v247,s[36:39],s55 offen offset:2048 sc1 lds\nv_exp_f32 v151,v151\n"
"v_mfma_f32_16x16x32_bf16 a[56:59],a[252:255],v[160:163],a[56:59]\ns_waitcnt lgkmcnt(1)\nv_add_f32 v232,v232,v151\nv_cvt_pk_bf16_f32 v187,v150,v151\nv_mfma_f32_16x16x32_bf16 v[96:99],a[192:195],v[32:35],v[192:195]\n"
"v_exp_f32 v152,v152\nv_mfma_f32_16x16x32_bf16 v[96:99],a[196:199],v[36:39],v[96:99]\nds_read_b128 a[224:227],v246 offset:24576\nv_add_f32 v233,v233,v152\nv_mfma_f32_16x16x32_bf16 v[96:99],a[200:203],v[40:43],v[96:99]\n"
"buffer_load_dwordx4 v247,s[36:39],s55 offen offset:3072 sc1 lds\nv_exp_f32 v153,v153\nv_mfma_f32_16x16x32_bf16 v[96:99],a[204:207],v[44:47],v[96:99]\ns_add_u32 m0,m0,0x1000\nv_add_f32 v233,v233,v153\n"
"v_cvt_pk_bf16_f32 v188,v152,v153\nv_mfma_f32_16x16x32_bf16 v[100:103],a[220:223],v[44:47],v[200:203]\ns_waitcnt lgkmcnt(1)\nv_exp_f32 v154,v154\nv_mfma_f32_16x16x32_bf16 v[100:103],a[216:219],v[40:43],v[100:103]\n"
"ds_read_b128 a[228:231],v246 offset:25600\nv_add_f32 v233,v233,v154\nv_mfma_f32_16x16x32_bf16 v[100:103],a[212:215],v[36:39],v[100:103]\ns_add_u32 s55,s49,0x1000\nv_exp_f32 v155,v155\n"
"v_mfma_f32_16x16x32_bf16 v[100:103],a[208:211],v[32:35],v[100:103]\nv_add_f32 v233,v233,v155\nv_cvt_pk_bf16_f32 v189,v154,v155\nv_mfma_f32_16x16x32_bf16 v[108:111],a[208:211],v[48:51],v[204:207]\nv_exp_f32 v156,v156\n"
"v_mfma_f32_16x16x32_bf16 v[108:111],a[212:215],v[52:55],v[108:111]\nds_read_b128 a[232:235],v246 offset:26624\nv_add_f32 v233,v233,v156\nv_mfma_f32_16x16x32_bf16 v[108:111],a[216:219],v[56:59],v[108:111]\n"
"buffer_load_dwordx4 v247,s[36:39],s49 offen offset:0 sc1 lds\nv_exp_f32 v157,v157\nv_mfma_f32_16x16x32_bf16 v[108:111],a[220:223],v[60:63],v[108:111]\nv_add_f32 v233,v233,v157\nv_cvt_pk_bf16_f32 v190,v156,v157\n"
"v_mfma_f32_16x16x32_bf16 v[104:107],a[204:207],v[60:63],v[196:199]\nv_exp_f32 v158,v158\nv_mfma_f32_16x16x32_bf16 v[104:107],a[200:203],v[56:59],v[104:107]\nds_read_b128 a[236:239],v246 offset:27648\nv_add_f32 v233,v233,v158\n"
"v_mfma_f32_16x16x32_bf16 v[104:107],a[196:199],v[52:55],v[104:107]\nbuffer_load_dwordx4 v247,s[36:39],s49 offen offset:1024 sc1 lds\nv_exp_f32 v159,v159\nv_mfma_f32_16x16x32_bf16 v[104:107],a[192:195],v[48:51],v[104:107]\n"
"v_add_f32 v233,v233,v159\nv_cvt_pk_bf16_f32 v191,v158,v159\nv_mfma_f32_16x16x32_bf16 v[112:115],a[192:195],a[128:131],v[224:227]\nv_exp_f32 v96,v96\nv_mfma_f32_16x16x32_bf16 v[112:115],a[196:199],a[132:135],v[112:115]\n"
"ds_read_b128 a[240:243],v246 offset:28672\nv_add_f32 v234,v234,v96\nv_mfma_f32_16x16x32_bf16 v[112:115],a[200:203],a[136:139],v[112:115]\nbuffer_load_dwordx4 v247,s[36:39],s49 offen offset:2048 sc1 lds\nv_exp_f32 v97,v97\n"
"v_mfma_f32_16x16x32_bf16 v[112:115],a[204:207],a[140:143],v[112:115]\nv_add_f32 v234,v234,v97\nv_cvt_pk_bf16_f32 v160,v96,v97\nv_mfma_f32_16x16x32_bf16 v[116:119],a[220:223],a[140:143],v[224:227]\nv_exp_f32 v98,v98\n"
"v_mfma_f32_16x16x32_bf16 v[116:119],a[216:219],a[136:139],v[116:119]\nds_read_b128 a[244:247],v246 offset:29696\nv_add_f32 v234,v234,v98\nv_mfma_f32_16x16x32_bf16 v[116:119],a[212:215],a[132:135],v[116:119]\n"
"buffer_load_dwordx4 v247,s[36:39],s49 offen offset:3072 sc1 lds\nv_exp_f32 v99,v99\nv_mfma_f32_16x16x32_bf16 v[116:119],a[208:211],a[128:131],v[116:119]\ns_add_u32 m0,m0,0x1000\nv_add_f32 v234,v234,v99\n"
"v_cvt_pk_bf16_f32 v161,v98,v99\nv_mfma_f32_16x16x32_bf16 v[124:127],a[208:211],a[144:147],v[228:231]\nv_exp_f32 v100,v100\nv_mfma_f32_16x16x32_bf16 v[124:127],a[212:215],a[148:151],v[124:127]\n"
"ds_read_b128 a[248:251],v246 offset:30720\nv_add_f32 v234,v234,v100\nv_mfma_f32_16x16x32_bf16 v[124:127],a[216:219],a[152:155],v[124:127]\nbuffer_load_dwordx4 v247,s[36:39],s55 offen offset:0 sc1 lds\nv_exp_f32 v101,v101\n"
"v_mfma_f32_16x16x32_bf16 v[124:127],a[220:223],a[156:159],v[124:127]\nv_add_f32 v234,v234,v101\nv_cvt_pk_bf16_f32 v162,v100,v101\nv_mfma_f32_16x16x32_bf16 v[120:123],a[204:207],a[156:159],v[228:231]\nv_exp_f32 v102,v102\n"
"v_mfma_f32_16x16x32_bf16 v[120:123],a[200:203],a[152:155],v[120:123]\nds_read_b128 a[252:255],v246 offset:31744\nv_add_f32 v234,v234,v102\nv_mfma_f32_16x16x32_bf16 v[120:123],a[196:199],a[148:151],v[120:123]\n"
"buffer_load_dwordx4 v247,s[36:39],s55 offen offset:1024 sc1 lds\nv_exp_f32 v103,v103\nv_mfma_f32_16x16x32_bf16 v[120:123],a[192:195],a[144:147],v[120:123]\ns_waitcnt lgkmcnt(1)\nv_add_f32 v234,v234,v103\n"
"v_cvt_pk_bf16_f32 v163,v102,v103\nv_mfma_f32_16x16x32_bf16 a[0:3],a[224:227],v[176:179],a[0:3]\nv_exp_f32 v104,v104\nv_mfma_f32_16x16x32_bf16 a[4:7],a[224:227],v[180:183],a[4:7]\nds_read_b128 a[192:195],v246 offset:8192\n"
"v_add_f32 v235,v235,v104\nv_mfma_f32_16x16x32_bf16 v[0:3],a[224:227],v[184:187],v[0:3]\nbuffer_load_dwordx4 v247,s[36:39],s55 offen offset:2048 sc1 lds\nv_exp_f32 v105,v105\n"
"v_mfma_f32_16x16x32_bf16 v[4:7],a[224:227],v[188:191],v[4:7]\nv_add_f32 v235,v235,v105\nv_cvt_pk_bf16_f32 v164,v104,v105\nv_mfma_f32_16x16x32_bf16 v[12:15],a[228:231],v[188:191],v[12:15]\ns_waitcnt lgkmcnt(1)\n"
"v_exp_f32 v106,v106\nv_mfma_f32_16x16x32_bf16 v[8:11],a[228:231],v[184:187],v[8:11]\nds_read_b128 a[196:199],v246 offset:9216\nv_add_f32 v235,v235,v106\nv_mfma_f32_16x16x32_bf16 a[12:15],a[228:231],v[180:183],a[12:15]\n"
"buffer_load_dwordx4 v247,s[36:39],s55 offen offset:3072 sc1 lds\nv_exp_f32 v107,v107\nv_mfma_f32_16x16x32_bf16 a[8:11],a[228:231],v[176:179],a[8:11]\ns_add_u32 s49,s48,s51\nv_add_f32 v235,v235,v107\n"
"v_cvt_pk_bf16_f32 v165,v106,v107\nv_mfma_f32_16x16x32_bf16 a[16:19],a[232:235],v[176:179],a[16:19]\ns_add_u32 s48,s48,0x10000\nv_exp_f32 v108,v108\nv_mfma_f32_16x16x32_bf16 a[20:23],a[232:235],v[180:183],a[20:23]\n"
"ds_read_b128 a[200:203],v246 offset:10240\nv_add_f32 v235,v235,v108\nv_mfma_f32_16x16x32_bf16 v[16:19],a[232:235],v[184:187],v[16:19]\nv_exp_f32 v109,v109\nv_mfma_f32_16x16x32_bf16 v[20:23],a[232:235],v[188:191],v[20:23]\n"
"v_add_f32 v235,v235,v109\nv_cvt_pk_bf16_f32 v166,v108,v109\nv_mfma_f32_16x16x32_bf16 v[28:31],a[236:239],v[188:191],v[28:31]\nv_exp_f32 v110,v110\nv_mfma_f32_16x16x32_bf16 v[24:27],a[236:239],v[184:187],v[24:27]\n"
"ds_read_b128 a[204:207],v246 offset:11264\nv_add_f32 v235,v235,v110\nv_mfma_f32_16x16x32_bf16 a[28:31],a[236:239],v[180:183],a[28:31]\nv_exp_f32 v111,v111\nv_mfma_f32_16x16x32_bf16 a[24:27],a[236:239],v[176:179],a[24:27]\n"
"v_add_f32 v235,v235,v111\nv_cvt_pk_bf16_f32 v167,v110,v111\nv_mfma_f32_16x16x32_bf16 a[32:35],a[240:243],v[176:179],a[32:35]\nv_exp_f32 v112,v112\nv_mfma_f32_16x16x32_bf16 a[36:39],a[240:243],v[180:183],a[36:39]\n"
"ds_read_b128 a[220:223],v246 offset:15360\nv_add_f32 v232,v232,v112\nv_mfma_f32_16x16x32_bf16 v[64:67],a[240:243],v[184:187],v[64:67]\nv_exp_f32 v113,v113\nv_mfma_f32_16x16x32_bf16 v[68:71],a[240:243],v[188:191],v[68:71]\n"
"v_add_f32 v232,v232,v113\nv_cvt_pk_bf16_f32 v168,v112,v113\nv_mfma_f32_16x16x32_bf16 v[76:79],a[244:247],v[188:191],v[76:79]\nv_exp_f32 v114,v114\nv_mfma_f32_16x16x32_bf16 v[72:75],a[244:247],v[184:187],v[72:75]\n"
"ds_read_b128 a[216:219],v246 offset:14336\nv_add_f32 v232,v232,v114\nv_mfma_f32_16x16x32_bf16 a[44:47],a[244:247],v[180:183],a[44:47]\nv_exp_f32 v115,v115\nv_mfma_f32_16x16x32_bf16 a[40:43],a[244:247],v[176:179],a[40:43]\n"
"v_add_f32 v232,v232,v115\nv_cvt_pk_bf16_f32 v169,v114,v115\nv_mfma_f32_16x16x32_bf16 a[48:51],a[248:251],v[176:179],a[48:51]\nv_exp_f32 v116,v116\nv_mfma_f32_16x16x32_bf16 a[52:55],a[248:251],v[180:183],a[52:55]\n"
"ds_read_b128 a[212:215],v246 offset:13312\nv_add_f32 v232,v232,v116\nv_mfma_f32_16x16x32_bf16 v[80:83],a[248:251],v[184:187],v[80:83]\nv_exp_f32 v117,v117\nv_mfma_f32_16x16x32_bf16 v[84:87],a[248:251],v[188:191],v[84:87]\n"
"v_add_f32 v232,v232,v117\nv_cvt_pk_bf16_f32 v170,v116,v117\nv_mfma_f32_16x16x32_bf16 v[92:95],a[252:255],v[188:191],v[92:95]\nv_exp_f32 v118,v118\nv_mfma_f32_16x16x32_bf16 v[88:91],a[252:255],v[184:187],v[88:91]\n"
"ds_read_b128 a[208:211],v246 offset:12288\nv_add_f32 v232,v232,v118\nv_mfma_f32_16x16x32_bf16 a[60:63],a[252:255],v[180:183],a[60:63]\nv_exp_f32 v119,v119\nv_mfma_f32_16x16x32_bf16 a[56:59],a[252:255],v[176:179],a[56:59]\n"
"s_waitcnt lgkmcnt(1)\nv_add_f32 v232,v232,v119\nv_cvt_pk_bf16_f32 v171,v118,v119\nv_mfma_f32_16x16x32_bf16 v[128:131],a[192:195],v[32:35],v[208:211]\nv_exp_f32 v120,v120\n"
"v_mfma_f32_16x16x32_bf16 v[128:131],a[196:199],v[36:39],v[128:131]\nds_read_b128 a[224:227],v246 offset:16384\nv_add_f32 v233,v233,v120\nv_mfma_f32_16x16x32_bf16 v[128:131],a[200:203],v[40:43],v[128:131]\nv_exp_f32 v121,v121\n"
"v_mfma_f32_16x16x32_bf16 v[128:131],a[204:207],v[44:47],v[128:131]\nv_add_f32 v233,v233,v121\nv_cvt_pk_bf16_f32 v172,v120,v121\nv_mfma_f32_16x16x32_bf16 v[132:135],a[220:223],v[44:47],v[216:219]\ns_waitcnt lgkmcnt(1)\n"
"v_exp_f32 v122,v122\nv_mfma_f32_16x16x32_bf16 v[132:135],a[216:219],v[40:43],v[132:135]\nds_read_b128 a[228:231],v246 offset:17408\nv_add_f32 v233,v233,v122\nv_mfma_f32_16x16x32_bf16 v[132:135],a[212:215],v[36:39],v[132:135]\n"
"v_exp_f32 v123,v123\nv_mfma_f32_16x16x32_bf16 v[132:135],a[208:211],v[32:35],v[132:135]\nv_add_f32 v233,v233,v123\nv_cvt_pk_bf16_f32 v173,v122,v123\nv_mfma_f32_16x16x32_bf16 v[140:143],a[208:211],v[48:51],v[220:223]\n"
"v_exp_f32 v124,v124\nv_mfma_f32_16x16x32_bf16 v[140:143],a[212:215],v[52:55],v[140:143]\nds_read_b128 a[232:235],v246 offset:18432\nv_add_f32 v233,v233,v124\nv_mfma_f32_16x16x32_bf16 v[140:143],a[216:219],v[56:59],v[140:143]\n"
"v_exp_f32 v125,v125\nv_mfma_f32_16x16x32_bf16 v[140:143],a[220:223],v[60:63],v[140:143]\nv_add_f32 v233,v233,v125\nv_cvt_pk_bf16_f32 v174,v124,v125\nv_mfma_f32_16x16x32_bf16 v[136:139],a[204:207],v[60:63],v[212:215]\n"
"v_exp_f32 v126,v126\nv_mfma_f32_16x16x32_bf16 v[136:139],a[200:203],v[56:59],v[136:139]\nds_read_b128 a[236:239],v246 offset:19456\nv_add_f32 v233,v233,v126\nv_mfma_f32_16x16x32_bf16 v[136:139],a[196:199],v[52:55],v[136:139]\n"
"v_exp_f32 v127,v127\nv_mfma_f32_16x16x32_bf16 v[136:139],a[192:195],v[48:51],v[136:139]\nv_add_f32 v233,v233,v127\nv_cvt_pk_bf16_f32 v175,v126,v127\nv_mfma_f32_16x16x32_bf16 v[144:147],a[192:195],a[128:131],v[224:227]\n"
"v_exp_f32 v128,v128\nv_mfma_f32_16x16x32_bf16 v[144:147],a[196:199],a[132:135],v[144:147]\nds_read_b128 a[240:243],v246 offset:20480\nv_add_f32 v234,v234,v128\nv_mfma_f32_16x16x32_bf16 v[144:147],a[200:203],a[136:139],v[144:147]\n"
"v_exp_f32 v129,v129\nv_mfma_f32_16x16x32_bf16 v[144:147],a[204:207],a[140:143],v[144:147]\nv_add_f32 v234,v234,v129\nv_cvt_pk_bf16_f32 v176,v128,v129\nv_mfma_f32_16x16x32_bf16 v[148:151],a[220:223],a[140:143],v[224:227]\n"
"v_exp_f32 v130,v130\nv_mfma_f32_16x16x32_bf16 v[148:151],a[216:219],a[136:139],v[148:151]\nds_read_b128 a[244:247],v246 offset:21504\nv_add_f32 v234,v234,v130\nv_mfma_f32_16x16x32_bf16 v[148:151],a[212:215],a[132:135],v[148:151]\n"
"v_exp_f32 v131,v131\nv_mfma_f32_16x16x32_bf16 v[148:151],a[208:211],a[128:131],v[148:151]\nv_add_f32 v234,v234,v131\nv_cvt_pk_bf16_f32 v177,v130,v131\nv_mfma_f32_16x16x32_bf16 v[156:159],a[208:211],a[144:147],v[228:231]\n"
"v_exp_f32 v132,v132\nv_mfma_f32_16x16x32_bf16 v[156:159],a[212:215],a[148:151],v[156:159]\nds_read_b128 a[248:251],v246 offset:22528\nv_add_f32 v234,v234,v132\nv_mfma_f32_16x16x32_bf16 v[156:159],a[216:219],a[152:155],v[156:159]\n"
"v_exp_f32 v133,v133\nv_mfma_f32_16x16x32_bf16 v[156:159],a[220:223],a[156:159],v[156:159]\nds_read_b128 a[252:255],v246 offset:23552\nv_add_f32 v234,v234,v133\nv_cvt_pk_bf16_f32 v178,v132,v133\n"
"v_mfma_f32_16x16x32_bf16 v[152:155],a[204:207],a[156:159],v[228:231]\nv_exp_f32 v134,v134\nv_mfma_f32_16x16x32_bf16 v[152:155],a[200:203],a[152:155],v[152:155]\nv_add_f32 v234,v234,v134\n"
"v_mfma_f32_16x16x32_bf16 v[152:155],a[196:199],a[148:151],v[152:155]\nv_exp_f32 v135,v135\nv_mfma_f32_16x16x32_bf16 v[152:155],a[192:195],a[144:147],v[152:155]\ns_waitcnt vmcnt(0) lgkmcnt(0)\nv_add_f32 v234,v234,v135\n"
"v_cvt_pk_bf16_f32 v179,v134,v135\nv_and_b32 v248,0xf0,v247\nv_lshrrev_b32 v248,2,v248\ns_lshl_b32 s56,s58,6\ns_add_u32 s56,s56,70025216\nbuffer_load_dword v238,v248,s[44:47],s56 offen sc1\n"
"buffer_load_dword v239,v248,s[44:47],s56 offen offset:64 sc1\nv_mov_b32 v192,v240\nv_mov_b32 v193,v240\nv_mov_b32 v194,v240\nv_mov_b32 v195,v240\nv_mov_b32 v196,v241\nv_mov_b32 v197,v241\nv_mov_b32 v198,v241\nv_mov_b32 v199,v241\n"
"s_lshr_b32 s55,s54,1\ns_lshl_b32 s55,s55,15\nv_add_u32 v246,s55,v246\ns_cmp_lg_u32 s53,16\ns_cbranch_scc1 LA2_%=\ns_getpc_b64 s[72:73]\n.Lgm_pc1:\ns_add_u32 s72,s72,.Lgm_tail-.Lgm_pc1\ns_addc_u32 s73,s73,0\n"
"s_and_b32 s73,s73,0xffff\ns_mov_b32 s74,.Lgm_code_end-.Lgm_tail\ns_mov_b32 s75,0x20000\ns_lshr_b32 s55,s4,3\ns_lshl_b32 s55,s55,2\ns_add_u32 s55,s55,s54\ns_lshl_b32 s55,s55,9\nv_lshrrev_b32 v249,1,v247\n"
"buffer_load_dword v251,v249,s[72:75],s55 offen\ns_cmp_lg_u32 s54,0\ns_cbranch_scc1 LW4_0_%=\nv_min_f32 v32,v224,v242\nv_min_f32 v33,v224,v243\nv_min_f32 v34,v224,v244\nv_min_f32 v35,v224,v245\nv_mov_b32 v36,v228\n"
"v_mov_b32 v37,v228\nv_mov_b32 v38,v228\nv_mov_b32 v39,v228\nv_mov_b32 v40,s63\nv_mov_b32 v41,s63\nv_mov_b32 v42,s63\nv_mov_b32 v43,s63\nv_min_f32 v44,v228,v242\nv_min_f32 v45,v228,v243\nv_min_f32 v46,v228,v244\n"
"v_min_f32 v47,v228,v245\nv_mov_b32 v48,s63\nv_mov_b32 v49,s63\nv_mov_b32 v50,s63\nv_mov_b32 v51,s63\nv_mov_b32 v52,s63\nv_mov_b32 v53,s63\nv_mov_b32 v54,s63\nv_mov_b32 v55,s63\nv_mov_b32 v56,s63\nv_mov_b32 v57,s63\n"
"v_mov_b32 v58,s63\nv_mov_b32 v59,s63\nv_mov_b32 v60,s63\nv_mov_b32 v61,s63\nv_mov_b32 v62,s63\nv_mov_b32 v63,s63\nv_mov_b32 v200,v240\nv_mov_b32 v201,v240\nv_mov_b32 v202,v240\nv_mov_b32 v203,v240\nv_mov_b32 v204,v241\n"
"v_mov_b32 v205,v241\nv_mov_b32 v206,v241\nv_mov_b32 v207,v241\nv_min_f32 v208,v240,v242\nv_min_f32 v209,v240,v243\nv_min_f32 v210,v240,v244\nv_min_f32 v211,v240,v245\nv_mov_b32 v212,v241\nv_mov_b32 v213,v241\nv_mov_b32 v214,v241\n"
"v_mov_b32 v215,v241\nv_mov_b32 v216,s63\nv_mov_b32 v217,s63\nv_mov_b32 v218,s63\nv_mov_b32 v219,s63\nv_min_f32 v220,v241,v242\nv_min_f32 v221,v241,v243\nv_min_f32 v222,v241,v244\nv_min_f32 v223,v241,v245\nLW4_0_%=:\n"
"s_cmp_lg_u32 s54,1\ns_cbranch_scc1 LW4_1_%=\nv_mov_b32 v32,v224\nv_mov_b32 v33,v224\nv_mov_b32 v34,v224\nv_mov_b32 v35,v224\nv_mov_b32 v36,v228\nv_mov_b32 v37,v228\nv_mov_b32 v38,v228\nv_mov_b32 v39,v228\nv_mov_b32 v40,v224\n"
"v_mov_b32 v41,v224\nv_mov_b32 v42,v224\nv_mov_b32 v43,v224\nv_mov_b32 v44,v228\nv_mov_b32 v45,v228\nv_mov_b32 v46,v228\nv_mov_b32 v47,v228\nv_mov_b32 v48,v224\nv_mov_b32 v49,v224\nv_mov_b32 v50,v224\nv_mov_b32 v51,v224\n"
"v_mov_b32 v52,v228\nv_mov_b32 v53,v228\nv_mov_b32 v54,v228\nv_mov_b32 v55,v228\nv_mov_b32 v56,v224\nv_mov_b32 v57,v224\nv_mov_b32 v58,v224\nv_mov_b32 v59,v224\nv_mov_b32 v60,v228\nv_mov_b32 v61,v228\nv_mov_b32 v62,v228\n"
"v_mov_b32 v63,v228\nv_mov_b32 v200,v240\nv_mov_b32 v201,v240\nv_mov_b32 v202,v240\nv_mov_b32 v203,v240\nv_mov_b32 v204,v241\nv_mov_b32 v205,v241\nv_mov_b32 v206,v241\nv_mov_b32 v207,v241\nv_mov_b32 v208,v240\nv_mov_b32 v209,v240\n"
"v_mov_b32 v210,v240\nv_mov_b32 v211,v240\nv_mov_b32 v212,v241\nv_mov_b32 v213,v241\nv_mov_b32 v214,v241\nv_mov_b32 v215,v241\nv_mov_b32 v216,v240\nv_mov_b32 v217,v240\nv_mov_b32 v218,v240\nv_mov_b32 v219,v240\n"
"v_mov_b32 v220,v241\nv_mov_b32 v221,v241\nv_mov_b32 v222,v241\nv_mov_b32 v223,v241\nLW4_1_%=:\ns_cmp_lg_u32 s54,2\ns_cbranch_scc1 LW4_2_%=\nv_mov_b32 v32,s63\nv_mov_b32 v33,s63\nv_mov_b32 v34,s63\nv_mov_b32 v35,s63\n"
"v_mov_b32 v36,s63\nv_mov_b32 v37,s63\nv_mov_b32 v38,s63\nv_mov_b32 v39,s63\nv_mov_b32 v40,s63\nv_mov_b32 v41,s63\nv_mov_b32 v42,s63\nv_mov_b32 v43,s63\nv_mov_b32 v44,s63\nv_mov_b32 v45,s63\nv_mov_b32 v46,s63\nv_mov_b32 v47,s63\n"
"v_mov_b32 v48,s63\nv_mov_b32 v49,s63\nv_mov_b32 v50,s63\nv_mov_b32 v51,s63\nv_mov_b32 v52,s63\nv_mov_b32 v53,s63\nv_mov_b32 v54,s63\nv_mov_b32 v55,s63\nv_mov_b32 v56,s63\nv_mov_b32 v57,s63\nv_mov_b32 v58,s63\nv_mov_b32 v59,s63\n"
"v_mov_b32 v60,s63\nv_mov_b32 v61,s63\nv_mov_b32 v62,s63\nv_mov_b32 v63,s63\nv_mov_b32 v192,s63\nv_mov_b32 v193,s63\nv_mov_b32 v194,s63\nv_mov_b32 v195,s63\nv_mov_b32 v196,s63\nv_mov_b32 v197,s63\nv_mov_b32 v198,s63\n"
"v_mov_b32 v199,s63\nv_mov_b32 v200,s63\nv_mov_b32 v201,s63\nv_mov_b32 v202,s63\nv_mov_b32 v203,s63\nv_mov_b32 v204,s63\nv_mov_b32 v205,s63\nv_mov_b32 v206,s63\nv_mov_b32 v207,s63\nv_mov_b32 v208,s63\nv_mov_b32 v209,s63\n"
"v_mov_b32 v210,s63\nv_mov_b32 v211,s63\nv_mov_b32 v212,s63\nv_mov_b32 v213,s63\nv_mov_b32 v214,s63\nv_mov_b32 v215,s63\nv_mov_b32 v216,s63\nv_mov_b32 v217,s63\nv_mov_b32 v218,s63\nv_mov_b32 v219,s63\nv_mov_b32 v220,s63\n"
"v_mov_b32 v221,s63\nv_mov_b32 v222,s63\nv_mov_b32 v223,s63\nLW4_2_%=:\ns_cmp_lg_u32 s54,3\ns_cbranch_scc1 LW4_3_%=\nv_mov_b32 v32,v224\nv_mov_b32 v33,v224\nv_mov_b32 v34,v224\nv_mov_b32 v35,v224\nv_mov_b32 v36,v228\n"
"v_mov_b32 v37,v228\nv_mov_b32 v38,v228\nv_mov_b32 v39,v228\nv_mov_b32 v40,v224\nv_mov_b32 v41,v224\nv_mov_b32 v42,v224\nv_mov_b32 v43,v224\nv_mov_b32 v44,v228\nv_mov_b32 v45,v228\nv_mov_b32 v46,v228\nv_mov_b32 v47,v228\n"
"v_min_f32 v48,v224,v242\nv_min_f32 v49,v224,v243\nv_min_f32 v50,v224,v244\nv_min_f32 v51,v224,v245\nv_mov_b32 v52,v228\nv_mov_b32 v53,v228\nv_mov_b32 v54,v228\nv_mov_b32 v55,v228\nv_mov_b32 v56,s63\nv_mov_b32 v57,s63\n"
"v_mov_b32 v58,s63\nv_mov_b32 v59,s63\nv_min_f32 v60,v228,v242\nv_min_f32 v61,v228,v243\nv_min_f32 v62,v228,v244\nv_min_f32 v63,v228,v245\nv_min_f32 v192,v240,v242\nv_min_f32 v193,v240,v243\nv_min_f32 v194,v240,v244\n"
"v_min_f32 v195,v240,v245\nv_mov_b32 v196,v241\nv_mov_b32 v197,v241\nv_mov_b32 v198,v241\nv_mov_b32 v199,v241\nv_mov_b32 v200,s63\nv_mov_b32 v201,s63\nv_mov_b32 v202,s63\nv_mov_b32 v203,s63\nv_min_f32 v204,v241,v242\n"
"v_min_f32 v205,v241,v243\nv_min_f32 v206,v241,v244\nv_min_f32 v207,v241,v245\nv_mov_b32 v208,s63\nv_mov_b32 v209,s63\nv_mov_b32 v210,s63\nv_mov_b32 v211,s63\nv_mov_b32 v212,s63\nv_mov_b32 v213,s63\nv_mov_b32 v214,s63\n"
"v_mov_b32 v215,s63\nv_mov_b32 v216,s63\nv_mov_b32 v217,s63\nv_mov_b32 v218,s63\nv_mov_b32 v219,s63\nv_mov_b32 v220,s63\nv_mov_b32 v221,s63\nv_mov_b32 v222,s63\nv_mov_b32 v223,s63\nLW4_3_%=:\nv_lshrrev_b32 v248,1,v247\n"
"v_add_u32 v248,s21,v248\nglobal_load_dwordx2 v[250:251],v248,s[60:61] offset:0 sc1\ns_and_b32 s56,s54,1\ns_mul_i32 s55,s56,12288\ns_sub_u32 s48,s48,s55\ns_sub_u32 s48,s48,40960\ns_lshl_b32 s56,s56,12\ns_lshr_b32 s55,s54,1\n"
"s_lshl_b32 s55,s55,15\ns_add_u32 s55,s55,s56\ns_add_u32 m0,s55,90112\nv_mfma_f32_16x16x32_bf16 a[0:3],a[224:227],v[160:163],a[0:3]\ns_barrier\nv_exp_f32 v136,v136\nv_xor_b32 v246,0x10000,v246\n"
"v_mfma_f32_16x16x32_bf16 a[4:7],a[224:227],v[164:167],a[4:7]\nds_read_b128 a[192:195],v246 offset:0\nv_add_f32 v235,v235,v136\nv_mfma_f32_16x16x32_bf16 v[0:3],a[224:227],v[168:171],v[0:3]\n"
"buffer_load_dwordx4 v247,s[36:39],s48 offen offset:0 sc1 lds\nv_exp_f32 v137,v137\nv_mfma_f32_16x16x32_bf16 v[4:7],a[224:227],v[172:175],v[4:7]\nv_add_f32 v235,v235,v137\nv_cvt_pk_bf16_f32 v180,v136,v137\n"
"v_mfma_f32_16x16x32_bf16 v[12:15],a[228:231],v[172:175],v[12:15]\nv_exp_f32 v138,v138\nv_mfma_f32_16x16x32_bf16 v[8:11],a[228:231],v[168:171],v[8:11]\nds_read_b128 a[196:199],v246 offset:1024\nv_add_f32 v235,v235,v138\n"
"v_mfma_f32_16x16x32_bf16 a[12:15],a[228:231],v[164:167],a[12:15]\nbuffer_load_dwordx4 v247,s[36:39],s48 offen offset:1024 sc1 lds\nv_exp_f32 v139,v139\nv_mfma_f32_16x16x32_bf16 a[8:11],a[228:231],v[160:163],a[8:11]\n"
"v_add_f32 v235,v235,v139\nv_cvt_pk_bf16_f32 v181,v138,v139\nv_mfma_f32_16x16x32_bf16 a[16:19],a[232:235],v[160:163],a[16:19]\nv_exp_f32 v140,v140\nv_mfma_f32_16x16x32_bf16 a[20:23],a[232:235],v[164:167],a[20:23]\n"
"ds_read_b128 a[200:203],v246 offset:2048\nv_add_f32 v235,v235,v140\nv_mfma_f32_16x16x32_bf16 v[16:19],a[232:235],v[168:171],v[16:19]\nbuffer_load_dwordx4 v247,s[36:39],s48 offen offset:2048 sc1 lds\nv_exp_f32 v141,v141\n"
"v_mfma_f32_16x16x32_bf16 v[20:23],a[232:235],v[172:175],v[20:23]\nv_add_f32 v235,v235,v141\nv_cvt_pk_bf16_f32 v182,v140,v141\nv_mfma_f32_16x16x32_bf16 v[28:31],a[236:239],v[172:175],v[28:31]\nv_exp_f32 v142,v142\n"
"v_mfma_f32_16x16x32_bf16 v[24:27],a[236:239],v[168:171],v[24:27]\nds_read_b128 a[204:207],v246 offset:3072\nv_add_f32 v235,v235,v142\nv_mfma_f32_16x16x32_bf16 a[28:31],a[236:239],v[164:167],a[28:31]\n"
"buffer_load_dwordx4 v247,s[36:39],s48 offen offset:3072 sc1 lds\nv_exp_f32 v143,v143\nv_mfma_f32_16x16x32_bf16 a[24:27],a[236:239],v[160:163],a[24:27]\nv_add_f32 v235,v235,v143\nv_cvt_pk_bf16_f32 v183,v142,v143\n"
"v_mfma_f32_16x16x32_bf16 a[32:35],a[240:243],v[160:163],a[32:35]\nv_exp_f32 v144,v144\nv_mfma_f32_16x16x32_bf16 a[36:39],a[240:243],v[164:167],a[36:39]\nds_read_b128 a[220:223],v246 offset:7168\nv_add_f32 v232,v232,v144\n"
"v_mfma_f32_16x16x32_bf16 v[64:67],a[240:243],v[168:171],v[64:67]\nv_exp_f32 v145,v145\nv_mfma_f32_16x16x32_bf16 v[68:71],a[240:243],v[172:175],v[68:71]\nv_add_f32 v232,v232,v145\nv_cvt_pk_bf16_f32 v184,v144,v145\n"
"v_mfma_f32_16x16x32_bf16 v[76:79],a[244:247],v[172:175],v[76:79]\nv_exp_f32 v146,v146\nv_mfma_f32_16x16x32_bf16 v[72:75],a[244:247],v[168:171],v[72:75]\nds_read_b128 a[216:219],v246 offset:6144\nv_add_f32 v232,v232,v146\n"
"v_mfma_f32_16x16x32_bf16 a[44:47],a[244:247],v[164:167],a[44:47]\nv_exp_f32 v147,v147\nv_mfma_f32_16x16x32_bf16 a[40:43],a[244:247],v[160:163],a[40:43]\nv_add_f32 v232,v232,v147\nv_cvt_pk_bf16_f32 v185,v146,v147\n"
"v_mfma_f32_16x16x32_bf16 a[48:51],a[248:251],v[160:163],a[48:51]\nv_exp_f32 v148,v148\nv_mfma_f32_16x16x32_bf16 a[52:55],a[248:251],v[164:167],a[52:55]\nds_read_b128 a[212:215],v246 offset:5120\nv_add_f32 v232,v232,v148\n"
"v_mfma_f32_16x16x32_bf16 v[80:83],a[248:251],v[168:171],v[80:83]\nv_exp_f32 v149,v149\nv_mfma_f32_16x16x32_bf16 v[84:87],a[248:251],v[172:175],v[84:87]\nv_add_f32 v232,v232,v149\nv_cvt_pk_bf16_f32 v186,v148,v149\n"
"v_mfma_f32_16x16x32_bf16 v[92:95],a[252:255],v[172:175],v[92:95]\nv_exp_f32 v150,v150\nv_mfma_f32_16x16x32_bf16 v[88:91],a[252:255],v[168:171],v[88:91]\nds_read_b128 a[208:211],v246 offset:4096\nv_add_f32 v232,v232,v150\n"
"v_mfma_f32_16x16x32_bf16 a[60:63],a[252:255],v[164:167],a[60:63]\nv_exp_f32 v151,v151\nv_mfma_f32_16x16x32_bf16 a[56:59],a[252:255],v[160:163],a[56:59]\ns_waitcnt lgkmcnt(1)\nv_add_f32 v232,v232,v151\n"
"v_cvt_pk_bf16_f32 v187,v150,v151\nv_mfma_f32_16x16x32_bf16 v[96:99],a[192:195],a[160:163],v[192:195]\nv_exp_f32 v152,v152\nv_mfma_f32_16x16x32_bf16 v[96:99],a[196:199],a[164:167],v[96:99]\n"
"ds_read_b128 a[224:227],v246 offset:24576\nv_add_f32 v233,v233,v152\nv_mfma_f32_16x16x32_bf16 v[96:99],a[200:203],a[168:171],v[96:99]\nv_exp_f32 v153,v153\nv_mfma_f32_16x16x32_bf16 v[96:99],a[204:207],a[172:175],v[96:99]\n"
"v_add_f32 v233,v233,v153\nv_cvt_pk_bf16_f32 v188,v152,v153\nv_mfma_f32_16x16x32_bf16 v[100:103],a[220:223],a[172:175],v[200:203]\ns_waitcnt lgkmcnt(1)\nv_exp_f32 v154,v154\n"
"v_mfma_f32_16x16x32_bf16 v[100:103],a[216:219],a[168:171],v[100:103]\nds_read_b128 a[228:231],v246 offset:25600\nv_add_f32 v233,v233,v154\nv_mfma_f32_16x16x32_bf16 v[100:103],a[212:215],a[164:167],v[100:103]\nv_exp_f32 v155,v155\n"
"v_mfma_f32_16x16x32_bf16 v[100:103],a[208:211],a[160:163],v[100:103]\nv_add_f32 v233,v233,v155\nv_cvt_pk_bf16_f32 v189,v154,v155\nv_mfma_f32_16x16x32_bf16 v[108:111],a[208:211],a[176:179],v[204:207]\ns_waitcnt vmcnt(4)\n"
"v_cmp_ne_u64 vcc,s[98:99],v[250:251]\ns_cmp_eq_u64 vcc,0\ns_cselect_b32 s49,0,0x40000000\ns_add_u32 s51,s49,s22\ns_lshl_b32 s50,s54,10\nv_exp_f32 v156,v156\nv_mfma_f32_16x16x32_bf16 v[108:111],a[212:215],a[180:183],v[108:111]\n"
"ds_read_b128 a[232:235],v246 offset:26624\nv_add_f32 v233,v233,v156\nv_mfma_f32_16x16x32_bf16 v[108:111],a[216:219],a[184:187],v[108:111]\ns_add_u32 m0,s50,65536\ns_add_u32 s48,s51,0\n"
"buffer_load_dwordx4 v247,s[44:47],s48 offen sc1 lds\nv_exp_f32 v157,v157\nv_mfma_f32_16x16x32_bf16 v[108:111],a[220:223],a[188:191],v[108:111]\ns_add_u32 m0,s50,69632\nv_add_f32 v233,v233,v157\nv_cvt_pk_bf16_f32 v190,v156,v157\n"
"v_mfma_f32_16x16x32_bf16 v[104:107],a[204:207],a[188:191],v[196:199]\ns_add_u32 s48,s51,147456\nv_exp_f32 v158,v158\nv_mfma_f32_16x16x32_bf16 v[104:107],a[200:203],a[184:187],v[104:107]\nds_read_b128 a[236:239],v246 offset:27648\n"
"v_add_f32 v233,v233,v158\nv_mfma_f32_16x16x32_bf16 v[104:107],a[196:199],a[180:183],v[104:107]\nbuffer_load_dwordx4 v247,s[44:47],s48 offen sc1 lds\nv_exp_f32 v159,v159\n"
"v_mfma_f32_16x16x32_bf16 v[104:107],a[192:195],a[176:179],v[104:107]\ns_add_u32 m0,s50,73728\nv_add_f32 v233,v233,v159\nv_cvt_pk_bf16_f32 v191,v158,v159\nv_mfma_f32_16x16x32_bf16 v[112:115],a[192:195],a[128:131],v[32:35]\n"
"s_add_u32 s48,s51,294912\nv_exp_f32 v96,v96\nv_mfma_f32_16x16x32_bf16 v[112:115],a[196:199],a[132:135],v[112:115]\nds_read_b128 a[240:243],v246 offset:28672\nv_add_f32 v236,v236,v96\n"
"v_mfma_f32_16x16x32_bf16 v[112:115],a[200:203],a[136:139],v[112:115]\nbuffer_load_dwordx4 v247,s[44:47],s48 offen sc1 lds\nv_exp_f32 v97,v97\nv_mfma_f32_16x16x32_bf16 v[112:115],a[204:207],a[140:143],v[112:115]\n"
"s_add_u32 m0,s50,77824\nv_add_f32 v236,v236,v97\nv_cvt_pk_bf16_f32 v160,v96,v97\nv_mfma_f32_16x16x32_bf16 v[116:119],a[220:223],a[140:143],v[40:43]\ns_add_u32 s48,s51,4096\nv_exp_f32 v98,v98\n"
"v_mfma_f32_16x16x32_bf16 v[116:119],a[216:219],a[136:139],v[116:119]\nds_read_b128 a[244:247],v246 offset:29696\nv_add_f32 v236,v236,v98\nv_mfma_f32_16x16x32_bf16 v[116:119],a[212:215],a[132:135],v[116:119]\n"
"buffer_load_dwordx4 v247,s[44:47],s48 offen sc1 lds\nv_exp_f32 v99,v99\nv_mfma_f32_16x16x32_bf16 v[116:119],a[208:211],a[128:131],v[116:119]\ns_add_u32 m0,s50,81920\nv_add_f32 v236,v236,v99\nv_cvt_pk_bf16_f32 v161,v98,v99\n"
"v_mfma_f32_16x16x32_bf16 v[124:127],a[208:211],a[144:147],v[44:47]\ns_add_u32 s48,s51,151552\nv_exp_f32 v100,v100\nv_mfma_f32_16x16x32_bf16 v[124:127],a[212:215],a[148:151],v[124:127]\nds_read_b128 a[248:251],v246 offset:30720\n"
"v_add_f32 v236,v236,v100\nv_mfma_f32_16x16x32_bf16 v[124:127],a[216:219],a[152:155],v[124:127]\nbuffer_load_dwordx4 v247,s[44:47],s48 offen sc1 lds\nv_exp_f32 v101,v101\n"
"v_mfma_f32_16x16x32_bf16 v[124:127],a[220:223],a[156:159],v[124:127]\ns_add_u32 m0,s50,86016\nv_add_f32 v236,v236,v101\nv_cvt_pk_bf16_f32 v162,v100,v101\nv_mfma_f32_16x16x32_bf16 v[120:123],a[204:207],a[156:159],v[36:39]\n"
"s_add_u32 s48,s51,299008\nv_exp_f32 v102,v102\nv_mfma_f32_16x16x32_bf16 v[120:123],a[200:203],a[152:155],v[120:123]\nds_read_b128 a[252:255],v246 offset:31744\nv_add_f32 v236,v236,v102\n"
"v_mfma_f32_16x16x32_bf16 v[120:123],a[196:199],a[148:151],v[120:123]\nbuffer_load_dwordx4 v247,s[44:47],s48 offen sc1 lds\nv_exp_f32 v103,v103\nv_mfma_f32_16x16x32_bf16 v[120:123],a[192:195],a[144:147],v[120:123]\n"
"s_waitcnt lgkmcnt(1)\nv_add_f32 v236,v236,v103\nv_cvt_pk_bf16_f32 v163,v102,v103\nv_mfma_f32_16x16x32_bf16 a[0:3],a[224:227],v[176:179],a[0:3]\ns_add_u32 m0,s50,98304\nv_exp_f32 v104,v104\ns_cmp_lg_u32 s54,0\n"
"s_cbranch_scc1 LCL2_%=\ns_xor_b32 s55,s24,0\ns_lshl_b32 s55,s55,7\ns_add_u32 s55,s55,151552\nv_mov_b32 v249,s55\nv_mov_b32 v242,s98\nv_mov_b32 v243,s99\ns_mov_b64 exec,1\n"
"global_atomic_swap_x2 v[242:243],v249,v[242:243],s[60:61] sc0 sc1\ns_mov_b64 exec,-1\nLCL2_%=:\nv_mfma_f32_16x16x32_bf16 a[4:7],a[224:227],v[180:183],a[4:7]\nds_read_b128 a[192:195],v246 offset:8192\nv_add_f32 v237,v237,v104\n"
"v_mfma_f32_16x16x32_bf16 v[0:3],a[224:227],v[184:187],v[0:3]\ns_add_u32 s48,s51,8192\nbuffer_load_dwordx4 v247,s[44:47],s48 offen sc1 lds\nv_exp_f32 v105,v105\nv_mfma_f32_16x16x32_bf16 v[4:7],a[224:227],v[188:191],v[4:7]\n"
"s_add_u32 m0,s50,102400\nv_add_f32 v237,v237,v105\nv_cvt_pk_bf16_f32 v164,v104,v105\nv_mfma_f32_16x16x32_bf16 v[12:15],a[228:231],v[188:191],v[12:15]\ns_waitcnt lgkmcnt(1)\nv_exp_f32 v106,v106\n"
"v_mfma_f32_16x16x32_bf16 v[8:11],a[228:231],v[184:187],v[8:11]\nds_read_b128 a[196:199],v246 offset:9216\nv_add_f32 v237,v237,v106\nv_mfma_f32_16x16x32_bf16 a[12:15],a[228:231],v[180:183],a[12:15]\ns_add_u32 s48,s51,155648\n"
"buffer_load_dwordx4 v247,s[44:47],s48 offen sc1 lds\nv_exp_f32 v107,v107\nv_mfma_f32_16x16x32_bf16 a[8:11],a[228:231],v[176:179],a[8:11]\ns_add_u32 m0,s50,106496\nv_add_f32 v237,v237,v107\nv_cvt_pk_bf16_f32 v165,v106,v107\n"
"v_mfma_f32_16x16x32_bf16 a[16:19],a[232:235],v[176:179],a[16:19]\ns_add_u32 s48,s51,303104\nv_exp_f32 v108,v108\nv_mfma_f32_16x16x32_bf16 a[20:23],a[232:235],v[180:183],a[20:23]\nds_read_b128 a[200:203],v246 offset:10240\n"
"v_add_f32 v237,v237,v108\nv_mfma_f32_16x16x32_bf16 v[16:19],a[232:235],v[184:187],v[16:19]\nbuffer_load_dwordx4 v247,s[44:47],s48 offen sc1 lds\nv_exp_f32 v109,v109\n"
"v_mfma_f32_16x16x32_bf16 v[20:23],a[232:235],v[188:191],v[20:23]\ns_add_u32 m0,s50,110592\nv_add_f32 v237,v237,v109\nv_cvt_pk_bf16_f32 v166,v108,v109\nv_mfma_f32_16x16x32_bf16 v[28:31],a[236:239],v[188:191],v[28:31]\n"
"s_add_u32 s48,s51,12288\nv_exp_f32 v110,v110\nv_mfma_f32_16x16x32_bf16 v[24:27],a[236:239],v[184:187],v[24:27]\nds_read_b128 a[204:207],v246 offset:11264\nv_add_f32 v237,v237,v110\n"
"v_mfma_f32_16x16x32_bf16 a[28:31],a[236:239],v[180:183],a[28:31]\nbuffer_load_dwordx4 v247,s[44:47],s48 offen sc1 lds\nv_exp_f32 v111,v111\nv_mfma_f32_16x16x32_bf16 a[24:27],a[236:239],v[176:179],a[24:27]\n"
"s_add_u32 m0,s50,114688\nv_add_f32 v237,v237,v111\nv_cvt_pk_bf16_f32 v167,v110,v111\nv_mfma_f32_16x16x32_bf16 a[32:35],a[240:243],v[176:179],a[32:35]\ns_add_u32 s48,s51,159744\nv_exp_f32 v112,v112\n"
"v_mfma_f32_16x16x32_bf16 a[36:39],a[240:243],v[180:183],a[36:39]\nds_read_b128 a[220:223],v246 offset:15360\nv_add_f32 v232,v232,v112\nv_mfma_f32_16x16x32_bf16 v[64:67],a[240:243],v[184:187],v[64:67]\n"
"buffer_load_dwordx4 v247,s[44:47],s48 offen sc1 lds\nv_exp_f32 v113,v113\nv_mfma_f32_16x16x32_bf16 v[68:71],a[240:243],v[188:191],v[68:71]\nv_add_f32 v232,v232,v113\nv_cvt_pk_bf16_f32 v168,v112,v113\n"
"v_mfma_f32_16x16x32_bf16 v[76:79],a[244:247],v[188:191],v[76:79]\nv_exp_f32 v114,v114\nv_mfma_f32_16x16x32_bf16 v[72:75],a[244:247],v[184:187],v[72:75]\nds_read_b128 a[216:219],v246 offset:14336\nv_add_f32 v232,v232,v114\n"
"v_mfma_f32_16x16x32_bf16 a[44:47],a[244:247],v[180:183],a[44:47]\nv_exp_f32 v115,v115\nv_mfma_f32_16x16x32_bf16 a[40:43],a[244:247],v[176:179],a[40:43]\nv_add_f32 v232,v232,v115\nv_cvt_pk_bf16_f32 v169,v114,v115\n"
"v_mfma_f32_16x16x32_bf16 a[48:51],a[248:251],v[176:179],a[48:51]\nv_exp_f32 v116,v116\nv_mfma_f32_16x16x32_bf16 a[52:55],a[248:251],v[180:183],a[52:55]\nds_read_b128 a[212:215],v246 offset:13312\nv_add_f32 v232,v232,v116\n"
"v_mfma_f32_16x16x32_bf16 v[80:83],a[248:251],v[184:187],v[80:83]\nv_exp_f32 v117,v117\nv_mfma_f32_16x16x32_bf16 v[84:87],a[248:251],v[188:191],v[84:87]\nv_add_f32 v232,v232,v117\nv_cvt_pk_bf16_f32 v170,v116,v117\n"
"v_mfma_f32_16x16x32_bf16 v[92:95],a[252:255],v[188:191],v[92:95]\nv_exp_f32 v118,v118\nv_mfma_f32_16x16x32_bf16 v[88:91],a[252:255],v[184:187],v[88:91]\nds_read_b128 a[208:211],v246 offset:12288\nv_add_f32 v232,v232,v118\n"
"v_mfma_f32_16x16x32_bf16 a[60:63],a[252:255],v[180:183],a[60:63]\nv_exp_f32 v119,v119\nv_mfma_f32_16x16x32_bf16 a[56:59],a[252:255],v[176:179],a[56:59]\ns_waitcnt lgkmcnt(1)\nv_add_f32 v232,v232,v119\n"
"v_cvt_pk_bf16_f32 v171,v118,v119\nv_mfma_f32_16x16x32_bf16 v[128:131],a[192:195],a[160:163],v[208:211]\nv_exp_f32 v120,v120\nv_mfma_f32_16x16x32_bf16 v[128:131],a[196:199],a[164:167],v[128:131]\n"
"ds_read_b128 a[224:227],v246 offset:16384\nv_add_f32 v233,v233,v120\nv_mfma_f32_16x16x32_bf16 v[128:131],a[200:203],a[168:171],v[128:131]\nv_exp_f32 v121,v121\nv_mfma_f32_16x16x32_bf16 v[128:131],a[204:207],a[172:175],v[128:131]\n"
"v_add_f32 v233,v233,v121\nv_cvt_pk_bf16_f32 v172,v120,v121\nv_mfma_f32_16x16x32_bf16 v[132:135],a[220:223],a[172:175],v[216:219]\ns_waitcnt lgkmcnt(1)\nv_exp_f32 v122,v122\n"
"v_mfma_f32_16x16x32_bf16 v[132:135],a[216:219],a[168:171],v[132:135]\nds_read_b128 a[228:231],v246 offset:17408\nv_add_f32 v233,v233,v122\nv_mfma_f32_16x16x32_bf16 v[132:135],a[212:215],a[164:167],v[132:135]\nv_exp_f32 v123,v123\n"
"v_mfma_f32_16x16x32_bf16 v[132:135],a[208:211],a[160:163],v[132:135]\nv_add_f32 v233,v233,v123\nv_cvt_pk_bf16_f32 v173,v122,v123\nv_mfma_f32_16x16x32_bf16 v[140:143],a[208:211],a[176:179],v[220:223]\nv_exp_f32 v124,v124\n"
"v_mfma_f32_16x16x32_bf16 v[140:143],a[212:215],a[180:183],v[140:143]\nds_read_b128 a[232:235],v246 offset:18432\nv_add_f32 v233,v233,v124\nv_mfma_f32_16x16x32_bf16 v[140:143],a[216:219],a[184:187],v[140:143]\nv_exp_f32 v125,v125\n"
"v_mfma_f32_16x16x32_bf16 v[140:143],a[220:223],a[188:191],v[140:143]\nv_add_f32 v233,v233,v125\nv_cvt_pk_bf16_f32 v174,v124,v125\nv_mfma_f32_16x16x32_bf16 v[136:139],a[204:207],a[188:191],v[212:215]\nv_exp_f32 v126,v126\n"
"v_mfma_f32_16x16x32_bf16 v[136:139],a[200:203],a[184:187],v[136:139]\nds_read_b128 a[236:239],v246 offset:19456\nv_add_f32 v233,v233,v126\nv_mfma_f32_16x16x32_bf16 v[136:139],a[196:199],a[180:183],v[136:139]\nv_exp_f32 v127,v127\n"
"v_mfma_f32_16x16x32_bf16 v[136:139],a[192:195],a[176:179],v[136:139]\nv_add_f32 v233,v233,v127\nv_cvt_pk_bf16_f32 v175,v126,v127\nv_mfma_f32_16x16x32_bf16 v[144:147],a[192:195],a[128:131],v[48:51]\nv_exp_f32 v128,v128\n"
"v_mfma_f32_16x16x32_bf16 v[144:147],a[196:199],a[132:135],v[144:147]\nds_read_b128 a[240:243],v246 offset:20480\nv_add_f32 v236,v236,v128\nv_mfma_f32_16x16x32_bf16 v[144:147],a[200:203],a[136:139],v[144:147]\nv_exp_f32 v129,v129\n"
"v_mfma_f32_16x16x32_bf16 v[144:147],a[204:207],a[140:143],v[144:147]\nv_add_f32 v236,v236,v129\nv_cvt_pk_bf16_f32 v176,v128,v129\nv_mfma_f32_16x16x32_bf16 v[148:151],a[220:223],a[140:143],v[56:59]\nv_exp_f32 v130,v130\n"
"v_mfma_f32_16x16x32_bf16 v[148:151],a[216:219],a[136:139],v[148:151]\nds_read_b128 a[244:247],v246 offset:21504\nv_add_f32 v236,v236,v130\nv_mfma_f32_16x16x32_bf16 v[148:151],a[212:215],a[132:135],v[148:151]\nv_exp_f32 v131,v131\n"
"v_mfma_f32_16x16x32_bf16 v[148:151],a[208:211],a[128:131],v[148:151]\nv_add_f32 v236,v236,v131\nv_cvt_pk_bf16_f32 v177,v130,v131\nv_mfma_f32_16x16x32_bf16 v[156:159],a[208:211],a[144:147],v[60:63]\nv_exp_f32 v132,v132\n"
"v_mfma_f32_16x16x32_bf16 v[156:159],a[212:215],a[148:151],v[156:159]\nds_read_b128 a[248:251],v246 offset:22528\nv_add_f32 v236,v236,v132\nv_mfma_f32_16x16x32_bf16 v[156:159],a[216:219],a[152:155],v[156:159]\nv_exp_f32 v133,v133\n"
"v_mfma_f32_16x16x32_bf16 v[156:159],a[220:223],a[156:159],v[156:159]\nds_read_b128 a[252:255],v246 offset:23552\nv_add_f32 v236,v236,v133\nv_cvt_pk_bf16_f32 v178,v132,v133\n"
"v_mfma_f32_16x16x32_bf16 v[152:155],a[204:207],a[156:159],v[52:55]\nv_exp_f32 v134,v134\nv_mfma_f32_16x16x32_bf16 v[152:155],a[200:203],a[152:155],v[152:155]\nv_add_f32 v236,v236,v134\n"
"v_mfma_f32_16x16x32_bf16 v[152:155],a[196:199],a[148:151],v[152:155]\nv_exp_f32 v135,v135\nv_mfma_f32_16x16x32_bf16 v[152:155],a[192:195],a[144:147],v[152:155]\ns_waitcnt vmcnt(11) lgkmcnt(0)\nv_add_f32 v236,v236,v135\n"
"v_cvt_pk_bf16_f32 v179,v134,v135\ns_mul_i32 s66,s58,147456\ns_add_u32 s66,s66,s59\ns_add_u32 s67,s66,147456\ns_mov_b32 s62,0\nv_mov_b32 v96,v234\nv_mov_b32 v97,v235\ns_nop 1\nv_permlane16_swap_b32 v96,v234\n"
"v_permlane16_swap_b32 v97,v235\ns_nop 1\nv_add_f32 v234,v234,v96\nv_add_f32 v235,v235,v97\nv_mov_b32 v96,v234\nv_mov_b32 v97,v235\ns_nop 1\nv_permlane32_swap_b32 v96,v234\nv_permlane32_swap_b32 v97,v235\ns_nop 1\n"
"v_add_f32 v234,v234,v96\nv_add_f32 v235,v235,v97\nv_mov_b32 v96,0x6f800000\nv_mov_b32 v97,0x1f800000\nv_cmp_nle_f32 s[68:69],v234,v96\nv_cmp_nge_f32 s[70:71],v234,v97\nv_rcp_f32 v234,v234\nv_sub_f32 v238,1.0,v238\n"
"s_or_b64 s[68:69],s[68:69],s[70:71]\ns_cselect_b32 s55,4,0\ns_or_b32 s62,s62,s55\nv_mul_f32 v248,v238,v234\nv_mul_f32 v249,v238,v234\nv_cmp_nle_f32 s[68:69],v235,v96\nv_cmp_nge_f32 s[70:71],v235,v97\nv_rcp_f32 v235,v235\n"
"v_sub_f32 v239,1.0,v239\ns_or_b64 s[68:69],s[68:69],s[70:71]\ns_cselect_b32 s55,8,0\ns_or_b32 s62,s62,s55\nv_mul_f32 v250,v239,v235\nv_mul_f32 v251,v239,v235\nv_accvgpr_read_b32 v96,a0\nv_accvgpr_read_b32 v97,a1\n"
"v_accvgpr_read_b32 v98,a2\nv_accvgpr_read_b32 v99,a3\nv_accvgpr_read_b32 v100,a8\nv_accvgpr_read_b32 v101,a9\nv_accvgpr_read_b32 v102,a10\nv_accvgpr_read_b32 v103,a11\nv_pk_mul_f32 v[96:97],v[96:97],v[248:249]\n"
"v_pk_mul_f32 v[98:99],v[98:99],v[248:249]\nv_pk_mul_f32 v[100:101],v[100:101],v[248:249]\nv_pk_mul_f32 v[102:103],v[102:103],v[248:249]\nv_cvt_pk_bf16_f32 v96,v96,v97\nv_cvt_pk_bf16_f32 v97,v98,v99\n"
"v_cvt_pk_bf16_f32 v98,v100,v101\nv_cvt_pk_bf16_f32 v99,v102,v103\nbuffer_store_dwordx4 v[96:99],v247,s[44:47],s66 offen offset:0 sc1\nv_accvgpr_read_b32 v104,a4\nv_accvgpr_read_b32 v105,a5\nv_accvgpr_read_b32 v106,a6\n"
"v_accvgpr_read_b32 v107,a7\nv_accvgpr_read_b32 v108,a12\nv_accvgpr_read_b32 v109,a13\nv_accvgpr_read_b32 v110,a14\nv_accvgpr_read_b32 v111,a15\nv_pk_mul_f32 v[104:105],v[104:105],v[250:251]\n"
"v_pk_mul_f32 v[106:107],v[106:107],v[250:251]\nv_pk_mul_f32 v[108:109],v[108:109],v[250:251]\nv_pk_mul_f32 v[110:111],v[110:111],v[250:251]\nv_cvt_pk_bf16_f32 v104,v104,v105\nv_cvt_pk_bf16_f32 v105,v106,v107\n"
"v_cvt_pk_bf16_f32 v106,v108,v109\nv_cvt_pk_bf16_f32 v107,v110,v111\nbuffer_store_dwordx4 v[104:107],v247,s[44:47],s67 offen offset:0 sc1\ns_add_u32 s56,s20,0\nbuffer_load_dwordx4 a[0:3],v247,s[44:47],s56 offen offset:0 sc1\n"
"buffer_load_dwordx4 a[4:7],v247,s[44:47],s56 offen offset:1024 sc1\ns_add_u32 s56,s20,8192\nbuffer_load_dwordx4 a[8:11],v247,s[44:47],s56 offen offset:0 sc1\nbuffer_load_dwordx4 a[12:15],v247,s[44:47],s56 offen offset:1024 sc1\n"
"v_accvgpr_read_b32 v112,a16\nv_accvgpr_read_b32 v113,a17\nv_accvgpr_read_b32 v114,a18\nv_accvgpr_read_b32 v115,a19\nv_accvgpr_read_b32 v116,a24\nv_accvgpr_read_b32 v117,a25\nv_accvgpr_read_b32 v118,a26\n"
"v_accvgpr_read_b32 v119,a27\nv_pk_mul_f32 v[112:113],v[112:113],v[248:249]\nv_pk_mul_f32 v[114:115],v[114:115],v[248:249]\nv_pk_mul_f32 v[116:117],v[116:117],v[248:249]\nv_pk_mul_f32 v[118:119],v[118:119],v[248:249]\n"
"v_cvt_pk_bf16_f32 v112,v112,v113\nv_cvt_pk_bf16_f32 v113,v114,v115\nv_cvt_pk_bf16_f32 v114,v116,v117\nv_cvt_pk_bf16_f32 v115,v118,v119\nbuffer_store_dwordx4 v[112:115],v247,s[44:47],s66 offen offset:1024 sc1\n"
"v_accvgpr_read_b32 v120,a20\nv_accvgpr_read_b32 v121,a21\nv_accvgpr_read_b32 v122,a22\nv_accvgpr_read_b32 v123,a23\nv_accvgpr_read_b32 v124,a28\nv_accvgpr_read_b32 v125,a29\nv_accvgpr_read_b32 v126,a30\n"
"v_accvgpr_read_b32 v127,a31\nv_pk_mul_f32 v[120:121],v[120:121],v[250:251]\nv_pk_mul_f32 v[122:123],v[122:123],v[250:251]\nv_pk_mul_f32 v[124:125],v[124:125],v[250:251]\nv_pk_mul_f32 v[126:127],v[126:127],v[250:251]\n"
"v_cvt_pk_bf16_f32 v120,v120,v121\nv_cvt_pk_bf16_f32 v121,v122,v123\nv_cvt_pk_bf16_f32 v122,v124,v125\nv_cvt_pk_bf16_f32 v123,v126,v127\nbuffer_store_dwordx4 v[120:123],v247,s[44:47],s67 offen offset:1024 sc1\n"
"s_add_u32 s56,s20,16384\nbuffer_load_dwordx4 a[16:19],v247,s[44:47],s56 offen offset:0 sc1\nbuffer_load_dwordx4 a[20:23],v247,s[44:47],s56 offen offset:1024 sc1\ns_add_u32 s56,s20,24576\n"
"buffer_load_dwordx4 a[24:27],v247,s[44:47],s56 offen offset:0 sc1\nbuffer_load_dwordx4 a[28:31],v247,s[44:47],s56 offen offset:1024 sc1\nv_accvgpr_read_b32 v96,a32\nv_accvgpr_read_b32 v97,a33\nv_accvgpr_read_b32 v98,a34\n"
"v_accvgpr_read_b32 v99,a35\nv_accvgpr_read_b32 v100,a40\nv_accvgpr_read_b32 v101,a41\nv_accvgpr_read_b32 v102,a42\nv_accvgpr_read_b32 v103,a43\nv_pk_mul_f32 v[96:97],v[96:97],v[248:249]\nv_pk_mul_f32 v[98:99],v[98:99],v[248:249]\n"
"v_pk_mul_f32 v[100:101],v[100:101],v[248:249]\nv_pk_mul_f32 v[102:103],v[102:103],v[248:249]\nv_cvt_pk_bf16_f32 v96,v96,v97\nv_cvt_pk_bf16_f32 v97,v98,v99\nv_cvt_pk_bf16_f32 v98,v100,v101\nv_cvt_pk_bf16_f32 v99,v102,v103\n"
"buffer_store_dwordx4 v[96:99],v247,s[44:47],s66 offen offset:2048 sc1\nv_accvgpr_read_b32 v104,a36\nv_accvgpr_read_b32 v105,a37\nv_accvgpr_read_b32 v106,a38\nv_accvgpr_read_b32 v107,a39\nv_accvgpr_read_b32 v108,a44\n"
"v_accvgpr_read_b32 v109,a45\nv_accvgpr_read_b32 v110,a46\nv_accvgpr_read_b32 v111,a47\nv_pk_mul_f32 v[104:105],v[104:105],v[250:251]\nv_pk_mul_f32 v[106:107],v[106:107],v[250:251]\nv_pk_mul_f32 v[108:109],v[108:109],v[250:251]\n"
"v_pk_mul_f32 v[110:111],v[110:111],v[250:251]\nv_cvt_pk_bf16_f32 v104,v104,v105\nv_cvt_pk_bf16_f32 v105,v106,v107\nv_cvt_pk_bf16_f32 v106,v108,v109\nv_cvt_pk_bf16_f32 v107,v110,v111\n"
"buffer_store_dwordx4 v[104:107],v247,s[44:47],s67 offen offset:2048 sc1\ns_add_u32 s56,s20,32768\nbuffer_load_dwordx4 a[32:35],v247,s[44:47],s56 offen offset:0 sc1\n"
"buffer_load_dwordx4 a[36:39],v247,s[44:47],s56 offen offset:1024 sc1\ns_add_u32 s56,s20,40960\nbuffer_load_dwordx4 a[40:43],v247,s[44:47],s56 offen offset:0 sc1\n"
"buffer_load_dwordx4 a[44:47],v247,s[44:47],s56 offen offset:1024 sc1\nv_accvgpr_read_b32 v112,a48\nv_accvgpr_read_b32 v113,a49\nv_accvgpr_read_b32 v114,a50\nv_accvgpr_read_b32 v115,a51\nv_accvgpr_read_b32 v116,a56\n"
"v_accvgpr_read_b32 v117,a57\nv_accvgpr_read_b32 v118,a58\nv_accvgpr_read_b32 v119,a59\nv_pk_mul_f32 v[112:113],v[112:113],v[248:249]\nv_pk_mul_f32 v[114:115],v[114:115],v[248:249]\nv_pk_mul_f32 v[116:117],v[116:117],v[248:249]\n"
"v_pk_mul_f32 v[118:119],v[118:119],v[248:249]\nv_cvt_pk_bf16_f32 v112,v112,v113\nv_cvt_pk_bf16_f32 v113,v114,v115\nv_cvt_pk_bf16_f32 v114,v116,v117\nv_cvt_pk_bf16_f32 v115,v118,v119\n"
"buffer_store_dwordx4 v[112:115],v247,s[44:47],s66 offen offset:3072 sc1\nv_accvgpr_read_b32 v120,a52\nv_accvgpr_read_b32 v121,a53\nv_accvgpr_read_b32 v122,a54\nv_accvgpr_read_b32 v123,a55\nv_accvgpr_read_b32 v124,a60\n"
"v_accvgpr_read_b32 v125,a61\nv_accvgpr_read_b32 v126,a62\nv_accvgpr_read_b32 v127,a63\nv_pk_mul_f32 v[120:121],v[120:121],v[250:251]\nv_pk_mul_f32 v[122:123],v[122:123],v[250:251]\nv_pk_mul_f32 v[124:125],v[124:125],v[250:251]\n"
"v_pk_mul_f32 v[126:127],v[126:127],v[250:251]\nv_cvt_pk_bf16_f32 v120,v120,v121\nv_cvt_pk_bf16_f32 v121,v122,v123\nv_cvt_pk_bf16_f32 v122,v124,v125\nv_cvt_pk_bf16_f32 v123,v126,v127\n"
"buffer_store_dwordx4 v[120:123],v247,s[44:47],s67 offen offset:3072 sc1\ns_add_u32 s56,s20,49152\nbuffer_load_dwordx4 a[48:51],v247,s[44:47],s56 offen offset:0 sc1\n"
"buffer_load_dwordx4 a[52:55],v247,s[44:47],s56 offen offset:1024 sc1\ns_add_u32 s56,s20,57344\nbuffer_load_dwordx4 a[56:59],v247,s[44:47],s56 offen offset:0 sc1\n"
"buffer_load_dwordx4 a[60:63],v247,s[44:47],s56 offen offset:1024 sc1\ns_branch LA3_%=\nLA2_%=:\nv_mfma_f32_16x16x32_bf16 a[0:3],a[224:227],v[160:163],a[0:3]\ns_barrier\nv_exp_f32 v136,v136\nv_xor_b32 v246,0x10000,v246\n"
"v_mfma_f32_16x16x32_bf16 a[4:7],a[224:227],v[164:167],a[4:7]\nds_read_b128 a[192:195],v246 offset:0\nv_add_f32 v235,v235,v136\nv_mfma_f32_16x16x32_bf16 v[0:3],a[224:227],v[168:171],v[0:3]\ns_xor_b32 s50,s50,0x10000\n"
"v_exp_f32 v137,v137\nv_mfma_f32_16x16x32_bf16 v[4:7],a[224:227],v[172:175],v[4:7]\ns_mov_b32 m0,s50\nv_add_f32 v235,v235,v137\nv_cvt_pk_bf16_f32 v180,v136,v137\nv_mfma_f32_16x16x32_bf16 v[12:15],a[228:231],v[172:175],v[12:15]\n"
"s_add_u32 s55,s48,0x1000\nv_exp_f32 v138,v138\nv_mfma_f32_16x16x32_bf16 v[8:11],a[228:231],v[168:171],v[8:11]\nds_read_b128 a[196:199],v246 offset:1024\nv_add_f32 v235,v235,v138\n"
"v_mfma_f32_16x16x32_bf16 a[12:15],a[228:231],v[164:167],a[12:15]\nbuffer_load_dwordx4 v247,s[36:39],s48 offen offset:0 sc1 lds\nv_exp_f32 v139,v139\nv_mfma_f32_16x16x32_bf16 a[8:11],a[228:231],v[160:163],a[8:11]\n"
"v_add_f32 v235,v235,v139\nv_cvt_pk_bf16_f32 v181,v138,v139\nv_mfma_f32_16x16x32_bf16 a[16:19],a[232:235],v[160:163],a[16:19]\nv_exp_f32 v140,v140\nv_mfma_f32_16x16x32_bf16 a[20:23],a[232:235],v[164:167],a[20:23]\n"
"ds_read_b128 a[200:203],v246 offset:2048\nv_add_f32 v235,v235,v140\nv_mfma_f32_16x16x32_bf16 v[16:19],a[232:235],v[168:171],v[16:19]\nbuffer_load_dwordx4 v247,s[36:39],s48 offen offset:1024 sc1 lds\nv_exp_f32 v141,v141\n"
"v_mfma_f32_16x16x32_bf16 v[20:23],a[232:235],v[172:175],v[20:23]\nv_add_f32 v235,v235,v141\nv_cvt_pk_bf16_f32 v182,v140,v141\nv_mfma_f32_16x16x32_bf16 v[28:31],a[236:239],v[172:175],v[28:31]\nv_exp_f32 v142,v142\n"
"v_mfma_f32_16x16x32_bf16 v[24:27],a[236:239],v[168:171],v[24:27]\nds_read_b128 a[204:207],v246 offset:3072\nv_add_f32 v235,v235,v142\nv_mfma_f32_16x16x32_bf16 a[28:31],a[236:239],v[164:167],a[28:31]\n"
"buffer_load_dwordx4 v247,s[36:39],s48 offen offset:2048 sc1 lds\nv_exp_f32 v143,v143\nv_mfma_f32_16x16x32_bf16 a[24:27],a[236:239],v[160:163],a[24:27]\nv_add_f32 v235,v235,v143\nv_cvt_pk_bf16_f32 v183,v142,v143\n"
"v_mfma_f32_16x16x32_bf16 a[32:35],a[240:243],v[160:163],a[32:35]\nv_exp_f32 v144,v144\nv_mfma_f32_16x16x32_bf16 a[36:39],a[240:243],v[164:167],a[36:39]\nds_read_b128 a[220:223],v246 offset:7168\nv_add_f32 v232,v232,v144\n"
"v_mfma_f32_16x16x32_bf16 v[64:67],a[240:243],v[168:171],v[64:67]\nbuffer_load_dwordx4 v247,s[36:39],s48 offen offset:3072 sc1 lds\nv_exp_f32 v145,v145\nv_mfma_f32_16x16x32_bf16 v[68:71],a[240:243],v[172:175],v[68:71]\n"
"s_add_u32 m0,m0,0x1000\nv_add_f32 v232,v232,v145\nv_cvt_pk_bf16_f32 v184,v144,v145\nv_mfma_f32_16x16x32_bf16 v[76:79],a[244:247],v[172:175],v[76:79]\nv_exp_f32 v146,v146\n"
"v_mfma_f32_16x16x32_bf16 v[72:75],a[244:247],v[168:171],v[72:75]\nds_read_b128 a[216:219],v246 offset:6144\nv_add_f32 v232,v232,v146\nv_mfma_f32_16x16x32_bf16 a[44:47],a[244:247],v[164:167],a[44:47]\n"
"buffer_load_dwordx4 v247,s[36:39],s55 offen offset:0 sc1 lds\nv_exp_f32 v147,v147\nv_mfma_f32_16x16x32_bf16 a[40:43],a[244:247],v[160:163],a[40:43]\nv_add_f32 v232,v232,v147\nv_cvt_pk_bf16_f32 v185,v146,v147\n"
"v_mfma_f32_16x16x32_bf16 a[48:51],a[248:251],v[160:163],a[48:51]\nv_exp_f32 v148,v148\nv_mfma_f32_16x16x32_bf16 a[52:55],a[248:251],v[164:167],a[52:55]\nds_read_b128 a[212:215],v246 offset:5120\nv_add_f32 v232,v232,v148\n"
"v_mfma_f32_16x16x32_bf16 v[80:83],a[248:251],v[168:171],v[80:83]\nbuffer_load_dwordx4 v247,s[36:39],s55 offen offset:1024 sc1 lds\nv_exp_f32 v149,v149\nv_mfma_f32_16x16x32_bf16 v[84:87],a[248:251],v[172:175],v[84:87]\n"
"v_add_f32 v232,v232,v149\nv_cvt_pk_bf16_f32 v186,v148,v149\nv_mfma_f32_16x16x32_bf16 v[92:95],a[252:255],v[172:175],v[92:95]\nv_exp_f32 v150,v150\nv_mfma_f32_16x16x32_bf16 v[88:91],a[252:255],v[168:171],v[88:91]\n"
"ds_read_b128 a[208:211],v246 offset:4096\nv_add_f32 v232,v232,v150\nv_mfma_f32_16x16x32_bf16 a[60:63],a[252:255],v[164:167],a[60:63]\nbuffer_load_dwordx4 v247,s[36:39],s55 offen offset:2048 sc1 lds\nv_exp_f32 v151,v151\n"
"v_mfma_f32_16x16x32_bf16 a[56:59],a[252:255],v[160:163],a[56:59]\ns_waitcnt lgkmcnt(1)\nv_add_f32 v232,v232,v151\nv_cvt_pk_bf16_f32 v187,v150,v151\nv_mfma_f32_16x16x32_bf16 v[96:99],a[192:195],a[160:163],v[192:195]\n"
"v_exp_f32 v152,v152\nv_mfma_f32_16x16x32_bf16 v[96:99],a[196:199],a[164:167],v[96:99]\nds_read_b128 a[224:227],v246 offset:24576\nv_add_f32 v233,v233,v152\nv_mfma_f32_16x16x32_bf16 v[96:99],a[200:203],a[168:171],v[96:99]\n"
"buffer_load_dwordx4 v247,s[36:39],s55 offen offset:3072 sc1 lds\nv_exp_f32 v153,v153\nv_mfma_f32_16x16x32_bf16 v[96:99],a[204:207],a[172:175],v[96:99]\ns_add_u32 m0,m0,0x1000\nv_add_f32 v233,v233,v153\n"
"v_cvt_pk_bf16_f32 v188,v152,v153\nv_mfma_f32_16x16x32_bf16 v[100:103],a[220:223],a[172:175],v[192:195]\ns_waitcnt lgkmcnt(1)\nv_exp_f32 v154,v154\nv_mfma_f32_16x16x32_bf16 v[100:103],a[216:219],a[168:171],v[100:103]\n"
"ds_read_b128 a[228:231],v246 offset:25600\nv_add_f32 v233,v233,v154\nv_mfma_f32_16x16x32_bf16 v[100:103],a[212:215],a[164:167],v[100:103]\ns_add_u32 s55,s49,0x1000\nv_exp_f32 v155,v155\n"
"v_mfma_f32_16x16x32_bf16 v[100:103],a[208:211],a[160:163],v[100:103]\nv_add_f32 v233,v233,v155\nv_cvt_pk_bf16_f32 v189,v154,v155\nv_mfma_f32_16x16x32_bf16 v[108:111],a[208:211],a[176:179],v[196:199]\nv_exp_f32 v156,v156\n"
"v_mfma_f32_16x16x32_bf16 v[108:111],a[212:215],a[180:183],v[108:111]\nds_read_b128 a[232:235],v246 offset:26624\nv_add_f32 v233,v233,v156\nv_mfma_f32_16x16x32_bf16 v[108:111],a[216:219],a[184:187],v[108:111]\n"
"buffer_load_dwordx4 v247,s[36:39],s49 offen offset:0 sc1 lds\nv_exp_f32 v157,v157\nv_mfma_f32_16x16x32_bf16 v[108:111],a[220:223],a[188:191],v[108:111]\nv_add_f32 v233,v233,v157\nv_cvt_pk_bf16_f32 v190,v156,v157\n"
"v_mfma_f32_16x16x32_bf16 v[104:107],a[204:207],a[188:191],v[196:199]\nv_exp_f32 v158,v158\nv_mfma_f32_16x16x32_bf16 v[104:107],a[200:203],a[184:187],v[104:107]\nds_read_b128 a[236:239],v246 offset:27648\nv_add_f32 v233,v233,v158\n"
"v_mfma_f32_16x16x32_bf16 v[104:107],a[196:199],a[180:183],v[104:107]\nbuffer_load_dwordx4 v247,s[36:39],s49 offen offset:1024 sc1 lds\nv_exp_f32 v159,v159\nv_mfma_f32_16x16x32_bf16 v[104:107],a[192:195],a[176:179],v[104:107]\n"
"v_add_f32 v233,v233,v159\nv_cvt_pk_bf16_f32 v191,v158,v159\nv_mfma_f32_16x16x32_bf16 v[112:115],a[192:195],a[128:131],v[224:227]\nv_exp_f32 v96,v96\nv_mfma_f32_16x16x32_bf16 v[112:115],a[196:199],a[132:135],v[112:115]\n"
"ds_read_b128 a[240:243],v246 offset:28672\nv_add_f32 v236,v236,v96\nv_mfma_f32_16x16x32_bf16 v[112:115],a[200:203],a[136:139],v[112:115]\nbuffer_load_dwordx4 v247,s[36:39],s49 offen offset:2048 sc1 lds\nv_exp_f32 v97,v97\n"
"v_mfma_f32_16x16x32_bf16 v[112:115],a[204:207],a[140:143],v[112:115]\nv_add_f32 v236,v236,v97\nv_cvt_pk_bf16_f32 v160,v96,v97\nv_mfma_f32_16x16x32_bf16 v[116:119],a[220:223],a[140:143],v[224:227]\nv_exp_f32 v98,v98\n"
"v_mfma_f32_16x16x32_bf16 v[116:119],a[216:219],a[136:139],v[116:119]\nds_read_b128 a[244:247],v246 offset:29696\nv_add_f32 v236,v236,v98\nv_mfma_f32_16x16x32_bf16 v[116:119],a[212:215],a[132:135],v[116:119]\n"
"buffer_load_dwordx4 v247,s[36:39],s49 offen offset:3072 sc1 lds\nv_exp_f32 v99,v99\nv_mfma_f32_16x16x32_bf16 v[116:119],a[208:211],a[128:131],v[116:119]\ns_add_u32 m0,m0,0x1000\nv_add_f32 v236,v236,v99\n"
"v_cvt_pk_bf16_f32 v161,v98,v99\nv_mfma_f32_16x16x32_bf16 v[124:127],a[208:211],a[144:147],v[228:231]\nv_exp_f32 v100,v100\nv_mfma_f32_16x16x32_bf16 v[124:127],a[212:215],a[148:151],v[124:127]\n"
"ds_read_b128 a[248:251],v246 offset:30720\nv_add_f32 v236,v236,v100\nv_mfma_f32_16x16x32_bf16 v[124:127],a[216:219],a[152:155],v[124:127]\nbuffer_load_dwordx4 v247,s[36:39],s55 offen offset:0 sc1 lds\nv_exp_f32 v101,v101\n"
"v_mfma_f32_16x16x32_bf16 v[124:127],a[220:223],a[156:159],v[124:127]\nv_add_f32 v236,v236,v101\nv_cvt_pk_bf16_f32 v162,v100,v101\nv_mfma_f32_16x16x32_bf16 v[120:123],a[204:207],a[156:159],v[228:231]\nv_exp_f32 v102,v102\n"
"v_mfma_f32_16x16x32_bf16 v[120:123],a[200:203],a[152:155],v[120:123]\nds_read_b128 a[252:255],v246 offset:31744\nv_add_f32 v236,v236,v102\nv_mfma_f32_16x16x32_bf16 v[120:123],a[196:199],a[148:151],v[120:123]\n"
"buffer_load_dwordx4 v247,s[36:39],s55 offen offset:1024 sc1 lds\nv_exp_f32 v103,v103\nv_mfma_f32_16x16x32_bf16 v[120:123],a[192:195],a[144:147],v[120:123]\ns_waitcnt lgkmcnt(1)\nv_add_f32 v236,v236,v103\n"
"v_cvt_pk_bf16_f32 v163,v102,v103\nv_mfma_f32_16x16x32_bf16 a[0:3],a[224:227],v[176:179],a[0:3]\nv_exp_f32 v104,v104\nv_mfma_f32_16x16x32_bf16 a[4:7],a[224:227],v[180:183],a[4:7]\nds_read_b128 a[192:195],v246 offset:8192\n"
"v_add_f32 v237,v237,v104\nv_mfma_f32_16x16x32_bf16 v[0:3],a[224:227],v[184:187],v[0:3]\nbuffer_load_dwordx4 v247,s[36:39],s55 offen offset:2048 sc1 lds\nv_exp_f32 v105,v105\n"
"v_mfma_f32_16x16x32_bf16 v[4:7],a[224:227],v[188:191],v[4:7]\nv_add_f32 v237,v237,v105\nv_cvt_pk_bf16_f32 v164,v104,v105\nv_mfma_f32_16x16x32_bf16 v[12:15],a[228:231],v[188:191],v[12:15]\ns_waitcnt lgkmcnt(1)\n"
"v_exp_f32 v106,v106\nv_mfma_f32_16x16x32_bf16 v[8:11],a[228:231],v[184:187],v[8:11]\nds_read_b128 a[196:199],v246 offset:9216\nv_add_f32 v237,v237,v106\nv_mfma_f32_16x16x32_bf16 a[12:15],a[228:231],v[180:183],a[12:15]\n"
"buffer_load_dwordx4 v247,s[36:39],s55 offen offset:3072 sc1 lds\nv_exp_f32 v107,v107\nv_mfma_f32_16x16x32_bf16 a[8:11],a[228:231],v[176:179],a[8:11]\ns_add_u32 s49,s48,s51\nv_add_f32 v237,v237,v107\n"
"v_cvt_pk_bf16_f32 v165,v106,v107\nv_mfma_f32_16x16x32_bf16 a[16:19],a[232:235],v[176:179],a[16:19]\ns_add_u32 s48,s48,0x10000\nv_exp_f32 v108,v108\nv_mfma_f32_16x16x32_bf16 a[20:23],a[232:235],v[180:183],a[20:23]\n"
"ds_read_b128 a[200:203],v246 offset:10240\nv_add_f32 v237,v237,v108\nv_mfma_f32_16x16x32_bf16 v[16:19],a[232:235],v[184:187],v[16:19]\nv_exp_f32 v109,v109\nv_mfma_f32_16x16x32_bf16 v[20:23],a[232:235],v[188:191],v[20:23]\n"
"v_add_f32 v237,v237,v109\nv_cvt_pk_bf16_f32 v166,v108,v109\nv_mfma_f32_16x16x32_bf16 v[28:31],a[236:239],v[188:191],v[28:31]\nv_exp_f32 v110,v110\nv_mfma_f32_16x16x32_bf16 v[24:27],a[236:239],v[184:187],v[24:27]\n"
"ds_read_b128 a[204:207],v246 offset:11264\nv_add_f32 v237,v237,v110\nv_mfma_f32_16x16x32_bf16 a[28:31],a[236:239],v[180:183],a[28:31]\nv_exp_f32 v111,v111\nv_mfma_f32_16x16x32_bf16 a[24:27],a[236:239],v[176:179],a[24:27]\n"
"v_add_f32 v237,v237,v111\nv_cvt_pk_bf16_f32 v167,v110,v111\nv_mfma_f32_16x16x32_bf16 a[32:35],a[240:243],v[176:179],a[32:35]\nv_exp_f32 v112,v112\nv_mfma_f32_16x16x32_bf16 a[36:39],a[240:243],v[180:183],a[36:39]\n"
"ds_read_b128 a[220:223],v246 offset:15360\nv_add_f32 v232,v232,v112\nv_mfma_f32_16x16x32_bf16 v[64:67],a[240:243],v[184:187],v[64:67]\nv_exp_f32 v113,v113\nv_mfma_f32_16x16x32_bf16 v[68:71],a[240:243],v[188:191],v[68:71]\n"
"v_add_f32 v232,v232,v113\nv_cvt_pk_bf16_f32 v168,v112,v113\nv_mfma_f32_16x16x32_bf16 v[76:79],a[244:247],v[188:191],v[76:79]\nv_exp_f32 v114,v114\nv_mfma_f32_16x16x32_bf16 v[72:75],a[244:247],v[184:187],v[72:75]\n"
"ds_read_b128 a[216:219],v246 offset:14336\nv_add_f32 v232,v232,v114\nv_mfma_f32_16x16x32_bf16 a[44:47],a[244:247],v[180:183],a[44:47]\nv_exp_f32 v115,v115\nv_mfma_f32_16x16x32_bf16 a[40:43],a[244:247],v[176:179],a[40:43]\n"
"v_add_f32 v232,v232,v115\nv_cvt_pk_bf16_f32 v169,v114,v115\nv_mfma_f32_16x16x32_bf16 a[48:51],a[248:251],v[176:179],a[48:51]\nv_exp_f32 v116,v116\nv_mfma_f32_16x16x32_bf16 a[52:55],a[248:251],v[180:183],a[52:55]\n"
"ds_read_b128 a[212:215],v246 offset:13312\nv_add_f32 v232,v232,v116\nv_mfma_f32_16x16x32_bf16 v[80:83],a[248:251],v[184:187],v[80:83]\nv_exp_f32 v117,v117\nv_mfma_f32_16x16x32_bf16 v[84:87],a[248:251],v[188:191],v[84:87]\n"
"v_add_f32 v232,v232,v117\nv_cvt_pk_bf16_f32 v170,v116,v117\nv_mfma_f32_16x16x32_bf16 v[92:95],a[252:255],v[188:191],v[92:95]\nv_exp_f32 v118,v118\nv_mfma_f32_16x16x32_bf16 v[88:91],a[252:255],v[184:187],v[88:91]\n"
"ds_read_b128 a[208:211],v246 offset:12288\nv_add_f32 v232,v232,v118\nv_mfma_f32_16x16x32_bf16 a[60:63],a[252:255],v[180:183],a[60:63]\nv_exp_f32 v119,v119\nv_mfma_f32_16x16x32_bf16 a[56:59],a[252:255],v[176:179],a[56:59]\n"
"s_waitcnt lgkmcnt(1)\nv_add_f32 v232,v232,v119\nv_cvt_pk_bf16_f32 v171,v118,v119\nv_mfma_f32_16x16x32_bf16 v[128:131],a[192:195],a[160:163],v[192:195]\nv_exp_f32 v120,v120\n"
"v_mfma_f32_16x16x32_bf16 v[128:131],a[196:199],a[164:167],v[128:131]\nds_read_b128 a[224:227],v246 offset:16384\nv_add_f32 v233,v233,v120\nv_mfma_f32_16x16x32_bf16 v[128:131],a[200:203],a[168:171],v[128:131]\nv_exp_f32 v121,v121\n"
"v_mfma_f32_16x16x32_bf16 v[128:131],a[204:207],a[172:175],v[128:131]\nv_add_f32 v233,v233,v121\nv_cvt_pk_bf16_f32 v172,v120,v121\nv_mfma_f32_16x16x32_bf16 v[132:135],a[220:223],a[172:175],v[192:195]\ns_waitcnt lgkmcnt(1)\n"
"v_exp_f32 v122,v122\nv_mfma_f32_16x16x32_bf16 v[132:135],a[216:219],a[168:171],v[132:135]\nds_read_b128 a[228:231],v246 offset:17408\nv_add_f32 v233,v233,v122\nv_mfma_f32_16x16x32_bf16 v[132:135],a[212:215],a[164:167],v[132:135]\n"
"v_exp_f32 v123,v123\nv_mfma_f32_16x16x32_bf16 v[132:135],a[208:211],a[160:163],v[132:135]\nv_add_f32 v233,v233,v123\nv_cvt_pk_bf16_f32 v173,v122,v123\nv_mfma_f32_16x16x32_bf16 v[140:143],a[208:211],a[176:179],v[196:199]\n"
"v_exp_f32 v124,v124\nv_mfma_f32_16x16x32_bf16 v[140:143],a[212:215],a[180:183],v[140:143]\nds_read_b128 a[232:235],v246 offset:18432\nv_add_f32 v233,v233,v124\nv_mfma_f32_16x16x32_bf16 v[140:143],a[216:219],a[184:187],v[140:143]\n"
"v_exp_f32 v125,v125\nv_mfma_f32_16x16x32_bf16 v[140:143],a[220:223],a[188:191],v[140:143]\nv_add_f32 v233,v233,v125\nv_cvt_pk_bf16_f32 v174,v124,v125\nv_mfma_f32_16x16x32_bf16 v[136:139],a[204:207],a[188:191],v[196:199]\n"
"v_exp_f32 v126,v126\nv_mfma_f32_16x16x32_bf16 v[136:139],a[200:203],a[184:187],v[136:139]\nds_read_b128 a[236:239],v246 offset:19456\nv_add_f32 v233,v233,v126\nv_mfma_f32_16x16x32_bf16 v[136:139],a[196:199],a[180:183],v[136:139]\n"
"v_exp_f32 v127,v127\nv_mfma_f32_16x16x32_bf16 v[136:139],a[192:195],a[176:179],v[136:139]\nv_add_f32 v233,v233,v127\nv_cvt_pk_bf16_f32 v175,v126,v127\nv_mfma_f32_16x16x32_bf16 v[144:147],a[192:195],a[128:131],v[224:227]\n"
"v_exp_f32 v128,v128\nv_mfma_f32_16x16x32_bf16 v[144:147],a[196:199],a[132:135],v[144:147]\nds_read_b128 a[240:243],v246 offset:20480\nv_add_f32 v236,v236,v128\nv_mfma_f32_16x16x32_bf16 v[144:147],a[200:203],a[136:139],v[144:147]\n"
"v_exp_f32 v129,v129\nv_mfma_f32_16x16x32_bf16 v[144:147],a[204:207],a[140:143],v[144:147]\nv_add_f32 v236,v236,v129\nv_cvt_pk_bf16_f32 v176,v128,v129\nv_mfma_f32_16x16x32_bf16 v[148:151],a[220:223],a[140:143],v[224:227]\n"
"v_exp_f32 v130,v130\nv_mfma_f32_16x16x32_bf16 v[148:151],a[216:219],a[136:139],v[148:151]\nds_read_b128 a[244:247],v246 offset:21504\nv_add_f32 v236,v236,v130\nv_mfma_f32_16x16x32_bf16 v[148:151],a[212:215],a[132:135],v[148:151]\n"
"v_exp_f32 v131,v131\nv_mfma_f32_16x16x32_bf16 v[148:151],a[208:211],a[128:131],v[148:151]\nv_add_f32 v236,v236,v131\nv_cvt_pk_bf16_f32 v177,v130,v131\nv_mfma_f32_16x16x32_bf16 v[156:159],a[208:211],a[144:147],v[228:231]\n"
"v_exp_f32 v132,v132\nv_mfma_f32_16x16x32_bf16 v[156:159],a[212:215],a[148:151],v[156:159]\nds_read_b128 a[248:251],v246 offset:22528\nv_add_f32 v236,v236,v132\nv_mfma_f32_16x16x32_bf16 v[156:159],a[216:219],a[152:155],v[156:159]\n"
"v_exp_f32 v133,v133\nv_mfma_f32_16x16x32_bf16 v[156:159],a[220:223],a[156:159],v[156:159]\nds_read_b128 a[252:255],v246 offset:23552\nv_add_f32 v236,v236,v133\nv_cvt_pk_bf16_f32 v178,v132,v133\n"
"v_mfma_f32_16x16x32_bf16 v[152:155],a[204:207],a[156:159],v[228:231]\nv_exp_f32 v134,v134\nv_mfma_f32_16x16x32_bf16 v[152:155],a[200:203],a[152:155],v[152:155]\nv_add_f32 v236,v236,v134\n"
"v_mfma_f32_16x16x32_bf16 v[152:155],a[196:199],a[148:151],v[152:155]\nv_exp_f32 v135,v135\nv_mfma_f32_16x16x32_bf16 v[152:155],a[192:195],a[144:147],v[152:155]\ns_waitcnt vmcnt(0) lgkmcnt(0)\nv_add_f32 v236,v236,v135\n"
"v_cvt_pk_bf16_f32 v179,v134,v135\ns_mul_i32 s66,s58,147456\ns_add_u32 s66,s66,s59\ns_add_u32 s67,s66,147456\ns_mov_b32 s62,0\nv_mov_b32 v96,v234\nv_mov_b32 v97,v235\ns_nop 1\nv_permlane16_swap_b32 v96,v234\n"
"v_permlane16_swap_b32 v97,v235\ns_nop 1\nv_add_f32 v234,v234,v96\nv_add_f32 v235,v235,v97\nv_mov_b32 v96,v234\nv_mov_b32 v97,v235\ns_nop 1\nv_permlane32_swap_b32 v96,v234\nv_permlane32_swap_b32 v97,v235\ns_nop 1\n"
"v_add_f32 v234,v234,v96\nv_add_f32 v235,v235,v97\nv_mov_b32 v96,0x6f800000\nv_mov_b32 v97,0x1f800000\nv_cmp_nle_f32 s[68:69],v234,v96\nv_cmp_nge_f32 s[70:71],v234,v97\nv_rcp_f32 v234,v234\nv_sub_f32 v238,1.0,v238\n"
"s_or_b64 s[68:69],s[68:69],s[70:71]\ns_cselect_b32 s55,4,0\ns_or_b32 s62,s62,s55\nv_mul_f32 v248,v238,v234\nv_mul_f32 v249,v238,v234\nv_cmp_nle_f32 s[68:69],v235,v96\nv_cmp_nge_f32 s[70:71],v235,v97\nv_rcp_f32 v235,v235\n"
"v_sub_f32 v239,1.0,v239\ns_or_b64 s[68:69],s[68:69],s[70:71]\ns_cselect_b32 s55,8,0\ns_or_b32 s62,s62,s55\nv_mul_f32 v250,v239,v235\nv_mul_f32 v251,v239,v235\nv_accvgpr_read_b32 v96,a0\nv_accvgpr_read_b32 v97,a1\n"
"v_accvgpr_read_b32 v98,a2\nv_accvgpr_read_b32 v99,a3\nv_accvgpr_read_b32 v100,a8\nv_accvgpr_read_b32 v101,a9\nv_accvgpr_read_b32 v102,a10\nv_accvgpr_read_b32 v103,a11\nv_pk_mul_f32 v[96:97],v[96:97],v[248:249]\n"
"v_pk_mul_f32 v[98:99],v[98:99],v[248:249]\nv_pk_mul_f32 v[100:101],v[100:101],v[248:249]\nv_pk_mul_f32 v[102:103],v[102:103],v[248:249]\nv_cvt_pk_bf16_f32 v96,v96,v97\nv_cvt_pk_bf16_f32 v97,v98,v99\n"
"v_cvt_pk_bf16_f32 v98,v100,v101\nv_cvt_pk_bf16_f32 v99,v102,v103\nbuffer_store_dwordx4 v[96:99],v247,s[44:47],s66 offen offset:0 sc1\nv_accvgpr_read_b32 v104,a4\nv_accvgpr_read_b32 v105,a5\nv_accvgpr_read_b32 v106,a6\n"
"v_accvgpr_read_b32 v107,a7\nv_accvgpr_read_b32 v108,a12\nv_accvgpr_read_b32 v109,a13\nv_accvgpr_read_b32 v110,a14\nv_accvgpr_read_b32 v111,a15\nv_pk_mul_f32 v[104:105],v[104:105],v[250:251]\n"
"v_pk_mul_f32 v[106:107],v[106:107],v[250:251]\nv_pk_mul_f32 v[108:109],v[108:109],v[250:251]\nv_pk_mul_f32 v[110:111],v[110:111],v[250:251]\nv_cvt_pk_bf16_f32 v104,v104,v105\nv_cvt_pk_bf16_f32 v105,v106,v107\n"
"v_cvt_pk_bf16_f32 v106,v108,v109\nv_cvt_pk_bf16_f32 v107,v110,v111\nbuffer_store_dwordx4 v[104:107],v247,s[44:47],s67 offen offset:0 sc1\nv_accvgpr_read_b32 v112,a16\nv_accvgpr_read_b32 v113,a17\nv_accvgpr_read_b32 v114,a18\n"
"v_accvgpr_read_b32 v115,a19\nv_accvgpr_read_b32 v116,a24\nv_accvgpr_read_b32 v117,a25\nv_accvgpr_read_b32 v118,a26\nv_accvgpr_read_b32 v119,a27\nv_pk_mul_f32 v[112:113],v[112:113],v[248:249]\n"
"v_pk_mul_f32 v[114:115],v[114:115],v[248:249]\nv_pk_mul_f32 v[116:117],v[116:117],v[248:249]\nv_pk_mul_f32 v[118:119],v[118:119],v[248:249]\nv_cvt_pk_bf16_f32 v112,v112,v113\nv_cvt_pk_bf16_f32 v113,v114,v115\n"
"v_cvt_pk_bf16_f32 v114,v116,v117\nv_cvt_pk_bf16_f32 v115,v118,v119\nbuffer_store_dwordx4 v[112:115],v247,s[44:47],s66 offen offset:1024 sc1\nv_accvgpr_read_b32 v120,a20\nv_accvgpr_read_b32 v121,a21\nv_accvgpr_read_b32 v122,a22\n"
"v_accvgpr_read_b32 v123,a23\nv_accvgpr_read_b32 v124,a28\nv_accvgpr_read_b32 v125,a29\nv_accvgpr_read_b32 v126,a30\nv_accvgpr_read_b32 v127,a31\nv_pk_mul_f32 v[120:121],v[120:121],v[250:251]\n"
"v_pk_mul_f32 v[122:123],v[122:123],v[250:251]\nv_pk_mul_f32 v[124:125],v[124:125],v[250:251]\nv_pk_mul_f32 v[126:127],v[126:127],v[250:251]\nv_cvt_pk_bf16_f32 v120,v120,v121\nv_cvt_pk_bf16_f32 v121,v122,v123\n"
"v_cvt_pk_bf16_f32 v122,v124,v125\nv_cvt_pk_bf16_f32 v123,v126,v127\nbuffer_store_dwordx4 v[120:123],v247,s[44:47],s67 offen offset:1024 sc1\nv_accvgpr_read_b32 v96,a32\nv_accvgpr_read_b32 v97,a33\nv_accvgpr_read_b32 v98,a34\n"
"v_accvgpr_read_b32 v99,a35\nv_accvgpr_read_b32 v100,a40\nv_accvgpr_read_b32 v101,a41\nv_accvgpr_read_b32 v102,a42\nv_accvgpr_read_b32 v103,a43\nv_pk_mul_f32 v[96:97],v[96:97],v[248:249]\nv_pk_mul_f32 v[98:99],v[98:99],v[248:249]\n"
"v_pk_mul_f32 v[100:101],v[100:101],v[248:249]\nv_pk_mul_f32 v[102:103],v[102:103],v[248:249]\nv_cvt_pk_bf16_f32 v96,v96,v97\nv_cvt_pk_bf16_f32 v97,v98,v99\nv_cvt_pk_bf16_f32 v98,v100,v101\nv_cvt_pk_bf16_f32 v99,v102,v103\n"
"buffer_store_dwordx4 v[96:99],v247,s[44:47],s66 offen offset:2048 sc1\nv_accvgpr_read_b32 v104,a36\nv_accvgpr_read_b32 v105,a37\nv_accvgpr_read_b32 v106,a38\nv_accvgpr_read_b32 v107,a39\nv_accvgpr_read_b32 v108,a44\n"
"v_accvgpr_read_b32 v109,a45\nv_accvgpr_read_b32 v110,a46\nv_accvgpr_read_b32 v111,a47\nv_pk_mul_f32 v[104:105],v[104:105],v[250:251]\nv_pk_mul_f32 v[106:107],v[106:107],v[250:251]\nv_pk_mul_f32 v[108:109],v[108:109],v[250:251]\n"
"v_pk_mul_f32 v[110:111],v[110:111],v[250:251]\nv_cvt_pk_bf16_f32 v104,v104,v105\nv_cvt_pk_bf16_f32 v105,v106,v107\nv_cvt_pk_bf16_f32 v106,v108,v109\nv_cvt_pk_bf16_f32 v107,v110,v111\n"
"buffer_store_dwordx4 v[104:107],v247,s[44:47],s67 offen offset:2048 sc1\nv_accvgpr_read_b32 v112,a48\nv_accvgpr_read_b32 v113,a49\nv_accvgpr_read_b32 v114,a50\nv_accvgpr_read_b32 v115,a51\nv_accvgpr_read_b32 v116,a56\n"
"v_accvgpr_read_b32 v117,a57\nv_accvgpr_read_b32 v118,a58\nv_accvgpr_read_b32 v119,a59\nv_pk_mul_f32 v[112:113],v[112:113],v[248:249]\nv_pk_mul_f32 v[114:115],v[114:115],v[248:249]\nv_pk_mul_f32 v[116:117],v[116:117],v[248:249]\n"
"v_pk_mul_f32 v[118:119],v[118:119],v[248:249]\nv_cvt_pk_bf16_f32 v112,v112,v113\nv_cvt_pk_bf16_f32 v113,v114,v115\nv_cvt_pk_bf16_f32 v114,v116,v117\nv_cvt_pk_bf16_f32 v115,v118,v119\n"
"buffer_store_dwordx4 v[112:115],v247,s[44:47],s66 offen offset:3072 sc1\nv_accvgpr_read_b32 v120,a52\nv_accvgpr_read_b32 v121,a53\nv_accvgpr_read_b32 v122,a54\nv_accvgpr_read_b32 v123,a55\nv_accvgpr_read_b32 v124,a60\n"
"v_accvgpr_read_b32 v125,a61\nv_accvgpr_read_b32 v126,a62\nv_accvgpr_read_b32 v127,a63\nv_pk_mul_f32 v[120:121],v[120:121],v[250:251]\nv_pk_mul_f32 v[122:123],v[122:123],v[250:251]\nv_pk_mul_f32 v[124:125],v[124:125],v[250:251]\n"
"v_pk_mul_f32 v[126:127],v[126:127],v[250:251]\nv_cvt_pk_bf16_f32 v120,v120,v121\nv_cvt_pk_bf16_f32 v121,v122,v123\nv_cvt_pk_bf16_f32 v122,v124,v125\nv_cvt_pk_bf16_f32 v123,v126,v127\n"
"buffer_store_dwordx4 v[120:123],v247,s[44:47],s67 offen offset:3072 sc1\ns_mov_b32 s52,1\ns_sub_u32 s64,14,s53\ns_mov_b32 s65,1\n.p2align 6\nLL2_%=:\nv_mfma_f32_16x16x32_bf16 a[64:67],a[224:227],v[160:163],a[64:67]\ns_barrier\n"
"v_exp_f32 v136,v136\nv_xor_b32 v246,0x10000,v246\nv_mfma_f32_16x16x32_bf16 a[68:71],a[224:227],v[164:167],a[68:71]\nds_read_b128 a[192:195],v246 offset:0\nv_add_f32 v237,v237,v136\n"
"v_mfma_f32_16x16x32_bf16 v[0:3],a[224:227],v[168:171],v[0:3]\ns_xor_b32 s50,s50,0x10000\nv_exp_f32 v137,v137\nv_mfma_f32_16x16x32_bf16 v[4:7],a[224:227],v[172:175],v[4:7]\ns_mov_b32 m0,s50\nv_add_f32 v237,v237,v137\n"
"v_cvt_pk_bf16_f32 v180,v136,v137\nv_mfma_f32_16x16x32_bf16 v[12:15],a[228:231],v[172:175],v[12:15]\ns_add_u32 s55,s48,0x1000\nv_exp_f32 v138,v138\nv_mfma_f32_16x16x32_bf16 v[8:11],a[228:231],v[168:171],v[8:11]\n"
"ds_read_b128 a[196:199],v246 offset:1024\nv_add_f32 v237,v237,v138\nv_mfma_f32_16x16x32_bf16 a[76:79],a[228:231],v[164:167],a[76:79]\nbuffer_load_dwordx4 v247,s[36:39],s48 offen offset:0 sc1 lds\nv_exp_f32 v139,v139\n"
"v_mfma_f32_16x16x32_bf16 a[72:75],a[228:231],v[160:163],a[72:75]\nv_add_f32 v237,v237,v139\nv_cvt_pk_bf16_f32 v181,v138,v139\nv_mfma_f32_16x16x32_bf16 a[80:83],a[232:235],v[160:163],a[80:83]\nv_exp_f32 v140,v140\n"
"v_mfma_f32_16x16x32_bf16 a[84:87],a[232:235],v[164:167],a[84:87]\nds_read_b128 a[200:203],v246 offset:2048\nv_add_f32 v237,v237,v140\nv_mfma_f32_16x16x32_bf16 v[16:19],a[232:235],v[168:171],v[16:19]\n"
"buffer_load_dwordx4 v247,s[36:39],s48 offen offset:1024 sc1 lds\nv_exp_f32 v141,v141\nv_mfma_f32_16x16x32_bf16 v[20:23],a[232:235],v[172:175],v[20:23]\nv_add_f32 v237,v237,v141\nv_cvt_pk_bf16_f32 v182,v140,v141\n"
"v_mfma_f32_16x16x32_bf16 v[28:31],a[236:239],v[172:175],v[28:31]\nv_exp_f32 v142,v142\nv_mfma_f32_16x16x32_bf16 v[24:27],a[236:239],v[168:171],v[24:27]\nds_read_b128 a[204:207],v246 offset:3072\nv_add_f32 v237,v237,v142\n"
"v_mfma_f32_16x16x32_bf16 a[92:95],a[236:239],v[164:167],a[92:95]\nbuffer_load_dwordx4 v247,s[36:39],s48 offen offset:2048 sc1 lds\nv_exp_f32 v143,v143\nv_mfma_f32_16x16x32_bf16 a[88:91],a[236:239],v[160:163],a[88:91]\n"
"v_add_f32 v237,v237,v143\nv_cvt_pk_bf16_f32 v183,v142,v143\nv_mfma_f32_16x16x32_bf16 a[96:99],a[240:243],v[160:163],a[96:99]\nv_exp_f32 v144,v144\nv_mfma_f32_16x16x32_bf16 a[100:103],a[240:243],v[164:167],a[100:103]\n"
"ds_read_b128 a[220:223],v246 offset:7168\nv_add_f32 v232,v232,v144\nv_mfma_f32_16x16x32_bf16 v[64:67],a[240:243],v[168:171],v[64:67]\nbuffer_load_dwordx4 v247,s[36:39],s48 offen offset:3072 sc1 lds\nv_exp_f32 v145,v145\n"
"v_mfma_f32_16x16x32_bf16 v[68:71],a[240:243],v[172:175],v[68:71]\ns_add_u32 m0,m0,0x1000\nv_add_f32 v232,v232,v145\nv_cvt_pk_bf16_f32 v184,v144,v145\nv_mfma_f32_16x16x32_bf16 v[76:79],a[244:247],v[172:175],v[76:79]\n"
"v_exp_f32 v146,v146\nv_mfma_f32_16x16x32_bf16 v[72:75],a[244:247],v[168:171],v[72:75]\nds_read_b128 a[216:219],v246 offset:6144\nv_add_f32 v232,v232,v146\nv_mfma_f32_16x16x32_bf16 a[108:111],a[244:247],v[164:167],a[108:111]\n"
"buffer_load_dwordx4 v247,s[36:39],s55 offen offset:0 sc1 lds\nv_exp_f32 v147,v147\nv_mfma_f32_16x16x32_bf16 a[104:107],a[244:247],v[160:163],a[104:107]\nv_add_f32 v232,v232,v147\nv_cvt_pk_bf16_f32 v185,v146,v147\n"
"v_mfma_f32_16x16x32_bf16 a[112:115],a[248:251],v[160:163],a[112:115]\nv_exp_f32 v148,v148\nv_mfma_f32_16x16x32_bf16 a[116:119],a[248:251],v[164:167],a[116:119]\nds_read_b128 a[212:215],v246 offset:5120\nv_add_f32 v232,v232,v148\n"
"v_mfma_f32_16x16x32_bf16 v[80:83],a[248:251],v[168:171],v[80:83]\nbuffer_load_dwordx4 v247,s[36:39],s55 offen offset:1024 sc1 lds\nv_exp_f32 v149,v149\nv_mfma_f32_16x16x32_bf16 v[84:87],a[248:251],v[172:175],v[84:87]\n"
"v_add_f32 v232,v232,v149\nv_cvt_pk_bf16_f32 v186,v148,v149\nv_mfma_f32_16x16x32_bf16 v[92:95],a[252:255],v[172:175],v[92:95]\nv_exp_f32 v150,v150\nv_mfma_f32_16x16x32_bf16 v[88:91],a[252:255],v[168:171],v[88:91]\n"
"ds_read_b128 a[208:211],v246 offset:4096\nv_add_f32 v232,v232,v150\nv_mfma_f32_16x16x32_bf16 a[124:127],a[252:255],v[164:167],a[124:127]\nbuffer_load_dwordx4 v247,s[36:39],s55 offen offset:2048 sc1 lds\nv_exp_f32 v151,v151\n"
"v_mfma_f32_16x16x32_bf16 a[120:123],a[252:255],v[160:163],a[120:123]\ns_waitcnt lgkmcnt(1)\nv_add_f32 v232,v232,v151\nv_cvt_pk_bf16_f32 v187,v150,v151\nv_mfma_f32_16x16x32_bf16 v[96:99],a[192:195],a[160:163],v[192:195]\n"
"v_exp_f32 v152,v152\nv_mfma_f32_16x16x32_bf16 v[96:99],a[196:199],a[164:167],v[96:99]\nds_read_b128 a[224:227],v246 offset:24576\nv_add_f32 v233,v233,v152\nv_mfma_f32_16x16x32_bf16 v[96:99],a[200:203],a[168:171],v[96:99]\n"
"buffer_load_dwordx4 v247,s[36:39],s55 offen offset:3072 sc1 lds\nv_exp_f32 v153,v153\nv_mfma_f32_16x16x32_bf16 v[96:99],a[204:207],a[172:175],v[96:99]\ns_add_u32 m0,m0,0x1000\nv_add_f32 v233,v233,v153\n"
"v_cvt_pk_bf16_f32 v188,v152,v153\nv_mfma_f32_16x16x32_bf16 v[100:103],a[220:223],a[172:175],v[192:195]\ns_waitcnt lgkmcnt(1)\nv_exp_f32 v154,v154\nv_mfma_f32_16x16x32_bf16 v[100:103],a[216:219],a[168:171],v[100:103]\n"
"ds_read_b128 a[228:231],v246 offset:25600\nv_add_f32 v233,v233,v154\nv_mfma_f32_16x16x32_bf16 v[100:103],a[212:215],a[164:167],v[100:103]\ns_add_u32 s55,s49,0x1000\nv_exp_f32 v155,v155\n"
"v_mfma_f32_16x16x32_bf16 v[100:103],a[208:211],a[160:163],v[100:103]\nv_add_f32 v233,v233,v155\nv_cvt_pk_bf16_f32 v189,v154,v155\nv_mfma_f32_16x16x32_bf16 v[108:111],a[208:211],a[176:179],v[196:199]\nv_exp_f32 v156,v156\n"
"v_mfma_f32_16x16x32_bf16 v[108:111],a[212:215],a[180:183],v[108:111]\nds_read_b128 a[232:235],v246 offset:26624\nv_add_f32 v233,v233,v156\nv_mfma_f32_16x16x32_bf16 v[108:111],a[216:219],a[184:187],v[108:111]\n"
"buffer_load_dwordx4 v247,s[36:39],s49 offen offset:0 sc1 lds\nv_exp_f32 v157,v157\nv_mfma_f32_16x16x32_bf16 v[108:111],a[220:223],a[188:191],v[108:111]\nv_add_f32 v233,v233,v157\nv_cvt_pk_bf16_f32 v190,v156,v157\n"
"v_mfma_f32_16x16x32_bf16 v[104:107],a[204:207],a[188:191],v[196:199]\nv_exp_f32 v158,v158\nv_mfma_f32_16x16x32_bf16 v[104:107],a[200:203],a[184:187],v[104:107]\nds_read_b128 a[236:239],v246 offset:27648\nv_add_f32 v233,v233,v158\n"
"v_mfma_f32_16x16x32_bf16 v[104:107],a[196:199],a[180:183],v[104:107]\nbuffer_load_dwordx4 v247,s[36:39],s49 offen offset:1024 sc1 lds\nv_exp_f32 v159,v159\nv_mfma_f32_16x16x32_bf16 v[104:107],a[192:195],a[176:179],v[104:107]\n"
"v_add_f32 v233,v233,v159\nv_cvt_pk_bf16_f32 v191,v158,v159\nv_mfma_f32_16x16x32_bf16 v[112:115],a[192:195],a[128:131],v[224:227]\nv_exp_f32 v96,v96\nv_mfma_f32_16x16x32_bf16 v[112:115],a[196:199],a[132:135],v[112:115]\n"
"ds_read_b128 a[240:243],v246 offset:28672\nv_add_f32 v236,v236,v96\nv_mfma_f32_16x16x32_bf16 v[112:115],a[200:203],a[136:139],v[112:115]\nbuffer_load_dwordx4 v247,s[36:39],s49 offen offset:2048 sc1 lds\nv_exp_f32 v97,v97\n"
"v_mfma_f32_16x16x32_bf16 v[112:115],a[204:207],a[140:143],v[112:115]\nv_add_f32 v236,v236,v97\nv_cvt_pk_bf16_f32 v160,v96,v97\nv_mfma_f32_16x16x32_bf16 v[116:119],a[220:223],a[140:143],v[224:227]\nv_exp_f32 v98,v98\n"
"v_mfma_f32_16x16x32_bf16 v[116:119],a[216:219],a[136:139],v[116:119]\nds_read_b128 a[244:247],v246 offset:29696\nv_add_f32 v236,v236,v98\nv_mfma_f32_16x16x32_bf16 v[116:119],a[212:215],a[132:135],v[116:119]\n"
"buffer_load_dwordx4 v247,s[36:39],s49 offen offset:3072 sc1 lds\nv_exp_f32 v99,v99\nv_mfma_f32_16x16x32_bf16 v[116:119],a[208:211],a[128:131],v[116:119]\ns_add_u32 m0,m0,0x1000\nv_add_f32 v236,v236,v99\n"
"v_cvt_pk_bf16_f32 v161,v98,v99\nv_mfma_f32_16x16x32_bf16 v[124:127],a[208:211],a[144:147],v[228:231]\nv_exp_f32 v100,v100\nv_mfma_f32_16x16x32_bf16 v[124:127],a[212:215],a[148:151],v[124:127]\n"
"ds_read_b128 a[248:251],v246 offset:30720\nv_add_f32 v236,v236,v100\nv_mfma_f32_16x16x32_bf16 v[124:127],a[216:219],a[152:155],v[124:127]\nbuffer_load_dwordx4 v247,s[36:39],s55 offen offset:0 sc1 lds\nv_exp_f32 v101,v101\n"
"v_mfma_f32_16x16x32_bf16 v[124:127],a[220:223],a[156:159],v[124:127]\nv_add_f32 v236,v236,v101\nv_cvt_pk_bf16_f32 v162,v100,v101\nv_mfma_f32_16x16x32_bf16 v[120:123],a[204:207],a[156:159],v[228:231]\nv_exp_f32 v102,v102\n"
"v_mfma_f32_16x16x32_bf16 v[120:123],a[200:203],a[152:155],v[120:123]\nds_read_b128 a[252:255],v246 offset:31744\nv_add_f32 v236,v236,v102\nv_mfma_f32_16x16x32_bf16 v[120:123],a[196:199],a[148:151],v[120:123]\n"
"buffer_load_dwordx4 v247,s[36:39],s55 offen offset:1024 sc1 lds\nv_exp_f32 v103,v103\nv_mfma_f32_16x16x32_bf16 v[120:123],a[192:195],a[144:147],v[120:123]\ns_waitcnt lgkmcnt(1)\nv_add_f32 v236,v236,v103\n"
"v_cvt_pk_bf16_f32 v163,v102,v103\nv_mfma_f32_16x16x32_bf16 a[64:67],a[224:227],v[176:179],a[64:67]\nv_exp_f32 v104,v104\nv_mfma_f32_16x16x32_bf16 a[68:71],a[224:227],v[180:183],a[68:71]\nds_read_b128 a[192:195],v246 offset:8192\n"
"v_add_f32 v237,v237,v104\nv_mfma_f32_16x16x32_bf16 v[0:3],a[224:227],v[184:187],v[0:3]\nbuffer_load_dwordx4 v247,s[36:39],s55 offen offset:2048 sc1 lds\nv_exp_f32 v105,v105\n"
"v_mfma_f32_16x16x32_bf16 v[4:7],a[224:227],v[188:191],v[4:7]\nv_add_f32 v237,v237,v105\nv_cvt_pk_bf16_f32 v164,v104,v105\nv_mfma_f32_16x16x32_bf16 v[12:15],a[228:231],v[188:191],v[12:15]\ns_waitcnt lgkmcnt(1)\n"
"v_exp_f32 v106,v106\nv_mfma_f32_16x16x32_bf16 v[8:11],a[228:231],v[184:187],v[8:11]\nds_read_b128 a[196:199],v246 offset:9216\nv_add_f32 v237,v237,v106\nv_mfma_f32_16x16x32_bf16 a[76:79],a[228:231],v[180:183],a[76:79]\n"
"buffer_load_dwordx4 v247,s[36:39],s55 offen offset:3072 sc1 lds\nv_exp_f32 v107,v107\nv_mfma_f32_16x16x32_bf16 a[72:75],a[228:231],v[176:179],a[72:75]\ns_add_u32 s49,s48,s51\nv_add_f32 v237,v237,v107\n"
"v_cvt_pk_bf16_f32 v165,v106,v107\nv_mfma_f32_16x16x32_bf16 a[80:83],a[232:235],v[176:179],a[80:83]\ns_add_u32 s48,s48,0x10000\nv_exp_f32 v108,v108\nv_mfma_f32_16x16x32_bf16 a[84:87],a[232:235],v[180:183],a[84:87]\n"
"ds_read_b128 a[200:203],v246 offset:10240\nv_add_f32 v237,v237,v108\nv_mfma_f32_16x16x32_bf16 v[16:19],a[232:235],v[184:187],v[16:19]\nv_exp_f32 v109,v109\nv_mfma_f32_16x16x32_bf16 v[20:23],a[232:235],v[188:191],v[20:23]\n"
"v_add_f32 v237,v237,v109\nv_cvt_pk_bf16_f32 v166,v108,v109\nv_mfma_f32_16x16x32_bf16 v[28:31],a[236:239],v[188:191],v[28:31]\nv_exp_f32 v110,v110\nv_mfma_f32_16x16x32_bf16 v[24:27],a[236:239],v[184:187],v[24:27]\n"
"ds_read_b128 a[204:207],v246 offset:11264\nv_add_f32 v237,v237,v110\nv_mfma_f32_16x16x32_bf16 a[92:95],a[236:239],v[180:183],a[92:95]\nv_exp_f32 v111,v111\nv_mfma_f32_16x16x32_bf16 a[88:91],a[236:239],v[176:179],a[88:91]\n"
"v_add_f32 v237,v237,v111\nv_cvt_pk_bf16_f32 v167,v110,v111\nv_mfma_f32_16x16x32_bf16 a[96:99],a[240:243],v[176:179],a[96:99]\nv_exp_f32 v112,v112\nv_mfma_f32_16x16x32_bf16 a[100:103],a[240:243],v[180:183],a[100:103]\n"
"ds_read_b128 a[220:223],v246 offset:15360\nv_add_f32 v232,v232,v112\nv_mfma_f32_16x16x32_bf16 v[64:67],a[240:243],v[184:187],v[64:67]\nv_exp_f32 v113,v113\nv_mfma_f32_16x16x32_bf16 v[68:71],a[240:243],v[188:191],v[68:71]\n"
"v_add_f32 v232,v232,v113\nv_cvt_pk_bf16_f32 v168,v112,v113\nv_mfma_f32_16x16x32_bf16 v[76:79],a[244:247],v[188:191],v[76:79]\nv_exp_f32 v114,v114\nv_mfma_f32_16x16x32_bf16 v[72:75],a[244:247],v[184:187],v[72:75]\n"
"ds_read_b128 a[216:219],v246 offset:14336\nv_add_f32 v232,v232,v114\nv_mfma_f32_16x16x32_bf16 a[108:111],a[244:247],v[180:183],a[108:111]\nv_exp_f32 v115,v115\nv_mfma_f32_16x16x32_bf16 a[104:107],a[244:247],v[176:179],a[104:107]\n"
"v_add_f32 v232,v232,v115\nv_cvt_pk_bf16_f32 v169,v114,v115\nv_mfma_f32_16x16x32_bf16 a[112:115],a[248:251],v[176:179],a[112:115]\nv_exp_f32 v116,v116\nv_mfma_f32_16x16x32_bf16 a[116:119],a[248:251],v[180:183],a[116:119]\n"
"ds_read_b128 a[212:215],v246 offset:13312\nv_add_f32 v232,v232,v116\nv_mfma_f32_16x16x32_bf16 v[80:83],a[248:251],v[184:187],v[80:83]\nv_exp_f32 v117,v117\nv_mfma_f32_16x16x32_bf16 v[84:87],a[248:251],v[188:191],v[84:87]\n"
"v_add_f32 v232,v232,v117\nv_cvt_pk_bf16_f32 v170,v116,v117\nv_mfma_f32_16x16x32_bf16 v[92:95],a[252:255],v[188:191],v[92:95]\nv_exp_f32 v118,v118\nv_mfma_f32_16x16x32_bf16 v[88:91],a[252:255],v[184:187],v[88:91]\n"
"ds_read_b128 a[208:211],v246 offset:12288\nv_add_f32 v232,v232,v118\nv_mfma_f32_16x16x32_bf16 a[124:127],a[252:255],v[180:183],a[124:127]\nv_exp_f32 v119,v119\nv_mfma_f32_16x16x32_bf16 a[120:123],a[252:255],v[176:179],a[120:123]\n"
"s_waitcnt lgkmcnt(1)\nv_add_f32 v232,v232,v119\nv_cvt_pk_bf16_f32 v171,v118,v119\nv_mfma_f32_16x16x32_bf16 v[128:131],a[192:195],a[160:163],v[192:195]\nv_exp_f32 v120,v120\n"
"v_mfma_f32_16x16x32_bf16 v[128:131],a[196:199],a[164:167],v[128:131]\nds_read_b128 a[224:227],v246 offset:16384\nv_add_f32 v233,v233,v120\nv_mfma_f32_16x16x32_bf16 v[128:131],a[200:203],a[168:171],v[128:131]\nv_exp_f32 v121,v121\n"
"v_mfma_f32_16x16x32_bf16 v[128:131],a[204:207],a[172:175],v[128:131]\nv_add_f32 v233,v233,v121\nv_cvt_pk_bf16_f32 v172,v120,v121\nv_mfma_f32_16x16x32_bf16 v[132:135],a[220:223],a[172:175],v[192:195]\ns_waitcnt lgkmcnt(1)\n"
"v_exp_f32 v122,v122\nv_mfma_f32_16x16x32_bf16 v[132:135],a[216:219],a[168:171],v[132:135]\nds_read_b128 a[228:231],v246 offset:17408\nv_add_f32 v233,v233,v122\nv_mfma_f32_16x16x32_bf16 v[132:135],a[212:215],a[164:167],v[132:135]\n"
"v_exp_f32 v123,v123\nv_mfma_f32_16x16x32_bf16 v[132:135],a[208:211],a[160:163],v[132:135]\nv_add_f32 v233,v233,v123\nv_cvt_pk_bf16_f32 v173,v122,v123\nv_mfma_f32_16x16x32_bf16 v[140:143],a[208:211],a[176:179],v[196:199]\n"
"v_exp_f32 v124,v124\nv_mfma_f32_16x16x32_bf16 v[140:143],a[212:215],a[180:183],v[140:143]\nds_read_b128 a[232:235],v246 offset:18432\nv_add_f32 v233,v233,v124\nv_mfma_f32_16x16x32_bf16 v[140:143],a[216:219],a[184:187],v[140:143]\n"
"v_exp_f32 v125,v125\nv_mfma_f32_16x16x32_bf16 v[140:143],a[220:223],a[188:191],v[140:143]\nv_add_f32 v233,v233,v125\nv_cvt_pk_bf16_f32 v174,v124,v125\nv_mfma_f32_16x16x32_bf16 v[136:139],a[204:207],a[188:191],v[196:199]\n"
"v_exp_f32 v126,v126\nv_mfma_f32_16x16x32_bf16 v[136:139],a[200:203],a[184:187],v[136:139]\nds_read_b128 a[236:239],v246 offset:19456\nv_add_f32 v233,v233,v126\nv_mfma_f32_16x16x32_bf16 v[136:139],a[196:199],a[180:183],v[136:139]\n"
"v_exp_f32 v127,v127\nv_mfma_f32_16x16x32_bf16 v[136:139],a[192:195],a[176:179],v[136:139]\nv_add_f32 v233,v233,v127\nv_cvt_pk_bf16_f32 v175,v126,v127\nv_mfma_f32_16x16x32_bf16 v[144:147],a[192:195],a[128:131],v[224:227]\n"
"v_exp_f32 v128,v128\nv_mfma_f32_16x16x32_bf16 v[144:147],a[196:199],a[132:135],v[144:147]\nds_read_b128 a[240:243],v246 offset:20480\nv_add_f32 v236,v236,v128\nv_mfma_f32_16x16x32_bf16 v[144:147],a[200:203],a[136:139],v[144:147]\n"
"v_exp_f32 v129,v129\nv_mfma_f32_16x16x32_bf16 v[144:147],a[204:207],a[140:143],v[144:147]\nv_add_f32 v236,v236,v129\nv_cvt_pk_bf16_f32 v176,v128,v129\nv_mfma_f32_16x16x32_bf16 v[148:151],a[220:223],a[140:143],v[224:227]\n"
"v_exp_f32 v130,v130\nv_mfma_f32_16x16x32_bf16 v[148:151],a[216:219],a[136:139],v[148:151]\nds_read_b128 a[244:247],v246 offset:21504\nv_add_f32 v236,v236,v130\nv_mfma_f32_16x16x32_bf16 v[148:151],a[212:215],a[132:135],v[148:151]\n"
"v_exp_f32 v131,v131\nv_mfma_f32_16x16x32_bf16 v[148:151],a[208:211],a[128:131],v[148:151]\nv_add_f32 v236,v236,v131\nv_cvt_pk_bf16_f32 v177,v130,v131\nv_mfma_f32_16x16x32_bf16 v[156:159],a[208:211],a[144:147],v[228:231]\n"
"s_sub_u32 s52,s52,1\nv_exp_f32 v132,v132\nv_mfma_f32_16x16x32_bf16 v[156:159],a[212:215],a[148:151],v[156:159]\nds_read_b128 a[248:251],v246 offset:22528\nv_add_f32 v236,v236,v132\n"
"v_mfma_f32_16x16x32_bf16 v[156:159],a[216:219],a[152:155],v[156:159]\ns_cmp_lg_u32 s52,0\nv_exp_f32 v133,v133\nv_mfma_f32_16x16x32_bf16 v[156:159],a[220:223],a[156:159],v[156:159]\nds_read_b128 a[252:255],v246 offset:23552\n"
"v_add_f32 v236,v236,v133\nv_cvt_pk_bf16_f32 v178,v132,v133\nv_mfma_f32_16x16x32_bf16 v[152:155],a[204:207],a[156:159],v[228:231]\nv_exp_f32 v134,v134\nv_mfma_f32_16x16x32_bf16 v[152:155],a[200:203],a[152:155],v[152:155]\n"
"v_add_f32 v236,v236,v134\nv_mfma_f32_16x16x32_bf16 v[152:155],a[196:199],a[148:151],v[152:155]\nv_exp_f32 v135,v135\nv_mfma_f32_16x16x32_bf16 v[152:155],a[192:195],a[144:147],v[152:155]\ns_waitcnt vmcnt(0) lgkmcnt(0)\n"
"v_add_f32 v236,v236,v135\nv_cvt_pk_bf16_f32 v179,v134,v135\ns_cbranch_scc1 LL2_%=\ns_cmp_eq_u32 s65,0\ns_cbranch_scc1 LL2c_%=\ns_cmp_lg_u32 s62,0\ns_cbranch_scc1 LPE1_%=\nv_mov_b32 v248,s19\nv_mov_b32 v250,s98\n"
"v_mov_b32 v251,s99\ns_mov_b64 exec,1\nglobal_store_dwordx2 v248,v[250:251],s[60:61] sc1\ns_mov_b64 exec,-1\nLPE1_%=:\ns_mov_b32 s65,0\ns_mov_b32 s52,s64\ns_cmp_lg_u32 s52,0\ns_cbranch_scc1 LL2_%=\nLL2c_%=:\ns_getpc_b64 s[72:73]\n"
".Lgm_pc3:\ns_add_u32 s72,s72,.Lgm_tail-.Lgm_pc3\ns_addc_u32 s73,s73,0\ns_and_b32 s73,s73,0xffff\ns_mov_b32 s74,.Lgm_code_end-.Lgm_tail\ns_mov_b32 s75,0x20000\ns_lshr_b32 s55,s4,3\ns_lshl_b32 s55,s55,2\ns_add_u32 s55,s55,s54\n"
"s_lshl_b32 s55,s55,9\nv_lshrrev_b32 v249,1,v247\nbuffer_load_dword v251,v249,s[72:75],s55 offen\ns_cmp_lg_u32 s54,0\ns_cbranch_scc1 LW5_0_%=\nv_min_f32 v32,v224,v242\nv_min_f32 v33,v224,v243\nv_min_f32 v34,v224,v244\n"
"v_min_f32 v35,v224,v245\nv_mov_b32 v36,v228\nv_mov_b32 v37,v228\nv_mov_b32 v38,v228\nv_mov_b32 v39,v228\nv_mov_b32 v40,s63\nv_mov_b32 v41,s63\nv_mov_b32 v42,s63\nv_mov_b32 v43,s63\nv_min_f32 v44,v228,v242\n"
"v_min_f32 v45,v228,v243\nv_min_f32 v46,v228,v244\nv_min_f32 v47,v228,v245\nv_mov_b32 v48,s63\nv_mov_b32 v49,s63\nv_mov_b32 v50,s63\nv_mov_b32 v51,s63\nv_mov_b32 v52,s63\nv_mov_b32 v53,s63\nv_mov_b32 v54,s63\nv_mov_b32 v55,s63\n"
"v_mov_b32 v56,s63\nv_mov_b32 v57,s63\nv_mov_b32 v58,s63\nv_mov_b32 v59,s63\nv_mov_b32 v60,s63\nv_mov_b32 v61,s63\nv_mov_b32 v62,s63\nv_mov_b32 v63,s63\nv_mov_b32 v200,v240\nv_mov_b32 v201,v240\nv_mov_b32 v202,v240\n"
"v_mov_b32 v203,v240\nv_mov_b32 v204,v241\nv_mov_b32 v205,v241\nv_mov_b32 v206,v241\nv_mov_b32 v207,v241\nv_min_f32 v208,v240,v242\nv_min_f32 v209,v240,v243\nv_min_f32 v210,v240,v244\nv_min_f32 v211,v240,v245\nv_mov_b32 v212,v241\n"
"v_mov_b32 v213,v241\nv_mov_b32 v214,v241\nv_mov_b32 v215,v241\nv_mov_b32 v216,s63\nv_mov_b32 v217,s63\nv_mov_b32 v218,s63\nv_mov_b32 v219,s63\nv_min_f32 v220,v241,v242\nv_min_f32 v221,v241,v243\nv_min_f32 v222,v241,v244\n"
"v_min_f32 v223,v241,v245\nLW5_0_%=:\ns_cmp_lg_u32 s54,1\ns_cbranch_scc1 LW5_1_%=\nv_mov_b32 v32,v224\nv_mov_b32 v33,v224\nv_mov_b32 v34,v224\nv_mov_b32 v35,v224\nv_mov_b32 v36,v228\nv_mov_b32 v37,v228\nv_mov_b32 v38,v228\n"
"v_mov_b32 v39,v228\nv_mov_b32 v40,v224\nv_mov_b32 v41,v224\nv_mov_b32 v42,v224\nv_mov_b32 v43,v224\nv_mov_b32 v44,v228\nv_mov_b32 v45,v228\nv_mov_b32 v46,v228\nv_mov_b32 v47,v228\nv_mov_b32 v48,v224\nv_mov_b32 v49,v224\n"
"v_mov_b32 v50,v224\nv_mov_b32 v51,v224\nv_mov_b32 v52,v228\nv_mov_b32 v53,v228\nv_mov_b32 v54,v228\nv_mov_b32 v55,v228\nv_mov_b32 v56,v224\nv_mov_b32 v57,v224\nv_mov_b32 v58,v224\nv_mov_b32 v59,v224\nv_mov_b32 v60,v228\n"
"v_mov_b32 v61,v228\nv_mov_b32 v62,v228\nv_mov_b32 v63,v228\nv_mov_b32 v200,v240\nv_mov_b32 v201,v240\nv_mov_b32 v202,v240\nv_mov_b32 v203,v240\nv_mov_b32 v204,v241\nv_mov_b32 v205,v241\nv_mov_b32 v206,v241\nv_mov_b32 v207,v241\n"
"v_mov_b32 v208,v240\nv_mov_b32 v209,v240\nv_mov_b32 v210,v240\nv_mov_b32 v211,v240\nv_mov_b32 v212,v241\nv_mov_b32 v213,v241\nv_mov_b32 v214,v241\nv_mov_b32 v215,v241\nv_mov_b32 v216,v240\nv_mov_b32 v217,v240\n"
"v_mov_b32 v218,v240\nv_mov_b32 v219,v240\nv_mov_b32 v220,v241\nv_mov_b32 v221,v241\nv_mov_b32 v222,v241\nv_mov_b32 v223,v241\nLW5_1_%=:\ns_cmp_lg_u32 s54,2\ns_cbranch_scc1 LW5_2_%=\nv_mov_b32 v32,s63\nv_mov_b32 v33,s63\n"
"v_mov_b32 v34,s63\nv_mov_b32 v35,s63\nv_mov_b32 v36,s63\nv_mov_b32 v37,s63\nv_mov_b32 v38,s63\nv_mov_b32 v39,s63\nv_mov_b32 v40,s63\nv_mov_b32 v41,s63\nv_mov_b32 v42,s63\nv_mov_b32 v43,s63\nv_mov_b32 v44,s63\nv_mov_b32 v45,s63\n"
"v_mov_b32 v46,s63\nv_mov_b32 v47,s63\nv_mov_b32 v48,s63\nv_mov_b32 v49,s63\nv_mov_b32 v50,s63\nv_mov_b32 v51,s63\nv_mov_b32 v52,s63\nv_mov_b32 v53,s63\nv_mov_b32 v54,s63\nv_mov_b32 v55,s63\nv_mov_b32 v56,s63\nv_mov_b32 v57,s63\n"
"v_mov_b32 v58,s63\nv_mov_b32 v59,s63\nv_mov_b32 v60,s63\nv_mov_b32 v61,s63\nv_mov_b32 v62,s63\nv_mov_b32 v63,s63\nv_mov_b32 v192,s63\nv_mov_b32 v193,s63\nv_mov_b32 v194,s63\nv_mov_b32 v195,s63\nv_mov_b32 v196,s63\n"
"v_mov_b32 v197,s63\nv_mov_b32 v198,s63\nv_mov_b32 v199,s63\nv_mov_b32 v200,s63\nv_mov_b32 v201,s63\nv_mov_b32 v202,s63\nv_mov_b32 v203,s63\nv_mov_b32 v204,s63\nv_mov_b32 v205,s63\nv_mov_b32 v206,s63\nv_mov_b32 v207,s63\n"
"v_mov_b32 v208,s63\nv_mov_b32 v209,s63\nv_mov_b32 v210,s63\nv_mov_b32 v211,s63\nv_mov_b32 v212,s63\nv_mov_b32 v213,s63\nv_mov_b32 v214,s63\nv_mov_b32 v215,s63\nv_mov_b32 v216,s63\nv_mov_b32 v217,s63\nv_mov_b32 v218,s63\n"
"v_mov_b32 v219,s63\nv_mov_b32 v220,s63\nv_mov_b32 v221,s63\nv_mov_b32 v222,s63\nv_mov_b32 v223,s63\nLW5_2_%=:\ns_cmp_lg_u32 s54,3\ns_cbranch_scc1 LW5_3_%=\nv_mov_b32 v32,v224\nv_mov_b32 v33,v224\nv_mov_b32 v34,v224\n"
"v_mov_b32 v35,v224\nv_mov_b32 v36,v228\nv_mov_b32 v37,v228\nv_mov_b32 v38,v228\nv_mov_b32 v39,v228\nv_mov_b32 v40,v224\nv_mov_b32 v41,v224\nv_mov_b32 v42,v224\nv_mov_b32 v43,v224\nv_mov_b32 v44,v228\nv_mov_b32 v45,v228\n"
"v_mov_b32 v46,v228\nv_mov_b32 v47,v228\nv_min_f32 v48,v224,v242\nv_min_f32 v49,v224,v243\nv_min_f32 v50,v224,v244\nv_min_f32 v51,v224,v245\nv_mov_b32 v52,v228\nv_mov_b32 v53,v228\nv_mov_b32 v54,v228\nv_mov_b32 v55,v228\n"
"v_mov_b32 v56,s63\nv_mov_b32 v57,s63\nv_mov_b32 v58,s63\nv_mov_b32 v59,s63\nv_min_f32 v60,v228,v242\nv_min_f32 v61,v228,v243\nv_min_f32 v62,v228,v244\nv_min_f32 v63,v228,v245\nv_min_f32 v192,v240,v242\nv_min_f32 v193,v240,v243\n"
"v_min_f32 v194,v240,v244\nv_min_f32 v195,v240,v245\nv_mov_b32 v196,v241\nv_mov_b32 v197,v241\nv_mov_b32 v198,v241\nv_mov_b32 v199,v241\nv_mov_b32 v200,s63\nv_mov_b32 v201,s63\nv_mov_b32 v202,s63\nv_mov_b32 v203,s63\n"
"v_min_f32 v204,v241,v242\nv_min_f32 v205,v241,v243\nv_min_f32 v206,v241,v244\nv_min_f32 v207,v241,v245\nv_mov_b32 v208,s63\nv_mov_b32 v209,s63\nv_mov_b32 v210,s63\nv_mov_b32 v211,s63\nv_mov_b32 v212,s63\nv_mov_b32 v213,s63\n"
"v_mov_b32 v214,s63\nv_mov_b32 v215,s63\nv_mov_b32 v216,s63\nv_mov_b32 v217,s63\nv_mov_b32 v218,s63\nv_mov_b32 v219,s63\nv_mov_b32 v220,s63\nv_mov_b32 v221,s63\nv_mov_b32 v222,s63\nv_mov_b32 v223,s63\nLW5_3_%=:\n"
"v_lshrrev_b32 v248,1,v247\nv_add_u32 v248,s21,v248\nglobal_load_dwordx2 v[250:251],v248,s[60:61] offset:0 sc1\ns_and_b32 s56,s54,1\ns_mul_i32 s55,s56,12288\ns_sub_u32 s48,s48,s55\ns_sub_u32 s48,s48,40960\ns_lshl_b32 s56,s56,12\n"
"s_lshr_b32 s55,s54,1\ns_lshl_b32 s55,s55,15\ns_add_u32 s55,s55,s56\ns_add_u32 m0,s55,90112\nv_mfma_f32_16x16x32_bf16 a[64:67],a[224:227],v[160:163],a[64:67]\ns_barrier\nv_exp_f32 v136,v136\nv_xor_b32 v246,0x10000,v246\n"
"v_mfma_f32_16x16x32_bf16 a[68:71],a[224:227],v[164:167],a[68:71]\nds_read_b128 a[192:195],v246 offset:0\nv_add_f32 v237,v237,v136\nv_mfma_f32_16x16x32_bf16 v[0:3],a[224:227],v[168:171],v[0:3]\n"
"buffer_load_dwordx4 v247,s[36:39],s48 offen offset:0 sc1 lds\nv_exp_f32 v137,v137\nv_mfma_f32_16x16x32_bf16 v[4:7],a[224:227],v[172:175],v[4:7]\nv_add_f32 v237,v237,v137\nv_cvt_pk_bf16_f32 v180,v136,v137\n"
"v_mfma_f32_16x16x32_bf16 v[12:15],a[228:231],v[172:175],v[12:15]\nv_exp_f32 v138,v138\nv_mfma_f32_16x16x32_bf16 v[8:11],a[228:231],v[168:171],v[8:11]\nds_read_b128 a[196:199],v246 offset:1024\nv_add_f32 v237,v237,v138\n"
"v_mfma_f32_16x16x32_bf16 a[76:79],a[228:231],v[164:167],a[76:79]\nbuffer_load_dwordx4 v247,s[36:39],s48 offen offset:1024 sc1 lds\nv_exp_f32 v139,v139\nv_mfma_f32_16x16x32_bf16 a[72:75],a[228:231],v[160:163],a[72:75]\n"
"v_add_f32 v237,v237,v139\nv_cvt_pk_bf16_f32 v181,v138,v139\nv_mfma_f32_16x16x32_bf16 a[80:83],a[232:235],v[160:163],a[80:83]\nv_exp_f32 v140,v140\nv_mfma_f32_16x16x32_bf16 a[84:87],a[232:235],v[164:167],a[84:87]\n"
"ds_read_b128 a[200:203],v246 offset:2048\nv_add_f32 v237,v237,v140\nv_mfma_f32_16x16x32_bf16 v[16:19],a[232:235],v[168:171],v[16:19]\nbuffer_load_dwordx4 v247,s[36:39],s48 offen offset:2048 sc1 lds\nv_exp_f32 v141,v141\n"
"v_mfma_f32_16x16x32_bf16 v[20:23],a[232:235],v[172:175],v[20:23]\nv_add_f32 v237,v237,v141\nv_cvt_pk_bf16_f32 v182,v140,v141\nv_mfma_f32_16x16x32_bf16 v[28:31],a[236:239],v[172:175],v[28:31]\nv_exp_f32 v142,v142\n"
"v_mfma_f32_16x16x32_bf16 v[24:27],a[236:239],v[168:171],v[24:27]\nds_read_b128 a[204:207],v246 offset:3072\nv_add_f32 v237,v237,v142\nv_mfma_f32_16x16x32_bf16 a[92:95],a[236:239],v[164:167],a[92:95]\n"
"buffer_load_dwordx4 v247,s[36:39],s48 offen offset:3072 sc1 lds\nv_exp_f32 v143,v143\nv_mfma_f32_16x16x32_bf16 a[88:91],a[236:239],v[160:163],a[88:91]\ns_add_u32 s56,s20,0\nv_add_f32 v237,v237,v143\n"
"v_cvt_pk_bf16_f32 v183,v142,v143\nv_mfma_f32_16x16x32_bf16 a[96:99],a[240:243],v[160:163],a[96:99]\nv_exp_f32 v144,v144\nv_mfma_f32_16x16x32_bf16 a[100:103],a[240:243],v[164:167],a[100:103]\n"
"ds_read_b128 a[220:223],v246 offset:7168\nv_add_f32 v232,v232,v144\nv_mfma_f32_16x16x32_bf16 v[64:67],a[240:243],v[168:171],v[64:67]\nbuffer_load_dwordx4 a[0:3],v247,s[44:47],s56 offen offset:0 sc1\nv_exp_f32 v145,v145\n"
"v_mfma_f32_16x16x32_bf16 v[68:71],a[240:243],v[172:175],v[68:71]\nv_add_f32 v232,v232,v145\nv_cvt_pk_bf16_f32 v184,v144,v145\nv_mfma_f32_16x16x32_bf16 v[76:79],a[244:247],v[172:175],v[76:79]\nv_exp_f32 v146,v146\n"
"v_mfma_f32_16x16x32_bf16 v[72:75],a[244:247],v[168:171],v[72:75]\nds_read_b128 a[216:219],v246 offset:6144\nv_add_f32 v232,v232,v146\nv_mfma_f32_16x16x32_bf16 a[108:111],a[244:247],v[164:167],a[108:111]\n"
"buffer_load_dwordx4 a[4:7],v247,s[44:47],s56 offen offset:1024 sc1\nv_exp_f32 v147,v147\nv_mfma_f32_16x16x32_bf16 a[104:107],a[244:247],v[160:163],a[104:107]\ns_add_u32 s56,s20,8192\nv_add_f32 v232,v232,v147\n"
"v_cvt_pk_bf16_f32 v185,v146,v147\nv_mfma_f32_16x16x32_bf16 a[112:115],a[248:251],v[160:163],a[112:115]\nv_exp_f32 v148,v148\nv_mfma_f32_16x16x32_bf16 a[116:119],a[248:251],v[164:167],a[116:119]\n"
"ds_read_b128 a[212:215],v246 offset:5120\nv_add_f32 v232,v232,v148\nv_mfma_f32_16x16x32_bf16 v[80:83],a[248:251],v[168:171],v[80:83]\nbuffer_load_dwordx4 a[8:11],v247,s[44:47],s56 offen offset:0 sc1\nv_exp_f32 v149,v149\n"
"v_mfma_f32_16x16x32_bf16 v[84:87],a[248:251],v[172:175],v[84:87]\nv_add_f32 v232,v232,v149\nv_cvt_pk_bf16_f32 v186,v148,v149\nv_mfma_f32_16x16x32_bf16 v[92:95],a[252:255],v[172:175],v[92:95]\nv_exp_f32 v150,v150\n"
"v_mfma_f32_16x16x32_bf16 v[88:91],a[252:255],v[168:171],v[88:91]\nds_read_b128 a[208:211],v246 offset:4096\nv_add_f32 v232,v232,v150\nv_mfma_f32_16x16x32_bf16 a[124:127],a[252:255],v[164:167],a[124:127]\n"
"buffer_load_dwordx4 a[12:15],v247,s[44:47],s56 offen offset:1024 sc1\nv_exp_f32 v151,v151\nv_mfma_f32_16x16x32_bf16 a[120:123],a[252:255],v[160:163],a[120:123]\ns_waitcnt lgkmcnt(1)\nv_add_f32 v232,v232,v151\n"
"v_cvt_pk_bf16_f32 v187,v150,v151\nv_mfma_f32_16x16x32_bf16 v[96:99],a[192:195],a[160:163],v[192:195]\ns_add_u32 s56,s20,16384\nv_exp_f32 v152,v152\nv_mfma_f32_16x16x32_bf16 v[96:99],a[196:199],a[164:167],v[96:99]\n"
"ds_read_b128 a[224:227],v246 offset:24576\nv_add_f32 v233,v233,v152\nv_mfma_f32_16x16x32_bf16 v[96:99],a[200:203],a[168:171],v[96:99]\nbuffer_load_dwordx4 a[16:19],v247,s[44:47],s56 offen offset:0 sc1\nv_exp_f32 v153,v153\n"
"v_mfma_f32_16x16x32_bf16 v[96:99],a[204:207],a[172:175],v[96:99]\nv_add_f32 v233,v233,v153\nv_cvt_pk_bf16_f32 v188,v152,v153\nv_mfma_f32_16x16x32_bf16 v[100:103],a[220:223],a[172:175],v[200:203]\ns_waitcnt lgkmcnt(1)\n"
"v_exp_f32 v154,v154\nv_mfma_f32_16x16x32_bf16 v[100:103],a[216:219],a[168:171],v[100:103]\nds_read_b128 a[228:231],v246 offset:25600\nv_add_f32 v233,v233,v154\nv_mfma_f32_16x16x32_bf16 v[100:103],a[212:215],a[164:167],v[100:103]\n"
"buffer_load_dwordx4 a[20:23],v247,s[44:47],s56 offen offset:1024 sc1\nv_exp_f32 v155,v155\nv_mfma_f32_16x16x32_bf16 v[100:103],a[208:211],a[160:163],v[100:103]\ns_add_u32 s56,s20,24576\nv_add_f32 v233,v233,v155\n"
"v_cvt_pk_bf16_f32 v189,v154,v155\nv_mfma_f32_16x16x32_bf16 v[108:111],a[208:211],a[176:179],v[204:207]\nv_exp_f32 v156,v156\nv_mfma_f32_16x16x32_bf16 v[108:111],a[212:215],a[180:183],v[108:111]\n"
"ds_read_b128 a[232:235],v246 offset:26624\nv_add_f32 v233,v233,v156\nv_mfma_f32_16x16x32_bf16 v[108:111],a[216:219],a[184:187],v[108:111]\nbuffer_load_dwordx4 a[24:27],v247,s[44:47],s56 offen offset:0 sc1\nv_exp_f32 v157,v157\n"
"v_mfma_f32_16x16x32_bf16 v[108:111],a[220:223],a[188:191],v[108:111]\nv_add_f32 v233,v233,v157\nv_cvt_pk_bf16_f32 v190,v156,v157\nv_mfma_f32_16x16x32_bf16 v[104:107],a[204:207],a[188:191],v[196:199]\nv_exp_f32 v158,v158\n"
"v_mfma_f32_16x16x32_bf16 v[104:107],a[200:203],a[184:187],v[104:107]\nds_read_b128 a[236:239],v246 offset:27648\nv_add_f32 v233,v233,v158\nv_mfma_f32_16x16x32_bf16 v[104:107],a[196:199],a[180:183],v[104:107]\n"
"buffer_load_dwordx4 a[28:31],v247,s[44:47],s56 offen offset:1024 sc1\nv_exp_f32 v159,v159\nv_mfma_f32_16x16x32_bf16 v[104:107],a[192:195],a[176:179],v[104:107]\ns_add_u32 s56,s20,32768\nv_add_f32 v233,v233,v159\n"
"v_cvt_pk_bf16_f32 v191,v158,v159\nv_mfma_f32_16x16x32_bf16 v[112:115],a[192:195],a[128:131],v[32:35]\nv_exp_f32 v96,v96\nv_mfma_f32_16x16x32_bf16 v[112:115],a[196:199],a[132:135],v[112:115]\n"
"ds_read_b128 a[240:243],v246 offset:28672\nv_add_f32 v236,v236,v96\nv_mfma_f32_16x16x32_bf16 v[112:115],a[200:203],a[136:139],v[112:115]\nbuffer_load_dwordx4 a[32:35],v247,s[44:47],s56 offen offset:0 sc1\nv_exp_f32 v97,v97\n"
"v_mfma_f32_16x16x32_bf16 v[112:115],a[204:207],a[140:143],v[112:115]\nv_add_f32 v236,v236,v97\nv_cvt_pk_bf16_f32 v160,v96,v97\nv_mfma_f32_16x16x32_bf16 v[116:119],a[220:223],a[140:143],v[40:43]\nv_exp_f32 v98,v98\n"
"v_mfma_f32_16x16x32_bf16 v[116:119],a[216:219],a[136:139],v[116:119]\nds_read_b128 a[244:247],v246 offset:29696\nv_add_f32 v236,v236,v98\nv_mfma_f32_16x16x32_bf16 v[116:119],a[212:215],a[132:135],v[116:119]\n"
"buffer_load_dwordx4 a[36:39],v247,s[44:47],s56 offen offset:1024 sc1\nv_exp_f32 v99,v99\nv_mfma_f32_16x16x32_bf16 v[116:119],a[208:211],a[128:131],v[116:119]\ns_add_u32 s56,s20,40960\nv_add_f32 v236,v236,v99\n"
"v_cvt_pk_bf16_f32 v161,v98,v99\nv_mfma_f32_16x16x32_bf16 v[124:127],a[208:211],a[144:147],v[44:47]\nv_exp_f32 v100,v100\nv_mfma_f32_16x16x32_bf16 v[124:127],a[212:215],a[148:151],v[124:127]\n"
"ds_read_b128 a[248:251],v246 offset:30720\nv_add_f32 v236,v236,v100\nv_mfma_f32_16x16x32_bf16 v[124:127],a[216:219],a[152:155],v[124:127]\nbuffer_load_dwordx4 a[40:43],v247,s[44:47],s56 offen offset:0 sc1\nv_exp_f32 v101,v101\n"
"v_mfma_f32_16x16x32_bf16 v[124:127],a[220:223],a[156:159],v[124:127]\nv_add_f32 v236,v236,v101\nv_cvt_pk_bf16_f32 v162,v100,v101\nv_mfma_f32_16x16x32_bf16 v[120:123],a[204:207],a[156:159],v[36:39]\nv_exp_f32 v102,v102\n"
"v_mfma_f32_16x16x32_bf16 v[120:123],a[200:203],a[152:155],v[120:123]\nds_read_b128 a[252:255],v246 offset:31744\nv_add_f32 v236,v236,v102\nv_mfma_f32_16x16x32_bf16 v[120:123],a[196:199],a[148:151],v[120:123]\n"
"buffer_load_dwordx4 a[44:47],v247,s[44:47],s56 offen offset:1024 sc1\nv_exp_f32 v103,v103\nv_mfma_f32_16x16x32_bf16 v[120:123],a[192:195],a[144:147],v[120:123]\ns_waitcnt lgkmcnt(1)\nv_add_f32 v236,v236,v103\n"
"v_cvt_pk_bf16_f32 v163,v102,v103\nv_mfma_f32_16x16x32_bf16 a[64:67],a[224:227],v[176:179],a[64:67]\ns_add_u32 s56,s20,49152\nv_exp_f32 v104,v104\ns_cmp_lg_u32 s54,0\ns_cbranch_scc1 LCL4_%=\ns_xor_b32 s55,s24,0\n"
"s_lshl_b32 s55,s55,7\ns_add_u32 s55,s55,151552\nv_mov_b32 v249,s55\nv_mov_b32 v242,s98\nv_mov_b32 v243,s99\ns_mov_b64 exec,1\nglobal_atomic_swap_x2 v[242:243],v249,v[242:243],s[60:61] sc0 sc1\ns_mov_b64 exec,-1\nLCL4_%=:\n"
"v_mfma_f32_16x16x32_bf16 a[68:71],a[224:227],v[180:183],a[68:71]\nds_read_b128 a[192:195],v246 offset:8192\nv_add_f32 v237,v237,v104\nv_mfma_f32_16x16x32_bf16 v[0:3],a[224:227],v[184:187],v[0:3]\n"
"buffer_load_dwordx4 a[48:51],v247,s[44:47],s56 offen offset:0 sc1\nv_exp_f32 v105,v105\nv_mfma_f32_16x16x32_bf16 v[4:7],a[224:227],v[188:191],v[4:7]\nv_add_f32 v237,v237,v105\nv_cvt_pk_bf16_f32 v164,v104,v105\n"
"v_mfma_f32_16x16x32_bf16 v[12:15],a[228:231],v[188:191],v[12:15]\ns_waitcnt lgkmcnt(1)\nv_exp_f32 v106,v106\nv_mfma_f32_16x16x32_bf16 v[8:11],a[228:231],v[184:187],v[8:11]\nds_read_b128 a[196:199],v246 offset:9216\n"
"v_add_f32 v237,v237,v106\nv_mfma_f32_16x16x32_bf16 a[76:79],a[228:231],v[180:183],a[76:79]\nbuffer_load_dwordx4 a[52:55],v247,s[44:47],s56 offen offset:1024 sc1\nv_exp_f32 v107,v107\n"
"v_mfma_f32_16x16x32_bf16 a[72:75],a[228:231],v[176:179],a[72:75]\ns_add_u32 s56,s20,57344\nv_add_f32 v237,v237,v107\nv_cvt_pk_bf16_f32 v165,v106,v107\nv_mfma_f32_16x16x32_bf16 a[80:83],a[232:235],v[176:179],a[80:83]\n"
"v_exp_f32 v108,v108\nv_mfma_f32_16x16x32_bf16 a[84:87],a[232:235],v[180:183],a[84:87]\nds_read_b128 a[200:203],v246 offset:10240\nv_add_f32 v237,v237,v108\nv_mfma_f32_16x16x32_bf16 v[16:19],a[232:235],v[184:187],v[16:19]\n"
"buffer_load_dwordx4 a[56:59],v247,s[44:47],s56 offen offset:0 sc1\nv_exp_f32 v109,v109\nv_mfma_f32_16x16x32_bf16 v[20:23],a[232:235],v[188:191],v[20:23]\nv_add_f32 v237,v237,v109\nv_cvt_pk_bf16_f32 v166,v108,v109\n"
"v_mfma_f32_16x16x32_bf16 v[28:31],a[236:239],v[188:191],v[28:31]\nv_exp_f32 v110,v110\nv_mfma_f32_16x16x32_bf16 v[24:27],a[236:239],v[184:187],v[24:27]\nds_read_b128 a[204:207],v246 offset:11264\nv_add_f32 v237,v237,v110\n"
"v_mfma_f32_16x16x32_bf16 a[92:95],a[236:239],v[180:183],a[92:95]\nbuffer_load_dwordx4 a[60:63],v247,s[44:47],s56 offen offset:1024 sc1\nv_exp_f32 v111,v111\nv_mfma_f32_16x16x32_bf16 a[88:91],a[236:239],v[176:179],a[88:91]\n"
"s_waitcnt vmcnt(20)\nv_cmp_ne_u64 vcc,s[98:99],v[250:251]\ns_cmp_eq_u64 vcc,0\ns_cselect_b32 s49,0,0x40000000\ns_add_u32 s51,s49,s22\ns_lshl_b32 s50,s54,10\nv_add_f32 v237,v237,v111\nv_cvt_pk_bf16_f32 v167,v110,v111\n"
"v_mfma_f32_16x16x32_bf16 a[96:99],a[240:243],v[176:179],a[96:99]\ns_add_u32 m0,s50,65536\nv_exp_f32 v112,v112\nv_mfma_f32_16x16x32_bf16 a[100:103],a[240:243],v[180:183],a[100:103]\nds_read_b128 a[220:223],v246 offset:15360\n"
"v_add_f32 v232,v232,v112\nv_mfma_f32_16x16x32_bf16 v[64:67],a[240:243],v[184:187],v[64:67]\ns_add_u32 s48,s51,0\nbuffer_load_dwordx4 v247,s[44:47],s48 offen sc1 lds\nv_exp_f32 v113,v113\n"
"v_mfma_f32_16x16x32_bf16 v[68:71],a[240:243],v[188:191],v[68:71]\ns_add_u32 m0,s50,69632\nv_add_f32 v232,v232,v113\nv_cvt_pk_bf16_f32 v168,v112,v113\nv_mfma_f32_16x16x32_bf16 v[76:79],a[244:247],v[188:191],v[76:79]\n"
"s_add_u32 s48,s51,147456\nv_exp_f32 v114,v114\nv_mfma_f32_16x16x32_bf16 v[72:75],a[244:247],v[184:187],v[72:75]\nds_read_b128 a[216:219],v246 offset:14336\nv_add_f32 v232,v232,v114\n"
"v_mfma_f32_16x16x32_bf16 a[108:111],a[244:247],v[180:183],a[108:111]\nbuffer_load_dwordx4 v247,s[44:47],s48 offen sc1 lds\nv_exp_f32 v115,v115\nv_mfma_f32_16x16x32_bf16 a[104:107],a[244:247],v[176:179],a[104:107]\n"
"s_add_u32 m0,s50,73728\nv_add_f32 v232,v232,v115\nv_cvt_pk_bf16_f32 v169,v114,v115\nv_mfma_f32_16x16x32_bf16 a[112:115],a[248:251],v[176:179],a[112:115]\ns_add_u32 s48,s51,294912\nv_exp_f32 v116,v116\n"
"v_mfma_f32_16x16x32_bf16 a[116:119],a[248:251],v[180:183],a[116:119]\nds_read_b128 a[212:215],v246 offset:13312\nv_add_f32 v232,v232,v116\nv_mfma_f32_16x16x32_bf16 v[80:83],a[248:251],v[184:187],v[80:83]\n"
"buffer_load_dwordx4 v247,s[44:47],s48 offen sc1 lds\nv_exp_f32 v117,v117\nv_mfma_f32_16x16x32_bf16 v[84:87],a[248:251],v[188:191],v[84:87]\ns_add_u32 m0,s50,77824\nv_add_f32 v232,v232,v117\nv_cvt_pk_bf16_f32 v170,v116,v117\n"
"v_mfma_f32_16x16x32_bf16 v[92:95],a[252:255],v[188:191],v[92:95]\ns_add_u32 s48,s51,4096\nv_exp_f32 v118,v118\nv_mfma_f32_16x16x32_bf16 v[88:91],a[252:255],v[184:187],v[88:91]\nds_read_b128 a[208:211],v246 offset:12288\n"
"v_add_f32 v232,v232,v118\nv_mfma_f32_16x16x32_bf16 a[124:127],a[252:255],v[180:183],a[124:127]\nbuffer_load_dwordx4 v247,s[44:47],s48 offen sc1 lds\nv_exp_f32 v119,v119\n"
"v_mfma_f32_16x16x32_bf16 a[120:123],a[252:255],v[176:179],a[120:123]\ns_waitcnt lgkmcnt(1)\nv_add_f32 v232,v232,v119\nv_cvt_pk_bf16_f32 v171,v118,v119\nv_mfma_f32_16x16x32_bf16 v[128:131],a[192:195],a[160:163],v[208:211]\n"
"s_add_u32 m0,s50,81920\nv_exp_f32 v120,v120\nv_mfma_f32_16x16x32_bf16 v[128:131],a[196:199],a[164:167],v[128:131]\nds_read_b128 a[224:227],v246 offset:16384\nv_add_f32 v233,v233,v120\n"
"v_mfma_f32_16x16x32_bf16 v[128:131],a[200:203],a[168:171],v[128:131]\ns_add_u32 s48,s51,151552\nbuffer_load_dwordx4 v247,s[44:47],s48 offen sc1 lds\nv_exp_f32 v121,v121\n"
"v_mfma_f32_16x16x32_bf16 v[128:131],a[204:207],a[172:175],v[128:131]\ns_add_u32 m0,s50,86016\nv_add_f32 v233,v233,v121\nv_cvt_pk_bf16_f32 v172,v120,v121\nv_mfma_f32_16x16x32_bf16 v[132:135],a[220:223],a[172:175],v[216:219]\n"
"s_waitcnt lgkmcnt(1)\nv_exp_f32 v122,v122\nv_mfma_f32_16x16x32_bf16 v[132:135],a[216:219],a[168:171],v[132:135]\nds_read_b128 a[228:231],v246 offset:17408\nv_add_f32 v233,v233,v122\n"
"v_mfma_f32_16x16x32_bf16 v[132:135],a[212:215],a[164:167],v[132:135]\ns_add_u32 s48,s51,299008\nbuffer_load_dwordx4 v247,s[44:47],s48 offen sc1 lds\nv_exp_f32 v123,v123\n"
"v_mfma_f32_16x16x32_bf16 v[132:135],a[208:211],a[160:163],v[132:135]\ns_add_u32 m0,s50,98304\nv_add_f32 v233,v233,v123\nv_cvt_pk_bf16_f32 v173,v122,v123\nv_mfma_f32_16x16x32_bf16 v[140:143],a[208:211],a[176:179],v[220:223]\n"
"s_add_u32 s48,s51,8192\nv_exp_f32 v124,v124\nv_mfma_f32_16x16x32_bf16 v[140:143],a[212:215],a[180:183],v[140:143]\nds_read_b128 a[232:235],v246 offset:18432\nv_add_f32 v233,v233,v124\n"
"v_mfma_f32_16x16x32_bf16 v[140:143],a[216:219],a[184:187],v[140:143]\nbuffer_load_dwordx4 v247,s[44:47],s48 offen sc1 lds\nv_exp_f32 v125,v125\nv_mfma_f32_16x16x32_bf16 v[140:143],a[220:223],a[188:191],v[140:143]\n"
"s_add_u32 m0,s50,102400\nv_add_f32 v233,v233,v125\nv_cvt_pk_bf16_f32 v174,v124,v125\nv_mfma_f32_16x16x32_bf16 v[136:139],a[204:207],a[188:191],v[212:215]\ns_add_u32 s48,s51,155648\nv_exp_f32 v126,v126\n"
"v_mfma_f32_16x16x32_bf16 v[136:139],a[200:203],a[184:187],v[136:139]\nds_read_b128 a[236:239],v246 offset:19456\nv_add_f32 v233,v233,v126\nv_mfma_f32_16x16x32_bf16 v[136:139],a[196:199],a[180:183],v[136:139]\n"
"buffer_load_dwordx4 v247,s[44:47],s48 offen sc1 lds\nv_exp_f32 v127,v127\nv_mfma_f32_16x16x32_bf16 v[136:139],a[192:195],a[176:179],v[136:139]\ns_add_u32 m0,s50,106496\nv_add_f32 v233,v233,v127\nv_cvt_pk_bf16_f32 v175,v126,v127\n"
"v_mfma_f32_16x16x32_bf16 v[144:147],a[192:195],a[128:131],v[48:51]\ns_add_u32 s48,s51,303104\nv_exp_f32 v128,v128\nv_mfma_f32_16x16x32_bf16 v[144:147],a[196:199],a[132:135],v[144:147]\nds_read_b128 a[240:243],v246 offset:20480\n"
"v_add_f32 v236,v236,v128\nv_mfma_f32_16x16x32_bf16 v[144:147],a[200:203],a[136:139],v[144:147]\nbuffer_load_dwordx4 v247,s[44:47],s48 offen sc1 lds\nv_exp_f32 v129,v129\n"
"v_mfma_f32_16x16x32_bf16 v[144:147],a[204:207],a[140:143],v[144:147]\ns_add_u32 m0,s50,110592\nv_add_f32 v236,v236,v129\nv_cvt_pk_bf16_f32 v176,v128,v129\nv_mfma_f32_16x16x32_bf16 v[148:151],a[220:223],a[140:143],v[56:59]\n"
"s_add_u32 s48,s51,12288\nv_exp_f32 v130,v130\nv_mfma_f32_16x16x32_bf16 v[148:151],a[216:219],a[136:139],v[148:151]\nds_read_b128 a[244:247],v246 offset:21504\nv_add_f32 v236,v236,v130\n"
"v_mfma_f32_16x16x32_bf16 v[148:151],a[212:215],a[132:135],v[148:151]\nbuffer_load_dwordx4 v247,s[44:47],s48 offen sc1 lds\nv_exp_f32 v131,v131\nv_mfma_f32_16x16x32_bf16 v[148:151],a[208:211],a[128:131],v[148:151]\n"
"s_add_u32 m0,s50,114688\nv_add_f32 v236,v236,v131\nv_cvt_pk_bf16_f32 v177,v130,v131\nv_mfma_f32_16x16x32_bf16 v[156:159],a[208:211],a[144:147],v[60:63]\ns_add_u32 s48,s51,159744\nv_exp_f32 v132,v132\n"
"v_mfma_f32_16x16x32_bf16 v[156:159],a[212:215],a[148:151],v[156:159]\nds_read_b128 a[248:251],v246 offset:22528\nv_add_f32 v236,v236,v132\nv_mfma_f32_16x16x32_bf16 v[156:159],a[216:219],a[152:155],v[156:159]\n"
"buffer_load_dwordx4 v247,s[44:47],s48 offen sc1 lds\nv_exp_f32 v133,v133\nv_mfma_f32_16x16x32_bf16 v[156:159],a[220:223],a[156:159],v[156:159]\nds_read_b128 a[252:255],v246 offset:23552\nv_add_f32 v236,v236,v133\n"
"v_cvt_pk_bf16_f32 v178,v132,v133\nv_mfma_f32_16x16x32_bf16 v[152:155],a[204:207],a[156:159],v[52:55]\nv_exp_f32 v134,v134\nv_mfma_f32_16x16x32_bf16 v[152:155],a[200:203],a[152:155],v[152:155]\nv_add_f32 v236,v236,v134\n"
"v_mfma_f32_16x16x32_bf16 v[152:155],a[196:199],a[148:151],v[152:155]\nv_exp_f32 v135,v135\nv_mfma_f32_16x16x32_bf16 v[152:155],a[192:195],a[144:147],v[152:155]\ns_waitcnt vmcnt(27) lgkmcnt(0)\nv_add_f32 v236,v236,v135\n"
"v_cvt_pk_bf16_f32 v179,v134,v135\nLA3_%=:\n.Lgm_tail:\nv_mfma_f32_16x16x32_bf16 a[64:67],a[224:227],v[160:163],a[64:67]\ns_barrier\nv_exp_f32 v136,v136\nv_xor_b32 v246,0x10000,v246\nv_and_b32 v203,0xf0,v247\n"
"v_lshrrev_b32 v203,2,v203\nv_mfma_f32_16x16x32_bf16 a[68:71],a[224:227],v[164:167],a[68:71]\nds_read_b128 a[192:195],v246 offset:24576\nv_add_f32 v237,v237,v136\nv_mfma_f32_16x16x32_bf16 a[76:79],a[228:231],v[164:167],a[76:79]\n"
"ds_read_b128 a[196:199],v246 offset:25600\nv_exp_f32 v137,v137\nv_mfma_f32_16x16x32_bf16 a[72:75],a[228:231],v[160:163],a[72:75]\nds_read_b128 a[200:203],v246 offset:26624\nv_add_f32 v237,v237,v137\n"
"v_cvt_pk_bf16_f32 v180,v136,v137\nv_mfma_f32_16x16x32_bf16 a[80:83],a[232:235],v[160:163],a[80:83]\nds_read_b128 a[204:207],v246 offset:27648\nv_exp_f32 v138,v138\nv_mfma_f32_16x16x32_bf16 a[84:87],a[232:235],v[164:167],a[84:87]\n"
"ds_read_b128 a[208:211],v246 offset:28672\nv_add_f32 v237,v237,v138\nv_mfma_f32_16x16x32_bf16 a[92:95],a[236:239],v[164:167],a[92:95]\nds_read_b128 a[212:215],v246 offset:29696\nv_exp_f32 v139,v139\n"
"v_mfma_f32_16x16x32_bf16 a[88:91],a[236:239],v[160:163],a[88:91]\nds_read_b128 a[216:219],v246 offset:30720\nv_add_f32 v237,v237,v139\nv_cvt_pk_bf16_f32 v181,v138,v139\n"
"v_mfma_f32_16x16x32_bf16 a[96:99],a[240:243],v[160:163],a[96:99]\nds_read_b128 a[220:223],v246 offset:31744\nv_exp_f32 v140,v140\ns_cmp_lg_u32 s54,0\ns_cbranch_scc1 LCL5_%=\ns_xor_b32 s55,s24,1\ns_lshl_b32 s55,s55,7\n"
"s_add_u32 s55,s55,151552\nv_mov_b32 v249,s55\nv_mov_b32 v244,s98\nv_mov_b32 v245,s99\ns_mov_b64 exec,1\nglobal_atomic_swap_x2 v[244:245],v249,v[244:245],s[60:61] sc0 sc1\ns_mov_b64 exec,-1\nLCL5_%=:\n"
"v_mfma_f32_16x16x32_bf16 a[100:103],a[240:243],v[164:167],a[100:103]\ns_lshl_b32 s55,s57,6\nv_add_f32 v237,v237,v140\nv_mfma_f32_16x16x32_bf16 a[108:111],a[244:247],v[164:167],a[108:111]\ns_add_u32 s55,s55,70025216\n"
"v_exp_f32 v141,v141\nv_mfma_f32_16x16x32_bf16 a[104:107],a[244:247],v[160:163],a[104:107]\nbuffer_load_dword v224,v203,s[44:47],s55 offen sc1\nv_add_f32 v237,v237,v141\nv_cvt_pk_bf16_f32 v182,v140,v141\n"
"v_mfma_f32_16x16x32_bf16 a[112:115],a[248:251],v[160:163],a[112:115]\nbuffer_load_dword v225,v203,s[44:47],s55 offen offset:64 sc1\nv_exp_f32 v142,v142\nv_mfma_f32_16x16x32_bf16 a[116:119],a[248:251],v[164:167],a[116:119]\n"
"s_mul_i32 s64,s57,147456\nv_add_f32 v237,v237,v142\nv_mfma_f32_16x16x32_bf16 a[124:127],a[252:255],v[164:167],a[124:127]\ns_add_u32 s64,s64,s59\nv_exp_f32 v143,v143\n"
"v_mfma_f32_16x16x32_bf16 a[120:123],a[252:255],v[160:163],a[120:123]\ns_waitcnt lgkmcnt(0)\nv_add_f32 v237,v237,v143\nv_cvt_pk_bf16_f32 v183,v142,v143\nv_mfma_f32_16x16x32_bf16 a[64:67],a[192:195],v[176:179],a[64:67]\n"
"s_add_u32 s56,s20,65536\nbuffer_load_dwordx4 a[128:131],v247,s[44:47],s56 offen offset:0 sc1\nv_exp_f32 v144,v144\nv_mfma_f32_16x16x32_bf16 a[68:71],a[192:195],v[180:183],a[68:71]\ns_add_u32 s65,s64,147456\n"
"v_add_f32 v232,v232,v144\nv_mfma_f32_16x16x32_bf16 a[76:79],a[196:199],v[180:183],a[76:79]\ns_lshl_b32 s55,s54,14\nv_exp_f32 v145,v145\nv_mfma_f32_16x16x32_bf16 a[72:75],a[196:199],v[176:179],a[72:75]\ns_lshl_b32 s66,s54,9\n"
"v_add_f32 v232,v232,v145\nv_cvt_pk_bf16_f32 v184,v144,v145\nv_mfma_f32_16x16x32_bf16 a[80:83],a[200:203],v[176:179],a[80:83]\nbuffer_load_dwordx4 a[132:135],v247,s[44:47],s56 offen offset:1024 sc1\nv_exp_f32 v146,v146\n"
"v_mfma_f32_16x16x32_bf16 a[84:87],a[200:203],v[180:183],a[84:87]\ns_add_u32 s66,s66,0x27800\nv_add_f32 v232,v232,v146\nv_mfma_f32_16x16x32_bf16 a[92:95],a[204:207],v[180:183],a[92:95]\nv_exp_f32 v147,v147\n"
"v_mfma_f32_16x16x32_bf16 a[88:91],a[204:207],v[176:179],a[88:91]\nv_add_f32 v232,v232,v147\nv_cvt_pk_bf16_f32 v185,v146,v147\nv_mfma_f32_16x16x32_bf16 a[96:99],a[208:211],v[176:179],a[96:99]\ns_add_u32 s56,s20,73728\n"
"buffer_load_dwordx4 a[136:139],v247,s[44:47],s56 offen offset:0 sc1\nv_exp_f32 v148,v148\nv_mfma_f32_16x16x32_bf16 a[100:103],a[208:211],v[180:183],a[100:103]\nv_add_f32 v232,v232,v148\n"
"v_mfma_f32_16x16x32_bf16 a[108:111],a[212:215],v[180:183],a[108:111]\nv_exp_f32 v149,v149\nv_mfma_f32_16x16x32_bf16 a[104:107],a[212:215],v[176:179],a[104:107]\nv_add_f32 v232,v232,v149\nv_cvt_pk_bf16_f32 v186,v148,v149\n"
"v_mfma_f32_16x16x32_bf16 a[112:115],a[216:219],v[176:179],a[112:115]\nbuffer_load_dwordx4 a[140:143],v247,s[44:47],s56 offen offset:1024 sc1\nv_exp_f32 v150,v150\n"
"v_mfma_f32_16x16x32_bf16 a[116:119],a[216:219],v[180:183],a[116:119]\nv_add_f32 v232,v232,v150\nv_mfma_f32_16x16x32_bf16 a[124:127],a[220:223],v[180:183],a[124:127]\nv_exp_f32 v151,v151\nv_add_u32 v200,s55,v247\n"
"v_xor_b32 v201,0x8000,v200\nv_lshrrev_b32 v202,1,v247\nv_add_u32 v202,s66,v202\nv_mfma_f32_16x16x32_bf16 a[120:123],a[220:223],v[176:179],a[120:123]\nds_write_b64 v202,v[236:237]\nv_add_f32 v232,v232,v151\n"
"v_cvt_pk_bf16_f32 v187,v150,v151\nv_mfma_f32_16x16x32_bf16 v[0:3],a[224:227],v[168:171],v[0:3]\ns_add_u32 s56,s20,81920\nbuffer_load_dwordx4 a[144:147],v247,s[44:47],s56 offen offset:0 sc1\nv_exp_f32 v152,v152\n"
"v_mfma_f32_16x16x32_bf16 v[4:7],a[224:227],v[172:175],v[4:7]\nds_write_b128 v200,a[64:67] offset:0\nv_add_f32 v233,v233,v152\nv_mfma_f32_16x16x32_bf16 v[12:15],a[228:231],v[172:175],v[12:15]\nv_exp_f32 v153,v153\n"
"v_mfma_f32_16x16x32_bf16 v[8:11],a[228:231],v[168:171],v[8:11]\nds_write_b128 v200,a[68:71] offset:1024\nv_add_f32 v233,v233,v153\nv_cvt_pk_bf16_f32 v188,v152,v153\n"
"v_mfma_f32_16x16x32_bf16 v[16:19],a[232:235],v[168:171],v[16:19]\nbuffer_load_dwordx4 a[148:151],v247,s[44:47],s56 offen offset:1024 sc1\nv_exp_f32 v154,v154\nv_mfma_f32_16x16x32_bf16 v[20:23],a[232:235],v[172:175],v[20:23]\n"
"ds_write_b128 v200,a[72:75] offset:2048\nv_add_f32 v233,v233,v154\nv_mfma_f32_16x16x32_bf16 v[28:31],a[236:239],v[172:175],v[28:31]\nv_exp_f32 v155,v155\nv_mfma_f32_16x16x32_bf16 v[24:27],a[236:239],v[168:171],v[24:27]\n"
"ds_write_b128 v200,a[76:79] offset:3072\nv_add_f32 v233,v233,v155\nv_cvt_pk_bf16_f32 v189,v154,v155\nv_mfma_f32_16x16x32_bf16 v[64:67],a[240:243],v[168:171],v[64:67]\ns_add_u32 s56,s20,90112\n"
"buffer_load_dwordx4 a[152:155],v247,s[44:47],s56 offen offset:0 sc1\nv_exp_f32 v156,v156\nv_mfma_f32_16x16x32_bf16 v[68:71],a[240:243],v[172:175],v[68:71]\nds_write_b128 v200,a[80:83] offset:4096\nv_add_f32 v233,v233,v156\n"
"v_mfma_f32_16x16x32_bf16 v[76:79],a[244:247],v[172:175],v[76:79]\nv_exp_f32 v157,v157\nv_mfma_f32_16x16x32_bf16 v[72:75],a[244:247],v[168:171],v[72:75]\nds_write_b128 v200,a[84:87] offset:5120\nv_add_f32 v233,v233,v157\n"
"v_cvt_pk_bf16_f32 v190,v156,v157\nv_mfma_f32_16x16x32_bf16 v[80:83],a[248:251],v[168:171],v[80:83]\nbuffer_load_dwordx4 a[156:159],v247,s[44:47],s56 offen offset:1024 sc1\nv_exp_f32 v158,v158\n"
"v_mfma_f32_16x16x32_bf16 v[84:87],a[248:251],v[172:175],v[84:87]\nds_write_b128 v200,a[88:91] offset:6144\nv_add_f32 v233,v233,v158\nv_mfma_f32_16x16x32_bf16 v[92:95],a[252:255],v[172:175],v[92:95]\nv_exp_f32 v159,v159\n"
"v_mfma_f32_16x16x32_bf16 v[88:91],a[252:255],v[168:171],v[88:91]\nds_write_b128 v200,a[92:95] offset:7168\nv_add_f32 v233,v233,v159\nv_cvt_pk_bf16_f32 v191,v158,v159\nv_mfma_f32_16x16x32_bf16 v[0:3],a[192:195],v[184:187],v[0:3]\n"
"s_add_u32 s56,s20,98304\nbuffer_load_dwordx4 a[160:163],v247,s[44:47],s56 offen offset:0 sc1\nv_mfma_f32_16x16x32_bf16 v[4:7],a[192:195],v[188:191],v[4:7]\nds_write_b128 v200,a[96:99] offset:8192\n"
"v_mfma_f32_16x16x32_bf16 v[12:15],a[196:199],v[188:191],v[12:15]\nv_mfma_f32_16x16x32_bf16 v[8:11],a[196:199],v[184:187],v[8:11]\nds_write_b128 v200,a[100:103] offset:9216\n"
"v_mfma_f32_16x16x32_bf16 v[16:19],a[200:203],v[184:187],v[16:19]\nbuffer_load_dwordx4 a[164:167],v247,s[44:47],s56 offen offset:1024 sc1\nv_mfma_f32_16x16x32_bf16 v[20:23],a[200:203],v[188:191],v[20:23]\n"
"ds_write_b128 v200,a[104:107] offset:10240\nv_mfma_f32_16x16x32_bf16 v[28:31],a[204:207],v[188:191],v[28:31]\nv_mfma_f32_16x16x32_bf16 v[24:27],a[204:207],v[184:187],v[24:27]\nds_write_b128 v200,a[108:111] offset:11264\n"
"v_mfma_f32_16x16x32_bf16 v[64:67],a[208:211],v[184:187],v[64:67]\ns_add_u32 s56,s20,106496\nbuffer_load_dwordx4 a[168:171],v247,s[44:47],s56 offen offset:0 sc1\nv_mfma_f32_16x16x32_bf16 v[68:71],a[208:211],v[188:191],v[68:71]\n"
"ds_write_b128 v200,a[112:115] offset:12288\nv_mfma_f32_16x16x32_bf16 v[76:79],a[212:215],v[188:191],v[76:79]\nv_mfma_f32_16x16x32_bf16 v[72:75],a[212:215],v[184:187],v[72:75]\nds_write_b128 v200,a[116:119] offset:13312\n"
"v_mfma_f32_16x16x32_bf16 v[80:83],a[216:219],v[184:187],v[80:83]\nbuffer_load_dwordx4 a[172:175],v247,s[44:47],s56 offen offset:1024 sc1\nv_mfma_f32_16x16x32_bf16 v[84:87],a[216:219],v[188:191],v[84:87]\n"
"ds_write_b128 v200,a[120:123] offset:14336\nv_mfma_f32_16x16x32_bf16 v[92:95],a[220:223],v[188:191],v[92:95]\nv_mfma_f32_16x16x32_bf16 v[88:91],a[220:223],v[184:187],v[88:91]\nds_write_b128 v200,a[124:127] offset:15360\n"
"s_waitcnt lgkmcnt(0)\ns_barrier\nv_xor_b32 v202,0x400,v202\nds_read_b64 v[236:237],v202\nds_read_b128 v[96:99],v201 offset:0\nds_read_b128 v[100:103],v201 offset:1024\nds_read_b128 v[104:107],v201 offset:2048\n"
"ds_read_b128 v[108:111],v201 offset:3072\nds_read_b128 v[112:115],v201 offset:4096\nds_read_b128 v[116:119],v201 offset:5120\nds_read_b128 v[120:123],v201 offset:6144\nds_read_b128 v[124:127],v201 offset:7168\n"
"ds_read_b128 v[128:131],v201 offset:8192\nds_read_b128 v[132:135],v201 offset:9216\nds_read_b128 v[136:139],v201 offset:10240\nds_read_b128 v[140:143],v201 offset:11264\nds_read_b128 v[144:147],v201 offset:12288\n"
"ds_read_b128 v[148:151],v201 offset:13312\nds_read_b128 v[152:155],v201 offset:14336\nds_read_b128 v[156:159],v201 offset:15360\ns_waitcnt vmcnt(12)\ns_cmp_lg_u32 s53,16\ns_cbranch_scc1 LPE2_%=\ns_cmp_lg_u32 s62,0\n"
"s_cbranch_scc1 LPE2_%=\nv_mov_b32 v248,s19\nv_mov_b32 v250,s98\nv_mov_b32 v251,s99\ns_mov_b64 exec,1\nglobal_store_dwordx2 v248,v[250:251],s[60:61] sc1\ns_mov_b64 exec,-1\nLPE2_%=:\ns_cmp_lg_u32 s54,0\ns_cbranch_scc1 LCD_%=\n"
"v_cmp_ne_u64 vcc,s[98:99],v[244:245]\nv_cmp_eq_u64 s[68:69],s[98:99],v[242:243]\nv_mov_b32 v243,0x27000\ns_and_b32 s55,vcc_lo,1\ns_bitcmp1_b32 s68,0\ns_cselect_b32 s55,2,s55\nv_mov_b32 v242,s55\nds_write_b32 v243,v242\nLCD_%=:\n"
"s_add_u32 s56,s20,114688\nbuffer_load_dwordx4 a[176:179],v247,s[44:47],s56 offen offset:0 sc1\nbuffer_load_dwordx4 a[180:183],v247,s[44:47],s56 offen offset:1024 sc1\ns_add_u32 s56,s20,122880\n"
"buffer_load_dwordx4 a[184:187],v247,s[44:47],s56 offen offset:0 sc1\nbuffer_load_dwordx4 a[188:191],v247,s[44:47],s56 offen offset:1024 sc1\ns_add_u32 m0,s50,118784\ns_add_u32 s48,s51,307200\n"
"buffer_load_dwordx4 v247,s[44:47],s48 offen sc1 lds\ns_add_u32 s56,s20,131072\nbuffer_load_dwordx4 a[192:195],v247,s[44:47],s56 offen offset:0 sc1\nbuffer_load_dwordx4 a[196:199],v247,s[44:47],s56 offen offset:1024 sc1\n"
"s_waitcnt lgkmcnt(15)\nv_add_f32 v232,v232,v236\nv_add_f32 v233,v233,v237\nv_mov_b32 v160,v232\nv_mov_b32 v161,v233\ns_nop 1\nv_permlane16_swap_b32 v160,v232\nv_permlane16_swap_b32 v161,v233\ns_nop 1\nv_add_f32 v232,v232,v160\n"
"v_add_f32 v233,v233,v161\nv_mov_b32 v160,v232\nv_mov_b32 v161,v233\ns_nop 1\nv_permlane32_swap_b32 v160,v232\nv_permlane32_swap_b32 v161,v233\ns_nop 1\nv_add_f32 v232,v232,v160\nv_add_f32 v233,v233,v161\n"
"v_mov_b32 v160,0x6f800000\nv_mov_b32 v161,0x1f800000\nv_cmp_nle_f32 s[68:69],v232,v160\nv_cmp_nge_f32 s[70:71],v232,v161\nv_rcp_f32 v232,v232\nv_sub_f32 v224,1.0,v224\ns_or_b64 s[68:69],s[68:69],s[70:71]\ns_cselect_b32 s55,1,0\n"
"s_or_b32 s62,s62,s55\nv_mul_f32 v248,v224,v232\nv_mul_f32 v249,v224,v232\nv_cmp_nle_f32 s[68:69],v233,v160\nv_cmp_nge_f32 s[70:71],v233,v161\nv_rcp_f32 v233,v233\nv_sub_f32 v225,1.0,v225\ns_or_b64 s[68:69],s[68:69],s[70:71]\n"
"s_cselect_b32 s55,2,0\ns_or_b32 s62,s62,s55\nv_mul_f32 v250,v225,v233\nv_mul_f32 v251,v225,v233\ns_waitcnt lgkmcnt(0)\nv_pk_add_f32 v[0:1],v[0:1],v[96:97]\nv_pk_add_f32 v[2:3],v[2:3],v[98:99]\n"
"v_pk_add_f32 v[8:9],v[8:9],v[104:105]\nv_pk_add_f32 v[10:11],v[10:11],v[106:107]\nv_pk_mul_f32 v[0:1],v[0:1],v[248:249]\nv_pk_mul_f32 v[2:3],v[2:3],v[248:249]\nv_pk_mul_f32 v[8:9],v[8:9],v[248:249]\n"
"v_pk_mul_f32 v[10:11],v[10:11],v[248:249]\nv_cvt_pk_bf16_f32 v96,v0,v1\nv_cvt_pk_bf16_f32 v97,v2,v3\nv_cvt_pk_bf16_f32 v98,v8,v9\nv_cvt_pk_bf16_f32 v99,v10,v11\nbuffer_store_dwordx4 v[96:99],v247,s[44:47],s64 offen offset:0 sc1\n"
"v_pk_add_f32 v[16:17],v[16:17],v[112:113]\nv_pk_add_f32 v[18:19],v[18:19],v[114:115]\nv_pk_add_f32 v[24:25],v[24:25],v[120:121]\nv_pk_add_f32 v[26:27],v[26:27],v[122:123]\nv_pk_mul_f32 v[16:17],v[16:17],v[248:249]\n"
"v_pk_mul_f32 v[18:19],v[18:19],v[248:249]\nv_pk_mul_f32 v[24:25],v[24:25],v[248:249]\nv_pk_mul_f32 v[26:27],v[26:27],v[248:249]\nv_cvt_pk_bf16_f32 v112,v16,v17\nv_cvt_pk_bf16_f32 v113,v18,v19\nv_cvt_pk_bf16_f32 v114,v24,v25\n"
"v_cvt_pk_bf16_f32 v115,v26,v27\nbuffer_store_dwordx4 v[112:115],v247,s[44:47],s64 offen offset:1024 sc1\nv_pk_add_f32 v[64:65],v[64:65],v[128:129]\nv_pk_add_f32 v[66:67],v[66:67],v[130:131]\n"
"v_pk_add_f32 v[72:73],v[72:73],v[136:137]\nv_pk_add_f32 v[74:75],v[74:75],v[138:139]\nv_pk_mul_f32 v[64:65],v[64:65],v[248:249]\nv_pk_mul_f32 v[66:67],v[66:67],v[248:249]\nv_pk_mul_f32 v[72:73],v[72:73],v[248:249]\n"
"v_pk_mul_f32 v[74:75],v[74:75],v[248:249]\nv_cvt_pk_bf16_f32 v128,v64,v65\nv_cvt_pk_bf16_f32 v129,v66,v67\nv_cvt_pk_bf16_f32 v130,v72,v73\nv_cvt_pk_bf16_f32 v131,v74,v75\n"
"buffer_store_dwordx4 v[128:131],v247,s[44:47],s64 offen offset:2048 sc1\nv_pk_add_f32 v[80:81],v[80:81],v[144:145]\nv_pk_add_f32 v[82:83],v[82:83],v[146:147]\nv_pk_add_f32 v[88:89],v[88:89],v[152:153]\n"
"v_pk_add_f32 v[90:91],v[90:91],v[154:155]\nv_pk_mul_f32 v[80:81],v[80:81],v[248:249]\nv_pk_mul_f32 v[82:83],v[82:83],v[248:249]\nv_pk_mul_f32 v[88:89],v[88:89],v[248:249]\nv_pk_mul_f32 v[90:91],v[90:91],v[248:249]\n"
"v_cvt_pk_bf16_f32 v144,v80,v81\nv_cvt_pk_bf16_f32 v145,v82,v83\nv_cvt_pk_bf16_f32 v146,v88,v89\nv_cvt_pk_bf16_f32 v147,v90,v91\nbuffer_store_dwordx4 v[144:147],v247,s[44:47],s64 offen offset:3072 sc1\n"
"v_pk_add_f32 v[4:5],v[4:5],v[100:101]\nv_pk_add_f32 v[6:7],v[6:7],v[102:103]\nv_pk_add_f32 v[12:13],v[12:13],v[108:109]\nv_pk_add_f32 v[14:15],v[14:15],v[110:111]\nv_pk_mul_f32 v[4:5],v[4:5],v[250:251]\n"
"v_pk_mul_f32 v[6:7],v[6:7],v[250:251]\nv_pk_mul_f32 v[12:13],v[12:13],v[250:251]\nv_pk_mul_f32 v[14:15],v[14:15],v[250:251]\nv_cvt_pk_bf16_f32 v100,v4,v5\nv_cvt_pk_bf16_f32 v101,v6,v7\nv_cvt_pk_bf16_f32 v102,v12,v13\n"
"v_cvt_pk_bf16_f32 v103,v14,v15\nbuffer_store_dwordx4 v[100:103],v247,s[44:47],s65 offen offset:0 sc1\nv_pk_add_f32 v[20:21],v[20:21],v[116:117]\nv_pk_add_f32 v[22:23],v[22:23],v[118:119]\n"
"v_pk_add_f32 v[28:29],v[28:29],v[124:125]\nv_pk_add_f32 v[30:31],v[30:31],v[126:127]\nv_pk_mul_f32 v[20:21],v[20:21],v[250:251]\nv_pk_mul_f32 v[22:23],v[22:23],v[250:251]\nv_pk_mul_f32 v[28:29],v[28:29],v[250:251]\n"
"v_pk_mul_f32 v[30:31],v[30:31],v[250:251]\nv_cvt_pk_bf16_f32 v116,v20,v21\nv_cvt_pk_bf16_f32 v117,v22,v23\nv_cvt_pk_bf16_f32 v118,v28,v29\nv_cvt_pk_bf16_f32 v119,v30,v31\n"
"buffer_store_dwordx4 v[116:119],v247,s[44:47],s65 offen offset:1024 sc1\nv_pk_add_f32 v[68:69],v[68:69],v[132:133]\nv_pk_add_f32 v[70:71],v[70:71],v[134:135]\nv_pk_add_f32 v[76:77],v[76:77],v[140:141]\n"
"v_pk_add_f32 v[78:79],v[78:79],v[142:143]\nv_pk_mul_f32 v[68:69],v[68:69],v[250:251]\nv_pk_mul_f32 v[70:71],v[70:71],v[250:251]\nv_pk_mul_f32 v[76:77],v[76:77],v[250:251]\nv_pk_mul_f32 v[78:79],v[78:79],v[250:251]\n"
"v_cvt_pk_bf16_f32 v132,v68,v69\nv_cvt_pk_bf16_f32 v133,v70,v71\nv_cvt_pk_bf16_f32 v134,v76,v77\nv_cvt_pk_bf16_f32 v135,v78,v79\nbuffer_store_dwordx4 v[132:135],v247,s[44:47],s65 offen offset:2048 sc1\n"
"v_pk_add_f32 v[84:85],v[84:85],v[148:149]\nv_pk_add_f32 v[86:87],v[86:87],v[150:151]\nv_pk_add_f32 v[92:93],v[92:93],v[156:157]\nv_pk_add_f32 v[94:95],v[94:95],v[158:159]\nv_pk_mul_f32 v[84:85],v[84:85],v[250:251]\n"
"v_pk_mul_f32 v[86:87],v[86:87],v[250:251]\nv_pk_mul_f32 v[92:93],v[92:93],v[250:251]\nv_pk_mul_f32 v[94:95],v[94:95],v[250:251]\nv_cvt_pk_bf16_f32 v148,v84,v85\nv_cvt_pk_bf16_f32 v149,v86,v87\nv_cvt_pk_bf16_f32 v150,v92,v93\n"
"v_cvt_pk_bf16_f32 v151,v94,v95\nbuffer_store_dwordx4 v[148:151],v247,s[44:47],s65 offen offset:3072 sc1\ns_cmp_lg_u32 s62,0\ns_cbranch_scc1 LEX_%=\n.Lgm_p3:\nv_add_u32 v244,0,v247\nv_add_u32 v245,65536,v247\n"
"v_add_u32 v246,131072,v247\ns_cmp_eq_u32 s49,0\ns_cbranch_scc1 LFAST_%=\ns_waitcnt vmcnt(0)\nv_mov_b32 v234,s98\nv_mov_b32 v235,s99\ns_mov_b64 exec,1\nv_mov_b32 v233,s18\n"
"global_store_dwordx2 v233,v[234:235],s[60:61] offset:0 sc1\nv_mov_b32 v233,s19\nglobal_store_dwordx2 v233,v[234:235],s[60:61] offset:0 sc1\ns_mov_b64 exec,-1\nv_lshrrev_b32 v236,1,v247\nv_add_u32 v236,s21,v236\n"
"s_mov_b32 s70,0x40000\nLPLE_%=:\nglobal_load_dwordx2 v[240:241],v236,s[60:61] offset:0 sc1\ns_waitcnt vmcnt(0)\nLPCE_%=:\nv_cmp_ne_u64 vcc,s[98:99],v[240:241]\ns_cmp_eq_u64 vcc,0\ns_cbranch_scc1 LPDE_%=\ns_sleep 2\n"
"s_sub_u32 s70,s70,1\ns_cmp_lg_u32 s70,0\ns_cbranch_scc1 LPLE_%=\nLPDE_%=:\ns_add_u32 m0,s50,65536\ns_add_u32 s48,s22,0\nbuffer_load_dwordx4 v247,s[44:47],s48 offen sc1 lds\ns_add_u32 m0,s50,69632\ns_add_u32 s48,s22,147456\n"
"buffer_load_dwordx4 v247,s[44:47],s48 offen sc1 lds\ns_add_u32 m0,s50,73728\ns_add_u32 s48,s22,294912\nbuffer_load_dwordx4 v247,s[44:47],s48 offen sc1 lds\ns_add_u32 m0,s50,77824\ns_add_u32 s48,s22,4096\n"
"buffer_load_dwordx4 v247,s[44:47],s48 offen sc1 lds\ns_add_u32 m0,s50,81920\ns_add_u32 s48,s22,151552\nbuffer_load_dwordx4 v247,s[44:47],s48 offen sc1 lds\ns_add_u32 m0,s50,86016\ns_add_u32 s48,s22,299008\n"
"buffer_load_dwordx4 v247,s[44:47],s48 offen sc1 lds\ns_add_u32 m0,s50,98304\ns_add_u32 s48,s22,8192\nbuffer_load_dwordx4 v247,s[44:47],s48 offen sc1 lds\ns_add_u32 m0,s50,102400\ns_add_u32 s48,s22,155648\n"
"buffer_load_dwordx4 v247,s[44:47],s48 offen sc1 lds\ns_add_u32 m0,s50,106496\ns_add_u32 s48,s22,303104\nbuffer_load_dwordx4 v247,s[44:47],s48 offen sc1 lds\ns_add_u32 m0,s50,110592\ns_add_u32 s48,s22,12288\n"
"buffer_load_dwordx4 v247,s[44:47],s48 offen sc1 lds\ns_add_u32 m0,s50,114688\ns_add_u32 s48,s22,159744\nbuffer_load_dwordx4 v247,s[44:47],s48 offen sc1 lds\ns_add_u32 m0,s50,118784\ns_add_u32 s48,s22,307200\n"
"buffer_load_dwordx4 v247,s[44:47],s48 offen sc1 lds\ns_waitcnt vmcnt(0)\nLFAST_%=:\ns_add_u32 s55,s20,139264\nbuffer_load_dwordx4 a[200:203],v247,s[44:47],s55 offen offset:0 sc1\n"
"buffer_load_dwordx4 a[204:207],v247,s[44:47],s55 offen offset:1024 sc1\ns_add_u32 s55,s20,147456\nbuffer_load_dwordx4 a[208:211],v247,s[44:47],s55 offen offset:0 sc1\n"
"buffer_load_dwordx4 a[212:215],v247,s[44:47],s55 offen offset:1024 sc1\ns_add_u32 s55,s20,155648\nbuffer_load_dwordx4 a[216:219],v247,s[44:47],s55 offen offset:0 sc1\n"
"buffer_load_dwordx4 a[220:223],v247,s[44:47],s55 offen offset:1024 sc1\ns_add_u32 m0,s50,131072\ns_add_u32 s48,s22,16384\nbuffer_load_dwordx4 v247,s[44:47],s48 offen sc1 lds\ns_add_u32 m0,s50,135168\ns_add_u32 s48,s22,163840\n"
"buffer_load_dwordx4 v247,s[44:47],s48 offen sc1 lds\ns_waitcnt vmcnt(45)\ns_barrier\nv_mov_b32 v243,0x27000\nds_read_b32 v243,v243\nds_read_b128 v[160:163],v245 offset:0\nds_read_b128 v[164:167],v245 offset:1024\n"
"ds_read_b128 v[168:171],v245 offset:2048\nds_read_b128 v[172:175],v245 offset:3072\nds_read_b128 v[176:179],v245 offset:4096\nds_read_b128 v[180:183],v245 offset:5120\nds_read_b128 v[184:187],v245 offset:6144\n"
"ds_read_b128 v[188:191],v245 offset:7168\ns_waitcnt lgkmcnt(4)\nv_mfma_f32_16x16x32_bf16 v[208:211],a[0:3],v[160:163],0\nds_read_b128 v[192:195],v245 offset:8192\ns_add_u32 s64,s64,s49\n"
"v_mfma_f32_16x16x32_bf16 v[208:211],a[8:11],v[164:167],v[208:211]\nds_read_b128 v[196:199],v245 offset:9216\ns_add_u32 s65,s65,s49\nv_mfma_f32_16x16x32_bf16 v[208:211],a[16:19],v[168:171],v[208:211]\ns_add_u32 m0,s50,139264\n"
"s_bfe_u32 s80,s24,0x20001\ns_add_u32 s48,s22,311296\nv_mfma_f32_16x16x32_bf16 v[208:211],a[24:27],v[172:175],v[208:211]\nbuffer_load_dwordx4 v247,s[44:47],s48 offen sc1 lds\n"
"v_mfma_f32_16x16x32_bf16 v[212:215],a[28:31],v[172:175],0\nds_read_b128 v[200:203],v245 offset:10240\ns_add_u32 s55,s20,163840\nv_mfma_f32_16x16x32_bf16 v[212:215],a[20:23],v[168:171],v[212:215]\n"
"ds_read_b128 v[204:207],v245 offset:11264\ns_lshr_b32 s36,s24,5\nv_mfma_f32_16x16x32_bf16 v[212:215],a[12:15],v[164:167],v[212:215]\ns_waitcnt vmcnt(43) lgkmcnt(4)\ns_lshl_b32 s36,s36,1\ns_and_b32 s37,s24,1\n"
"v_mfma_f32_16x16x32_bf16 v[212:215],a[4:7],v[160:163],v[212:215]\nbuffer_load_dwordx4 a[224:227],v247,s[44:47],s55 offen offset:0 sc1\nv_mfma_f32_16x16x32_bf16 v[220:223],a[4:7],v[176:179],0\ns_barrier\ns_or_b32 s38,s36,s37\n"
"s_sub_u32 s37,s37,s36\nv_mfma_f32_16x16x32_bf16 v[220:223],a[12:15],v[180:183],v[220:223]\nds_read_b128 v[160:163],v245 offset:12288\ns_lshl_b32 s37,s37,2\nv_mfma_f32_16x16x32_bf16 v[220:223],a[20:23],v[184:187],v[220:223]\n"
"ds_read_b128 v[164:167],v245 offset:13312\ns_add_u32 s37,s37,120\nv_mfma_f32_16x16x32_bf16 v[220:223],a[28:31],v[188:191],v[220:223]\nbuffer_load_dwordx4 a[228:231],v247,s[44:47],s55 offen offset:1024 sc1\n"
"v_mfma_f32_16x16x32_bf16 v[216:219],a[24:27],v[188:191],0\nds_read_b128 v[168:171],v245 offset:14336\ns_add_u32 s55,s20,172032\nv_mfma_f32_16x16x32_bf16 v[216:219],a[16:19],v[184:187],v[216:219]\n"
"ds_read_b128 v[172:175],v245 offset:15360\ns_mul_i32 s39,s80,36\nv_mfma_f32_16x16x32_bf16 v[216:219],a[8:11],v[180:183],v[216:219]\ns_waitcnt lgkmcnt(4)\ns_add_u32 s39,s39,s54\ns_lshl_b32 s39,s39,10\n"
"v_mfma_f32_16x16x32_bf16 v[216:219],a[0:3],v[176:179],v[216:219]\nbuffer_load_dwordx4 a[232:235],v247,s[44:47],s55 offen offset:0 sc1\nv_mfma_f32_16x16x32_bf16 v[224:227],a[0:3],v[192:195],0\n"
"ds_read_b128 v[176:179],v245 offset:16384\ns_add_u32 s39,s39,50855936\nv_mfma_f32_16x16x32_bf16 v[224:227],a[8:11],v[196:199],v[224:227]\nds_read_b128 v[180:183],v245 offset:17408\ns_mul_i32 s82,s37,147456\n"
"v_mfma_f32_16x16x32_bf16 v[224:227],a[16:19],v[200:203],v[224:227]\ns_add_u32 s82,s82,s39\ns_add_u32 s36,s38,48\ns_mul_i32 s81,s36,147456\nv_mfma_f32_16x16x32_bf16 v[224:227],a[24:27],v[204:207],v[224:227]\n"
"buffer_load_dwordx4 a[236:239],v247,s[44:47],s55 offen offset:1024 sc1\nv_mfma_f32_16x16x32_bf16 v[228:231],a[28:31],v[204:207],0\nds_read_b128 v[184:187],v245 offset:18432\ns_add_u32 s55,s20,180224\n"
"v_mfma_f32_16x16x32_bf16 v[228:231],a[20:23],v[200:203],v[228:231]\nds_read_b128 v[188:191],v245 offset:19456\ns_add_u32 s81,s81,s39\nv_mfma_f32_16x16x32_bf16 v[228:231],a[12:15],v[196:199],v[228:231]\ns_waitcnt lgkmcnt(4)\n"
"s_lshr_b32 s87,s36,1\ns_lshl_b32 s87,s87,8\nv_mfma_f32_16x16x32_bf16 v[228:231],a[4:7],v[192:195],v[228:231]\nbuffer_load_dwordx4 a[240:243],v247,s[44:47],s55 offen offset:0 sc1\n"
"v_mfma_f32_16x16x32_bf16 v[212:215],a[36:39],v[160:163],v[212:215]\nds_read_b128 v[192:195],v245 offset:20480\ns_add_u32 s87,s87,4096\nv_mfma_f32_16x16x32_bf16 v[212:215],a[44:47],v[164:167],v[212:215]\n"
"ds_read_b128 v[196:199],v245 offset:21504\ns_lshl_b32 s88,s38,9\nv_mfma_f32_16x16x32_bf16 v[212:215],a[52:55],v[168:171],v[212:215]\ns_add_u32 s88,s88,12288\ns_mul_i32 s39,s38,3\ns_add_u32 s39,s39,s80\n"
"v_mfma_f32_16x16x32_bf16 v[212:215],a[60:63],v[172:175],v[212:215]\nbuffer_load_dwordx4 a[244:247],v247,s[44:47],s55 offen offset:1024 sc1\nv_mfma_f32_16x16x32_bf16 v[208:211],a[56:59],v[172:175],v[208:211]\n"
"ds_read_b128 v[200:203],v245 offset:22528\ns_add_u32 s55,s20,188416\nv_mfma_f32_16x16x32_bf16 v[208:211],a[48:51],v[168:171],v[208:211]\nds_read_b128 v[204:207],v245 offset:23552\n"
"v_mfma_f32_16x16x32_bf16 v[208:211],a[40:43],v[164:167],v[208:211]\ns_waitcnt vmcnt(46) lgkmcnt(4)\ns_cmp_lt_u32 s80,3\ns_cselect_b32 s36,s39,s36\nv_mfma_f32_16x16x32_bf16 v[208:211],a[32:35],v[160:163],v[208:211]\n"
"buffer_load_dwordx4 a[248:251],v247,s[44:47],s55 offen offset:0 sc1\nv_mfma_f32_16x16x32_bf16 v[216:219],a[32:35],v[176:179],v[216:219]\ns_barrier\ns_add_u32 s37,s37,s80\ns_bfe_u32 s39,s24,0x20003\n"
"v_mfma_f32_16x16x32_bf16 v[216:219],a[40:43],v[180:183],v[216:219]\nds_read_b128 v[160:163],v245 offset:32768\ns_lshl_b32 s38,s38,2\nv_mfma_f32_16x16x32_bf16 v[216:219],a[48:51],v[184:187],v[216:219]\n"
"ds_read_b128 v[164:167],v245 offset:33792\ns_or_b32 s38,s38,s39\nv_mfma_f32_16x16x32_bf16 v[216:219],a[56:59],v[188:191],v[216:219]\nbuffer_load_dwordx4 a[252:255],v247,s[44:47],s55 offen offset:1024 sc1\n"
"v_mfma_f32_16x16x32_bf16 v[220:223],a[60:63],v[188:191],v[220:223]\nds_read_b128 v[168:171],v245 offset:34816\ns_add_u32 m0,s50,143360\nv_mfma_f32_16x16x32_bf16 v[220:223],a[52:55],v[184:187],v[220:223]\n"
"ds_read_b128 v[172:175],v245 offset:35840\ns_add_u32 s48,s22,20480\nv_mfma_f32_16x16x32_bf16 v[220:223],a[44:47],v[180:183],v[220:223]\ns_waitcnt lgkmcnt(4)\ns_lshl_b32 s39,s39,2\ns_add_u32 s39,s39,s54\n"
"v_mfma_f32_16x16x32_bf16 v[220:223],a[36:39],v[176:179],v[220:223]\nbuffer_load_dwordx4 v247,s[44:47],s48 offen sc1 lds\ns_waitcnt vmcnt(18)\nv_mov_b32 v234,s98\nv_mov_b32 v235,s99\ns_mov_b64 exec,1\nv_mov_b32 v233,s18\n"
"global_store_dwordx2 v233,v[234:235],s[60:61] offset:0 sc1\ns_mov_b64 exec,-1\nv_mfma_f32_16x16x32_bf16 v[228:231],a[36:39],v[192:195],v[228:231]\nds_read_b128 v[176:179],v245 offset:36864\ns_add_u32 m0,s50,147456\n"
"v_mfma_f32_16x16x32_bf16 v[228:231],a[44:47],v[196:199],v[228:231]\nds_read_b128 v[180:183],v245 offset:37888\ns_add_u32 s48,s22,167936\nv_mfma_f32_16x16x32_bf16 v[228:231],a[52:55],v[200:203],v[228:231]\ns_lshl_b32 s39,s39,6\n"
"s_lshl_b32 s36,s36,14\ns_add_u32 s83,s36,s39\nv_mfma_f32_16x16x32_bf16 v[228:231],a[60:63],v[204:207],v[228:231]\nbuffer_load_dwordx4 v247,s[44:47],s48 offen sc1 lds\n"
"v_mfma_f32_16x16x32_bf16 v[224:227],a[56:59],v[204:207],v[224:227]\nds_read_b128 v[184:187],v245 offset:38912\ns_add_u32 m0,s50,151552\nv_mfma_f32_16x16x32_bf16 v[224:227],a[48:51],v[200:203],v[224:227]\n"
"ds_read_b128 v[188:191],v245 offset:39936\ns_add_u32 s48,s22,315392\nv_mfma_f32_16x16x32_bf16 v[224:227],a[40:43],v[196:199],v[224:227]\ns_waitcnt lgkmcnt(4)\ns_lshl_b32 s37,s37,14\ns_add_u32 s84,s37,s39\n"
"v_mfma_f32_16x16x32_bf16 v[224:227],a[32:35],v[192:195],v[224:227]\nbuffer_load_dwordx4 v247,s[44:47],s48 offen sc1 lds\ns_waitcnt vmcnt(39)\nv_mfma_f32_16x16x32_bf16 v[208:211],a[128:131],v[160:163],v[208:211]\n"
"ds_read_b128 v[192:195],v245 offset:40960\ns_add_u32 s55,s20,196608\nv_mfma_f32_16x16x32_bf16 v[208:211],a[136:139],v[164:167],v[208:211]\nds_read_b128 v[196:199],v245 offset:41984\ns_lshl_b32 s36,s38,17\n"
"v_mfma_f32_16x16x32_bf16 v[208:211],a[144:147],v[168:171],v[208:211]\ns_add_u32 s36,s36,s50\ns_add_u32 s36,s36,42467328\ns_lshl_b32 s37,s80,12\nv_mfma_f32_16x16x32_bf16 v[208:211],a[152:155],v[172:175],v[208:211]\n"
"buffer_load_dwordx4 a[64:67],v247,s[44:47],s55 offen offset:0 sc1\nv_mfma_f32_16x16x32_bf16 v[212:215],a[156:159],v[172:175],v[212:215]\nds_read_b128 v[200:203],v245 offset:43008\ns_add_u32 s85,s36,s37\n"
"v_mfma_f32_16x16x32_bf16 v[212:215],a[148:151],v[168:171],v[212:215]\nds_read_b128 v[204:207],v245 offset:44032\ns_lshl_b32 s37,s80,14\nv_mfma_f32_16x16x32_bf16 v[212:215],a[140:143],v[164:167],v[212:215]\n"
"s_waitcnt vmcnt(31) lgkmcnt(4)\ns_add_u32 s86,s36,s37\ns_lshl_b32 s36,s38,2\nv_mfma_f32_16x16x32_bf16 v[212:215],a[132:135],v[160:163],v[212:215]\nbuffer_load_dwordx4 a[68:71],v247,s[44:47],s55 offen offset:1024 sc1\n"
"v_mfma_f32_16x16x32_bf16 v[220:223],a[132:135],v[176:179],v[220:223]\ns_barrier\ns_add_u32 s55,s20,204800\ns_add_u32 s36,s36,s54\nv_mfma_f32_16x16x32_bf16 v[220:223],a[140:143],v[180:183],v[220:223]\n"
"ds_read_b128 v[160:163],v245 offset:45056\ns_lshl_b32 s36,s36,9\nv_mfma_f32_16x16x32_bf16 v[220:223],a[148:151],v[184:187],v[220:223]\nds_read_b128 v[164:167],v245 offset:46080\ns_add_u32 s89,s36,20480\n"
"v_mfma_f32_16x16x32_bf16 v[220:223],a[156:159],v[188:191],v[220:223]\nbuffer_load_dwordx4 a[72:75],v247,s[44:47],s55 offen offset:0 sc1\nv_mfma_f32_16x16x32_bf16 v[216:219],a[152:155],v[188:191],v[216:219]\n"
"ds_read_b128 v[168:171],v245 offset:47104\ns_lshl_b32 s37,s80,7\nv_mfma_f32_16x16x32_bf16 v[216:219],a[144:147],v[184:187],v[216:219]\nds_read_b128 v[172:175],v245 offset:48128\ns_add_u32 s90,s89,s37\n"
"v_mfma_f32_16x16x32_bf16 v[216:219],a[136:139],v[180:183],v[216:219]\ns_waitcnt lgkmcnt(4)\ns_and_b32 s36,s24,1\ns_lshl_b32 s36,s36,1\nv_mfma_f32_16x16x32_bf16 v[216:219],a[128:131],v[176:179],v[216:219]\n"
"buffer_load_dwordx4 a[76:79],v247,s[44:47],s55 offen offset:1024 sc1\nv_mfma_f32_16x16x32_bf16 v[224:227],a[128:131],v[192:195],v[224:227]\nds_read_b128 v[176:179],v245 offset:49152\ns_add_u32 s55,s20,212992\n"
"v_mfma_f32_16x16x32_bf16 v[224:227],a[136:139],v[196:199],v[224:227]\nds_read_b128 v[180:183],v245 offset:50176\ns_sub_u32 s36,1,s36\nv_mfma_f32_16x16x32_bf16 v[224:227],a[144:147],v[200:203],v[224:227]\ns_mul_i32 s37,s36,147456\n"
"s_add_u32 s91,s81,s37\ns_lshl_b32 s37,s36,19\nv_mfma_f32_16x16x32_bf16 v[224:227],a[152:155],v[204:207],v[224:227]\nbuffer_load_dwordx4 a[80:83],v247,s[44:47],s55 offen offset:0 sc1\n"
"v_mfma_f32_16x16x32_bf16 v[228:231],a[156:159],v[204:207],v[228:231]\nds_read_b128 v[184:187],v245 offset:51200\ns_add_u32 s74,s85,s37\nv_mfma_f32_16x16x32_bf16 v[228:231],a[148:151],v[200:203],v[228:231]\n"
"ds_read_b128 v[188:191],v245 offset:52224\ns_lshl_b32 s36,s36,13\nv_mfma_f32_16x16x32_bf16 v[228:231],a[140:143],v[196:199],v[228:231]\ns_waitcnt lgkmcnt(4)\ns_cmp_eq_u32 s80,3\ns_cselect_b32 s75,s36,0\n"
"v_mfma_f32_16x16x32_bf16 v[228:231],a[132:135],v[192:195],v[228:231]\nbuffer_load_dwordx4 a[84:87],v247,s[44:47],s55 offen offset:1024 sc1\ns_waitcnt vmcnt(37)\n"
"v_mfma_f32_16x16x32_bf16 v[212:215],a[164:167],v[160:163],v[212:215]\nds_read_b128 v[192:195],v245 offset:53248\ns_add_u32 s55,s20,221184\nv_mfma_f32_16x16x32_bf16 v[212:215],a[172:175],v[164:167],v[212:215]\n"
"ds_read_b128 v[196:199],v245 offset:54272\ns_mov_b64 s[40:41],s[28:29]\nv_mfma_f32_16x16x32_bf16 v[212:215],a[180:183],v[168:171],v[212:215]\ns_and_b32 s41,s41,0xffff\ns_mov_b32 s42,0x200000\ns_mov_b32 s43,0x20000\n"
"v_mfma_f32_16x16x32_bf16 v[212:215],a[188:191],v[172:175],v[212:215]\nbuffer_load_dwordx4 a[88:91],v247,s[44:47],s55 offen offset:0 sc1\nv_mfma_f32_16x16x32_bf16 v[208:211],a[184:187],v[172:175],v[208:211]\n"
"ds_read_b128 v[200:203],v245 offset:55296\ns_add_u32 s76,s80,1\nv_mfma_f32_16x16x32_bf16 v[208:211],a[176:179],v[168:171],v[208:211]\nds_read_b128 v[204:207],v245 offset:56320\ns_and_b32 s76,s76,3\n"
"v_mfma_f32_16x16x32_bf16 v[208:211],a[168:171],v[164:167],v[208:211]\ns_waitcnt vmcnt(18) lgkmcnt(4)\ns_mul_i32 s76,s76,147456\ns_add_u32 s76,s76,s82\nv_mfma_f32_16x16x32_bf16 v[208:211],a[160:163],v[160:163],v[208:211]\n"
"buffer_load_dwordx4 a[92:95],v247,s[44:47],s55 offen offset:1024 sc1\nv_mfma_f32_16x16x32_bf16 v[216:219],a[160:163],v[176:179],v[216:219]\ns_barrier\ns_add_u32 m0,s50,155648\ns_add_u32 s77,s80,2\n"
"v_mfma_f32_16x16x32_bf16 v[216:219],a[168:171],v[180:183],v[216:219]\nds_read_b128 v[160:163],v246 offset:0\ns_add_u32 s48,s22,24576\nv_mfma_f32_16x16x32_bf16 v[216:219],a[176:179],v[184:187],v[216:219]\n"
"ds_read_b128 v[164:167],v246 offset:1024\ns_and_b32 s77,s77,3\nv_mfma_f32_16x16x32_bf16 v[216:219],a[184:187],v[188:191],v[216:219]\nbuffer_load_dwordx4 v247,s[44:47],s48 offen sc1 lds\n"
"v_mfma_f32_16x16x32_bf16 v[220:223],a[188:191],v[188:191],v[220:223]\nds_read_b128 v[168:171],v246 offset:2048\ns_add_u32 m0,s50,90112\nv_mfma_f32_16x16x32_bf16 v[220:223],a[180:183],v[184:187],v[220:223]\n"
"ds_read_b128 v[172:175],v246 offset:3072\ns_add_u32 s48,s22,172032\nv_mfma_f32_16x16x32_bf16 v[220:223],a[172:175],v[180:183],v[220:223]\ns_waitcnt lgkmcnt(4)\ns_mul_i32 s77,s77,147456\ns_add_u32 s77,s77,s82\n"
"v_mfma_f32_16x16x32_bf16 v[220:223],a[164:167],v[176:179],v[220:223]\nbuffer_load_dwordx4 v247,s[44:47],s48 offen sc1 lds\nv_mfma_f32_16x16x32_bf16 v[228:231],a[164:167],v[192:195],v[228:231]\n"
"ds_read_b128 v[176:179],v246 offset:4096\ns_add_u32 m0,s50,94208\nv_mfma_f32_16x16x32_bf16 v[228:231],a[172:175],v[196:199],v[228:231]\nds_read_b128 v[180:183],v246 offset:5120\ns_add_u32 s48,s22,319488\n"
"v_mfma_f32_16x16x32_bf16 v[228:231],a[180:183],v[200:203],v[228:231]\ns_add_u32 s78,s80,3\ns_and_b32 s78,s78,3\ns_mul_i32 s78,s78,147456\nv_mfma_f32_16x16x32_bf16 v[228:231],a[188:191],v[204:207],v[228:231]\n"
"buffer_load_dwordx4 v247,s[44:47],s48 offen sc1 lds\nv_mfma_f32_16x16x32_bf16 v[224:227],a[184:187],v[204:207],v[224:227]\nds_read_b128 v[184:187],v246 offset:6144\ns_add_u32 s55,s20,229376\n"
"v_mfma_f32_16x16x32_bf16 v[224:227],a[176:179],v[200:203],v[224:227]\nds_read_b128 v[188:191],v246 offset:7168\ns_add_u32 s78,s78,s82\nv_mfma_f32_16x16x32_bf16 v[224:227],a[168:171],v[196:199],v[224:227]\ns_waitcnt lgkmcnt(4)\n"
"s_add_u32 s79,s80,4\ns_and_b32 s79,s79,3\nv_mfma_f32_16x16x32_bf16 v[224:227],a[160:163],v[192:195],v[224:227]\nbuffer_load_dwordx4 a[96:99],v247,s[44:47],s55 offen offset:0 sc1\ns_waitcnt vmcnt(26)\n"
"v_mfma_f32_16x16x32_bf16 v[208:211],a[192:195],v[160:163],v[208:211]\nds_read_b128 v[192:195],v246 offset:8192\ns_mul_i32 s79,s79,147456\nv_mfma_f32_16x16x32_bf16 v[208:211],a[200:203],v[164:167],v[208:211]\n"
"ds_read_b128 v[196:199],v246 offset:9216\ns_add_u32 s79,s79,s82\nv_lshrrev_b32 v239,4,v247\nv_lshlrev_b32 v237,3,v239\nv_mfma_f32_16x16x32_bf16 v[208:211],a[208:211],v[168:171],v[208:211]\nv_and_b32 v236,0xf8,v237\n"
"v_add_u32 v237,s88,v237\nv_add_u32 v236,s87,v236\nv_and_b32 v238,3,v239\nv_mfma_f32_16x16x32_bf16 v[208:211],a[216:219],v[172:175],v[208:211]\nbuffer_load_dwordx4 a[100:103],v247,s[44:47],s55 offen offset:1024 sc1\n"
"v_lshlrev_b32 v238,7,v238\nv_add_u32 v238,s89,v238\nv_mfma_f32_16x16x32_bf16 v[212:215],a[220:223],v[172:175],v[212:215]\nds_read_b128 v[200:203],v246 offset:10240\ns_add_u32 s55,s20,237568\nv_bfe_u32 v239,v239,2,1\n"
"v_mul_lo_u32 v239,v239,s75\nv_mfma_f32_16x16x32_bf16 v[212:215],a[212:215],v[168:171],v[212:215]\nds_read_b128 v[204:207],v246 offset:11264\nv_add_u32 v238,v238,v239\nv_readfirstlane_b32 s71,v243\n"
"v_mfma_f32_16x16x32_bf16 v[212:215],a[204:207],v[164:167],v[212:215]\ns_waitcnt vmcnt(13) lgkmcnt(4)\nv_mfma_f32_16x16x32_bf16 v[212:215],a[196:199],v[160:163],v[212:215]\n"
"buffer_load_dwordx4 a[104:107],v247,s[44:47],s55 offen offset:0 sc1\nv_mfma_f32_16x16x32_bf16 v[220:223],a[196:199],v[176:179],v[220:223]\ns_barrier\nv_mfma_f32_16x16x32_bf16 v[220:223],a[204:207],v[180:183],v[220:223]\n"
"ds_read_b128 v[160:163],v246 offset:12288\nv_mfma_f32_16x16x32_bf16 v[220:223],a[212:215],v[184:187],v[220:223]\nds_read_b128 v[164:167],v246 offset:13312\nv_mfma_f32_16x16x32_bf16 v[220:223],a[220:223],v[188:191],v[220:223]\n"
"buffer_load_dwordx4 a[108:111],v247,s[44:47],s55 offen offset:1024 sc1\nv_mfma_f32_16x16x32_bf16 v[216:219],a[216:219],v[188:191],v[216:219]\nds_read_b128 v[168:171],v246 offset:14336\ns_add_u32 s55,s20,245760\n"
"v_mfma_f32_16x16x32_bf16 v[216:219],a[208:211],v[184:187],v[216:219]\nds_read_b128 v[172:175],v246 offset:15360\nv_mfma_f32_16x16x32_bf16 v[216:219],a[200:203],v[180:183],v[216:219]\ns_waitcnt lgkmcnt(4)\n"
"v_mfma_f32_16x16x32_bf16 v[216:219],a[192:195],v[176:179],v[216:219]\nbuffer_load_dwordx4 a[112:115],v247,s[44:47],s55 offen offset:0 sc1\nv_mfma_f32_16x16x32_bf16 v[224:227],a[192:195],v[192:195],v[224:227]\n"
"ds_read_b128 v[176:179],v246 offset:16384\nv_mfma_f32_16x16x32_bf16 v[224:227],a[200:203],v[196:199],v[224:227]\nds_read_b128 v[180:183],v246 offset:17408\nv_mfma_f32_16x16x32_bf16 v[224:227],a[208:211],v[200:203],v[224:227]\n"
"v_mfma_f32_16x16x32_bf16 v[224:227],a[216:219],v[204:207],v[224:227]\nbuffer_load_dwordx4 a[116:119],v247,s[44:47],s55 offen offset:1024 sc1\nv_mfma_f32_16x16x32_bf16 v[228:231],a[220:223],v[204:207],v[228:231]\n"
"ds_read_b128 v[184:187],v246 offset:18432\ns_add_u32 s55,s20,253952\nv_mfma_f32_16x16x32_bf16 v[228:231],a[212:215],v[200:203],v[228:231]\nds_read_b128 v[188:191],v246 offset:19456\n"
"v_mfma_f32_16x16x32_bf16 v[228:231],a[204:207],v[196:199],v[228:231]\ns_waitcnt lgkmcnt(4)\nv_mfma_f32_16x16x32_bf16 v[228:231],a[196:199],v[192:195],v[228:231]\n"
"buffer_load_dwordx4 a[120:123],v247,s[44:47],s55 offen offset:0 sc1\ns_cmp_eq_u32 s71,1\ns_cbranch_scc1 LMORE_%=\ns_cmp_eq_u32 s71,2\ns_cbranch_scc1 LLESS_%=\ns_waitcnt vmcnt(21)\ns_cmp_lg_u32 s54,0\ns_cbranch_scc1 LW0au0_%=\n"
"global_load_dwordx2 v[242:243],v236,s[60:61] offset:0 sc1\nLW0au0_%=:\nbuffer_load_dwordx4 a[124:127],v247,s[44:47],s55 offen offset:1024 sc1\ns_add_u32 m0,s50,122880\ns_add_u32 s48,s22,28672\n"
"buffer_load_dwordx4 v247,s[44:47],s48 offen sc1 lds\ns_add_u32 m0,s50,126976\ns_add_u32 s48,s22,176128\nbuffer_load_dwordx4 v247,s[44:47],s48 offen sc1 lds\nv_mfma_f32_16x16x32_bf16 v[212:215],a[228:231],v[160:163],v[212:215]\n"
"ds_read_b128 v[192:195],v246 offset:20480\ns_add_u32 m0,s50,0\nv_mfma_f32_16x16x32_bf16 v[212:215],a[236:239],v[164:167],v[212:215]\nds_read_b128 v[196:199],v246 offset:21504\ns_add_u32 s48,s22,323584\n"
"v_mfma_f32_16x16x32_bf16 v[212:215],a[244:247],v[168:171],v[212:215]\nv_mfma_f32_16x16x32_bf16 v[212:215],a[252:255],v[172:175],v[212:215]\nv_mfma_f32_16x16x32_bf16 v[208:211],a[248:251],v[172:175],v[208:211]\n"
"ds_read_b128 v[200:203],v246 offset:22528\nv_mfma_f32_16x16x32_bf16 v[208:211],a[240:243],v[168:171],v[208:211]\nds_read_b128 v[204:207],v246 offset:23552\nv_mfma_f32_16x16x32_bf16 v[208:211],a[232:235],v[164:167],v[208:211]\n"
"s_waitcnt vmcnt(10) lgkmcnt(4)\nv_mfma_f32_16x16x32_bf16 v[208:211],a[224:227],v[160:163],v[208:211]\nbuffer_load_dwordx4 v247,s[44:47],s48 offen sc1 lds\nv_mfma_f32_16x16x32_bf16 v[216:219],a[224:227],v[176:179],v[216:219]\n"
"s_barrier\ns_add_u32 s55,s20,262144\nv_mfma_f32_16x16x32_bf16 v[216:219],a[232:235],v[180:183],v[216:219]\nds_read_b128 v[160:163],v246 offset:24576\nv_mfma_f32_16x16x32_bf16 v[216:219],a[240:243],v[184:187],v[216:219]\n"
"ds_read_b128 v[164:167],v246 offset:25600\nv_mfma_f32_16x16x32_bf16 v[216:219],a[248:251],v[188:191],v[216:219]\nbuffer_load_dwordx4 v[32:35],v247,s[44:47],s55 offen offset:0 sc1\n"
"v_mfma_f32_16x16x32_bf16 v[220:223],a[252:255],v[188:191],v[220:223]\nds_read_b128 v[168:171],v246 offset:26624\nv_mfma_f32_16x16x32_bf16 v[220:223],a[244:247],v[184:187],v[220:223]\nds_read_b128 v[172:175],v246 offset:27648\n"
"v_mfma_f32_16x16x32_bf16 v[220:223],a[236:239],v[180:183],v[220:223]\ns_waitcnt lgkmcnt(4)\nv_mfma_f32_16x16x32_bf16 v[220:223],a[228:231],v[176:179],v[220:223]\n"
"buffer_load_dwordx4 v[36:39],v247,s[44:47],s55 offen offset:1024 sc1\nv_mfma_f32_16x16x32_bf16 v[228:231],a[228:231],v[192:195],v[228:231]\nds_read_b128 v[176:179],v245 offset:24576\ns_add_u32 s55,s20,270336\n"
"v_mfma_f32_16x16x32_bf16 v[228:231],a[236:239],v[196:199],v[228:231]\nds_read_b128 v[180:183],v245 offset:25600\nv_mfma_f32_16x16x32_bf16 v[228:231],a[244:247],v[200:203],v[228:231]\n"
"v_mfma_f32_16x16x32_bf16 v[228:231],a[252:255],v[204:207],v[228:231]\nbuffer_load_dwordx4 v[40:43],v247,s[44:47],s55 offen offset:0 sc1\nv_mfma_f32_16x16x32_bf16 v[224:227],a[248:251],v[204:207],v[224:227]\n"
"ds_read_b128 v[184:187],v245 offset:26624\nv_mfma_f32_16x16x32_bf16 v[224:227],a[240:243],v[200:203],v[224:227]\nds_read_b128 v[188:191],v245 offset:27648\nv_mfma_f32_16x16x32_bf16 v[224:227],a[232:235],v[196:199],v[224:227]\n"
"s_waitcnt lgkmcnt(4)\nv_mfma_f32_16x16x32_bf16 v[224:227],a[224:227],v[192:195],v[224:227]\nbuffer_load_dwordx4 v[44:47],v247,s[44:47],s55 offen offset:1024 sc1\ns_waitcnt vmcnt(18)\ns_add_u32 s55,s20,278528\n"
"buffer_load_dwordx4 v[48:51],v247,s[44:47],s55 offen offset:0 sc1\nbuffer_load_dwordx4 v[52:55],v247,s[44:47],s55 offen offset:1024 sc1\ns_add_u32 s55,s20,286720\nbuffer_load_dwordx4 v[56:59],v247,s[44:47],s55 offen offset:0 sc1\n"
"buffer_load_dwordx4 v[60:63],v247,s[44:47],s55 offen offset:1024 sc1\ns_add_u32 m0,s50,4096\ns_add_u32 s48,s22,32768\nbuffer_load_dwordx4 v247,s[44:47],s48 offen sc1 lds\ns_add_u32 m0,s50,8192\ns_add_u32 s48,s22,180224\n"
"buffer_load_dwordx4 v247,s[44:47],s48 offen sc1 lds\nv_mfma_f32_16x16x32_bf16 v[208:211],a[64:67],v[160:163],v[208:211]\nds_read_b128 v[192:195],v245 offset:28672\ns_add_u32 m0,s50,12288\n"
"v_mfma_f32_16x16x32_bf16 v[208:211],a[72:75],v[164:167],v[208:211]\nds_read_b128 v[196:199],v245 offset:29696\ns_add_u32 s48,s22,327680\nv_mfma_f32_16x16x32_bf16 v[208:211],a[80:83],v[168:171],v[208:211]\n"
"v_mfma_f32_16x16x32_bf16 v[208:211],a[88:91],v[172:175],v[208:211]\nv_mfma_f32_16x16x32_bf16 v[212:215],a[92:95],v[172:175],v[212:215]\nds_read_b128 v[200:203],v245 offset:30720\n"
"v_mfma_f32_16x16x32_bf16 v[212:215],a[84:87],v[168:171],v[212:215]\nds_read_b128 v[204:207],v245 offset:31744\nv_mfma_f32_16x16x32_bf16 v[212:215],a[76:79],v[164:167],v[212:215]\ns_waitcnt vmcnt(10) lgkmcnt(4)\n"
"v_mfma_f32_16x16x32_bf16 v[212:215],a[68:71],v[160:163],v[212:215]\nbuffer_load_dwordx4 v247,s[44:47],s48 offen sc1 lds\ns_cmp_lg_u32 s54,0\ns_cbranch_scc1 LW0bu0_%=\ns_mov_b32 s70,0x40000\ns_waitcnt vmcnt(15)\n"
"s_branch LPCLu0_%=\nLPLLu0_%=:\nglobal_load_dwordx2 v[242:243],v236,s[60:61] offset:0 sc1\ns_waitcnt vmcnt(0)\nLPCLu0_%=:\nv_cmp_ne_u64 vcc,s[98:99],v[242:243]\ns_cmp_eq_u64 vcc,0\ns_cbranch_scc1 LPDLu0_%=\ns_sleep 2\n"
"s_sub_u32 s70,s70,1\ns_cmp_lg_u32 s70,0\ns_cbranch_scc1 LPLLu0_%=\nLPDLu0_%=:\nLW0bu0_%=:\nv_mfma_f32_16x16x32_bf16 v[220:223],a[68:71],v[176:179],v[220:223]\ns_barrier\ns_add_u32 m0,s50,16384\ns_add_u32 s48,s81,0\n"
"v_mfma_f32_16x16x32_bf16 v[220:223],a[76:79],v[180:183],v[220:223]\nds_read_b128 v[160:163],v245 offset:57344\nv_mfma_f32_16x16x32_bf16 v[220:223],a[84:87],v[184:187],v[220:223]\nds_read_b128 v[164:167],v245 offset:58368\n"
"v_mfma_f32_16x16x32_bf16 v[220:223],a[92:95],v[188:191],v[220:223]\nbuffer_load_dwordx4 v247,s[44:47],s48 offen sc1 lds\nv_mfma_f32_16x16x32_bf16 v[216:219],a[88:91],v[188:191],v[216:219]\n"
"ds_read_b128 v[168:171],v245 offset:59392\ns_add_u32 m0,s50,20480\nv_mfma_f32_16x16x32_bf16 v[216:219],a[80:83],v[184:187],v[216:219]\nds_read_b128 v[172:175],v245 offset:60416\ns_add_u32 s48,s81,4096\n"
"v_mfma_f32_16x16x32_bf16 v[216:219],a[72:75],v[180:183],v[216:219]\ns_waitcnt lgkmcnt(4)\nv_mfma_f32_16x16x32_bf16 v[216:219],a[64:67],v[176:179],v[216:219]\nbuffer_load_dwordx4 v247,s[44:47],s48 offen sc1 lds\n"
"v_mfma_f32_16x16x32_bf16 v[224:227],a[64:67],v[192:195],v[224:227]\nds_read_b128 v[176:179],v245 offset:61440\ns_add_u32 m0,s50,24576\nv_mfma_f32_16x16x32_bf16 v[224:227],a[72:75],v[196:199],v[224:227]\n"
"ds_read_b128 v[180:183],v245 offset:62464\ns_add_u32 s48,s81,8192\nv_mfma_f32_16x16x32_bf16 v[224:227],a[80:83],v[200:203],v[224:227]\nv_mfma_f32_16x16x32_bf16 v[224:227],a[88:91],v[204:207],v[224:227]\n"
"buffer_load_dwordx4 v247,s[44:47],s48 offen sc1 lds\nv_mfma_f32_16x16x32_bf16 v[228:231],a[92:95],v[204:207],v[228:231]\nds_read_b128 v[184:187],v245 offset:63488\ns_add_u32 m0,s50,28672\n"
"v_mfma_f32_16x16x32_bf16 v[228:231],a[84:87],v[200:203],v[228:231]\nds_read_b128 v[188:191],v245 offset:64512\ns_add_u32 s48,s81,12288\nv_mfma_f32_16x16x32_bf16 v[228:231],a[76:79],v[196:199],v[228:231]\ns_waitcnt lgkmcnt(4)\n"
"v_mfma_f32_16x16x32_bf16 v[228:231],a[68:71],v[192:195],v[228:231]\nbuffer_load_dwordx4 v247,s[44:47],s48 offen sc1 lds\ns_waitcnt vmcnt(18)\ns_cmp_lg_u32 s54,0\ns_cbranch_scc1 LW0au1_%=\n"
"global_load_dwordx2 v[240:241],v237,s[60:61] offset:0 sc1\nLW0au1_%=:\nv_mfma_f32_16x16x32_bf16 v[212:215],a[100:103],v[160:163],v[212:215]\nds_read_b128 v[192:195],v244 offset:0\ns_add_u32 m0,s50,32768\n"
"v_mfma_f32_16x16x32_bf16 v[212:215],a[108:111],v[164:167],v[212:215]\nds_read_b128 v[196:199],v244 offset:1024\ns_add_u32 s48,s81,16384\nv_mfma_f32_16x16x32_bf16 v[212:215],a[116:119],v[168:171],v[212:215]\n"
"v_mfma_f32_16x16x32_bf16 v[212:215],a[124:127],v[172:175],v[212:215]\nbuffer_load_dwordx4 v247,s[44:47],s48 offen sc1 lds\nv_mfma_f32_16x16x32_bf16 v[208:211],a[120:123],v[172:175],v[208:211]\n"
"ds_read_b128 v[200:203],v244 offset:2048\ns_add_u32 m0,s50,36864\nv_mfma_f32_16x16x32_bf16 v[208:211],a[112:115],v[168:171],v[208:211]\nds_read_b128 v[204:207],v244 offset:3072\ns_add_u32 s48,s81,20480\n"
"v_mfma_f32_16x16x32_bf16 v[208:211],a[104:107],v[164:167],v[208:211]\ns_waitcnt vmcnt(5) lgkmcnt(4)\nv_mfma_f32_16x16x32_bf16 v[208:211],a[96:99],v[160:163],v[208:211]\nbuffer_load_dwordx4 v247,s[44:47],s48 offen sc1 lds\n"
"v_mfma_f32_16x16x32_bf16 v[216:219],a[96:99],v[176:179],v[216:219]\ns_barrier\ns_add_u32 m0,s50,40960\ns_add_u32 s48,s81,24576\nv_mfma_f32_16x16x32_bf16 v[216:219],a[104:107],v[180:183],v[216:219]\n"
"ds_read_b128 v[160:163],v244 offset:4096\nv_mfma_f32_16x16x32_bf16 v[216:219],a[112:115],v[184:187],v[216:219]\nds_read_b128 v[164:167],v244 offset:5120\nv_mfma_f32_16x16x32_bf16 v[216:219],a[120:123],v[188:191],v[216:219]\n"
"buffer_load_dwordx4 v247,s[44:47],s48 offen sc1 lds\nv_mfma_f32_16x16x32_bf16 v[220:223],a[124:127],v[188:191],v[220:223]\nds_read_b128 v[168:171],v244 offset:6144\ns_add_u32 m0,s50,45056\n"
"v_mfma_f32_16x16x32_bf16 v[220:223],a[116:119],v[184:187],v[220:223]\nds_read_b128 v[172:175],v244 offset:7168\ns_add_u32 s48,s81,28672\nv_mfma_f32_16x16x32_bf16 v[220:223],a[108:111],v[180:183],v[220:223]\ns_waitcnt lgkmcnt(4)\n"
"v_mfma_f32_16x16x32_bf16 v[220:223],a[100:103],v[176:179],v[220:223]\nbuffer_load_dwordx4 v247,s[44:47],s48 offen sc1 lds\nv_mfma_f32_16x16x32_bf16 v[228:231],a[100:103],v[192:195],v[228:231]\n"
"ds_read_b128 v[176:179],v244 offset:8192\ns_add_u32 m0,s50,49152\nv_mfma_f32_16x16x32_bf16 v[228:231],a[108:111],v[196:199],v[228:231]\nds_read_b128 v[180:183],v244 offset:9216\ns_add_u32 s48,s81,32768\n"
"v_mfma_f32_16x16x32_bf16 v[228:231],a[116:119],v[200:203],v[228:231]\nv_mfma_f32_16x16x32_bf16 v[228:231],a[124:127],v[204:207],v[228:231]\nbuffer_load_dwordx4 v247,s[44:47],s48 offen sc1 lds\n"
"v_mfma_f32_16x16x32_bf16 v[224:227],a[120:123],v[204:207],v[224:227]\nds_read_b128 v[184:187],v244 offset:10240\ns_add_u32 m0,s50,53248\nv_mfma_f32_16x16x32_bf16 v[224:227],a[112:115],v[200:203],v[224:227]\n"
"ds_read_b128 v[188:191],v244 offset:11264\ns_add_u32 s48,s76,0\nv_mfma_f32_16x16x32_bf16 v[224:227],a[104:107],v[196:199],v[224:227]\ns_waitcnt lgkmcnt(4)\nv_mfma_f32_16x16x32_bf16 v[224:227],a[96:99],v[192:195],v[224:227]\n"
"s_waitcnt vmcnt(12)\nv_mfma_f32_16x16x32_bf16 v[208:211],v[32:35],v[160:163],v[208:211]\nds_read_b128 v[192:195],v244 offset:12288\nv_mfma_f32_16x16x32_bf16 v[208:211],v[40:43],v[164:167],v[208:211]\n"
"ds_read_b128 v[196:199],v244 offset:13312\nv_mfma_f32_16x16x32_bf16 v[208:211],v[48:51],v[168:171],v[208:211]\nv_mfma_f32_16x16x32_bf16 v[208:211],v[56:59],v[172:175],v[208:211]\n"
"v_mfma_f32_16x16x32_bf16 v[212:215],v[60:63],v[172:175],v[212:215]\nds_read_b128 v[200:203],v244 offset:14336\nv_mfma_f32_16x16x32_bf16 v[212:215],v[52:55],v[168:171],v[212:215]\nds_read_b128 v[204:207],v244 offset:15360\n"
"v_mfma_f32_16x16x32_bf16 v[212:215],v[44:47],v[164:167],v[212:215]\ns_waitcnt vmcnt(3) lgkmcnt(4)\nv_mfma_f32_16x16x32_bf16 v[212:215],v[36:39],v[160:163],v[212:215]\ns_cmp_lg_u32 s54,0\ns_cbranch_scc1 LW0bu1_%=\n"
"s_mov_b32 s70,0x40000\ns_waitcnt vmcnt(5)\ns_branch LPCLu1_%=\nLPLLu1_%=:\nglobal_load_dwordx2 v[240:241],v237,s[60:61] offset:0 sc1\ns_waitcnt vmcnt(0)\nLPCLu1_%=:\nv_cmp_ne_u64 vcc,s[98:99],v[240:241]\ns_cmp_eq_u64 vcc,0\n"
"s_cbranch_scc1 LPDLu1_%=\ns_sleep 2\ns_sub_u32 s70,s70,1\ns_cmp_lg_u32 s70,0\ns_cbranch_scc1 LPLLu1_%=\nLPDLu1_%=:\nLW0bu1_%=:\nv_mfma_f32_16x16x32_bf16 v[220:223],v[36:39],v[176:179],v[220:223]\ns_barrier\ns_add_u32 s57,s85,0\n"
"v_mfma_f32_16x16x32_bf16 v[220:223],v[44:47],v[180:183],v[220:223]\nds_read_b128 v[160:163],v244 offset:16384\nv_mfma_f32_16x16x32_bf16 v[220:223],v[52:55],v[184:187],v[220:223]\nds_read_b128 v[164:167],v244 offset:17408\n"
"v_mfma_f32_16x16x32_bf16 v[220:223],v[60:63],v[188:191],v[220:223]\nbuffer_load_dwordx4 v247,s[44:47],s48 offen sc1 lds\nv_mfma_f32_16x16x32_bf16 v[216:219],v[56:59],v[188:191],v[216:219]\n"
"ds_read_b128 v[168:171],v244 offset:18432\ns_add_u32 m0,s50,57344\nv_mfma_f32_16x16x32_bf16 v[216:219],v[48:51],v[184:187],v[216:219]\nds_read_b128 v[172:175],v244 offset:19456\ns_add_u32 s48,s76,4096\n"
"v_mfma_f32_16x16x32_bf16 v[216:219],v[40:43],v[180:183],v[216:219]\ns_waitcnt lgkmcnt(4)\nv_cvt_pk_bf16_f32 v208,v208,v209\nv_cvt_pk_bf16_f32 v209,v210,v211\nv_mfma_f32_16x16x32_bf16 v[216:219],v[32:35],v[176:179],v[216:219]\n"
"buffer_load_dwordx4 v247,s[44:47],s48 offen sc1 lds\nv_mfma_f32_16x16x32_bf16 v[224:227],v[32:35],v[192:195],v[224:227]\nds_read_b128 v[176:179],v244 offset:20480\ns_add_u32 m0,s50,61440\n"
"v_mfma_f32_16x16x32_bf16 v[224:227],v[40:43],v[196:199],v[224:227]\nds_read_b128 v[180:183],v244 offset:21504\ns_add_u32 s48,s76,8192\nv_mfma_f32_16x16x32_bf16 v[224:227],v[48:51],v[200:203],v[224:227]\n"
"v_cvt_pk_bf16_f32 v210,v212,v213\nv_cvt_pk_bf16_f32 v211,v214,v215\nv_mfma_f32_16x16x32_bf16 v[224:227],v[56:59],v[204:207],v[224:227]\nbuffer_store_dwordx4 v[208:211],v247,s[44:47],s57 offen sc1\n"
"v_mfma_f32_16x16x32_bf16 v[228:231],v[60:63],v[204:207],v[228:231]\nds_read_b128 v[184:187],v244 offset:22528\ns_add_u32 s57,s85,16384\nv_mfma_f32_16x16x32_bf16 v[228:231],v[52:55],v[200:203],v[228:231]\n"
"ds_read_b128 v[188:191],v244 offset:23552\nv_cvt_pk_bf16_f32 v216,v216,v217\nv_mfma_f32_16x16x32_bf16 v[228:231],v[44:47],v[196:199],v[228:231]\ns_waitcnt lgkmcnt(4)\nv_cvt_pk_bf16_f32 v217,v218,v219\n"
"v_cvt_pk_bf16_f32 v218,v220,v221\nv_mfma_f32_16x16x32_bf16 v[228:231],v[36:39],v[192:195],v[228:231]\nbuffer_load_dwordx4 v247,s[44:47],s48 offen sc1 lds\nv_mfma_f32_16x16x32_bf16 v[96:99],a[0:3],v[160:163],0\n"
"ds_read_b128 v[192:195],v244 offset:24576\ns_add_u32 m0,s50,65536\nv_mfma_f32_16x16x32_bf16 v[96:99],a[8:11],v[164:167],v[96:99]\nds_read_b128 v[196:199],v244 offset:25600\ns_add_u32 s48,s76,12288\n"
"v_mfma_f32_16x16x32_bf16 v[96:99],a[16:19],v[168:171],v[96:99]\nv_cvt_pk_bf16_f32 v219,v222,v223\nv_mfma_f32_16x16x32_bf16 v[96:99],a[24:27],v[172:175],v[96:99]\nbuffer_store_dwordx4 v[216:219],v247,s[44:47],s57 offen sc1\n"
"v_mfma_f32_16x16x32_bf16 v[100:103],a[28:31],v[172:175],0\nds_read_b128 v[200:203],v244 offset:26624\ns_add_u32 s57,s85,32768\nv_mfma_f32_16x16x32_bf16 v[100:103],a[20:23],v[168:171],v[100:103]\n"
"ds_read_b128 v[204:207],v244 offset:27648\nv_cvt_pk_bf16_f32 v224,v224,v225\nv_mfma_f32_16x16x32_bf16 v[100:103],a[12:15],v[164:167],v[100:103]\ns_waitcnt lgkmcnt(4)\nv_cvt_pk_bf16_f32 v225,v226,v227\n"
"v_cvt_pk_bf16_f32 v226,v228,v229\nv_mfma_f32_16x16x32_bf16 v[100:103],a[4:7],v[160:163],v[100:103]\nbuffer_load_dwordx4 v247,s[44:47],s48 offen sc1 lds\nv_mfma_f32_16x16x32_bf16 v[100:103],a[36:39],v[176:179],v[100:103]\n"
"ds_read_b128 v[160:163],v244 offset:28672\ns_add_u32 m0,s50,69632\nv_mfma_f32_16x16x32_bf16 v[100:103],a[44:47],v[180:183],v[100:103]\nds_read_b128 v[164:167],v244 offset:29696\ns_add_u32 s48,s76,16384\n"
"v_mfma_f32_16x16x32_bf16 v[100:103],a[52:55],v[184:187],v[100:103]\nv_cvt_pk_bf16_f32 v227,v230,v231\nv_mfma_f32_16x16x32_bf16 v[100:103],a[60:63],v[188:191],v[100:103]\n"
"buffer_store_dwordx4 v[224:227],v247,s[44:47],s57 offen sc1\nv_mfma_f32_16x16x32_bf16 v[96:99],a[56:59],v[188:191],v[96:99]\nds_read_b128 v[168:171],v244 offset:30720\n"
"v_mfma_f32_16x16x32_bf16 v[96:99],a[48:51],v[184:187],v[96:99]\nds_read_b128 v[172:175],v244 offset:31744\nv_mfma_f32_16x16x32_bf16 v[96:99],a[40:43],v[180:183],v[96:99]\ns_waitcnt lgkmcnt(4)\n"
"v_mfma_f32_16x16x32_bf16 v[96:99],a[32:35],v[176:179],v[96:99]\nbuffer_load_dwordx4 v247,s[44:47],s48 offen sc1 lds\nv_mfma_f32_16x16x32_bf16 v[96:99],a[128:131],v[192:195],v[96:99]\nds_read_b128 v[176:179],v244 offset:32768\n"
"s_add_u32 m0,s50,73728\nv_mfma_f32_16x16x32_bf16 v[96:99],a[136:139],v[196:199],v[96:99]\nds_read_b128 v[180:183],v244 offset:33792\ns_add_u32 s48,s76,20480\nv_mfma_f32_16x16x32_bf16 v[96:99],a[144:147],v[200:203],v[96:99]\n"
"v_mfma_f32_16x16x32_bf16 v[96:99],a[152:155],v[204:207],v[96:99]\nbuffer_load_dwordx4 v247,s[44:47],s48 offen sc1 lds\nv_mfma_f32_16x16x32_bf16 v[100:103],a[156:159],v[204:207],v[100:103]\n"
"ds_read_b128 v[184:187],v244 offset:34816\ns_add_u32 m0,s50,77824\nv_mfma_f32_16x16x32_bf16 v[100:103],a[148:151],v[200:203],v[100:103]\nds_read_b128 v[188:191],v244 offset:35840\ns_add_u32 s48,s76,24576\n"
"v_mfma_f32_16x16x32_bf16 v[100:103],a[140:143],v[196:199],v[100:103]\ns_waitcnt lgkmcnt(4)\nv_mfma_f32_16x16x32_bf16 v[100:103],a[132:135],v[192:195],v[100:103]\nbuffer_load_dwordx4 v247,s[44:47],s48 offen sc1 lds\n"
"v_mfma_f32_16x16x32_bf16 v[100:103],a[164:167],v[160:163],v[100:103]\nds_read_b128 v[192:195],v244 offset:36864\ns_add_u32 m0,s50,81920\nv_mfma_f32_16x16x32_bf16 v[100:103],a[172:175],v[164:167],v[100:103]\n"
"ds_read_b128 v[196:199],v244 offset:37888\ns_add_u32 s48,s76,28672\nv_mfma_f32_16x16x32_bf16 v[100:103],a[180:183],v[168:171],v[100:103]\nv_mfma_f32_16x16x32_bf16 v[100:103],a[188:191],v[172:175],v[100:103]\n"
"buffer_load_dwordx4 v247,s[44:47],s48 offen sc1 lds\nv_mfma_f32_16x16x32_bf16 v[96:99],a[184:187],v[172:175],v[96:99]\nds_read_b128 v[200:203],v244 offset:38912\ns_add_u32 m0,s50,86016\n"
"v_mfma_f32_16x16x32_bf16 v[96:99],a[176:179],v[168:171],v[96:99]\nds_read_b128 v[204:207],v244 offset:39936\ns_add_u32 s48,s76,32768\nv_mfma_f32_16x16x32_bf16 v[96:99],a[168:171],v[164:167],v[96:99]\n"
"s_waitcnt vmcnt(7) lgkmcnt(4)\nv_mfma_f32_16x16x32_bf16 v[96:99],a[160:163],v[160:163],v[96:99]\nbuffer_load_dwordx4 v247,s[44:47],s48 offen sc1 lds\nv_mfma_f32_16x16x32_bf16 v[96:99],a[192:195],v[176:179],v[96:99]\ns_barrier\n"
"s_add_u32 m0,s50,98304\ns_add_u32 s48,s77,0\nv_mfma_f32_16x16x32_bf16 v[96:99],a[200:203],v[180:183],v[96:99]\nds_read_b128 v[160:163],v244 offset:40960\nv_mfma_f32_16x16x32_bf16 v[96:99],a[208:211],v[184:187],v[96:99]\n"
"ds_read_b128 v[164:167],v244 offset:41984\nv_mfma_f32_16x16x32_bf16 v[96:99],a[216:219],v[188:191],v[96:99]\nbuffer_load_dwordx4 v247,s[44:47],s48 offen sc1 lds\n"
"v_mfma_f32_16x16x32_bf16 v[100:103],a[220:223],v[188:191],v[100:103]\nds_read_b128 v[168:171],v244 offset:43008\ns_add_u32 m0,s50,102400\nv_mfma_f32_16x16x32_bf16 v[100:103],a[212:215],v[184:187],v[100:103]\n"
"ds_read_b128 v[172:175],v244 offset:44032\ns_add_u32 s48,s77,4096\nv_mfma_f32_16x16x32_bf16 v[100:103],a[204:207],v[180:183],v[100:103]\ns_waitcnt lgkmcnt(4)\nv_mfma_f32_16x16x32_bf16 v[100:103],a[196:199],v[176:179],v[100:103]\n"
"buffer_load_dwordx4 v247,s[44:47],s48 offen sc1 lds\nv_mfma_f32_16x16x32_bf16 v[100:103],a[228:231],v[192:195],v[100:103]\nds_read_b128 v[176:179],v244 offset:45056\ns_add_u32 m0,s50,106496\n"
"v_mfma_f32_16x16x32_bf16 v[100:103],a[236:239],v[196:199],v[100:103]\nds_read_b128 v[180:183],v244 offset:46080\ns_add_u32 s48,s77,8192\nv_mfma_f32_16x16x32_bf16 v[100:103],a[244:247],v[200:203],v[100:103]\n"
"v_mfma_f32_16x16x32_bf16 v[100:103],a[252:255],v[204:207],v[100:103]\nbuffer_load_dwordx4 v247,s[44:47],s48 offen sc1 lds\nv_mfma_f32_16x16x32_bf16 v[96:99],a[248:251],v[204:207],v[96:99]\n"
"ds_read_b128 v[184:187],v244 offset:47104\ns_add_u32 m0,s50,110592\nv_mfma_f32_16x16x32_bf16 v[96:99],a[240:243],v[200:203],v[96:99]\nds_read_b128 v[188:191],v244 offset:48128\ns_add_u32 s48,s77,12288\n"
"v_mfma_f32_16x16x32_bf16 v[96:99],a[232:235],v[196:199],v[96:99]\ns_waitcnt lgkmcnt(4)\nv_mfma_f32_16x16x32_bf16 v[96:99],a[224:227],v[192:195],v[96:99]\nbuffer_load_dwordx4 v247,s[44:47],s48 offen sc1 lds\n"
"v_mfma_f32_16x16x32_bf16 v[96:99],a[64:67],v[160:163],v[96:99]\nds_read_b128 v[192:195],v244 offset:49152\ns_add_u32 m0,s50,114688\nv_mfma_f32_16x16x32_bf16 v[96:99],a[72:75],v[164:167],v[96:99]\n"
"ds_read_b128 v[196:199],v244 offset:50176\ns_add_u32 s48,s77,16384\nv_mfma_f32_16x16x32_bf16 v[96:99],a[80:83],v[168:171],v[96:99]\nv_mfma_f32_16x16x32_bf16 v[96:99],a[88:91],v[172:175],v[96:99]\n"
"buffer_load_dwordx4 v247,s[44:47],s48 offen sc1 lds\nv_mfma_f32_16x16x32_bf16 v[100:103],a[92:95],v[172:175],v[100:103]\nds_read_b128 v[200:203],v244 offset:51200\ns_add_u32 m0,s50,118784\n"
"v_mfma_f32_16x16x32_bf16 v[100:103],a[84:87],v[168:171],v[100:103]\nds_read_b128 v[204:207],v244 offset:52224\ns_add_u32 s48,s77,20480\nv_mfma_f32_16x16x32_bf16 v[100:103],a[76:79],v[164:167],v[100:103]\ns_waitcnt lgkmcnt(4)\n"
"v_mfma_f32_16x16x32_bf16 v[100:103],a[68:71],v[160:163],v[100:103]\nv_mfma_f32_16x16x32_bf16 v[100:103],a[100:103],v[176:179],v[100:103]\nds_read_b128 v[160:163],v244 offset:53248\n"
"v_mfma_f32_16x16x32_bf16 v[100:103],a[108:111],v[180:183],v[100:103]\nds_read_b128 v[164:167],v244 offset:54272\nv_mfma_f32_16x16x32_bf16 v[100:103],a[116:119],v[184:187],v[100:103]\n"
"buffer_load_dwordx4 v247,s[44:47],s48 offen sc1 lds\nv_mfma_f32_16x16x32_bf16 v[100:103],a[124:127],v[188:191],v[100:103]\ns_add_u32 m0,s50,131072\ns_add_u32 s48,s77,24576\n"
"v_mfma_f32_16x16x32_bf16 v[96:99],a[120:123],v[188:191],v[96:99]\nds_read_b128 v[168:171],v244 offset:55296\nv_mfma_f32_16x16x32_bf16 v[96:99],a[112:115],v[184:187],v[96:99]\nds_read_b128 v[172:175],v244 offset:56320\n"
"v_mfma_f32_16x16x32_bf16 v[96:99],a[104:107],v[180:183],v[96:99]\ns_waitcnt lgkmcnt(4)\nv_mfma_f32_16x16x32_bf16 v[96:99],a[96:99],v[176:179],v[96:99]\nv_mfma_f32_16x16x32_bf16 v[96:99],v[32:35],v[192:195],v[96:99]\n"
"ds_read_b128 v[176:179],v244 offset:57344\nv_mfma_f32_16x16x32_bf16 v[96:99],v[40:43],v[196:199],v[96:99]\nds_read_b128 v[180:183],v244 offset:58368\nv_mfma_f32_16x16x32_bf16 v[96:99],v[48:51],v[200:203],v[96:99]\n"
"buffer_load_dwordx4 v247,s[44:47],s48 offen sc1 lds\nv_mfma_f32_16x16x32_bf16 v[96:99],v[56:59],v[204:207],v[96:99]\ns_add_u32 m0,s50,135168\ns_add_u32 s48,s77,28672\n"
"v_mfma_f32_16x16x32_bf16 v[100:103],v[60:63],v[204:207],v[100:103]\nds_read_b128 v[184:187],v244 offset:59392\nv_mfma_f32_16x16x32_bf16 v[100:103],v[52:55],v[200:203],v[100:103]\nds_read_b128 v[188:191],v244 offset:60416\n"
"v_mfma_f32_16x16x32_bf16 v[100:103],v[44:47],v[196:199],v[100:103]\ns_waitcnt lgkmcnt(4)\nv_mfma_f32_16x16x32_bf16 v[100:103],v[36:39],v[192:195],v[100:103]\nv_mfma_f32_16x16x32_bf16 v[104:107],a[0:3],v[160:163],0\n"
"ds_read_b128 v[192:195],v244 offset:61440\ns_add_u32 s57,s85,49152\nv_mfma_f32_16x16x32_bf16 v[104:107],a[8:11],v[164:167],v[104:107]\nds_read_b128 v[196:199],v244 offset:62464\n"
"v_mfma_f32_16x16x32_bf16 v[104:107],a[16:19],v[168:171],v[104:107]\nbuffer_load_dwordx4 v247,s[44:47],s48 offen sc1 lds\nv_mfma_f32_16x16x32_bf16 v[104:107],a[24:27],v[172:175],v[104:107]\ns_add_u32 m0,s50,139264\n"
"s_add_u32 s48,s77,32768\nv_mfma_f32_16x16x32_bf16 v[108:111],a[28:31],v[172:175],0\nds_read_b128 v[200:203],v244 offset:63488\nv_mfma_f32_16x16x32_bf16 v[108:111],a[20:23],v[168:171],v[108:111]\n"
"ds_read_b128 v[204:207],v244 offset:64512\nv_cvt_pk_bf16_f32 v96,v96,v97\nv_mfma_f32_16x16x32_bf16 v[108:111],a[12:15],v[164:167],v[108:111]\ns_waitcnt vmcnt(8) lgkmcnt(4)\nv_cvt_pk_bf16_f32 v97,v98,v99\n"
"v_cvt_pk_bf16_f32 v98,v100,v101\nv_mfma_f32_16x16x32_bf16 v[108:111],a[4:7],v[160:163],v[108:111]\nv_cvt_pk_bf16_f32 v99,v102,v103\nv_mfma_f32_16x16x32_bf16 v[108:111],a[36:39],v[176:179],v[108:111]\ns_barrier\n"
"v_mfma_f32_16x16x32_bf16 v[108:111],a[44:47],v[180:183],v[108:111]\nds_read_b128 v[160:163],v245 offset:0\nv_mfma_f32_16x16x32_bf16 v[108:111],a[52:55],v[184:187],v[108:111]\nds_read_b128 v[164:167],v245 offset:1024\n"
"v_mfma_f32_16x16x32_bf16 v[108:111],a[60:63],v[188:191],v[108:111]\nbuffer_store_dwordx4 v[96:99],v247,s[44:47],s57 offen sc1\nv_mfma_f32_16x16x32_bf16 v[104:107],a[56:59],v[188:191],v[104:107]\n"
"ds_read_b128 v[168:171],v245 offset:2048\nv_mfma_f32_16x16x32_bf16 v[104:107],a[48:51],v[184:187],v[104:107]\nds_read_b128 v[172:175],v245 offset:3072\nv_mfma_f32_16x16x32_bf16 v[104:107],a[40:43],v[180:183],v[104:107]\n"
"s_waitcnt lgkmcnt(4)\nv_mfma_f32_16x16x32_bf16 v[104:107],a[32:35],v[176:179],v[104:107]\nbuffer_load_dwordx4 v247,s[44:47],s48 offen sc1 lds\nv_mfma_f32_16x16x32_bf16 v[104:107],a[128:131],v[192:195],v[104:107]\n"
"ds_read_b128 v[176:179],v245 offset:4096\ns_add_u32 m0,s50,143360\nv_mfma_f32_16x16x32_bf16 v[104:107],a[136:139],v[196:199],v[104:107]\nds_read_b128 v[180:183],v245 offset:5120\ns_add_u32 s48,s78,0\n"
"v_mfma_f32_16x16x32_bf16 v[104:107],a[144:147],v[200:203],v[104:107]\nv_mfma_f32_16x16x32_bf16 v[104:107],a[152:155],v[204:207],v[104:107]\nbuffer_load_dwordx4 v247,s[44:47],s48 offen sc1 lds\n"
"v_mfma_f32_16x16x32_bf16 v[108:111],a[156:159],v[204:207],v[108:111]\nds_read_b128 v[184:187],v245 offset:6144\ns_add_u32 m0,s50,147456\nv_mfma_f32_16x16x32_bf16 v[108:111],a[148:151],v[200:203],v[108:111]\n"
"ds_read_b128 v[188:191],v245 offset:7168\ns_add_u32 s48,s78,4096\nv_mfma_f32_16x16x32_bf16 v[108:111],a[140:143],v[196:199],v[108:111]\ns_waitcnt lgkmcnt(4)\nv_mfma_f32_16x16x32_bf16 v[108:111],a[132:135],v[192:195],v[108:111]\n"
"v_mfma_f32_16x16x32_bf16 v[108:111],a[164:167],v[160:163],v[108:111]\nds_read_b128 v[192:195],v245 offset:8192\nv_mfma_f32_16x16x32_bf16 v[108:111],a[172:175],v[164:167],v[108:111]\nds_read_b128 v[196:199],v245 offset:9216\n"
"v_mfma_f32_16x16x32_bf16 v[108:111],a[180:183],v[168:171],v[108:111]\nbuffer_load_dwordx4 v247,s[44:47],s48 offen sc1 lds\nv_mfma_f32_16x16x32_bf16 v[108:111],a[188:191],v[172:175],v[108:111]\ns_add_u32 m0,s50,151552\n"
"s_add_u32 s48,s78,8192\nv_mfma_f32_16x16x32_bf16 v[104:107],a[184:187],v[172:175],v[104:107]\nds_read_b128 v[200:203],v245 offset:10240\nv_mfma_f32_16x16x32_bf16 v[104:107],a[176:179],v[168:171],v[104:107]\n"
"ds_read_b128 v[204:207],v245 offset:11264\nv_mfma_f32_16x16x32_bf16 v[104:107],a[168:171],v[164:167],v[104:107]\ns_waitcnt lgkmcnt(4)\nv_mfma_f32_16x16x32_bf16 v[104:107],a[160:163],v[160:163],v[104:107]\n"
"v_mfma_f32_16x16x32_bf16 v[104:107],a[192:195],v[176:179],v[104:107]\nds_read_b128 v[160:163],v245 offset:12288\nv_mfma_f32_16x16x32_bf16 v[104:107],a[200:203],v[180:183],v[104:107]\nds_read_b128 v[164:167],v245 offset:13312\n"
"v_mfma_f32_16x16x32_bf16 v[104:107],a[208:211],v[184:187],v[104:107]\nbuffer_load_dwordx4 v247,s[44:47],s48 offen sc1 lds\nv_mfma_f32_16x16x32_bf16 v[104:107],a[216:219],v[188:191],v[104:107]\ns_add_u32 m0,s50,155648\n"
"s_add_u32 s48,s78,12288\nv_mfma_f32_16x16x32_bf16 v[108:111],a[220:223],v[188:191],v[108:111]\nds_read_b128 v[168:171],v245 offset:14336\nv_mfma_f32_16x16x32_bf16 v[108:111],a[212:215],v[184:187],v[108:111]\n"
"ds_read_b128 v[172:175],v245 offset:15360\nv_mfma_f32_16x16x32_bf16 v[108:111],a[204:207],v[180:183],v[108:111]\ns_waitcnt lgkmcnt(4)\nv_mfma_f32_16x16x32_bf16 v[108:111],a[196:199],v[176:179],v[108:111]\n"
"v_mfma_f32_16x16x32_bf16 v[108:111],a[228:231],v[192:195],v[108:111]\nds_read_b128 v[176:179],v245 offset:16384\nv_mfma_f32_16x16x32_bf16 v[108:111],a[236:239],v[196:199],v[108:111]\nds_read_b128 v[180:183],v245 offset:17408\n"
"v_mfma_f32_16x16x32_bf16 v[108:111],a[244:247],v[200:203],v[108:111]\nbuffer_load_dwordx4 v247,s[44:47],s48 offen sc1 lds\nv_mfma_f32_16x16x32_bf16 v[108:111],a[252:255],v[204:207],v[108:111]\ns_add_u32 m0,s50,90112\n"
"s_add_u32 s48,s78,16384\nv_mfma_f32_16x16x32_bf16 v[104:107],a[248:251],v[204:207],v[104:107]\nds_read_b128 v[184:187],v245 offset:18432\nv_mfma_f32_16x16x32_bf16 v[104:107],a[240:243],v[200:203],v[104:107]\n"
"ds_read_b128 v[188:191],v245 offset:19456\nv_mfma_f32_16x16x32_bf16 v[104:107],a[232:235],v[196:199],v[104:107]\ns_waitcnt lgkmcnt(4)\nv_mfma_f32_16x16x32_bf16 v[104:107],a[224:227],v[192:195],v[104:107]\ns_waitcnt vmcnt(5)\n"
"v_mov_b32 v234,s98\nv_mov_b32 v235,s99\ns_mov_b64 exec,1\nv_mov_b32 v233,s90\nglobal_store_dwordx2 v233,v[234:235],s[60:61] offset:0 sc1\ns_mov_b64 exec,-1\nv_mfma_f32_16x16x32_bf16 v[104:107],a[64:67],v[160:163],v[104:107]\n"
"ds_read_b128 v[192:195],v245 offset:20480\nv_mfma_f32_16x16x32_bf16 v[104:107],a[72:75],v[164:167],v[104:107]\nds_read_b128 v[196:199],v245 offset:21504\nv_mfma_f32_16x16x32_bf16 v[104:107],a[80:83],v[168:171],v[104:107]\n"
"buffer_load_dwordx4 v247,s[44:47],s48 offen sc1 lds\nv_mfma_f32_16x16x32_bf16 v[104:107],a[88:91],v[172:175],v[104:107]\ns_add_u32 m0,s50,94208\ns_add_u32 s48,s78,20480\n"
"v_mfma_f32_16x16x32_bf16 v[108:111],a[92:95],v[172:175],v[108:111]\nds_read_b128 v[200:203],v245 offset:22528\nv_mfma_f32_16x16x32_bf16 v[108:111],a[84:87],v[168:171],v[108:111]\nds_read_b128 v[204:207],v245 offset:23552\n"
"v_mfma_f32_16x16x32_bf16 v[108:111],a[76:79],v[164:167],v[108:111]\ns_waitcnt vmcnt(9) lgkmcnt(4)\nv_mfma_f32_16x16x32_bf16 v[108:111],a[68:71],v[160:163],v[108:111]\n"
"v_mfma_f32_16x16x32_bf16 v[108:111],a[100:103],v[176:179],v[108:111]\ns_barrier\nv_mfma_f32_16x16x32_bf16 v[108:111],a[108:111],v[180:183],v[108:111]\nds_read_b128 v[160:163],v245 offset:32768\n"
"v_mfma_f32_16x16x32_bf16 v[108:111],a[116:119],v[184:187],v[108:111]\nds_read_b128 v[164:167],v245 offset:33792\nv_mfma_f32_16x16x32_bf16 v[108:111],a[124:127],v[188:191],v[108:111]\n"
"buffer_load_dwordx4 v247,s[44:47],s48 offen sc1 lds\nv_mfma_f32_16x16x32_bf16 v[104:107],a[120:123],v[188:191],v[104:107]\nds_read_b128 v[168:171],v245 offset:34816\ns_add_u32 m0,s50,122880\n"
"v_mfma_f32_16x16x32_bf16 v[104:107],a[112:115],v[184:187],v[104:107]\nds_read_b128 v[172:175],v245 offset:35840\ns_add_u32 s48,s78,24576\nv_mfma_f32_16x16x32_bf16 v[104:107],a[104:107],v[180:183],v[104:107]\ns_waitcnt lgkmcnt(4)\n"
"v_mfma_f32_16x16x32_bf16 v[104:107],a[96:99],v[176:179],v[104:107]\nv_mfma_f32_16x16x32_bf16 v[104:107],v[32:35],v[192:195],v[104:107]\nds_read_b128 v[176:179],v245 offset:36864\n"
"v_mfma_f32_16x16x32_bf16 v[104:107],v[40:43],v[196:199],v[104:107]\nds_read_b128 v[180:183],v245 offset:37888\nv_mfma_f32_16x16x32_bf16 v[104:107],v[48:51],v[200:203],v[104:107]\n"
"buffer_load_dwordx4 v247,s[44:47],s48 offen sc1 lds\nv_mfma_f32_16x16x32_bf16 v[104:107],v[56:59],v[204:207],v[104:107]\ns_add_u32 m0,s50,126976\ns_add_u32 s48,s78,28672\n"
"v_mfma_f32_16x16x32_bf16 v[108:111],v[60:63],v[204:207],v[108:111]\nds_read_b128 v[184:187],v245 offset:38912\nv_mfma_f32_16x16x32_bf16 v[108:111],v[52:55],v[200:203],v[108:111]\nds_read_b128 v[188:191],v245 offset:39936\n"
"v_mfma_f32_16x16x32_bf16 v[108:111],v[44:47],v[196:199],v[108:111]\ns_waitcnt lgkmcnt(4)\nv_mfma_f32_16x16x32_bf16 v[108:111],v[36:39],v[192:195],v[108:111]\nv_mfma_f32_16x16x32_bf16 v[112:115],a[0:3],v[160:163],0\n"
"ds_read_b128 v[192:195],v245 offset:40960\nv_mfma_f32_16x16x32_bf16 v[112:115],a[8:11],v[164:167],v[112:115]\nds_read_b128 v[196:199],v245 offset:41984\nv_mfma_f32_16x16x32_bf16 v[112:115],a[16:19],v[168:171],v[112:115]\n"
"s_add_u32 s57,s80,1\ns_and_b32 s57,s57,3\nv_mfma_f32_16x16x32_bf16 v[112:115],a[24:27],v[172:175],v[112:115]\ns_lshl_b32 s57,s57,14\ns_add_u32 s57,s57,s85\ns_add_u32 s57,s57,0x10000\n"
"v_mfma_f32_16x16x32_bf16 v[116:119],a[28:31],v[172:175],0\nds_read_b128 v[200:203],v245 offset:43008\nv_mfma_f32_16x16x32_bf16 v[116:119],a[20:23],v[168:171],v[116:119]\nds_read_b128 v[204:207],v245 offset:44032\n"
"v_cvt_pk_bf16_f32 v104,v104,v105\nv_mfma_f32_16x16x32_bf16 v[116:119],a[12:15],v[164:167],v[116:119]\ns_waitcnt lgkmcnt(4)\nv_cvt_pk_bf16_f32 v105,v106,v107\nv_cvt_pk_bf16_f32 v106,v108,v109\n"
"v_mfma_f32_16x16x32_bf16 v[116:119],a[4:7],v[160:163],v[116:119]\nbuffer_load_dwordx4 v247,s[44:47],s48 offen sc1 lds\nv_mfma_f32_16x16x32_bf16 v[116:119],a[36:39],v[176:179],v[116:119]\nds_read_b128 v[160:163],v245 offset:45056\n"
"s_add_u32 m0,s50,0\nv_mfma_f32_16x16x32_bf16 v[116:119],a[44:47],v[180:183],v[116:119]\nds_read_b128 v[164:167],v245 offset:46080\ns_add_u32 s48,s78,32768\nv_mfma_f32_16x16x32_bf16 v[116:119],a[52:55],v[184:187],v[116:119]\n"
"v_cvt_pk_bf16_f32 v107,v110,v111\nv_mfma_f32_16x16x32_bf16 v[116:119],a[60:63],v[188:191],v[116:119]\nbuffer_store_dwordx4 v[104:107],v247,s[44:47],s57 offen sc1\n"
"v_mfma_f32_16x16x32_bf16 v[112:115],a[56:59],v[188:191],v[112:115]\nds_read_b128 v[168:171],v245 offset:47104\nv_mfma_f32_16x16x32_bf16 v[112:115],a[48:51],v[184:187],v[112:115]\nds_read_b128 v[172:175],v245 offset:48128\n"
"v_mfma_f32_16x16x32_bf16 v[112:115],a[40:43],v[180:183],v[112:115]\ns_waitcnt lgkmcnt(4)\nv_mfma_f32_16x16x32_bf16 v[112:115],a[32:35],v[176:179],v[112:115]\nbuffer_load_dwordx4 v247,s[44:47],s48 offen sc1 lds\n"
"v_mfma_f32_16x16x32_bf16 v[112:115],a[128:131],v[192:195],v[112:115]\nds_read_b128 v[176:179],v245 offset:49152\ns_add_u32 m0,s50,4096\nv_mfma_f32_16x16x32_bf16 v[112:115],a[136:139],v[196:199],v[112:115]\n"
"ds_read_b128 v[180:183],v245 offset:50176\ns_add_u32 s48,s79,0\nv_mfma_f32_16x16x32_bf16 v[112:115],a[144:147],v[200:203],v[112:115]\nv_mfma_f32_16x16x32_bf16 v[112:115],a[152:155],v[204:207],v[112:115]\n"
"buffer_load_dwordx4 v247,s[44:47],s48 offen sc1 lds\nv_mfma_f32_16x16x32_bf16 v[116:119],a[156:159],v[204:207],v[116:119]\nds_read_b128 v[184:187],v245 offset:51200\ns_add_u32 m0,s50,8192\n"
"v_mfma_f32_16x16x32_bf16 v[116:119],a[148:151],v[200:203],v[116:119]\nds_read_b128 v[188:191],v245 offset:52224\ns_add_u32 s48,s79,4096\nv_mfma_f32_16x16x32_bf16 v[116:119],a[140:143],v[196:199],v[116:119]\ns_waitcnt lgkmcnt(4)\n"
"v_mfma_f32_16x16x32_bf16 v[116:119],a[132:135],v[192:195],v[116:119]\nv_mfma_f32_16x16x32_bf16 v[116:119],a[164:167],v[160:163],v[116:119]\nds_read_b128 v[192:195],v245 offset:53248\n"
"v_mfma_f32_16x16x32_bf16 v[116:119],a[172:175],v[164:167],v[116:119]\nds_read_b128 v[196:199],v245 offset:54272\nv_mfma_f32_16x16x32_bf16 v[116:119],a[180:183],v[168:171],v[116:119]\n"
"global_load_dwordx2 v[240:241],v238,s[60:61] offset:0 sc1\nv_mfma_f32_16x16x32_bf16 v[116:119],a[188:191],v[172:175],v[116:119]\nv_mfma_f32_16x16x32_bf16 v[112:115],a[184:187],v[172:175],v[112:115]\n"
"ds_read_b128 v[200:203],v245 offset:55296\nv_mfma_f32_16x16x32_bf16 v[112:115],a[176:179],v[168:171],v[112:115]\nds_read_b128 v[204:207],v245 offset:56320\nv_mfma_f32_16x16x32_bf16 v[112:115],a[168:171],v[164:167],v[112:115]\n"
"s_waitcnt vmcnt(9) lgkmcnt(4)\nv_mfma_f32_16x16x32_bf16 v[112:115],a[160:163],v[160:163],v[112:115]\nbuffer_load_dwordx4 v247,s[44:47],s48 offen sc1 lds\nv_mfma_f32_16x16x32_bf16 v[112:115],a[192:195],v[176:179],v[112:115]\n"
"s_barrier\ns_add_u32 m0,s50,12288\ns_add_u32 s48,s79,8192\nv_mfma_f32_16x16x32_bf16 v[112:115],a[200:203],v[180:183],v[112:115]\nds_read_b128 v[160:163],v246 offset:0\n"
"v_mfma_f32_16x16x32_bf16 v[112:115],a[208:211],v[184:187],v[112:115]\nds_read_b128 v[164:167],v246 offset:1024\nv_mfma_f32_16x16x32_bf16 v[112:115],a[216:219],v[188:191],v[112:115]\n"
"buffer_load_dwordx4 v247,s[44:47],s48 offen sc1 lds\nv_mfma_f32_16x16x32_bf16 v[116:119],a[220:223],v[188:191],v[116:119]\nds_read_b128 v[168:171],v246 offset:2048\ns_add_u32 m0,s50,16384\n"
"v_mfma_f32_16x16x32_bf16 v[116:119],a[212:215],v[184:187],v[116:119]\nds_read_b128 v[172:175],v246 offset:3072\ns_add_u32 s48,s79,12288\nv_mfma_f32_16x16x32_bf16 v[116:119],a[204:207],v[180:183],v[116:119]\ns_waitcnt lgkmcnt(4)\n"
"v_mfma_f32_16x16x32_bf16 v[116:119],a[196:199],v[176:179],v[116:119]\nv_mfma_f32_16x16x32_bf16 v[116:119],a[228:231],v[192:195],v[116:119]\nds_read_b128 v[176:179],v246 offset:4096\n"
"v_mfma_f32_16x16x32_bf16 v[116:119],a[236:239],v[196:199],v[116:119]\nds_read_b128 v[180:183],v246 offset:5120\nv_mfma_f32_16x16x32_bf16 v[116:119],a[244:247],v[200:203],v[116:119]\n"
"buffer_load_dwordx4 v247,s[44:47],s48 offen sc1 lds\nv_mfma_f32_16x16x32_bf16 v[116:119],a[252:255],v[204:207],v[116:119]\ns_add_u32 m0,s50,20480\ns_add_u32 s48,s79,16384\n"
"v_mfma_f32_16x16x32_bf16 v[112:115],a[248:251],v[204:207],v[112:115]\nds_read_b128 v[184:187],v246 offset:6144\nv_mfma_f32_16x16x32_bf16 v[112:115],a[240:243],v[200:203],v[112:115]\nds_read_b128 v[188:191],v246 offset:7168\n"
"v_mfma_f32_16x16x32_bf16 v[112:115],a[232:235],v[196:199],v[112:115]\ns_waitcnt lgkmcnt(4)\nv_mfma_f32_16x16x32_bf16 v[112:115],a[224:227],v[192:195],v[112:115]\nv_mfma_f32_16x16x32_bf16 v[112:115],a[64:67],v[160:163],v[112:115]\n"
"ds_read_b128 v[192:195],v246 offset:8192\nv_mfma_f32_16x16x32_bf16 v[112:115],a[72:75],v[164:167],v[112:115]\nds_read_b128 v[196:199],v246 offset:9216\nv_mfma_f32_16x16x32_bf16 v[112:115],a[80:83],v[168:171],v[112:115]\n"
"buffer_load_dwordx4 v247,s[44:47],s48 offen sc1 lds\nv_mfma_f32_16x16x32_bf16 v[112:115],a[88:91],v[172:175],v[112:115]\ns_add_u32 m0,s50,24576\ns_add_u32 s48,s79,20480\n"
"v_mfma_f32_16x16x32_bf16 v[116:119],a[92:95],v[172:175],v[116:119]\nds_read_b128 v[200:203],v246 offset:10240\nv_mfma_f32_16x16x32_bf16 v[116:119],a[84:87],v[168:171],v[116:119]\nds_read_b128 v[204:207],v246 offset:11264\n"
"v_mfma_f32_16x16x32_bf16 v[116:119],a[76:79],v[164:167],v[116:119]\ns_waitcnt lgkmcnt(4)\nv_mfma_f32_16x16x32_bf16 v[116:119],a[68:71],v[160:163],v[116:119]\nv_mfma_f32_16x16x32_bf16 v[116:119],a[100:103],v[176:179],v[116:119]\n"
"ds_read_b128 v[160:163],v246 offset:12288\nv_mfma_f32_16x16x32_bf16 v[116:119],a[108:111],v[180:183],v[116:119]\nds_read_b128 v[164:167],v246 offset:13312\nv_mfma_f32_16x16x32_bf16 v[116:119],a[116:119],v[184:187],v[116:119]\n"
"buffer_load_dwordx4 v247,s[44:47],s48 offen sc1 lds\nv_mfma_f32_16x16x32_bf16 v[116:119],a[124:127],v[188:191],v[116:119]\ns_add_u32 m0,s50,28672\ns_add_u32 s48,s79,24576\n"
"v_mfma_f32_16x16x32_bf16 v[112:115],a[120:123],v[188:191],v[112:115]\nds_read_b128 v[168:171],v246 offset:14336\nv_mfma_f32_16x16x32_bf16 v[112:115],a[112:115],v[184:187],v[112:115]\nds_read_b128 v[172:175],v246 offset:15360\n"
"v_mfma_f32_16x16x32_bf16 v[112:115],a[104:107],v[180:183],v[112:115]\ns_waitcnt lgkmcnt(4)\nv_mfma_f32_16x16x32_bf16 v[112:115],a[96:99],v[176:179],v[112:115]\ns_waitcnt vmcnt(5)\nv_cmp_ne_u64 vcc,s[98:99],v[240:241]\n"
"s_cmp_eq_u64 vcc,0\ns_cselect_b32 s72,0,0x40000000\nv_mfma_f32_16x16x32_bf16 v[112:115],v[32:35],v[192:195],v[112:115]\nds_read_b128 v[176:179],v246 offset:16384\nv_and_b32 v28,0xf0,v247\n"
"v_mfma_f32_16x16x32_bf16 v[112:115],v[40:43],v[196:199],v[112:115]\nds_read_b128 v[180:183],v246 offset:17408\nv_lshlrev_b32 v28,6,v28\nv_mfma_f32_16x16x32_bf16 v[112:115],v[48:51],v[200:203],v[112:115]\ns_add_u32 s56,s86,0\n"
"s_add_u32 s56,s56,s72\nv_lshrrev_b32 v29,4,v247\nv_mfma_f32_16x16x32_bf16 v[112:115],v[56:59],v[204:207],v[112:115]\nbuffer_load_dwordx4 v[0:3],v247,s[44:47],s56 offen sc1\n"
"v_mfma_f32_16x16x32_bf16 v[116:119],v[60:63],v[204:207],v[116:119]\nds_read_b128 v[184:187],v246 offset:18432\nv_and_b32 v29,48,v29\nv_mfma_f32_16x16x32_bf16 v[116:119],v[52:55],v[200:203],v[116:119]\n"
"ds_read_b128 v[188:191],v246 offset:19456\nv_add_u32 v28,v28,v29\nv_mfma_f32_16x16x32_bf16 v[116:119],v[44:47],v[196:199],v[116:119]\ns_waitcnt lgkmcnt(4)\ns_add_u32 s56,s86,4096\ns_add_u32 s56,s56,s72\n"
"v_mfma_f32_16x16x32_bf16 v[116:119],v[36:39],v[192:195],v[116:119]\nbuffer_load_dwordx4 v[4:7],v247,s[44:47],s56 offen sc1\nv_mfma_f32_16x16x32_bf16 v[120:123],a[0:3],v[160:163],0\nds_read_b128 v[192:195],v246 offset:20480\n"
"v_mfma_f32_16x16x32_bf16 v[120:123],a[8:11],v[164:167],v[120:123]\nds_read_b128 v[196:199],v246 offset:21504\nv_mfma_f32_16x16x32_bf16 v[120:123],a[16:19],v[168:171],v[120:123]\ns_add_u32 s56,s86,8192\ns_add_u32 s56,s56,s72\n"
"v_mfma_f32_16x16x32_bf16 v[120:123],a[24:27],v[172:175],v[120:123]\nbuffer_load_dwordx4 v[8:11],v247,s[44:47],s56 offen sc1\nv_mfma_f32_16x16x32_bf16 v[124:127],a[28:31],v[172:175],0\nds_read_b128 v[200:203],v246 offset:22528\n"
"v_mfma_f32_16x16x32_bf16 v[124:127],a[20:23],v[168:171],v[124:127]\nds_read_b128 v[204:207],v246 offset:23552\nv_cvt_pk_bf16_f32 v112,v112,v113\nv_mfma_f32_16x16x32_bf16 v[124:127],a[12:15],v[164:167],v[124:127]\n"
"s_waitcnt vmcnt(10) lgkmcnt(4)\ns_add_u32 s56,s86,12288\ns_add_u32 s56,s56,s72\nv_mfma_f32_16x16x32_bf16 v[124:127],a[4:7],v[160:163],v[124:127]\nbuffer_load_dwordx4 v[12:15],v247,s[44:47],s56 offen sc1\n"
"v_mfma_f32_16x16x32_bf16 v[124:127],a[36:39],v[176:179],v[124:127]\ns_barrier\ns_add_u32 s57,s80,2\ns_and_b32 s57,s57,3\nv_mfma_f32_16x16x32_bf16 v[124:127],a[44:47],v[180:183],v[124:127]\n"
"ds_read_b128 v[160:163],v246 offset:24576\nv_cvt_pk_bf16_f32 v113,v114,v115\nv_mfma_f32_16x16x32_bf16 v[124:127],a[52:55],v[184:187],v[124:127]\nds_read_b128 v[164:167],v246 offset:25600\nv_cvt_pk_bf16_f32 v114,v116,v117\n"
"v_mfma_f32_16x16x32_bf16 v[124:127],a[60:63],v[188:191],v[124:127]\ns_lshl_b32 s57,s57,14\ns_add_u32 s57,s57,s85\nv_cvt_pk_bf16_f32 v115,v118,v119\nv_mfma_f32_16x16x32_bf16 v[120:123],a[56:59],v[188:191],v[120:123]\n"
"ds_read_b128 v[168:171],v246 offset:26624\ns_add_u32 s57,s57,0x10000\nv_mfma_f32_16x16x32_bf16 v[120:123],a[48:51],v[184:187],v[120:123]\nds_read_b128 v[172:175],v246 offset:27648\n"
"v_mfma_f32_16x16x32_bf16 v[120:123],a[40:43],v[180:183],v[120:123]\ns_waitcnt lgkmcnt(4)\nv_mfma_f32_16x16x32_bf16 v[120:123],a[32:35],v[176:179],v[120:123]\nbuffer_store_dwordx4 v[112:115],v247,s[44:47],s57 offen sc1\n"
"v_mfma_f32_16x16x32_bf16 v[120:123],a[128:131],v[192:195],v[120:123]\nds_read_b128 v[176:179],v245 offset:24576\nv_mfma_f32_16x16x32_bf16 v[120:123],a[136:139],v[196:199],v[120:123]\nds_read_b128 v[180:183],v245 offset:25600\n"
"v_mfma_f32_16x16x32_bf16 v[120:123],a[144:147],v[200:203],v[120:123]\nv_mfma_f32_16x16x32_bf16 v[120:123],a[152:155],v[204:207],v[120:123]\nbuffer_load_dwordx4 v247,s[44:47],s48 offen sc1 lds\n"
"v_mfma_f32_16x16x32_bf16 v[124:127],a[156:159],v[204:207],v[124:127]\nds_read_b128 v[184:187],v245 offset:26624\ns_add_u32 m0,s50,32768\nv_mfma_f32_16x16x32_bf16 v[124:127],a[148:151],v[200:203],v[124:127]\n"
"ds_read_b128 v[188:191],v245 offset:27648\ns_add_u32 s48,s79,28672\nv_mfma_f32_16x16x32_bf16 v[124:127],a[140:143],v[196:199],v[124:127]\ns_waitcnt lgkmcnt(4)\nv_mfma_f32_16x16x32_bf16 v[124:127],a[132:135],v[192:195],v[124:127]\n"
"buffer_load_dwordx4 v247,s[44:47],s48 offen sc1 lds\nv_mfma_f32_16x16x32_bf16 v[124:127],a[164:167],v[160:163],v[124:127]\nds_read_b128 v[192:195],v245 offset:28672\ns_add_u32 m0,s50,36864\n"
"v_mfma_f32_16x16x32_bf16 v[124:127],a[172:175],v[164:167],v[124:127]\nds_read_b128 v[196:199],v245 offset:29696\ns_add_u32 s48,s79,32768\nv_mfma_f32_16x16x32_bf16 v[124:127],a[180:183],v[168:171],v[124:127]\n"
"v_mfma_f32_16x16x32_bf16 v[124:127],a[188:191],v[172:175],v[124:127]\nbuffer_load_dwordx4 v247,s[44:47],s48 offen sc1 lds\nv_mfma_f32_16x16x32_bf16 v[120:123],a[184:187],v[172:175],v[120:123]\n"
"ds_read_b128 v[200:203],v245 offset:30720\nv_mfma_f32_16x16x32_bf16 v[120:123],a[176:179],v[168:171],v[120:123]\nds_read_b128 v[204:207],v245 offset:31744\nv_mfma_f32_16x16x32_bf16 v[120:123],a[168:171],v[164:167],v[120:123]\n"
"s_waitcnt lgkmcnt(4)\nv_mfma_f32_16x16x32_bf16 v[120:123],a[160:163],v[160:163],v[120:123]\nv_mfma_f32_16x16x32_bf16 v[120:123],a[192:195],v[176:179],v[120:123]\nds_read_b128 v[160:163],v245 offset:57344\n"
"v_mfma_f32_16x16x32_bf16 v[120:123],a[200:203],v[180:183],v[120:123]\nds_read_b128 v[164:167],v245 offset:58368\nv_mfma_f32_16x16x32_bf16 v[120:123],a[208:211],v[184:187],v[120:123]\n"
"v_mfma_f32_16x16x32_bf16 v[120:123],a[216:219],v[188:191],v[120:123]\nv_mfma_f32_16x16x32_bf16 v[124:127],a[220:223],v[188:191],v[124:127]\nds_read_b128 v[168:171],v245 offset:59392\n"
"v_mfma_f32_16x16x32_bf16 v[124:127],a[212:215],v[184:187],v[124:127]\nds_read_b128 v[172:175],v245 offset:60416\nv_mfma_f32_16x16x32_bf16 v[124:127],a[204:207],v[180:183],v[124:127]\ns_waitcnt lgkmcnt(4)\n"
"v_mfma_f32_16x16x32_bf16 v[124:127],a[196:199],v[176:179],v[124:127]\nv_mfma_f32_16x16x32_bf16 v[124:127],a[228:231],v[192:195],v[124:127]\nds_read_b128 v[176:179],v245 offset:61440\n"
"v_mfma_f32_16x16x32_bf16 v[124:127],a[236:239],v[196:199],v[124:127]\nds_read_b128 v[180:183],v245 offset:62464\nv_mfma_f32_16x16x32_bf16 v[124:127],a[244:247],v[200:203],v[124:127]\n"
"v_mfma_f32_16x16x32_bf16 v[124:127],a[252:255],v[204:207],v[124:127]\nv_mfma_f32_16x16x32_bf16 v[120:123],a[248:251],v[204:207],v[120:123]\nds_read_b128 v[184:187],v245 offset:63488\n"
"v_mfma_f32_16x16x32_bf16 v[120:123],a[240:243],v[200:203],v[120:123]\nds_read_b128 v[188:191],v245 offset:64512\nv_mfma_f32_16x16x32_bf16 v[120:123],a[232:235],v[196:199],v[120:123]\ns_waitcnt lgkmcnt(4)\n"
"v_mfma_f32_16x16x32_bf16 v[120:123],a[224:227],v[192:195],v[120:123]\nv_mfma_f32_16x16x32_bf16 v[120:123],a[64:67],v[160:163],v[120:123]\nds_read_b128 v[192:195],v244 offset:0\n"
"v_mfma_f32_16x16x32_bf16 v[120:123],a[72:75],v[164:167],v[120:123]\nds_read_b128 v[196:199],v244 offset:1024\nv_mfma_f32_16x16x32_bf16 v[120:123],a[80:83],v[168:171],v[120:123]\n"
"v_mfma_f32_16x16x32_bf16 v[120:123],a[88:91],v[172:175],v[120:123]\nv_mfma_f32_16x16x32_bf16 v[124:127],a[92:95],v[172:175],v[124:127]\nds_read_b128 v[200:203],v244 offset:2048\n"
"v_mfma_f32_16x16x32_bf16 v[124:127],a[84:87],v[168:171],v[124:127]\nds_read_b128 v[204:207],v244 offset:3072\nv_mfma_f32_16x16x32_bf16 v[124:127],a[76:79],v[164:167],v[124:127]\ns_waitcnt vmcnt(8) lgkmcnt(4)\n"
"v_mfma_f32_16x16x32_bf16 v[124:127],a[68:71],v[160:163],v[124:127]\ns_waitcnt vmcnt(4)\ns_add_u32 s73,s72,s83\nv_lshlrev_b32 v64,16,v0\nv_mfma_f32_16x16x32_bf16 v[124:127],a[100:103],v[176:179],v[124:127]\ns_barrier\n"
"v_and_b32 v65,0xffff0000,v0\nv_lshlrev_b32 v66,16,v1\nv_mfma_f32_16x16x32_bf16 v[124:127],a[108:111],v[180:183],v[124:127]\nds_read_b128 v[160:163],v244 offset:4096\nv_and_b32 v67,0xffff0000,v1\n"
"v_mfma_f32_16x16x32_bf16 v[124:127],a[116:119],v[184:187],v[124:127]\nds_read_b128 v[164:167],v244 offset:5120\nv_lshlrev_b32 v68,16,v2\nv_mfma_f32_16x16x32_bf16 v[124:127],a[124:127],v[188:191],v[124:127]\n"
"v_and_b32 v69,0xffff0000,v2\nv_lshlrev_b32 v70,16,v3\nv_mfma_f32_16x16x32_bf16 v[120:123],a[120:123],v[188:191],v[120:123]\nds_read_b128 v[168:171],v244 offset:6144\nv_and_b32 v71,0xffff0000,v3\n"
"v_mfma_f32_16x16x32_bf16 v[120:123],a[112:115],v[184:187],v[120:123]\nds_read_b128 v[172:175],v244 offset:7168\nv_lshlrev_b32 v72,16,v4\nv_mfma_f32_16x16x32_bf16 v[120:123],a[104:107],v[180:183],v[120:123]\ns_waitcnt lgkmcnt(4)\n"
"v_and_b32 v73,0xffff0000,v4\nv_lshlrev_b32 v74,16,v5\nv_mfma_f32_16x16x32_bf16 v[120:123],a[96:99],v[176:179],v[120:123]\nv_and_b32 v75,0xffff0000,v5\nv_lshlrev_b32 v76,16,v6\n"
"v_mfma_f32_16x16x32_bf16 v[120:123],v[32:35],v[192:195],v[120:123]\nds_read_b128 v[176:179],v244 offset:8192\nv_and_b32 v77,0xffff0000,v6\nv_mfma_f32_16x16x32_bf16 v[120:123],v[40:43],v[196:199],v[120:123]\n"
"ds_read_b128 v[180:183],v244 offset:9216\nv_lshlrev_b32 v78,16,v7\nv_mfma_f32_16x16x32_bf16 v[120:123],v[48:51],v[200:203],v[120:123]\nv_and_b32 v79,0xffff0000,v7\nv_lshlrev_b32 v80,16,v8\n"
"v_mfma_f32_16x16x32_bf16 v[120:123],v[56:59],v[204:207],v[120:123]\nv_and_b32 v81,0xffff0000,v8\nv_lshlrev_b32 v82,16,v9\nv_mfma_f32_16x16x32_bf16 v[124:127],v[60:63],v[204:207],v[124:127]\n"
"ds_read_b128 v[184:187],v244 offset:10240\nv_and_b32 v83,0xffff0000,v9\nv_mfma_f32_16x16x32_bf16 v[124:127],v[52:55],v[200:203],v[124:127]\nds_read_b128 v[188:191],v244 offset:11264\nv_lshlrev_b32 v84,16,v10\n"
"v_mfma_f32_16x16x32_bf16 v[124:127],v[44:47],v[196:199],v[124:127]\ns_waitcnt lgkmcnt(4)\nv_and_b32 v85,0xffff0000,v10\nv_lshlrev_b32 v86,16,v11\nv_mfma_f32_16x16x32_bf16 v[124:127],v[36:39],v[192:195],v[124:127]\n"
"v_and_b32 v87,0xffff0000,v11\nv_lshlrev_b32 v88,16,v12\nv_mfma_f32_16x16x32_bf16 v[128:131],a[0:3],v[160:163],0\nds_read_b128 v[192:195],v244 offset:12288\nv_and_b32 v89,0xffff0000,v12\n"
"v_mfma_f32_16x16x32_bf16 v[128:131],a[8:11],v[164:167],v[128:131]\nds_read_b128 v[196:199],v244 offset:13312\nv_lshlrev_b32 v90,16,v13\nv_mfma_f32_16x16x32_bf16 v[128:131],a[16:19],v[168:171],v[128:131]\ns_add_u32 s57,s80,3\n"
"s_and_b32 s57,s57,3\nv_and_b32 v91,0xffff0000,v13\nv_mfma_f32_16x16x32_bf16 v[128:131],a[24:27],v[172:175],v[128:131]\ns_lshl_b32 s57,s57,14\ns_add_u32 s57,s57,s85\ns_add_u32 s57,s57,0x10000\n"
"v_mfma_f32_16x16x32_bf16 v[132:135],a[28:31],v[172:175],0\nds_read_b128 v[200:203],v244 offset:14336\nv_lshlrev_b32 v92,16,v14\nv_mfma_f32_16x16x32_bf16 v[132:135],a[20:23],v[168:171],v[132:135]\n"
"ds_read_b128 v[204:207],v244 offset:15360\nv_cvt_pk_bf16_f32 v120,v120,v121\nv_mfma_f32_16x16x32_bf16 v[132:135],a[12:15],v[164:167],v[132:135]\ns_waitcnt lgkmcnt(4)\nv_cvt_pk_bf16_f32 v121,v122,v123\n"
"v_cvt_pk_bf16_f32 v122,v124,v125\nv_mfma_f32_16x16x32_bf16 v[132:135],a[4:7],v[160:163],v[132:135]\nv_cvt_pk_bf16_f32 v123,v126,v127\nv_and_b32 v93,0xffff0000,v14\nv_lshlrev_b32 v94,16,v15\n"
"v_mfma_f32_16x16x32_bf16 v[132:135],a[36:39],v[176:179],v[132:135]\nds_read_b128 v[160:163],v244 offset:16384\nv_and_b32 v95,0xffff0000,v15\nv_mfma_f32_16x16x32_bf16 v[132:135],a[44:47],v[180:183],v[132:135]\n"
"ds_read_b128 v[164:167],v244 offset:17408\nv_add_f32 v88,v88,v64\nv_mfma_f32_16x16x32_bf16 v[132:135],a[52:55],v[184:187],v[132:135]\nbuffer_store_dwordx4 v[120:123],v247,s[44:47],s57 offen sc1\n"
"v_mfma_f32_16x16x32_bf16 v[132:135],a[60:63],v[188:191],v[132:135]\nv_add_f32 v89,v89,v65\nv_add_f32 v90,v90,v66\nv_mfma_f32_16x16x32_bf16 v[128:131],a[56:59],v[188:191],v[128:131]\nds_read_b128 v[168:171],v244 offset:18432\n"
"v_add_f32 v91,v91,v67\nv_mfma_f32_16x16x32_bf16 v[128:131],a[48:51],v[184:187],v[128:131]\nds_read_b128 v[172:175],v244 offset:19456\nv_add_f32 v92,v92,v68\nv_mfma_f32_16x16x32_bf16 v[128:131],a[40:43],v[180:183],v[128:131]\n"
"s_waitcnt lgkmcnt(4)\nv_add_f32 v93,v93,v69\nv_add_f32 v94,v94,v70\nv_mfma_f32_16x16x32_bf16 v[128:131],a[32:35],v[176:179],v[128:131]\nv_add_f32 v95,v95,v71\nv_add_f32 v72,v72,v80\n"
"v_mfma_f32_16x16x32_bf16 v[128:131],a[128:131],v[192:195],v[128:131]\nds_read_b128 v[176:179],v244 offset:20480\nv_add_f32 v73,v73,v81\nv_mfma_f32_16x16x32_bf16 v[128:131],a[136:139],v[196:199],v[128:131]\n"
"ds_read_b128 v[180:183],v244 offset:21504\nv_add_f32 v74,v74,v82\nv_mfma_f32_16x16x32_bf16 v[128:131],a[144:147],v[200:203],v[128:131]\nv_add_f32 v75,v75,v83\nv_add_f32 v76,v76,v84\n"
"v_mfma_f32_16x16x32_bf16 v[128:131],a[152:155],v[204:207],v[128:131]\nv_add_f32 v77,v77,v85\nv_add_f32 v78,v78,v86\nv_mfma_f32_16x16x32_bf16 v[132:135],a[156:159],v[204:207],v[132:135]\nds_read_b128 v[184:187],v244 offset:22528\n"
"v_add_f32 v79,v79,v87\nv_mfma_f32_16x16x32_bf16 v[132:135],a[148:151],v[200:203],v[132:135]\nds_read_b128 v[188:191],v244 offset:23552\nv_add_f32 v88,v88,v72\nv_mfma_f32_16x16x32_bf16 v[132:135],a[140:143],v[196:199],v[132:135]\n"
"s_waitcnt lgkmcnt(4)\nv_add_f32 v89,v89,v73\nv_add_f32 v90,v90,v74\nv_mfma_f32_16x16x32_bf16 v[132:135],a[132:135],v[192:195],v[132:135]\nv_add_f32 v91,v91,v75\nv_add_f32 v92,v92,v76\n"
"v_mfma_f32_16x16x32_bf16 v[132:135],a[164:167],v[160:163],v[132:135]\nds_read_b128 v[192:195],v244 offset:24576\nv_add_f32 v93,v93,v77\nv_mfma_f32_16x16x32_bf16 v[132:135],a[172:175],v[164:167],v[132:135]\n"
"ds_read_b128 v[196:199],v244 offset:25600\nv_add_f32 v94,v94,v78\nv_mfma_f32_16x16x32_bf16 v[132:135],a[180:183],v[168:171],v[132:135]\ns_add_u32 s56,s80,1\ns_and_b32 s56,s56,3\ns_lshl_b32 s56,s56,12\n"
"v_mfma_f32_16x16x32_bf16 v[132:135],a[188:191],v[172:175],v[132:135]\ns_add_u32 s56,s56,s86\ns_add_u32 s56,s56,0x10000\nv_add_f32 v95,v95,v79\nv_mfma_f32_16x16x32_bf16 v[128:131],a[184:187],v[172:175],v[128:131]\n"
"ds_read_b128 v[200:203],v244 offset:26624\nv_cvt_pk_bf16_f32 v0,v88,v89\nv_mfma_f32_16x16x32_bf16 v[128:131],a[176:179],v[168:171],v[128:131]\nds_read_b128 v[204:207],v244 offset:27648\nv_cvt_pk_bf16_f32 v1,v90,v91\n"
"v_mfma_f32_16x16x32_bf16 v[128:131],a[168:171],v[164:167],v[128:131]\ns_waitcnt vmcnt(1) lgkmcnt(4)\nv_cvt_pk_bf16_f32 v2,v92,v93\nv_cvt_pk_bf16_f32 v3,v94,v95\n"
"v_mfma_f32_16x16x32_bf16 v[128:131],a[160:163],v[160:163],v[128:131]\nbuffer_load_dwordx4 v[16:19],v247,s[44:47],s56 offen sc1\nv_mfma_f32_16x16x32_bf16 v[128:131],a[192:195],v[176:179],v[128:131]\ns_barrier\n"
"v_mfma_f32_16x16x32_bf16 v[128:131],a[200:203],v[180:183],v[128:131]\nds_read_b128 v[160:163],v244 offset:28672\nv_mfma_f32_16x16x32_bf16 v[128:131],a[208:211],v[184:187],v[128:131]\nds_read_b128 v[164:167],v244 offset:29696\n"
"v_mfma_f32_16x16x32_bf16 v[128:131],a[216:219],v[188:191],v[128:131]\ns_add_u32 s56,s80,2\ns_and_b32 s56,s56,3\ns_lshl_b32 s56,s56,12\nv_mfma_f32_16x16x32_bf16 v[132:135],a[220:223],v[188:191],v[132:135]\n"
"ds_read_b128 v[168:171],v244 offset:30720\nv_mfma_f32_16x16x32_bf16 v[132:135],a[212:215],v[184:187],v[132:135]\nds_read_b128 v[172:175],v244 offset:31744\nv_mfma_f32_16x16x32_bf16 v[132:135],a[204:207],v[180:183],v[132:135]\n"
"s_waitcnt lgkmcnt(4)\ns_add_u32 s56,s56,s86\ns_add_u32 s56,s56,0x10000\nv_mfma_f32_16x16x32_bf16 v[132:135],a[196:199],v[176:179],v[132:135]\nbuffer_load_dwordx4 v[20:23],v247,s[44:47],s56 offen sc1\n"
"v_mfma_f32_16x16x32_bf16 v[132:135],a[228:231],v[192:195],v[132:135]\nds_read_b128 v[176:179],v244 offset:32768\nv_mfma_f32_16x16x32_bf16 v[132:135],a[236:239],v[196:199],v[132:135]\nds_read_b128 v[180:183],v244 offset:33792\n"
"v_mfma_f32_16x16x32_bf16 v[132:135],a[244:247],v[200:203],v[132:135]\ns_add_u32 s56,s80,3\ns_and_b32 s56,s56,3\ns_lshl_b32 s56,s56,12\nv_mfma_f32_16x16x32_bf16 v[132:135],a[252:255],v[204:207],v[132:135]\ns_add_u32 s56,s56,s86\n"
"s_add_u32 s56,s56,0x10000\nv_mfma_f32_16x16x32_bf16 v[128:131],a[248:251],v[204:207],v[128:131]\nds_read_b128 v[184:187],v244 offset:34816\nv_mfma_f32_16x16x32_bf16 v[128:131],a[240:243],v[200:203],v[128:131]\n"
"ds_read_b128 v[188:191],v244 offset:35840\nv_mfma_f32_16x16x32_bf16 v[128:131],a[232:235],v[196:199],v[128:131]\ns_waitcnt lgkmcnt(4)\nv_mfma_f32_16x16x32_bf16 v[128:131],a[224:227],v[192:195],v[128:131]\n"
"buffer_load_dwordx4 v[24:27],v247,s[44:47],s56 offen sc1\nv_mfma_f32_16x16x32_bf16 v[128:131],a[64:67],v[160:163],v[128:131]\nds_read_b128 v[192:195],v244 offset:36864\n"
"v_mfma_f32_16x16x32_bf16 v[128:131],a[72:75],v[164:167],v[128:131]\nds_read_b128 v[196:199],v244 offset:37888\nv_mfma_f32_16x16x32_bf16 v[128:131],a[80:83],v[168:171],v[128:131]\n"
"v_mfma_f32_16x16x32_bf16 v[128:131],a[88:91],v[172:175],v[128:131]\nbuffer_store_dwordx4 v[0:3],v28,s[40:43],s73 offen\nv_mfma_f32_16x16x32_bf16 v[132:135],a[92:95],v[172:175],v[132:135]\n"
"ds_read_b128 v[200:203],v244 offset:38912\nv_mfma_f32_16x16x32_bf16 v[132:135],a[84:87],v[168:171],v[132:135]\nds_read_b128 v[204:207],v244 offset:39936\nv_mfma_f32_16x16x32_bf16 v[132:135],a[76:79],v[164:167],v[132:135]\n"
"s_waitcnt lgkmcnt(4)\nv_mfma_f32_16x16x32_bf16 v[132:135],a[68:71],v[160:163],v[132:135]\nv_mfma_f32_16x16x32_bf16 v[132:135],a[100:103],v[176:179],v[132:135]\nv_mfma_f32_16x16x32_bf16 v[132:135],a[108:111],v[180:183],v[132:135]\n"
"v_mfma_f32_16x16x32_bf16 v[132:135],a[116:119],v[184:187],v[132:135]\nv_mfma_f32_16x16x32_bf16 v[132:135],a[124:127],v[188:191],v[132:135]\nv_mfma_f32_16x16x32_bf16 v[128:131],a[120:123],v[188:191],v[128:131]\n"
"v_mfma_f32_16x16x32_bf16 v[128:131],a[112:115],v[184:187],v[128:131]\nv_mfma_f32_16x16x32_bf16 v[128:131],a[104:107],v[180:183],v[128:131]\ns_waitcnt lgkmcnt(0)\nv_mfma_f32_16x16x32_bf16 v[128:131],a[96:99],v[176:179],v[128:131]\n"
"v_mfma_f32_16x16x32_bf16 v[128:131],v[32:35],v[192:195],v[128:131]\nv_mfma_f32_16x16x32_bf16 v[128:131],v[40:43],v[196:199],v[128:131]\nv_mfma_f32_16x16x32_bf16 v[128:131],v[48:51],v[200:203],v[128:131]\n"
"v_mfma_f32_16x16x32_bf16 v[128:131],v[56:59],v[204:207],v[128:131]\nv_mfma_f32_16x16x32_bf16 v[132:135],v[60:63],v[204:207],v[132:135]\nv_mfma_f32_16x16x32_bf16 v[132:135],v[52:55],v[200:203],v[132:135]\n"
"v_mfma_f32_16x16x32_bf16 v[132:135],v[44:47],v[196:199],v[132:135]\nv_mfma_f32_16x16x32_bf16 v[132:135],v[36:39],v[192:195],v[132:135]\nLFIN_%=:\ns_cmp_eq_u32 s72,0\ns_cbranch_scc1 LGD0_%=\ns_mov_b32 s70,0x40000\nLPLX0_%=:\n"
"global_load_dwordx2 v[240:241],v238,s[60:61] offset:0 sc1\ns_waitcnt vmcnt(0)\nLPCX0_%=:\nv_cmp_ne_u64 vcc,s[98:99],v[240:241]\ns_cmp_eq_u64 vcc,0\ns_cbranch_scc1 LPDX0_%=\ns_sleep 2\ns_sub_u32 s70,s70,1\ns_cmp_lg_u32 s70,0\n"
"s_cbranch_scc1 LPLX0_%=\nLPDX0_%=:\ns_add_u32 s56,s86,0\nbuffer_load_dwordx4 v[0:3],v247,s[44:47],s56 offen sc1\ns_add_u32 s56,s86,4096\nbuffer_load_dwordx4 v[4:7],v247,s[44:47],s56 offen sc1\ns_add_u32 s56,s86,8192\n"
"buffer_load_dwordx4 v[8:11],v247,s[44:47],s56 offen sc1\ns_add_u32 s56,s86,12288\nbuffer_load_dwordx4 v[12:15],v247,s[44:47],s56 offen sc1\ns_waitcnt vmcnt(0)\nv_lshlrev_b32 v64,16,v0\nv_and_b32 v65,0xffff0000,v0\n"
"v_lshlrev_b32 v66,16,v1\nv_and_b32 v67,0xffff0000,v1\nv_lshlrev_b32 v68,16,v2\nv_and_b32 v69,0xffff0000,v2\nv_lshlrev_b32 v70,16,v3\nv_and_b32 v71,0xffff0000,v3\nv_lshlrev_b32 v72,16,v4\nv_and_b32 v73,0xffff0000,v4\n"
"v_lshlrev_b32 v74,16,v5\nv_and_b32 v75,0xffff0000,v5\nv_lshlrev_b32 v76,16,v6\nv_and_b32 v77,0xffff0000,v6\nv_lshlrev_b32 v78,16,v7\nv_and_b32 v79,0xffff0000,v7\nv_lshlrev_b32 v80,16,v8\nv_and_b32 v81,0xffff0000,v8\n"
"v_lshlrev_b32 v82,16,v9\nv_and_b32 v83,0xffff0000,v9\nv_lshlrev_b32 v84,16,v10\nv_and_b32 v85,0xffff0000,v10\nv_lshlrev_b32 v86,16,v11\nv_and_b32 v87,0xffff0000,v11\nv_lshlrev_b32 v88,16,v12\nv_and_b32 v89,0xffff0000,v12\n"
"v_lshlrev_b32 v90,16,v13\nv_and_b32 v91,0xffff0000,v13\nv_lshlrev_b32 v92,16,v14\nv_and_b32 v93,0xffff0000,v14\nv_lshlrev_b32 v94,16,v15\nv_and_b32 v95,0xffff0000,v15\nv_pk_add_f32 v[88:89],v[88:89],v[64:65]\n"
"v_pk_add_f32 v[90:91],v[90:91],v[66:67]\nv_pk_add_f32 v[92:93],v[92:93],v[68:69]\nv_pk_add_f32 v[94:95],v[94:95],v[70:71]\nv_pk_add_f32 v[72:73],v[72:73],v[80:81]\nv_pk_add_f32 v[74:75],v[74:75],v[82:83]\n"
"v_pk_add_f32 v[76:77],v[76:77],v[84:85]\nv_pk_add_f32 v[78:79],v[78:79],v[86:87]\nv_pk_add_f32 v[88:89],v[88:89],v[72:73]\nv_pk_add_f32 v[90:91],v[90:91],v[74:75]\nv_pk_add_f32 v[92:93],v[92:93],v[76:77]\n"
"v_pk_add_f32 v[94:95],v[94:95],v[78:79]\nv_cvt_pk_bf16_f32 v0,v88,v89\nv_cvt_pk_bf16_f32 v1,v90,v91\nv_cvt_pk_bf16_f32 v2,v92,v93\nv_cvt_pk_bf16_f32 v3,v94,v95\nbuffer_store_dwordx4 v[0:3],v28,s[40:43],s83 offen\nLGD0_%=:\n"
"s_waitcnt vmcnt(1)\ns_mov_b32 s70,0x40000\nLGL_%=:\nv_max3_u32 v240,v16,v17,v18\nv_max3_u32 v241,v19,v20,v21\nv_max3_u32 v242,v22,v23,v24\nv_max3_u32 v243,v25,v26,v27\nv_max3_u32 v240,v240,v241,v242\nv_max_u32 v240,v240,v243\n"
"v_cmp_eq_u32 vcc,-1,v240\ns_cmp_eq_u64 vcc,0\ns_cbranch_scc1 LGD1_%=\ns_add_u32 s56,s80,1\ns_and_b32 s56,s56,3\ns_lshl_b32 s56,s56,12\ns_add_u32 s56,s56,s86\ns_add_u32 s56,s56,0x10000\n"
"buffer_load_dwordx4 v[16:19],v247,s[44:47],s56 offen sc1\ns_add_u32 s56,s80,2\ns_and_b32 s56,s56,3\ns_lshl_b32 s56,s56,12\ns_add_u32 s56,s56,s86\ns_add_u32 s56,s56,0x10000\n"
"buffer_load_dwordx4 v[20:23],v247,s[44:47],s56 offen sc1\ns_add_u32 s56,s80,3\ns_and_b32 s56,s56,3\ns_lshl_b32 s56,s56,12\ns_add_u32 s56,s56,s86\ns_add_u32 s56,s56,0x10000\n"
"buffer_load_dwordx4 v[24:27],v247,s[44:47],s56 offen sc1\ns_sub_u32 s70,s70,1\ns_cmp_lg_u32 s70,0\ns_waitcnt vmcnt(0)\ns_cbranch_scc1 LGL_%=\nLGD1_%=:\nv_lshlrev_b32 v64,16,v16\nv_and_b32 v65,0xffff0000,v16\n"
"v_lshlrev_b32 v66,16,v17\nv_and_b32 v67,0xffff0000,v17\nv_lshlrev_b32 v68,16,v18\nv_and_b32 v69,0xffff0000,v18\nv_lshlrev_b32 v70,16,v19\nv_and_b32 v71,0xffff0000,v19\nv_lshlrev_b32 v72,16,v20\nv_and_b32 v73,0xffff0000,v20\n"
"v_lshlrev_b32 v74,16,v21\nv_and_b32 v75,0xffff0000,v21\nv_lshlrev_b32 v76,16,v22\nv_and_b32 v77,0xffff0000,v22\nv_lshlrev_b32 v78,16,v23\nv_and_b32 v79,0xffff0000,v23\nv_lshlrev_b32 v80,16,v24\nv_and_b32 v81,0xffff0000,v24\n"
"v_lshlrev_b32 v82,16,v25\nv_and_b32 v83,0xffff0000,v25\nv_lshlrev_b32 v84,16,v26\nv_and_b32 v85,0xffff0000,v26\nv_lshlrev_b32 v86,16,v27\nv_and_b32 v87,0xffff0000,v27\nv_pk_add_f32 v[128:129],v[128:129],v[64:65]\n"
"v_pk_add_f32 v[130:131],v[130:131],v[66:67]\nv_pk_add_f32 v[132:133],v[132:133],v[68:69]\nv_pk_add_f32 v[134:135],v[134:135],v[70:71]\nv_pk_add_f32 v[72:73],v[72:73],v[80:81]\nv_pk_add_f32 v[74:75],v[74:75],v[82:83]\n"
"v_pk_add_f32 v[76:77],v[76:77],v[84:85]\nv_pk_add_f32 v[78:79],v[78:79],v[86:87]\nv_pk_add_f32 v[128:129],v[128:129],v[72:73]\nv_pk_add_f32 v[130:131],v[130:131],v[74:75]\nv_pk_add_f32 v[132:133],v[132:133],v[76:77]\n"
"v_pk_add_f32 v[134:135],v[134:135],v[78:79]\nv_cvt_pk_bf16_f32 v16,v128,v129\nv_cvt_pk_bf16_f32 v17,v130,v131\nv_cvt_pk_bf16_f32 v18,v132,v133\nv_cvt_pk_bf16_f32 v19,v134,v135\n"
"buffer_store_dwordx4 v[16:19],v28,s[40:43],s84 offen\ns_endpgm\nLMORE_%=:\ns_waitcnt vmcnt(21)\ns_cmp_lg_u32 s54,0\ns_cbranch_scc1 LW0af0_%=\nglobal_load_dwordx2 v[242:243],v236,s[60:61] offset:0 sc1\nLW0af0_%=:\n"
"buffer_load_dwordx4 a[124:127],v247,s[44:47],s55 offen offset:1024 sc1\ns_add_u32 m0,s50,122880\ns_add_u32 s48,s22,28672\nbuffer_load_dwordx4 v247,s[44:47],s48 offen sc1 lds\ns_add_u32 m0,s50,126976\ns_add_u32 s48,s22,176128\n"
"buffer_load_dwordx4 v247,s[44:47],s48 offen sc1 lds\nv_mfma_f32_16x16x32_bf16 v[212:215],a[228:231],v[160:163],v[212:215]\nds_read_b128 v[192:195],v246 offset:20480\ns_add_u32 m0,s50,0\n"
"v_mfma_f32_16x16x32_bf16 v[212:215],a[236:239],v[164:167],v[212:215]\nds_read_b128 v[196:199],v246 offset:21504\ns_add_u32 s48,s22,323584\nv_mfma_f32_16x16x32_bf16 v[212:215],a[244:247],v[168:171],v[212:215]\n"
"v_mfma_f32_16x16x32_bf16 v[212:215],a[252:255],v[172:175],v[212:215]\nv_mfma_f32_16x16x32_bf16 v[208:211],a[248:251],v[172:175],v[208:211]\nds_read_b128 v[200:203],v246 offset:22528\n"
"v_mfma_f32_16x16x32_bf16 v[208:211],a[240:243],v[168:171],v[208:211]\nds_read_b128 v[204:207],v246 offset:23552\nv_mfma_f32_16x16x32_bf16 v[208:211],a[232:235],v[164:167],v[208:211]\ns_waitcnt vmcnt(10) lgkmcnt(4)\n"
"v_mfma_f32_16x16x32_bf16 v[208:211],a[224:227],v[160:163],v[208:211]\nbuffer_load_dwordx4 v247,s[44:47],s48 offen sc1 lds\nv_mfma_f32_16x16x32_bf16 v[216:219],a[224:227],v[176:179],v[216:219]\ns_barrier\ns_add_u32 s55,s20,262144\n"
"v_mfma_f32_16x16x32_bf16 v[216:219],a[232:235],v[180:183],v[216:219]\nds_read_b128 v[160:163],v246 offset:24576\nv_mfma_f32_16x16x32_bf16 v[216:219],a[240:243],v[184:187],v[216:219]\nds_read_b128 v[164:167],v246 offset:25600\n"
"v_mfma_f32_16x16x32_bf16 v[216:219],a[248:251],v[188:191],v[216:219]\nbuffer_load_dwordx4 v[32:35],v247,s[44:47],s55 offen offset:0 sc1\nv_mfma_f32_16x16x32_bf16 v[220:223],a[252:255],v[188:191],v[220:223]\n"
"ds_read_b128 v[168:171],v246 offset:26624\nv_mfma_f32_16x16x32_bf16 v[220:223],a[244:247],v[184:187],v[220:223]\nds_read_b128 v[172:175],v246 offset:27648\nv_mfma_f32_16x16x32_bf16 v[220:223],a[236:239],v[180:183],v[220:223]\n"
"s_waitcnt lgkmcnt(4)\nv_mfma_f32_16x16x32_bf16 v[220:223],a[228:231],v[176:179],v[220:223]\nbuffer_load_dwordx4 v[36:39],v247,s[44:47],s55 offen offset:1024 sc1\n"
"v_mfma_f32_16x16x32_bf16 v[228:231],a[228:231],v[192:195],v[228:231]\nds_read_b128 v[176:179],v245 offset:24576\ns_add_u32 s55,s20,270336\nv_mfma_f32_16x16x32_bf16 v[228:231],a[236:239],v[196:199],v[228:231]\n"
"ds_read_b128 v[180:183],v245 offset:25600\nv_mfma_f32_16x16x32_bf16 v[228:231],a[244:247],v[200:203],v[228:231]\nv_mfma_f32_16x16x32_bf16 v[228:231],a[252:255],v[204:207],v[228:231]\n"
"buffer_load_dwordx4 v[40:43],v247,s[44:47],s55 offen offset:0 sc1\nv_mfma_f32_16x16x32_bf16 v[224:227],a[248:251],v[204:207],v[224:227]\nds_read_b128 v[184:187],v245 offset:26624\n"
"v_mfma_f32_16x16x32_bf16 v[224:227],a[240:243],v[200:203],v[224:227]\nds_read_b128 v[188:191],v245 offset:27648\nv_mfma_f32_16x16x32_bf16 v[224:227],a[232:235],v[196:199],v[224:227]\ns_waitcnt lgkmcnt(4)\n"
"v_mfma_f32_16x16x32_bf16 v[224:227],a[224:227],v[192:195],v[224:227]\nbuffer_load_dwordx4 v[44:47],v247,s[44:47],s55 offen offset:1024 sc1\ns_waitcnt vmcnt(18)\ns_add_u32 s55,s20,278528\n"
"buffer_load_dwordx4 v[48:51],v247,s[44:47],s55 offen offset:0 sc1\nbuffer_load_dwordx4 v[52:55],v247,s[44:47],s55 offen offset:1024 sc1\ns_add_u32 s55,s20,286720\nbuffer_load_dwordx4 v[56:59],v247,s[44:47],s55 offen offset:0 sc1\n"
"buffer_load_dwordx4 v[60:63],v247,s[44:47],s55 offen offset:1024 sc1\ns_add_u32 m0,s50,4096\ns_add_u32 s48,s22,32768\nbuffer_load_dwordx4 v247,s[44:47],s48 offen sc1 lds\ns_add_u32 m0,s50,8192\ns_add_u32 s48,s22,180224\n"
"buffer_load_dwordx4 v247,s[44:47],s48 offen sc1 lds\nv_mfma_f32_16x16x32_bf16 v[208:211],a[64:67],v[160:163],v[208:211]\nds_read_b128 v[192:195],v245 offset:28672\ns_add_u32 m0,s50,12288\n"
"v_mfma_f32_16x16x32_bf16 v[208:211],a[72:75],v[164:167],v[208:211]\nds_read_b128 v[196:199],v245 offset:29696\ns_add_u32 s48,s22,327680\nv_mfma_f32_16x16x32_bf16 v[208:211],a[80:83],v[168:171],v[208:211]\n"
"v_mfma_f32_16x16x32_bf16 v[208:211],a[88:91],v[172:175],v[208:211]\nv_mfma_f32_16x16x32_bf16 v[212:215],a[92:95],v[172:175],v[212:215]\nds_read_b128 v[200:203],v245 offset:30720\n"
"v_mfma_f32_16x16x32_bf16 v[212:215],a[84:87],v[168:171],v[212:215]\nds_read_b128 v[204:207],v245 offset:31744\nv_mfma_f32_16x16x32_bf16 v[212:215],a[76:79],v[164:167],v[212:215]\ns_waitcnt vmcnt(10) lgkmcnt(4)\n"
"v_mfma_f32_16x16x32_bf16 v[212:215],a[68:71],v[160:163],v[212:215]\nbuffer_load_dwordx4 v247,s[44:47],s48 offen sc1 lds\ns_cmp_lg_u32 s54,0\ns_cbranch_scc1 LW0bf0_%=\ns_mov_b32 s70,0x40000\ns_waitcnt vmcnt(15)\n"
"s_branch LPCLf0_%=\nLPLLf0_%=:\nglobal_load_dwordx2 v[242:243],v236,s[60:61] offset:0 sc1\ns_waitcnt vmcnt(0)\nLPCLf0_%=:\nv_cmp_ne_u64 vcc,s[98:99],v[242:243]\ns_cmp_eq_u64 vcc,0\ns_cbranch_scc1 LPDLf0_%=\ns_sleep 2\n"
"s_sub_u32 s70,s70,1\ns_cmp_lg_u32 s70,0\ns_cbranch_scc1 LPLLf0_%=\nLPDLf0_%=:\nLW0bf0_%=:\nv_mfma_f32_16x16x32_bf16 v[220:223],a[68:71],v[176:179],v[220:223]\ns_barrier\ns_add_u32 m0,s50,16384\ns_add_u32 s48,s81,0\n"
"v_mfma_f32_16x16x32_bf16 v[220:223],a[76:79],v[180:183],v[220:223]\nds_read_b128 v[160:163],v245 offset:57344\nv_mfma_f32_16x16x32_bf16 v[220:223],a[84:87],v[184:187],v[220:223]\nds_read_b128 v[164:167],v245 offset:58368\n"
"v_mfma_f32_16x16x32_bf16 v[220:223],a[92:95],v[188:191],v[220:223]\nbuffer_load_dwordx4 v247,s[44:47],s48 offen sc1 lds\nv_mfma_f32_16x16x32_bf16 v[216:219],a[88:91],v[188:191],v[216:219]\n"
"ds_read_b128 v[168:171],v245 offset:59392\ns_add_u32 m0,s50,20480\nv_mfma_f32_16x16x32_bf16 v[216:219],a[80:83],v[184:187],v[216:219]\nds_read_b128 v[172:175],v245 offset:60416\ns_add_u32 s48,s81,4096\n"
"v_mfma_f32_16x16x32_bf16 v[216:219],a[72:75],v[180:183],v[216:219]\ns_waitcnt lgkmcnt(4)\nv_mfma_f32_16x16x32_bf16 v[216:219],a[64:67],v[176:179],v[216:219]\nbuffer_load_dwordx4 v247,s[44:47],s48 offen sc1 lds\n"
"v_mfma_f32_16x16x32_bf16 v[224:227],a[64:67],v[192:195],v[224:227]\nds_read_b128 v[176:179],v245 offset:61440\ns_add_u32 m0,s50,24576\nv_mfma_f32_16x16x32_bf16 v[224:227],a[72:75],v[196:199],v[224:227]\n"
"ds_read_b128 v[180:183],v245 offset:62464\ns_add_u32 s48,s81,8192\nv_mfma_f32_16x16x32_bf16 v[224:227],a[80:83],v[200:203],v[224:227]\nv_mfma_f32_16x16x32_bf16 v[224:227],a[88:91],v[204:207],v[224:227]\n"
"buffer_load_dwordx4 v247,s[44:47],s48 offen sc1 lds\nv_mfma_f32_16x16x32_bf16 v[228:231],a[92:95],v[204:207],v[228:231]\nds_read_b128 v[184:187],v245 offset:63488\ns_add_u32 m0,s50,28672\n"
"v_mfma_f32_16x16x32_bf16 v[228:231],a[84:87],v[200:203],v[228:231]\nds_read_b128 v[188:191],v245 offset:64512\ns_add_u32 s48,s81,12288\nv_mfma_f32_16x16x32_bf16 v[228:231],a[76:79],v[196:199],v[228:231]\ns_waitcnt lgkmcnt(4)\n"
"v_mfma_f32_16x16x32_bf16 v[228:231],a[68:71],v[192:195],v[228:231]\nbuffer_load_dwordx4 v247,s[44:47],s48 offen sc1 lds\ns_waitcnt vmcnt(18)\nv_mfma_f32_16x16x32_bf16 v[212:215],a[100:103],v[160:163],v[212:215]\n"
"ds_read_b128 v[192:195],v244 offset:0\ns_add_u32 m0,s50,32768\nv_mfma_f32_16x16x32_bf16 v[212:215],a[108:111],v[164:167],v[212:215]\nds_read_b128 v[196:199],v244 offset:1024\ns_add_u32 s48,s81,16384\n"
"v_mfma_f32_16x16x32_bf16 v[212:215],a[116:119],v[168:171],v[212:215]\nv_mfma_f32_16x16x32_bf16 v[212:215],a[124:127],v[172:175],v[212:215]\nbuffer_load_dwordx4 v247,s[44:47],s48 offen sc1 lds\n"
"v_mfma_f32_16x16x32_bf16 v[208:211],a[120:123],v[172:175],v[208:211]\nds_read_b128 v[200:203],v244 offset:2048\ns_add_u32 m0,s50,36864\nv_mfma_f32_16x16x32_bf16 v[208:211],a[112:115],v[168:171],v[208:211]\n"
"ds_read_b128 v[204:207],v244 offset:3072\ns_add_u32 s48,s81,20480\nv_mfma_f32_16x16x32_bf16 v[208:211],a[104:107],v[164:167],v[208:211]\ns_waitcnt vmcnt(5) lgkmcnt(4)\n"
"v_mfma_f32_16x16x32_bf16 v[208:211],a[96:99],v[160:163],v[208:211]\nbuffer_load_dwordx4 v247,s[44:47],s48 offen sc1 lds\nv_mfma_f32_16x16x32_bf16 v[216:219],a[96:99],v[176:179],v[216:219]\ns_barrier\ns_add_u32 m0,s50,40960\n"
"s_add_u32 s48,s81,24576\nv_mfma_f32_16x16x32_bf16 v[216:219],a[104:107],v[180:183],v[216:219]\nds_read_b128 v[160:163],v244 offset:4096\nv_mfma_f32_16x16x32_bf16 v[216:219],a[112:115],v[184:187],v[216:219]\n"
"ds_read_b128 v[164:167],v244 offset:5120\nv_mfma_f32_16x16x32_bf16 v[216:219],a[120:123],v[188:191],v[216:219]\nbuffer_load_dwordx4 v247,s[44:47],s48 offen sc1 lds\n"
"v_mfma_f32_16x16x32_bf16 v[220:223],a[124:127],v[188:191],v[220:223]\nds_read_b128 v[168:171],v244 offset:6144\ns_add_u32 m0,s50,45056\nv_mfma_f32_16x16x32_bf16 v[220:223],a[116:119],v[184:187],v[220:223]\n"
"ds_read_b128 v[172:175],v244 offset:7168\ns_add_u32 s48,s81,28672\nv_mfma_f32_16x16x32_bf16 v[220:223],a[108:111],v[180:183],v[220:223]\ns_waitcnt lgkmcnt(4)\nv_mfma_f32_16x16x32_bf16 v[220:223],a[100:103],v[176:179],v[220:223]\n"
"buffer_load_dwordx4 v247,s[44:47],s48 offen sc1 lds\nv_mfma_f32_16x16x32_bf16 v[228:231],a[100:103],v[192:195],v[228:231]\nds_read_b128 v[176:179],v244 offset:8192\ns_add_u32 m0,s50,49152\n"
"v_mfma_f32_16x16x32_bf16 v[228:231],a[108:111],v[196:199],v[228:231]\nds_read_b128 v[180:183],v244 offset:9216\ns_add_u32 s48,s81,32768\nv_mfma_f32_16x16x32_bf16 v[228:231],a[116:119],v[200:203],v[228:231]\n"
"v_mfma_f32_16x16x32_bf16 v[228:231],a[124:127],v[204:207],v[228:231]\nbuffer_load_dwordx4 v247,s[44:47],s48 offen sc1 lds\nv_mfma_f32_16x16x32_bf16 v[224:227],a[120:123],v[204:207],v[224:227]\n"
"ds_read_b128 v[184:187],v244 offset:10240\ns_add_u32 m0,s50,53248\nv_mfma_f32_16x16x32_bf16 v[224:227],a[112:115],v[200:203],v[224:227]\nds_read_b128 v[188:191],v244 offset:11264\ns_add_u32 s48,s91,0\n"
"v_mfma_f32_16x16x32_bf16 v[224:227],a[104:107],v[196:199],v[224:227]\ns_waitcnt lgkmcnt(4)\nv_mfma_f32_16x16x32_bf16 v[224:227],a[96:99],v[192:195],v[224:227]\nbuffer_load_dwordx4 v247,s[44:47],s48 offen sc1 lds\n"
"s_waitcnt vmcnt(13)\nv_mfma_f32_16x16x32_bf16 v[208:211],v[32:35],v[160:163],v[208:211]\nds_read_b128 v[192:195],v244 offset:12288\ns_add_u32 m0,s50,57344\nv_mfma_f32_16x16x32_bf16 v[208:211],v[40:43],v[164:167],v[208:211]\n"
"ds_read_b128 v[196:199],v244 offset:13312\ns_add_u32 s48,s91,4096\nv_mfma_f32_16x16x32_bf16 v[208:211],v[48:51],v[168:171],v[208:211]\nv_mfma_f32_16x16x32_bf16 v[208:211],v[56:59],v[172:175],v[208:211]\n"
"buffer_load_dwordx4 v247,s[44:47],s48 offen sc1 lds\nv_mfma_f32_16x16x32_bf16 v[212:215],v[60:63],v[172:175],v[212:215]\nds_read_b128 v[200:203],v244 offset:14336\ns_add_u32 m0,s50,61440\n"
"v_mfma_f32_16x16x32_bf16 v[212:215],v[52:55],v[168:171],v[212:215]\nds_read_b128 v[204:207],v244 offset:15360\ns_add_u32 s48,s91,8192\nv_mfma_f32_16x16x32_bf16 v[212:215],v[44:47],v[164:167],v[212:215]\n"
"s_waitcnt vmcnt(5) lgkmcnt(4)\nv_mfma_f32_16x16x32_bf16 v[212:215],v[36:39],v[160:163],v[212:215]\nbuffer_load_dwordx4 v247,s[44:47],s48 offen sc1 lds\nv_mfma_f32_16x16x32_bf16 v[220:223],v[36:39],v[176:179],v[220:223]\n"
"s_barrier\ns_add_u32 s57,s85,0\ns_add_u32 m0,s50,65536\nv_mfma_f32_16x16x32_bf16 v[220:223],v[44:47],v[180:183],v[220:223]\nds_read_b128 v[160:163],v244 offset:16384\ns_add_u32 s48,s91,12288\n"
"v_mfma_f32_16x16x32_bf16 v[220:223],v[52:55],v[184:187],v[220:223]\nds_read_b128 v[164:167],v244 offset:17408\nv_mfma_f32_16x16x32_bf16 v[220:223],v[60:63],v[188:191],v[220:223]\n"
"buffer_load_dwordx4 v247,s[44:47],s48 offen sc1 lds\nv_mfma_f32_16x16x32_bf16 v[216:219],v[56:59],v[188:191],v[216:219]\nds_read_b128 v[168:171],v244 offset:18432\ns_add_u32 m0,s50,69632\n"
"v_mfma_f32_16x16x32_bf16 v[216:219],v[48:51],v[184:187],v[216:219]\nds_read_b128 v[172:175],v244 offset:19456\ns_add_u32 s48,s91,16384\nv_mfma_f32_16x16x32_bf16 v[216:219],v[40:43],v[180:183],v[216:219]\ns_waitcnt lgkmcnt(4)\n"
"v_cvt_pk_bf16_f32 v208,v208,v209\nv_cvt_pk_bf16_f32 v209,v210,v211\nv_mfma_f32_16x16x32_bf16 v[216:219],v[32:35],v[176:179],v[216:219]\nbuffer_load_dwordx4 v247,s[44:47],s48 offen sc1 lds\n"
"v_mfma_f32_16x16x32_bf16 v[224:227],v[32:35],v[192:195],v[224:227]\nds_read_b128 v[176:179],v244 offset:20480\ns_add_u32 m0,s50,73728\nv_mfma_f32_16x16x32_bf16 v[224:227],v[40:43],v[196:199],v[224:227]\n"
"ds_read_b128 v[180:183],v244 offset:21504\ns_add_u32 s48,s91,20480\nv_mfma_f32_16x16x32_bf16 v[224:227],v[48:51],v[200:203],v[224:227]\nv_cvt_pk_bf16_f32 v210,v212,v213\nv_cvt_pk_bf16_f32 v211,v214,v215\n"
"v_mfma_f32_16x16x32_bf16 v[224:227],v[56:59],v[204:207],v[224:227]\nbuffer_store_dwordx4 v[208:211],v247,s[44:47],s57 offen sc1\nv_mfma_f32_16x16x32_bf16 v[228:231],v[60:63],v[204:207],v[228:231]\n"
"ds_read_b128 v[184:187],v244 offset:22528\ns_add_u32 s57,s85,16384\nv_mfma_f32_16x16x32_bf16 v[228:231],v[52:55],v[200:203],v[228:231]\nds_read_b128 v[188:191],v244 offset:23552\nv_cvt_pk_bf16_f32 v216,v216,v217\n"
"v_mfma_f32_16x16x32_bf16 v[228:231],v[44:47],v[196:199],v[228:231]\ns_waitcnt lgkmcnt(4)\nv_cvt_pk_bf16_f32 v217,v218,v219\nv_cvt_pk_bf16_f32 v218,v220,v221\nv_mfma_f32_16x16x32_bf16 v[228:231],v[36:39],v[192:195],v[228:231]\n"
"buffer_load_dwordx4 v247,s[44:47],s48 offen sc1 lds\nv_mfma_f32_16x16x32_bf16 v[96:99],a[0:3],v[160:163],0\nds_read_b128 v[192:195],v244 offset:24576\ns_add_u32 m0,s50,77824\n"
"v_mfma_f32_16x16x32_bf16 v[96:99],a[8:11],v[164:167],v[96:99]\nds_read_b128 v[196:199],v244 offset:25600\ns_add_u32 s48,s91,24576\nv_mfma_f32_16x16x32_bf16 v[96:99],a[16:19],v[168:171],v[96:99]\nv_cvt_pk_bf16_f32 v219,v222,v223\n"
"v_mfma_f32_16x16x32_bf16 v[96:99],a[24:27],v[172:175],v[96:99]\nbuffer_store_dwordx4 v[216:219],v247,s[44:47],s57 offen sc1\nv_mfma_f32_16x16x32_bf16 v[100:103],a[28:31],v[172:175],0\nds_read_b128 v[200:203],v244 offset:26624\n"
"s_add_u32 s57,s85,32768\nv_mfma_f32_16x16x32_bf16 v[100:103],a[20:23],v[168:171],v[100:103]\nds_read_b128 v[204:207],v244 offset:27648\nv_cvt_pk_bf16_f32 v224,v224,v225\n"
"v_mfma_f32_16x16x32_bf16 v[100:103],a[12:15],v[164:167],v[100:103]\ns_waitcnt lgkmcnt(4)\nv_cvt_pk_bf16_f32 v225,v226,v227\nv_cvt_pk_bf16_f32 v226,v228,v229\nv_mfma_f32_16x16x32_bf16 v[100:103],a[4:7],v[160:163],v[100:103]\n"
"buffer_load_dwordx4 v247,s[44:47],s48 offen sc1 lds\nv_mfma_f32_16x16x32_bf16 v[100:103],a[36:39],v[176:179],v[100:103]\nds_read_b128 v[160:163],v244 offset:28672\ns_add_u32 m0,s50,81920\n"
"v_mfma_f32_16x16x32_bf16 v[100:103],a[44:47],v[180:183],v[100:103]\nds_read_b128 v[164:167],v244 offset:29696\ns_add_u32 s48,s91,28672\nv_mfma_f32_16x16x32_bf16 v[100:103],a[52:55],v[184:187],v[100:103]\n"
"v_cvt_pk_bf16_f32 v227,v230,v231\nv_mfma_f32_16x16x32_bf16 v[100:103],a[60:63],v[188:191],v[100:103]\nbuffer_store_dwordx4 v[224:227],v247,s[44:47],s57 offen sc1\nv_mfma_f32_16x16x32_bf16 v[96:99],a[56:59],v[188:191],v[96:99]\n"
"ds_read_b128 v[168:171],v244 offset:30720\nv_mfma_f32_16x16x32_bf16 v[96:99],a[48:51],v[184:187],v[96:99]\nds_read_b128 v[172:175],v244 offset:31744\nv_mfma_f32_16x16x32_bf16 v[96:99],a[40:43],v[180:183],v[96:99]\n"
"s_waitcnt lgkmcnt(4)\nv_mfma_f32_16x16x32_bf16 v[96:99],a[32:35],v[176:179],v[96:99]\nbuffer_load_dwordx4 v247,s[44:47],s48 offen sc1 lds\nv_mfma_f32_16x16x32_bf16 v[96:99],a[128:131],v[192:195],v[96:99]\n"
"ds_read_b128 v[176:179],v244 offset:32768\ns_add_u32 m0,s50,86016\nv_mfma_f32_16x16x32_bf16 v[96:99],a[136:139],v[196:199],v[96:99]\nds_read_b128 v[180:183],v244 offset:33792\ns_add_u32 s48,s91,32768\n"
"v_mfma_f32_16x16x32_bf16 v[96:99],a[144:147],v[200:203],v[96:99]\nv_mfma_f32_16x16x32_bf16 v[96:99],a[152:155],v[204:207],v[96:99]\nbuffer_load_dwordx4 v247,s[44:47],s48 offen sc1 lds\n"
"v_mfma_f32_16x16x32_bf16 v[100:103],a[156:159],v[204:207],v[100:103]\nds_read_b128 v[184:187],v244 offset:34816\ns_add_u32 m0,s50,98304\nv_mfma_f32_16x16x32_bf16 v[100:103],a[148:151],v[200:203],v[100:103]\n"
"ds_read_b128 v[188:191],v244 offset:35840\ns_add_u32 s48,s76,0\nv_mfma_f32_16x16x32_bf16 v[100:103],a[140:143],v[196:199],v[100:103]\ns_waitcnt lgkmcnt(4)\nv_mfma_f32_16x16x32_bf16 v[100:103],a[132:135],v[192:195],v[100:103]\n"
"v_mfma_f32_16x16x32_bf16 v[100:103],a[164:167],v[160:163],v[100:103]\nds_read_b128 v[192:195],v244 offset:36864\nv_mfma_f32_16x16x32_bf16 v[100:103],a[172:175],v[164:167],v[100:103]\nds_read_b128 v[196:199],v244 offset:37888\n"
"v_mfma_f32_16x16x32_bf16 v[100:103],a[180:183],v[168:171],v[100:103]\nv_mfma_f32_16x16x32_bf16 v[100:103],a[188:191],v[172:175],v[100:103]\nv_mfma_f32_16x16x32_bf16 v[96:99],a[184:187],v[172:175],v[96:99]\n"
"ds_read_b128 v[200:203],v244 offset:38912\nv_mfma_f32_16x16x32_bf16 v[96:99],a[176:179],v[168:171],v[96:99]\nds_read_b128 v[204:207],v244 offset:39936\nv_mfma_f32_16x16x32_bf16 v[96:99],a[168:171],v[164:167],v[96:99]\n"
"s_waitcnt vmcnt(9) lgkmcnt(4)\nv_mfma_f32_16x16x32_bf16 v[96:99],a[160:163],v[160:163],v[96:99]\nv_mfma_f32_16x16x32_bf16 v[96:99],a[192:195],v[176:179],v[96:99]\ns_barrier\n"
"v_mfma_f32_16x16x32_bf16 v[96:99],a[200:203],v[180:183],v[96:99]\nds_read_b128 v[160:163],v244 offset:40960\nv_mfma_f32_16x16x32_bf16 v[96:99],a[208:211],v[184:187],v[96:99]\nds_read_b128 v[164:167],v244 offset:41984\n"
"v_mfma_f32_16x16x32_bf16 v[96:99],a[216:219],v[188:191],v[96:99]\nv_mfma_f32_16x16x32_bf16 v[100:103],a[220:223],v[188:191],v[100:103]\nds_read_b128 v[168:171],v244 offset:43008\n"
"v_mfma_f32_16x16x32_bf16 v[100:103],a[212:215],v[184:187],v[100:103]\nds_read_b128 v[172:175],v244 offset:44032\nv_mfma_f32_16x16x32_bf16 v[100:103],a[204:207],v[180:183],v[100:103]\ns_waitcnt lgkmcnt(4)\n"
"v_mfma_f32_16x16x32_bf16 v[100:103],a[196:199],v[176:179],v[100:103]\nv_mfma_f32_16x16x32_bf16 v[100:103],a[228:231],v[192:195],v[100:103]\nds_read_b128 v[176:179],v244 offset:45056\n"
"v_mfma_f32_16x16x32_bf16 v[100:103],a[236:239],v[196:199],v[100:103]\nds_read_b128 v[180:183],v244 offset:46080\nv_mfma_f32_16x16x32_bf16 v[100:103],a[244:247],v[200:203],v[100:103]\n"
"v_mfma_f32_16x16x32_bf16 v[100:103],a[252:255],v[204:207],v[100:103]\nv_mfma_f32_16x16x32_bf16 v[96:99],a[248:251],v[204:207],v[96:99]\nds_read_b128 v[184:187],v244 offset:47104\n"
"v_mfma_f32_16x16x32_bf16 v[96:99],a[240:243],v[200:203],v[96:99]\nds_read_b128 v[188:191],v244 offset:48128\nv_mfma_f32_16x16x32_bf16 v[96:99],a[232:235],v[196:199],v[96:99]\ns_waitcnt lgkmcnt(4)\n"
"v_mfma_f32_16x16x32_bf16 v[96:99],a[224:227],v[192:195],v[96:99]\ns_cmp_lg_u32 s54,0\ns_cbranch_scc1 LW0af1_%=\nglobal_load_dwordx2 v[240:241],v237,s[60:61] offset:0 sc1\nLW0af1_%=:\n"
"v_mfma_f32_16x16x32_bf16 v[96:99],a[64:67],v[160:163],v[96:99]\nds_read_b128 v[192:195],v244 offset:49152\nv_mfma_f32_16x16x32_bf16 v[96:99],a[72:75],v[164:167],v[96:99]\nds_read_b128 v[196:199],v244 offset:50176\n"
"v_mfma_f32_16x16x32_bf16 v[96:99],a[80:83],v[168:171],v[96:99]\nv_mfma_f32_16x16x32_bf16 v[96:99],a[88:91],v[172:175],v[96:99]\nv_mfma_f32_16x16x32_bf16 v[100:103],a[92:95],v[172:175],v[100:103]\n"
"ds_read_b128 v[200:203],v244 offset:51200\nv_mfma_f32_16x16x32_bf16 v[100:103],a[84:87],v[168:171],v[100:103]\nds_read_b128 v[204:207],v244 offset:52224\nv_mfma_f32_16x16x32_bf16 v[100:103],a[76:79],v[164:167],v[100:103]\n"
"s_waitcnt lgkmcnt(4)\nv_mfma_f32_16x16x32_bf16 v[100:103],a[68:71],v[160:163],v[100:103]\nv_mfma_f32_16x16x32_bf16 v[100:103],a[100:103],v[176:179],v[100:103]\nds_read_b128 v[160:163],v244 offset:53248\n"
"v_mfma_f32_16x16x32_bf16 v[100:103],a[108:111],v[180:183],v[100:103]\nds_read_b128 v[164:167],v244 offset:54272\nv_mfma_f32_16x16x32_bf16 v[100:103],a[116:119],v[184:187],v[100:103]\n"
"v_mfma_f32_16x16x32_bf16 v[100:103],a[124:127],v[188:191],v[100:103]\nv_mfma_f32_16x16x32_bf16 v[96:99],a[120:123],v[188:191],v[96:99]\nds_read_b128 v[168:171],v244 offset:55296\n"
"v_mfma_f32_16x16x32_bf16 v[96:99],a[112:115],v[184:187],v[96:99]\nds_read_b128 v[172:175],v244 offset:56320\nv_mfma_f32_16x16x32_bf16 v[96:99],a[104:107],v[180:183],v[96:99]\ns_waitcnt lgkmcnt(4)\n"
"v_mfma_f32_16x16x32_bf16 v[96:99],a[96:99],v[176:179],v[96:99]\nv_mfma_f32_16x16x32_bf16 v[96:99],v[32:35],v[192:195],v[96:99]\nds_read_b128 v[176:179],v244 offset:57344\n"
"v_mfma_f32_16x16x32_bf16 v[96:99],v[40:43],v[196:199],v[96:99]\nds_read_b128 v[180:183],v244 offset:58368\nv_mfma_f32_16x16x32_bf16 v[96:99],v[48:51],v[200:203],v[96:99]\n"
"v_mfma_f32_16x16x32_bf16 v[96:99],v[56:59],v[204:207],v[96:99]\nv_mfma_f32_16x16x32_bf16 v[100:103],v[60:63],v[204:207],v[100:103]\nds_read_b128 v[184:187],v244 offset:59392\n"
"v_mfma_f32_16x16x32_bf16 v[100:103],v[52:55],v[200:203],v[100:103]\nds_read_b128 v[188:191],v244 offset:60416\nv_mfma_f32_16x16x32_bf16 v[100:103],v[44:47],v[196:199],v[100:103]\ns_waitcnt lgkmcnt(4)\n"
"v_mfma_f32_16x16x32_bf16 v[100:103],v[36:39],v[192:195],v[100:103]\nv_mfma_f32_16x16x32_bf16 v[136:139],a[0:3],v[160:163],0\nds_read_b128 v[192:195],v244 offset:61440\ns_add_u32 s57,s85,49152\n"
"v_mfma_f32_16x16x32_bf16 v[136:139],a[8:11],v[164:167],v[136:139]\nds_read_b128 v[196:199],v244 offset:62464\nv_mfma_f32_16x16x32_bf16 v[136:139],a[16:19],v[168:171],v[136:139]\n"
"v_mfma_f32_16x16x32_bf16 v[136:139],a[24:27],v[172:175],v[136:139]\nv_mfma_f32_16x16x32_bf16 v[140:143],a[28:31],v[172:175],0\nds_read_b128 v[200:203],v244 offset:63488\n"
"v_mfma_f32_16x16x32_bf16 v[140:143],a[20:23],v[168:171],v[140:143]\nds_read_b128 v[204:207],v244 offset:64512\nv_cvt_pk_bf16_f32 v96,v96,v97\nv_mfma_f32_16x16x32_bf16 v[140:143],a[12:15],v[164:167],v[140:143]\n"
"s_waitcnt vmcnt(0) lgkmcnt(4)\nv_cvt_pk_bf16_f32 v97,v98,v99\nv_cvt_pk_bf16_f32 v98,v100,v101\nv_mfma_f32_16x16x32_bf16 v[140:143],a[4:7],v[160:163],v[140:143]\nv_cvt_pk_bf16_f32 v99,v102,v103\ns_cmp_lg_u32 s54,0\n"
"s_cbranch_scc1 LW0bf1_%=\ns_mov_b32 s70,0x40000\ns_waitcnt vmcnt(0)\ns_branch LPCLf1_%=\nLPLLf1_%=:\nglobal_load_dwordx2 v[240:241],v237,s[60:61] offset:0 sc1\ns_waitcnt vmcnt(0)\nLPCLf1_%=:\nv_cmp_ne_u64 vcc,s[98:99],v[240:241]\n"
"s_cmp_eq_u64 vcc,0\ns_cbranch_scc1 LPDLf1_%=\ns_sleep 2\ns_sub_u32 s70,s70,1\ns_cmp_lg_u32 s70,0\ns_cbranch_scc1 LPLLf1_%=\nLPDLf1_%=:\nLW0bf1_%=:\nv_mfma_f32_16x16x32_bf16 v[140:143],a[36:39],v[176:179],v[140:143]\ns_barrier\n"
"v_mfma_f32_16x16x32_bf16 v[140:143],a[44:47],v[180:183],v[140:143]\nds_read_b128 v[160:163],v245 offset:0\nv_mfma_f32_16x16x32_bf16 v[140:143],a[52:55],v[184:187],v[140:143]\nds_read_b128 v[164:167],v245 offset:1024\n"
"v_mfma_f32_16x16x32_bf16 v[140:143],a[60:63],v[188:191],v[140:143]\nbuffer_store_dwordx4 v[96:99],v247,s[44:47],s57 offen sc1\nv_mfma_f32_16x16x32_bf16 v[136:139],a[56:59],v[188:191],v[136:139]\n"
"ds_read_b128 v[168:171],v245 offset:2048\nv_mfma_f32_16x16x32_bf16 v[136:139],a[48:51],v[184:187],v[136:139]\nds_read_b128 v[172:175],v245 offset:3072\nv_mfma_f32_16x16x32_bf16 v[136:139],a[40:43],v[180:183],v[136:139]\n"
"s_waitcnt lgkmcnt(4)\nv_mfma_f32_16x16x32_bf16 v[136:139],a[32:35],v[176:179],v[136:139]\nbuffer_load_dwordx4 v247,s[44:47],s48 offen sc1 lds\nv_mfma_f32_16x16x32_bf16 v[136:139],a[128:131],v[192:195],v[136:139]\n"
"ds_read_b128 v[176:179],v245 offset:4096\ns_add_u32 m0,s50,102400\nv_mfma_f32_16x16x32_bf16 v[136:139],a[136:139],v[196:199],v[136:139]\nds_read_b128 v[180:183],v245 offset:5120\ns_add_u32 s48,s76,4096\n"
"v_mfma_f32_16x16x32_bf16 v[136:139],a[144:147],v[200:203],v[136:139]\nv_mfma_f32_16x16x32_bf16 v[136:139],a[152:155],v[204:207],v[136:139]\nbuffer_load_dwordx4 v247,s[44:47],s48 offen sc1 lds\n"
"v_mfma_f32_16x16x32_bf16 v[140:143],a[156:159],v[204:207],v[140:143]\nds_read_b128 v[184:187],v245 offset:6144\ns_add_u32 m0,s50,106496\nv_mfma_f32_16x16x32_bf16 v[140:143],a[148:151],v[200:203],v[140:143]\n"
"ds_read_b128 v[188:191],v245 offset:7168\ns_add_u32 s48,s76,8192\nv_mfma_f32_16x16x32_bf16 v[140:143],a[140:143],v[196:199],v[140:143]\ns_waitcnt lgkmcnt(4)\nv_mfma_f32_16x16x32_bf16 v[140:143],a[132:135],v[192:195],v[140:143]\n"
"buffer_load_dwordx4 v247,s[44:47],s48 offen sc1 lds\nv_mfma_f32_16x16x32_bf16 v[140:143],a[164:167],v[160:163],v[140:143]\nds_read_b128 v[192:195],v245 offset:8192\ns_add_u32 m0,s50,110592\n"
"v_mfma_f32_16x16x32_bf16 v[140:143],a[172:175],v[164:167],v[140:143]\nds_read_b128 v[196:199],v245 offset:9216\ns_add_u32 s48,s76,12288\nv_mfma_f32_16x16x32_bf16 v[140:143],a[180:183],v[168:171],v[140:143]\n"
"v_mfma_f32_16x16x32_bf16 v[140:143],a[188:191],v[172:175],v[140:143]\nbuffer_load_dwordx4 v247,s[44:47],s48 offen sc1 lds\nv_mfma_f32_16x16x32_bf16 v[136:139],a[184:187],v[172:175],v[136:139]\n"
"ds_read_b128 v[200:203],v245 offset:10240\ns_add_u32 m0,s50,114688\nv_mfma_f32_16x16x32_bf16 v[136:139],a[176:179],v[168:171],v[136:139]\nds_read_b128 v[204:207],v245 offset:11264\ns_add_u32 s48,s76,16384\n"
"v_mfma_f32_16x16x32_bf16 v[136:139],a[168:171],v[164:167],v[136:139]\ns_waitcnt lgkmcnt(4)\nv_mfma_f32_16x16x32_bf16 v[136:139],a[160:163],v[160:163],v[136:139]\nbuffer_load_dwordx4 v247,s[44:47],s48 offen sc1 lds\n"
"v_mfma_f32_16x16x32_bf16 v[136:139],a[192:195],v[176:179],v[136:139]\nds_read_b128 v[160:163],v245 offset:12288\ns_add_u32 m0,s50,118784\nv_mfma_f32_16x16x32_bf16 v[136:139],a[200:203],v[180:183],v[136:139]\n"
"ds_read_b128 v[164:167],v245 offset:13312\ns_add_u32 s48,s76,20480\nv_mfma_f32_16x16x32_bf16 v[136:139],a[208:211],v[184:187],v[136:139]\nv_mfma_f32_16x16x32_bf16 v[136:139],a[216:219],v[188:191],v[136:139]\n"
"buffer_load_dwordx4 v247,s[44:47],s48 offen sc1 lds\nv_mfma_f32_16x16x32_bf16 v[140:143],a[220:223],v[188:191],v[140:143]\nds_read_b128 v[168:171],v245 offset:14336\ns_add_u32 m0,s50,131072\n"
"v_mfma_f32_16x16x32_bf16 v[140:143],a[212:215],v[184:187],v[140:143]\nds_read_b128 v[172:175],v245 offset:15360\ns_add_u32 s48,s76,24576\nv_mfma_f32_16x16x32_bf16 v[140:143],a[204:207],v[180:183],v[140:143]\ns_waitcnt lgkmcnt(4)\n"
"v_mfma_f32_16x16x32_bf16 v[140:143],a[196:199],v[176:179],v[140:143]\nbuffer_load_dwordx4 v247,s[44:47],s48 offen sc1 lds\nv_mfma_f32_16x16x32_bf16 v[140:143],a[228:231],v[192:195],v[140:143]\n"
"ds_read_b128 v[176:179],v245 offset:16384\ns_add_u32 m0,s50,135168\nv_mfma_f32_16x16x32_bf16 v[140:143],a[236:239],v[196:199],v[140:143]\nds_read_b128 v[180:183],v245 offset:17408\ns_add_u32 s48,s76,28672\n"
"v_mfma_f32_16x16x32_bf16 v[140:143],a[244:247],v[200:203],v[140:143]\nv_mfma_f32_16x16x32_bf16 v[140:143],a[252:255],v[204:207],v[140:143]\nbuffer_load_dwordx4 v247,s[44:47],s48 offen sc1 lds\n"
"v_mfma_f32_16x16x32_bf16 v[136:139],a[248:251],v[204:207],v[136:139]\nds_read_b128 v[184:187],v245 offset:18432\ns_add_u32 m0,s50,139264\nv_mfma_f32_16x16x32_bf16 v[136:139],a[240:243],v[200:203],v[136:139]\n"
"ds_read_b128 v[188:191],v245 offset:19456\ns_add_u32 s48,s76,32768\nv_mfma_f32_16x16x32_bf16 v[136:139],a[232:235],v[196:199],v[136:139]\ns_waitcnt lgkmcnt(4)\nv_mfma_f32_16x16x32_bf16 v[136:139],a[224:227],v[192:195],v[136:139]\n"
"buffer_load_dwordx4 v247,s[44:47],s48 offen sc1 lds\nv_mfma_f32_16x16x32_bf16 v[136:139],a[64:67],v[160:163],v[136:139]\nds_read_b128 v[192:195],v245 offset:20480\ns_add_u32 m0,s50,143360\n"
"v_mfma_f32_16x16x32_bf16 v[136:139],a[72:75],v[164:167],v[136:139]\nds_read_b128 v[196:199],v245 offset:21504\ns_add_u32 s48,s77,0\nv_mfma_f32_16x16x32_bf16 v[136:139],a[80:83],v[168:171],v[136:139]\n"
"v_mfma_f32_16x16x32_bf16 v[136:139],a[88:91],v[172:175],v[136:139]\nbuffer_load_dwordx4 v247,s[44:47],s48 offen sc1 lds\nv_mfma_f32_16x16x32_bf16 v[140:143],a[92:95],v[172:175],v[140:143]\n"
"ds_read_b128 v[200:203],v245 offset:22528\ns_add_u32 m0,s50,147456\nv_mfma_f32_16x16x32_bf16 v[140:143],a[84:87],v[168:171],v[140:143]\nds_read_b128 v[204:207],v245 offset:23552\ns_add_u32 s48,s77,4096\n"
"v_mfma_f32_16x16x32_bf16 v[140:143],a[76:79],v[164:167],v[140:143]\ns_waitcnt vmcnt(4) lgkmcnt(4)\nv_mfma_f32_16x16x32_bf16 v[140:143],a[68:71],v[160:163],v[140:143]\nbuffer_load_dwordx4 v247,s[44:47],s48 offen sc1 lds\n"
"v_mfma_f32_16x16x32_bf16 v[140:143],a[100:103],v[176:179],v[140:143]\ns_barrier\ns_add_u32 m0,s50,151552\ns_add_u32 s48,s77,8192\nv_mfma_f32_16x16x32_bf16 v[140:143],a[108:111],v[180:183],v[140:143]\n"
"ds_read_b128 v[160:163],v245 offset:32768\nv_mfma_f32_16x16x32_bf16 v[140:143],a[116:119],v[184:187],v[140:143]\nds_read_b128 v[164:167],v245 offset:33792\nv_mfma_f32_16x16x32_bf16 v[140:143],a[124:127],v[188:191],v[140:143]\n"
"buffer_load_dwordx4 v247,s[44:47],s48 offen sc1 lds\nv_mfma_f32_16x16x32_bf16 v[136:139],a[120:123],v[188:191],v[136:139]\nds_read_b128 v[168:171],v245 offset:34816\ns_add_u32 m0,s50,155648\n"
"v_mfma_f32_16x16x32_bf16 v[136:139],a[112:115],v[184:187],v[136:139]\nds_read_b128 v[172:175],v245 offset:35840\ns_add_u32 s48,s77,12288\nv_mfma_f32_16x16x32_bf16 v[136:139],a[104:107],v[180:183],v[136:139]\ns_waitcnt lgkmcnt(4)\n"
"v_mfma_f32_16x16x32_bf16 v[136:139],a[96:99],v[176:179],v[136:139]\nbuffer_load_dwordx4 v247,s[44:47],s48 offen sc1 lds\nv_mfma_f32_16x16x32_bf16 v[136:139],v[32:35],v[192:195],v[136:139]\n"
"ds_read_b128 v[176:179],v245 offset:36864\ns_add_u32 m0,s50,90112\nv_mfma_f32_16x16x32_bf16 v[136:139],v[40:43],v[196:199],v[136:139]\nds_read_b128 v[180:183],v245 offset:37888\ns_add_u32 s48,s77,16384\n"
"v_mfma_f32_16x16x32_bf16 v[136:139],v[48:51],v[200:203],v[136:139]\nv_mfma_f32_16x16x32_bf16 v[136:139],v[56:59],v[204:207],v[136:139]\nbuffer_load_dwordx4 v247,s[44:47],s48 offen sc1 lds\n"
"v_mfma_f32_16x16x32_bf16 v[140:143],v[60:63],v[204:207],v[140:143]\nds_read_b128 v[184:187],v245 offset:38912\ns_add_u32 m0,s50,94208\nv_mfma_f32_16x16x32_bf16 v[140:143],v[52:55],v[200:203],v[140:143]\n"
"ds_read_b128 v[188:191],v245 offset:39936\ns_add_u32 s48,s77,20480\nv_mfma_f32_16x16x32_bf16 v[140:143],v[44:47],v[196:199],v[140:143]\ns_waitcnt lgkmcnt(4)\nv_mfma_f32_16x16x32_bf16 v[140:143],v[36:39],v[192:195],v[140:143]\n"
"buffer_load_dwordx4 v247,s[44:47],s48 offen sc1 lds\nv_mfma_f32_16x16x32_bf16 v[104:107],a[0:3],v[160:163],0\nds_read_b128 v[192:195],v245 offset:40960\ns_add_u32 s57,s74,49152\n"
"v_mfma_f32_16x16x32_bf16 v[104:107],a[8:11],v[164:167],v[104:107]\nds_read_b128 v[196:199],v245 offset:41984\ns_add_u32 m0,s50,122880\nv_mfma_f32_16x16x32_bf16 v[104:107],a[16:19],v[168:171],v[104:107]\ns_add_u32 s48,s77,24576\n"
"v_mfma_f32_16x16x32_bf16 v[104:107],a[24:27],v[172:175],v[104:107]\nbuffer_load_dwordx4 v247,s[44:47],s48 offen sc1 lds\nv_mfma_f32_16x16x32_bf16 v[108:111],a[28:31],v[172:175],0\nds_read_b128 v[200:203],v245 offset:43008\n"
"s_add_u32 m0,s50,126976\nv_mfma_f32_16x16x32_bf16 v[108:111],a[20:23],v[168:171],v[108:111]\nds_read_b128 v[204:207],v245 offset:44032\ns_add_u32 s48,s77,28672\nv_mfma_f32_16x16x32_bf16 v[108:111],a[12:15],v[164:167],v[108:111]\n"
"s_waitcnt lgkmcnt(4)\nv_cvt_pk_bf16_f32 v136,v136,v137\nv_cvt_pk_bf16_f32 v137,v138,v139\nv_mfma_f32_16x16x32_bf16 v[108:111],a[4:7],v[160:163],v[108:111]\nbuffer_load_dwordx4 v247,s[44:47],s48 offen sc1 lds\n"
"v_mfma_f32_16x16x32_bf16 v[108:111],a[36:39],v[176:179],v[108:111]\nds_read_b128 v[160:163],v245 offset:45056\ns_add_u32 m0,s50,0\nv_mfma_f32_16x16x32_bf16 v[108:111],a[44:47],v[180:183],v[108:111]\n"
"ds_read_b128 v[164:167],v245 offset:46080\ns_add_u32 s48,s77,32768\nv_mfma_f32_16x16x32_bf16 v[108:111],a[52:55],v[184:187],v[108:111]\nv_cvt_pk_bf16_f32 v138,v140,v141\nv_cvt_pk_bf16_f32 v139,v142,v143\n"
"v_mfma_f32_16x16x32_bf16 v[108:111],a[60:63],v[188:191],v[108:111]\nbuffer_store_dwordx4 v[136:139],v247,s[44:47],s57 offen sc1\nv_mfma_f32_16x16x32_bf16 v[104:107],a[56:59],v[188:191],v[104:107]\n"
"ds_read_b128 v[168:171],v245 offset:47104\nv_mfma_f32_16x16x32_bf16 v[104:107],a[48:51],v[184:187],v[104:107]\nds_read_b128 v[172:175],v245 offset:48128\nv_mfma_f32_16x16x32_bf16 v[104:107],a[40:43],v[180:183],v[104:107]\n"
"s_waitcnt lgkmcnt(4)\nv_mfma_f32_16x16x32_bf16 v[104:107],a[32:35],v[176:179],v[104:107]\nbuffer_load_dwordx4 v247,s[44:47],s48 offen sc1 lds\nv_mfma_f32_16x16x32_bf16 v[104:107],a[128:131],v[192:195],v[104:107]\n"
"ds_read_b128 v[176:179],v245 offset:49152\ns_add_u32 m0,s50,4096\nv_mfma_f32_16x16x32_bf16 v[104:107],a[136:139],v[196:199],v[104:107]\nds_read_b128 v[180:183],v245 offset:50176\ns_add_u32 s48,s78,0\n"
"v_mfma_f32_16x16x32_bf16 v[104:107],a[144:147],v[200:203],v[104:107]\nv_mfma_f32_16x16x32_bf16 v[104:107],a[152:155],v[204:207],v[104:107]\nbuffer_load_dwordx4 v247,s[44:47],s48 offen sc1 lds\n"
"v_mfma_f32_16x16x32_bf16 v[108:111],a[156:159],v[204:207],v[108:111]\nds_read_b128 v[184:187],v245 offset:51200\ns_add_u32 m0,s50,8192\nv_mfma_f32_16x16x32_bf16 v[108:111],a[148:151],v[200:203],v[108:111]\n"
"ds_read_b128 v[188:191],v245 offset:52224\ns_add_u32 s48,s78,4096\nv_mfma_f32_16x16x32_bf16 v[108:111],a[140:143],v[196:199],v[108:111]\ns_waitcnt lgkmcnt(4)\nv_mfma_f32_16x16x32_bf16 v[108:111],a[132:135],v[192:195],v[108:111]\n"
"v_mfma_f32_16x16x32_bf16 v[108:111],a[164:167],v[160:163],v[108:111]\nds_read_b128 v[192:195],v245 offset:53248\nv_mfma_f32_16x16x32_bf16 v[108:111],a[172:175],v[164:167],v[108:111]\nds_read_b128 v[196:199],v245 offset:54272\n"
"v_mfma_f32_16x16x32_bf16 v[108:111],a[180:183],v[168:171],v[108:111]\nbuffer_load_dwordx4 v247,s[44:47],s48 offen sc1 lds\nv_mfma_f32_16x16x32_bf16 v[108:111],a[188:191],v[172:175],v[108:111]\ns_add_u32 m0,s50,12288\n"
"s_add_u32 s48,s78,8192\nv_mfma_f32_16x16x32_bf16 v[104:107],a[184:187],v[172:175],v[104:107]\nds_read_b128 v[200:203],v245 offset:55296\nv_mfma_f32_16x16x32_bf16 v[104:107],a[176:179],v[168:171],v[104:107]\n"
"ds_read_b128 v[204:207],v245 offset:56320\nv_mfma_f32_16x16x32_bf16 v[104:107],a[168:171],v[164:167],v[104:107]\ns_waitcnt vmcnt(9) lgkmcnt(4)\nv_mfma_f32_16x16x32_bf16 v[104:107],a[160:163],v[160:163],v[104:107]\n"
"v_mfma_f32_16x16x32_bf16 v[104:107],a[192:195],v[176:179],v[104:107]\ns_barrier\nv_mfma_f32_16x16x32_bf16 v[104:107],a[200:203],v[180:183],v[104:107]\nds_read_b128 v[160:163],v246 offset:0\n"
"v_mfma_f32_16x16x32_bf16 v[104:107],a[208:211],v[184:187],v[104:107]\nds_read_b128 v[164:167],v246 offset:1024\nv_mfma_f32_16x16x32_bf16 v[104:107],a[216:219],v[188:191],v[104:107]\n"
"buffer_load_dwordx4 v247,s[44:47],s48 offen sc1 lds\nv_mfma_f32_16x16x32_bf16 v[108:111],a[220:223],v[188:191],v[108:111]\nds_read_b128 v[168:171],v246 offset:2048\ns_add_u32 m0,s50,16384\n"
"v_mfma_f32_16x16x32_bf16 v[108:111],a[212:215],v[184:187],v[108:111]\nds_read_b128 v[172:175],v246 offset:3072\ns_add_u32 s48,s78,12288\nv_mfma_f32_16x16x32_bf16 v[108:111],a[204:207],v[180:183],v[108:111]\ns_waitcnt lgkmcnt(4)\n"
"v_mfma_f32_16x16x32_bf16 v[108:111],a[196:199],v[176:179],v[108:111]\nv_mfma_f32_16x16x32_bf16 v[108:111],a[228:231],v[192:195],v[108:111]\nds_read_b128 v[176:179],v246 offset:4096\n"
"v_mfma_f32_16x16x32_bf16 v[108:111],a[236:239],v[196:199],v[108:111]\nds_read_b128 v[180:183],v246 offset:5120\nv_mfma_f32_16x16x32_bf16 v[108:111],a[244:247],v[200:203],v[108:111]\n"
"buffer_load_dwordx4 v247,s[44:47],s48 offen sc1 lds\nv_mfma_f32_16x16x32_bf16 v[108:111],a[252:255],v[204:207],v[108:111]\ns_add_u32 m0,s50,20480\ns_add_u32 s48,s78,16384\n"
"v_mfma_f32_16x16x32_bf16 v[104:107],a[248:251],v[204:207],v[104:107]\nds_read_b128 v[184:187],v246 offset:6144\nv_mfma_f32_16x16x32_bf16 v[104:107],a[240:243],v[200:203],v[104:107]\nds_read_b128 v[188:191],v246 offset:7168\n"
"v_mfma_f32_16x16x32_bf16 v[104:107],a[232:235],v[196:199],v[104:107]\ns_waitcnt lgkmcnt(4)\nv_mfma_f32_16x16x32_bf16 v[104:107],a[224:227],v[192:195],v[104:107]\ns_waitcnt vmcnt(5)\nv_mov_b32 v234,s98\nv_mov_b32 v235,s99\n"
"s_mov_b64 exec,1\nv_mov_b32 v233,s90\nglobal_store_dwordx2 v233,v[234:235],s[60:61] offset:0 sc1\ns_mov_b64 exec,-1\nv_mfma_f32_16x16x32_bf16 v[104:107],a[64:67],v[160:163],v[104:107]\nds_read_b128 v[192:195],v246 offset:8192\n"
"v_mfma_f32_16x16x32_bf16 v[104:107],a[72:75],v[164:167],v[104:107]\nds_read_b128 v[196:199],v246 offset:9216\nv_mfma_f32_16x16x32_bf16 v[104:107],a[80:83],v[168:171],v[104:107]\n"
"buffer_load_dwordx4 v247,s[44:47],s48 offen sc1 lds\nv_mfma_f32_16x16x32_bf16 v[104:107],a[88:91],v[172:175],v[104:107]\ns_add_u32 m0,s50,24576\ns_add_u32 s48,s78,20480\n"
"v_mfma_f32_16x16x32_bf16 v[108:111],a[92:95],v[172:175],v[108:111]\nds_read_b128 v[200:203],v246 offset:10240\nv_mfma_f32_16x16x32_bf16 v[108:111],a[84:87],v[168:171],v[108:111]\nds_read_b128 v[204:207],v246 offset:11264\n"
"v_mfma_f32_16x16x32_bf16 v[108:111],a[76:79],v[164:167],v[108:111]\ns_waitcnt lgkmcnt(4)\nv_mfma_f32_16x16x32_bf16 v[108:111],a[68:71],v[160:163],v[108:111]\nv_mfma_f32_16x16x32_bf16 v[108:111],a[100:103],v[176:179],v[108:111]\n"
"ds_read_b128 v[160:163],v246 offset:12288\nv_mfma_f32_16x16x32_bf16 v[108:111],a[108:111],v[180:183],v[108:111]\nds_read_b128 v[164:167],v246 offset:13312\nv_mfma_f32_16x16x32_bf16 v[108:111],a[116:119],v[184:187],v[108:111]\n"
"buffer_load_dwordx4 v247,s[44:47],s48 offen sc1 lds\nv_mfma_f32_16x16x32_bf16 v[108:111],a[124:127],v[188:191],v[108:111]\ns_add_u32 m0,s50,28672\ns_add_u32 s48,s78,24576\n"
"v_mfma_f32_16x16x32_bf16 v[104:107],a[120:123],v[188:191],v[104:107]\nds_read_b128 v[168:171],v246 offset:14336\nv_mfma_f32_16x16x32_bf16 v[104:107],a[112:115],v[184:187],v[104:107]\nds_read_b128 v[172:175],v246 offset:15360\n"
"v_mfma_f32_16x16x32_bf16 v[104:107],a[104:107],v[180:183],v[104:107]\ns_waitcnt lgkmcnt(4)\nv_mfma_f32_16x16x32_bf16 v[104:107],a[96:99],v[176:179],v[104:107]\nv_mfma_f32_16x16x32_bf16 v[104:107],v[32:35],v[192:195],v[104:107]\n"
"ds_read_b128 v[176:179],v246 offset:16384\nv_mfma_f32_16x16x32_bf16 v[104:107],v[40:43],v[196:199],v[104:107]\nds_read_b128 v[180:183],v246 offset:17408\nv_mfma_f32_16x16x32_bf16 v[104:107],v[48:51],v[200:203],v[104:107]\n"
"buffer_load_dwordx4 v247,s[44:47],s48 offen sc1 lds\nv_mfma_f32_16x16x32_bf16 v[104:107],v[56:59],v[204:207],v[104:107]\ns_add_u32 m0,s50,32768\ns_add_u32 s48,s78,28672\n"
"v_mfma_f32_16x16x32_bf16 v[108:111],v[60:63],v[204:207],v[108:111]\nds_read_b128 v[184:187],v246 offset:18432\nv_mfma_f32_16x16x32_bf16 v[108:111],v[52:55],v[200:203],v[108:111]\nds_read_b128 v[188:191],v246 offset:19456\n"
"v_mfma_f32_16x16x32_bf16 v[108:111],v[44:47],v[196:199],v[108:111]\ns_waitcnt lgkmcnt(4)\nv_mfma_f32_16x16x32_bf16 v[108:111],v[36:39],v[192:195],v[108:111]\nv_mfma_f32_16x16x32_bf16 v[112:115],a[0:3],v[160:163],0\n"
"ds_read_b128 v[192:195],v246 offset:20480\nv_mfma_f32_16x16x32_bf16 v[112:115],a[8:11],v[164:167],v[112:115]\nds_read_b128 v[196:199],v246 offset:21504\nv_mfma_f32_16x16x32_bf16 v[112:115],a[16:19],v[168:171],v[112:115]\n"
"s_add_u32 s57,s80,1\ns_and_b32 s57,s57,3\nv_mfma_f32_16x16x32_bf16 v[112:115],a[24:27],v[172:175],v[112:115]\ns_lshl_b32 s57,s57,14\ns_add_u32 s57,s57,s85\ns_add_u32 s57,s57,0x10000\n"
"v_mfma_f32_16x16x32_bf16 v[116:119],a[28:31],v[172:175],0\nds_read_b128 v[200:203],v246 offset:22528\nv_mfma_f32_16x16x32_bf16 v[116:119],a[20:23],v[168:171],v[116:119]\nds_read_b128 v[204:207],v246 offset:23552\n"
"v_cvt_pk_bf16_f32 v104,v104,v105\nv_mfma_f32_16x16x32_bf16 v[116:119],a[12:15],v[164:167],v[116:119]\ns_waitcnt vmcnt(7) lgkmcnt(4)\nv_cvt_pk_bf16_f32 v105,v106,v107\nv_cvt_pk_bf16_f32 v106,v108,v109\n"
"v_mfma_f32_16x16x32_bf16 v[116:119],a[4:7],v[160:163],v[116:119]\nbuffer_load_dwordx4 v247,s[44:47],s48 offen sc1 lds\nv_mfma_f32_16x16x32_bf16 v[116:119],a[36:39],v[176:179],v[116:119]\ns_barrier\ns_add_u32 m0,s50,36864\n"
"v_cvt_pk_bf16_f32 v107,v110,v111\nv_mfma_f32_16x16x32_bf16 v[116:119],a[44:47],v[180:183],v[116:119]\nds_read_b128 v[160:163],v246 offset:24576\ns_add_u32 s48,s78,32768\n"
"v_mfma_f32_16x16x32_bf16 v[116:119],a[52:55],v[184:187],v[116:119]\nds_read_b128 v[164:167],v246 offset:25600\nv_mfma_f32_16x16x32_bf16 v[116:119],a[60:63],v[188:191],v[116:119]\n"
"buffer_store_dwordx4 v[104:107],v247,s[44:47],s57 offen sc1\nv_mfma_f32_16x16x32_bf16 v[112:115],a[56:59],v[188:191],v[112:115]\nds_read_b128 v[168:171],v246 offset:26624\n"
"v_mfma_f32_16x16x32_bf16 v[112:115],a[48:51],v[184:187],v[112:115]\nds_read_b128 v[172:175],v246 offset:27648\nv_mfma_f32_16x16x32_bf16 v[112:115],a[40:43],v[180:183],v[112:115]\ns_waitcnt lgkmcnt(4)\n"
"v_mfma_f32_16x16x32_bf16 v[112:115],a[32:35],v[176:179],v[112:115]\nbuffer_load_dwordx4 v247,s[44:47],s48 offen sc1 lds\nv_mfma_f32_16x16x32_bf16 v[112:115],a[128:131],v[192:195],v[112:115]\n"
"ds_read_b128 v[176:179],v245 offset:24576\ns_add_u32 m0,s50,40960\nv_mfma_f32_16x16x32_bf16 v[112:115],a[136:139],v[196:199],v[112:115]\nds_read_b128 v[180:183],v245 offset:25600\ns_add_u32 s48,s79,0\n"
"v_mfma_f32_16x16x32_bf16 v[112:115],a[144:147],v[200:203],v[112:115]\nv_mfma_f32_16x16x32_bf16 v[112:115],a[152:155],v[204:207],v[112:115]\nbuffer_load_dwordx4 v247,s[44:47],s48 offen sc1 lds\n"
"v_mfma_f32_16x16x32_bf16 v[116:119],a[156:159],v[204:207],v[116:119]\nds_read_b128 v[184:187],v245 offset:26624\ns_add_u32 m0,s50,45056\nv_mfma_f32_16x16x32_bf16 v[116:119],a[148:151],v[200:203],v[116:119]\n"
"ds_read_b128 v[188:191],v245 offset:27648\ns_add_u32 s48,s79,4096\nv_mfma_f32_16x16x32_bf16 v[116:119],a[140:143],v[196:199],v[116:119]\ns_waitcnt lgkmcnt(4)\nv_mfma_f32_16x16x32_bf16 v[116:119],a[132:135],v[192:195],v[116:119]\n"
"v_mfma_f32_16x16x32_bf16 v[116:119],a[164:167],v[160:163],v[116:119]\nds_read_b128 v[192:195],v245 offset:28672\nv_mfma_f32_16x16x32_bf16 v[116:119],a[172:175],v[164:167],v[116:119]\nds_read_b128 v[196:199],v245 offset:29696\n"
"v_mfma_f32_16x16x32_bf16 v[116:119],a[180:183],v[168:171],v[116:119]\nglobal_load_dwordx2 v[240:241],v238,s[60:61] offset:0 sc1\nv_mfma_f32_16x16x32_bf16 v[116:119],a[188:191],v[172:175],v[116:119]\n"
"v_mfma_f32_16x16x32_bf16 v[112:115],a[184:187],v[172:175],v[112:115]\nds_read_b128 v[200:203],v245 offset:30720\nv_mfma_f32_16x16x32_bf16 v[112:115],a[176:179],v[168:171],v[112:115]\nds_read_b128 v[204:207],v245 offset:31744\n"
"v_mfma_f32_16x16x32_bf16 v[112:115],a[168:171],v[164:167],v[112:115]\ns_waitcnt lgkmcnt(4)\nv_mfma_f32_16x16x32_bf16 v[112:115],a[160:163],v[160:163],v[112:115]\nbuffer_load_dwordx4 v247,s[44:47],s48 offen sc1 lds\n"
"v_mfma_f32_16x16x32_bf16 v[112:115],a[192:195],v[176:179],v[112:115]\nds_read_b128 v[160:163],v245 offset:57344\ns_add_u32 m0,s50,49152\nv_mfma_f32_16x16x32_bf16 v[112:115],a[200:203],v[180:183],v[112:115]\n"
"ds_read_b128 v[164:167],v245 offset:58368\ns_add_u32 s48,s79,8192\nv_mfma_f32_16x16x32_bf16 v[112:115],a[208:211],v[184:187],v[112:115]\nv_mfma_f32_16x16x32_bf16 v[112:115],a[216:219],v[188:191],v[112:115]\n"
"buffer_load_dwordx4 v247,s[44:47],s48 offen sc1 lds\nv_mfma_f32_16x16x32_bf16 v[116:119],a[220:223],v[188:191],v[116:119]\nds_read_b128 v[168:171],v245 offset:59392\ns_add_u32 m0,s50,53248\n"
"v_mfma_f32_16x16x32_bf16 v[116:119],a[212:215],v[184:187],v[116:119]\nds_read_b128 v[172:175],v245 offset:60416\ns_add_u32 s48,s79,12288\nv_mfma_f32_16x16x32_bf16 v[116:119],a[204:207],v[180:183],v[116:119]\ns_waitcnt lgkmcnt(4)\n"
"v_mfma_f32_16x16x32_bf16 v[116:119],a[196:199],v[176:179],v[116:119]\nv_mfma_f32_16x16x32_bf16 v[116:119],a[228:231],v[192:195],v[116:119]\nds_read_b128 v[176:179],v245 offset:61440\n"
"v_mfma_f32_16x16x32_bf16 v[116:119],a[236:239],v[196:199],v[116:119]\nds_read_b128 v[180:183],v245 offset:62464\nv_mfma_f32_16x16x32_bf16 v[116:119],a[244:247],v[200:203],v[116:119]\n"
"buffer_load_dwordx4 v247,s[44:47],s48 offen sc1 lds\nv_mfma_f32_16x16x32_bf16 v[116:119],a[252:255],v[204:207],v[116:119]\ns_add_u32 m0,s50,57344\ns_add_u32 s48,s79,16384\n"
"v_mfma_f32_16x16x32_bf16 v[112:115],a[248:251],v[204:207],v[112:115]\nds_read_b128 v[184:187],v245 offset:63488\nv_mfma_f32_16x16x32_bf16 v[112:115],a[240:243],v[200:203],v[112:115]\nds_read_b128 v[188:191],v245 offset:64512\n"
"v_mfma_f32_16x16x32_bf16 v[112:115],a[232:235],v[196:199],v[112:115]\ns_waitcnt lgkmcnt(4)\nv_mfma_f32_16x16x32_bf16 v[112:115],a[224:227],v[192:195],v[112:115]\nv_mfma_f32_16x16x32_bf16 v[112:115],a[64:67],v[160:163],v[112:115]\n"
"ds_read_b128 v[192:195],v244 offset:0\nv_mfma_f32_16x16x32_bf16 v[112:115],a[72:75],v[164:167],v[112:115]\nds_read_b128 v[196:199],v244 offset:1024\nv_mfma_f32_16x16x32_bf16 v[112:115],a[80:83],v[168:171],v[112:115]\n"
"buffer_load_dwordx4 v247,s[44:47],s48 offen sc1 lds\nv_mfma_f32_16x16x32_bf16 v[112:115],a[88:91],v[172:175],v[112:115]\ns_add_u32 m0,s50,61440\ns_add_u32 s48,s79,20480\n"
"v_mfma_f32_16x16x32_bf16 v[116:119],a[92:95],v[172:175],v[116:119]\nds_read_b128 v[200:203],v244 offset:2048\nv_mfma_f32_16x16x32_bf16 v[116:119],a[84:87],v[168:171],v[116:119]\nds_read_b128 v[204:207],v244 offset:3072\n"
"v_mfma_f32_16x16x32_bf16 v[116:119],a[76:79],v[164:167],v[116:119]\ns_waitcnt vmcnt(10) lgkmcnt(4)\nv_mfma_f32_16x16x32_bf16 v[116:119],a[68:71],v[160:163],v[116:119]\n"
"v_mfma_f32_16x16x32_bf16 v[116:119],a[100:103],v[176:179],v[116:119]\ns_barrier\nv_mfma_f32_16x16x32_bf16 v[116:119],a[108:111],v[180:183],v[116:119]\nds_read_b128 v[160:163],v244 offset:4096\n"
"v_mfma_f32_16x16x32_bf16 v[116:119],a[116:119],v[184:187],v[116:119]\nds_read_b128 v[164:167],v244 offset:5120\nv_mfma_f32_16x16x32_bf16 v[116:119],a[124:127],v[188:191],v[116:119]\n"
"buffer_load_dwordx4 v247,s[44:47],s48 offen sc1 lds\nv_mfma_f32_16x16x32_bf16 v[112:115],a[120:123],v[188:191],v[112:115]\nds_read_b128 v[168:171],v244 offset:6144\ns_add_u32 m0,s50,65536\n"
"v_mfma_f32_16x16x32_bf16 v[112:115],a[112:115],v[184:187],v[112:115]\nds_read_b128 v[172:175],v244 offset:7168\ns_add_u32 s48,s79,24576\nv_mfma_f32_16x16x32_bf16 v[112:115],a[104:107],v[180:183],v[112:115]\ns_waitcnt lgkmcnt(4)\n"
"v_mfma_f32_16x16x32_bf16 v[112:115],a[96:99],v[176:179],v[112:115]\ns_waitcnt vmcnt(5)\nv_cmp_ne_u64 vcc,s[98:99],v[240:241]\ns_cmp_eq_u64 vcc,0\ns_cselect_b32 s72,0,0x40000000\n"
"v_mfma_f32_16x16x32_bf16 v[112:115],v[32:35],v[192:195],v[112:115]\nds_read_b128 v[176:179],v244 offset:8192\nv_and_b32 v28,0xf0,v247\nv_mfma_f32_16x16x32_bf16 v[112:115],v[40:43],v[196:199],v[112:115]\n"
"ds_read_b128 v[180:183],v244 offset:9216\nv_lshlrev_b32 v28,6,v28\nv_mfma_f32_16x16x32_bf16 v[112:115],v[48:51],v[200:203],v[112:115]\ns_add_u32 s56,s86,0\ns_add_u32 s56,s56,s72\nv_lshrrev_b32 v29,4,v247\n"
"v_mfma_f32_16x16x32_bf16 v[112:115],v[56:59],v[204:207],v[112:115]\nbuffer_load_dwordx4 v[0:3],v247,s[44:47],s56 offen sc1\nv_mfma_f32_16x16x32_bf16 v[116:119],v[60:63],v[204:207],v[116:119]\n"
"ds_read_b128 v[184:187],v244 offset:10240\nv_and_b32 v29,48,v29\nv_mfma_f32_16x16x32_bf16 v[116:119],v[52:55],v[200:203],v[116:119]\nds_read_b128 v[188:191],v244 offset:11264\nv_add_u32 v28,v28,v29\n"
"v_mfma_f32_16x16x32_bf16 v[116:119],v[44:47],v[196:199],v[116:119]\ns_waitcnt lgkmcnt(4)\ns_add_u32 s56,s86,4096\ns_add_u32 s56,s56,s72\nv_mfma_f32_16x16x32_bf16 v[116:119],v[36:39],v[192:195],v[116:119]\n"
"buffer_load_dwordx4 v[4:7],v247,s[44:47],s56 offen sc1\nv_mfma_f32_16x16x32_bf16 v[120:123],a[0:3],v[160:163],0\nds_read_b128 v[192:195],v244 offset:12288\nv_mfma_f32_16x16x32_bf16 v[120:123],a[8:11],v[164:167],v[120:123]\n"
"ds_read_b128 v[196:199],v244 offset:13312\nv_mfma_f32_16x16x32_bf16 v[120:123],a[16:19],v[168:171],v[120:123]\ns_add_u32 s56,s86,8192\ns_add_u32 s56,s56,s72\nv_mfma_f32_16x16x32_bf16 v[120:123],a[24:27],v[172:175],v[120:123]\n"
"buffer_load_dwordx4 v[8:11],v247,s[44:47],s56 offen sc1\nv_mfma_f32_16x16x32_bf16 v[124:127],a[28:31],v[172:175],0\nds_read_b128 v[200:203],v244 offset:14336\nv_mfma_f32_16x16x32_bf16 v[124:127],a[20:23],v[168:171],v[124:127]\n"
"ds_read_b128 v[204:207],v244 offset:15360\nv_cvt_pk_bf16_f32 v112,v112,v113\nv_mfma_f32_16x16x32_bf16 v[124:127],a[12:15],v[164:167],v[124:127]\ns_waitcnt lgkmcnt(4)\ns_add_u32 s56,s86,12288\ns_add_u32 s56,s56,s72\n"
"v_mfma_f32_16x16x32_bf16 v[124:127],a[4:7],v[160:163],v[124:127]\nbuffer_load_dwordx4 v[12:15],v247,s[44:47],s56 offen sc1\nv_mfma_f32_16x16x32_bf16 v[124:127],a[36:39],v[176:179],v[124:127]\n"
"ds_read_b128 v[160:163],v244 offset:16384\nv_cvt_pk_bf16_f32 v113,v114,v115\nv_mfma_f32_16x16x32_bf16 v[124:127],a[44:47],v[180:183],v[124:127]\nds_read_b128 v[164:167],v244 offset:17408\nv_cvt_pk_bf16_f32 v114,v116,v117\n"
"v_mfma_f32_16x16x32_bf16 v[124:127],a[52:55],v[184:187],v[124:127]\ns_add_u32 s57,s80,2\ns_and_b32 s57,s57,3\nv_cvt_pk_bf16_f32 v115,v118,v119\nv_mfma_f32_16x16x32_bf16 v[124:127],a[60:63],v[188:191],v[124:127]\n"
"s_lshl_b32 s57,s57,14\ns_add_u32 s57,s57,s85\ns_add_u32 s57,s57,0x10000\nv_mfma_f32_16x16x32_bf16 v[120:123],a[56:59],v[188:191],v[120:123]\nds_read_b128 v[168:171],v244 offset:18432\n"
"v_mfma_f32_16x16x32_bf16 v[120:123],a[48:51],v[184:187],v[120:123]\nds_read_b128 v[172:175],v244 offset:19456\nv_mfma_f32_16x16x32_bf16 v[120:123],a[40:43],v[180:183],v[120:123]\ns_waitcnt lgkmcnt(4)\n"
"v_mfma_f32_16x16x32_bf16 v[120:123],a[32:35],v[176:179],v[120:123]\nbuffer_store_dwordx4 v[112:115],v247,s[44:47],s57 offen sc1\nv_mfma_f32_16x16x32_bf16 v[120:123],a[128:131],v[192:195],v[120:123]\n"
"ds_read_b128 v[176:179],v244 offset:20480\nv_mfma_f32_16x16x32_bf16 v[120:123],a[136:139],v[196:199],v[120:123]\nds_read_b128 v[180:183],v244 offset:21504\nv_mfma_f32_16x16x32_bf16 v[120:123],a[144:147],v[200:203],v[120:123]\n"
"v_mfma_f32_16x16x32_bf16 v[120:123],a[152:155],v[204:207],v[120:123]\nbuffer_load_dwordx4 v247,s[44:47],s48 offen sc1 lds\nv_mfma_f32_16x16x32_bf16 v[124:127],a[156:159],v[204:207],v[124:127]\n"
"ds_read_b128 v[184:187],v244 offset:22528\ns_add_u32 m0,s50,69632\nv_mfma_f32_16x16x32_bf16 v[124:127],a[148:151],v[200:203],v[124:127]\nds_read_b128 v[188:191],v244 offset:23552\ns_add_u32 s48,s79,28672\n"
"v_mfma_f32_16x16x32_bf16 v[124:127],a[140:143],v[196:199],v[124:127]\ns_waitcnt lgkmcnt(4)\nv_mfma_f32_16x16x32_bf16 v[124:127],a[132:135],v[192:195],v[124:127]\nbuffer_load_dwordx4 v247,s[44:47],s48 offen sc1 lds\n"
"v_mfma_f32_16x16x32_bf16 v[124:127],a[164:167],v[160:163],v[124:127]\nds_read_b128 v[192:195],v244 offset:24576\ns_add_u32 m0,s50,73728\nv_mfma_f32_16x16x32_bf16 v[124:127],a[172:175],v[164:167],v[124:127]\n"
"ds_read_b128 v[196:199],v244 offset:25600\ns_add_u32 s48,s79,32768\nv_mfma_f32_16x16x32_bf16 v[124:127],a[180:183],v[168:171],v[124:127]\nv_mfma_f32_16x16x32_bf16 v[124:127],a[188:191],v[172:175],v[124:127]\n"
"buffer_load_dwordx4 v247,s[44:47],s48 offen sc1 lds\nv_mfma_f32_16x16x32_bf16 v[120:123],a[184:187],v[172:175],v[120:123]\nds_read_b128 v[200:203],v244 offset:26624\n"
"v_mfma_f32_16x16x32_bf16 v[120:123],a[176:179],v[168:171],v[120:123]\nds_read_b128 v[204:207],v244 offset:27648\nv_mfma_f32_16x16x32_bf16 v[120:123],a[168:171],v[164:167],v[120:123]\ns_waitcnt vmcnt(11) lgkmcnt(4)\n"
"v_mfma_f32_16x16x32_bf16 v[120:123],a[160:163],v[160:163],v[120:123]\nv_mfma_f32_16x16x32_bf16 v[120:123],a[192:195],v[176:179],v[120:123]\ns_barrier\nv_mfma_f32_16x16x32_bf16 v[120:123],a[200:203],v[180:183],v[120:123]\n"
"ds_read_b128 v[160:163],v244 offset:28672\nv_mfma_f32_16x16x32_bf16 v[120:123],a[208:211],v[184:187],v[120:123]\nds_read_b128 v[164:167],v244 offset:29696\nv_mfma_f32_16x16x32_bf16 v[120:123],a[216:219],v[188:191],v[120:123]\n"
"v_mfma_f32_16x16x32_bf16 v[124:127],a[220:223],v[188:191],v[124:127]\nds_read_b128 v[168:171],v244 offset:30720\nv_mfma_f32_16x16x32_bf16 v[124:127],a[212:215],v[184:187],v[124:127]\nds_read_b128 v[172:175],v244 offset:31744\n"
"v_mfma_f32_16x16x32_bf16 v[124:127],a[204:207],v[180:183],v[124:127]\ns_waitcnt lgkmcnt(4)\nv_mfma_f32_16x16x32_bf16 v[124:127],a[196:199],v[176:179],v[124:127]\n"
"v_mfma_f32_16x16x32_bf16 v[124:127],a[228:231],v[192:195],v[124:127]\nds_read_b128 v[176:179],v244 offset:32768\nv_mfma_f32_16x16x32_bf16 v[124:127],a[236:239],v[196:199],v[124:127]\nds_read_b128 v[180:183],v244 offset:33792\n"
"v_mfma_f32_16x16x32_bf16 v[124:127],a[244:247],v[200:203],v[124:127]\nv_mfma_f32_16x16x32_bf16 v[124:127],a[252:255],v[204:207],v[124:127]\nv_mfma_f32_16x16x32_bf16 v[120:123],a[248:251],v[204:207],v[120:123]\n"
"ds_read_b128 v[184:187],v244 offset:34816\nv_mfma_f32_16x16x32_bf16 v[120:123],a[240:243],v[200:203],v[120:123]\nds_read_b128 v[188:191],v244 offset:35840\nv_mfma_f32_16x16x32_bf16 v[120:123],a[232:235],v[196:199],v[120:123]\n"
"s_waitcnt lgkmcnt(4)\nv_mfma_f32_16x16x32_bf16 v[120:123],a[224:227],v[192:195],v[120:123]\nv_mfma_f32_16x16x32_bf16 v[120:123],a[64:67],v[160:163],v[120:123]\nds_read_b128 v[192:195],v244 offset:36864\n"
"v_mfma_f32_16x16x32_bf16 v[120:123],a[72:75],v[164:167],v[120:123]\nds_read_b128 v[196:199],v244 offset:37888\nv_mfma_f32_16x16x32_bf16 v[120:123],a[80:83],v[168:171],v[120:123]\n"
"v_mfma_f32_16x16x32_bf16 v[120:123],a[88:91],v[172:175],v[120:123]\nv_mfma_f32_16x16x32_bf16 v[124:127],a[92:95],v[172:175],v[124:127]\nds_read_b128 v[200:203],v244 offset:38912\n"
"v_mfma_f32_16x16x32_bf16 v[124:127],a[84:87],v[168:171],v[124:127]\nds_read_b128 v[204:207],v244 offset:39936\nv_mfma_f32_16x16x32_bf16 v[124:127],a[76:79],v[164:167],v[124:127]\ns_waitcnt lgkmcnt(4)\n"
"v_mfma_f32_16x16x32_bf16 v[124:127],a[68:71],v[160:163],v[124:127]\ns_waitcnt vmcnt(4)\ns_add_u32 s73,s72,s83\nv_lshlrev_b32 v64,16,v0\nv_mfma_f32_16x16x32_bf16 v[124:127],a[100:103],v[176:179],v[124:127]\n"
"ds_read_b128 v[160:163],v244 offset:40960\nv_and_b32 v65,0xffff0000,v0\nv_mfma_f32_16x16x32_bf16 v[124:127],a[108:111],v[180:183],v[124:127]\nds_read_b128 v[164:167],v244 offset:41984\nv_lshlrev_b32 v66,16,v1\n"
"v_mfma_f32_16x16x32_bf16 v[124:127],a[116:119],v[184:187],v[124:127]\nv_and_b32 v67,0xffff0000,v1\nv_lshlrev_b32 v68,16,v2\nv_mfma_f32_16x16x32_bf16 v[124:127],a[124:127],v[188:191],v[124:127]\nv_and_b32 v69,0xffff0000,v2\n"
"v_lshlrev_b32 v70,16,v3\nv_mfma_f32_16x16x32_bf16 v[120:123],a[120:123],v[188:191],v[120:123]\nds_read_b128 v[168:171],v244 offset:43008\nv_and_b32 v71,0xffff0000,v3\n"
"v_mfma_f32_16x16x32_bf16 v[120:123],a[112:115],v[184:187],v[120:123]\nds_read_b128 v[172:175],v244 offset:44032\nv_lshlrev_b32 v72,16,v4\nv_mfma_f32_16x16x32_bf16 v[120:123],a[104:107],v[180:183],v[120:123]\ns_waitcnt lgkmcnt(4)\n"
"v_and_b32 v73,0xffff0000,v4\nv_lshlrev_b32 v74,16,v5\nv_mfma_f32_16x16x32_bf16 v[120:123],a[96:99],v[176:179],v[120:123]\nv_and_b32 v75,0xffff0000,v5\nv_lshlrev_b32 v76,16,v6\n"
"v_mfma_f32_16x16x32_bf16 v[120:123],v[32:35],v[192:195],v[120:123]\nds_read_b128 v[176:179],v244 offset:45056\nv_and_b32 v77,0xffff0000,v6\nv_mfma_f32_16x16x32_bf16 v[120:123],v[40:43],v[196:199],v[120:123]\n"
"ds_read_b128 v[180:183],v244 offset:46080\nv_lshlrev_b32 v78,16,v7\nv_mfma_f32_16x16x32_bf16 v[120:123],v[48:51],v[200:203],v[120:123]\nv_and_b32 v79,0xffff0000,v7\nv_lshlrev_b32 v80,16,v8\n"
"v_mfma_f32_16x16x32_bf16 v[120:123],v[56:59],v[204:207],v[120:123]\nv_and_b32 v81,0xffff0000,v8\nv_lshlrev_b32 v82,16,v9\nv_mfma_f32_16x16x32_bf16 v[124:127],v[60:63],v[204:207],v[124:127]\n"
"ds_read_b128 v[184:187],v244 offset:47104\nv_and_b32 v83,0xffff0000,v9\nv_mfma_f32_16x16x32_bf16 v[124:127],v[52:55],v[200:203],v[124:127]\nds_read_b128 v[188:191],v244 offset:48128\nv_lshlrev_b32 v84,16,v10\n"
"v_mfma_f32_16x16x32_bf16 v[124:127],v[44:47],v[196:199],v[124:127]\ns_waitcnt lgkmcnt(4)\nv_and_b32 v85,0xffff0000,v10\nv_lshlrev_b32 v86,16,v11\nv_mfma_f32_16x16x32_bf16 v[124:127],v[36:39],v[192:195],v[124:127]\n"
"v_and_b32 v87,0xffff0000,v11\nv_lshlrev_b32 v88,16,v12\nv_mfma_f32_16x16x32_bf16 v[128:131],a[0:3],v[160:163],0\nds_read_b128 v[192:195],v244 offset:49152\nv_and_b32 v89,0xffff0000,v12\n"
"v_mfma_f32_16x16x32_bf16 v[128:131],a[8:11],v[164:167],v[128:131]\nds_read_b128 v[196:199],v244 offset:50176\nv_lshlrev_b32 v90,16,v13\nv_mfma_f32_16x16x32_bf16 v[128:131],a[16:19],v[168:171],v[128:131]\ns_add_u32 s57,s80,3\n"
"s_and_b32 s57,s57,3\nv_and_b32 v91,0xffff0000,v13\nv_mfma_f32_16x16x32_bf16 v[128:131],a[24:27],v[172:175],v[128:131]\ns_lshl_b32 s57,s57,14\ns_add_u32 s57,s57,s85\ns_add_u32 s57,s57,0x10000\n"
"v_mfma_f32_16x16x32_bf16 v[132:135],a[28:31],v[172:175],0\nds_read_b128 v[200:203],v244 offset:51200\nv_lshlrev_b32 v92,16,v14\nv_mfma_f32_16x16x32_bf16 v[132:135],a[20:23],v[168:171],v[132:135]\n"
"ds_read_b128 v[204:207],v244 offset:52224\nv_cvt_pk_bf16_f32 v120,v120,v121\nv_mfma_f32_16x16x32_bf16 v[132:135],a[12:15],v[164:167],v[132:135]\ns_waitcnt vmcnt(0) lgkmcnt(4)\nv_cvt_pk_bf16_f32 v121,v122,v123\n"
"v_cvt_pk_bf16_f32 v122,v124,v125\nv_mfma_f32_16x16x32_bf16 v[132:135],a[4:7],v[160:163],v[132:135]\nv_cvt_pk_bf16_f32 v123,v126,v127\nv_and_b32 v93,0xffff0000,v14\nv_lshlrev_b32 v94,16,v15\n"
"v_mfma_f32_16x16x32_bf16 v[132:135],a[36:39],v[176:179],v[132:135]\ns_barrier\nv_and_b32 v95,0xffff0000,v15\nv_add_f32 v88,v88,v64\nv_mfma_f32_16x16x32_bf16 v[132:135],a[44:47],v[180:183],v[132:135]\n"
"ds_read_b128 v[160:163],v244 offset:53248\nv_add_f32 v89,v89,v65\nv_mfma_f32_16x16x32_bf16 v[132:135],a[52:55],v[184:187],v[132:135]\nds_read_b128 v[164:167],v244 offset:54272\nv_add_f32 v90,v90,v66\n"
"v_mfma_f32_16x16x32_bf16 v[132:135],a[60:63],v[188:191],v[132:135]\nbuffer_store_dwordx4 v[120:123],v247,s[44:47],s57 offen sc1\nv_mfma_f32_16x16x32_bf16 v[128:131],a[56:59],v[188:191],v[128:131]\n"
"ds_read_b128 v[168:171],v244 offset:55296\nv_add_f32 v91,v91,v67\nv_mfma_f32_16x16x32_bf16 v[128:131],a[48:51],v[184:187],v[128:131]\nds_read_b128 v[172:175],v244 offset:56320\nv_add_f32 v92,v92,v68\n"
"v_mfma_f32_16x16x32_bf16 v[128:131],a[40:43],v[180:183],v[128:131]\ns_waitcnt lgkmcnt(4)\nv_add_f32 v93,v93,v69\nv_add_f32 v94,v94,v70\nv_mfma_f32_16x16x32_bf16 v[128:131],a[32:35],v[176:179],v[128:131]\nv_add_f32 v95,v95,v71\n"
"v_add_f32 v72,v72,v80\nv_mfma_f32_16x16x32_bf16 v[128:131],a[128:131],v[192:195],v[128:131]\nds_read_b128 v[176:179],v244 offset:57344\nv_add_f32 v73,v73,v81\nv_mfma_f32_16x16x32_bf16 v[128:131],a[136:139],v[196:199],v[128:131]\n"
"ds_read_b128 v[180:183],v244 offset:58368\nv_add_f32 v74,v74,v82\nv_mfma_f32_16x16x32_bf16 v[128:131],a[144:147],v[200:203],v[128:131]\nv_add_f32 v75,v75,v83\nv_add_f32 v76,v76,v84\n"
"v_mfma_f32_16x16x32_bf16 v[128:131],a[152:155],v[204:207],v[128:131]\nv_add_f32 v77,v77,v85\nv_add_f32 v78,v78,v86\nv_mfma_f32_16x16x32_bf16 v[132:135],a[156:159],v[204:207],v[132:135]\nds_read_b128 v[184:187],v244 offset:59392\n"
"v_add_f32 v79,v79,v87\nv_mfma_f32_16x16x32_bf16 v[132:135],a[148:151],v[200:203],v[132:135]\nds_read_b128 v[188:191],v244 offset:60416\nv_add_f32 v88,v88,v72\nv_mfma_f32_16x16x32_bf16 v[132:135],a[140:143],v[196:199],v[132:135]\n"
"s_waitcnt lgkmcnt(4)\nv_add_f32 v89,v89,v73\nv_add_f32 v90,v90,v74\nv_mfma_f32_16x16x32_bf16 v[132:135],a[132:135],v[192:195],v[132:135]\nv_add_f32 v91,v91,v75\nv_add_f32 v92,v92,v76\n"
"v_mfma_f32_16x16x32_bf16 v[132:135],a[164:167],v[160:163],v[132:135]\nds_read_b128 v[192:195],v244 offset:61440\nv_add_f32 v93,v93,v77\nv_mfma_f32_16x16x32_bf16 v[132:135],a[172:175],v[164:167],v[132:135]\n"
"ds_read_b128 v[196:199],v244 offset:62464\nv_add_f32 v94,v94,v78\nv_mfma_f32_16x16x32_bf16 v[132:135],a[180:183],v[168:171],v[132:135]\ns_add_u32 s56,s80,1\ns_and_b32 s56,s56,3\ns_lshl_b32 s56,s56,12\n"
"v_mfma_f32_16x16x32_bf16 v[132:135],a[188:191],v[172:175],v[132:135]\ns_add_u32 s56,s56,s86\ns_add_u32 s56,s56,0x10000\nv_add_f32 v95,v95,v79\nv_mfma_f32_16x16x32_bf16 v[128:131],a[184:187],v[172:175],v[128:131]\n"
"ds_read_b128 v[200:203],v244 offset:63488\nv_cvt_pk_bf16_f32 v0,v88,v89\nv_mfma_f32_16x16x32_bf16 v[128:131],a[176:179],v[168:171],v[128:131]\nds_read_b128 v[204:207],v244 offset:64512\nv_cvt_pk_bf16_f32 v1,v90,v91\n"
"v_mfma_f32_16x16x32_bf16 v[128:131],a[168:171],v[164:167],v[128:131]\ns_waitcnt lgkmcnt(4)\nv_cvt_pk_bf16_f32 v2,v92,v93\nv_cvt_pk_bf16_f32 v3,v94,v95\nv_mfma_f32_16x16x32_bf16 v[128:131],a[160:163],v[160:163],v[128:131]\n"
"buffer_load_dwordx4 v[16:19],v247,s[44:47],s56 offen sc1\nv_mfma_f32_16x16x32_bf16 v[128:131],a[192:195],v[176:179],v[128:131]\nds_read_b128 v[160:163],v245 offset:0\n"
"v_mfma_f32_16x16x32_bf16 v[128:131],a[200:203],v[180:183],v[128:131]\nds_read_b128 v[164:167],v245 offset:1024\nv_mfma_f32_16x16x32_bf16 v[128:131],a[208:211],v[184:187],v[128:131]\ns_add_u32 s56,s80,2\ns_and_b32 s56,s56,3\n"
"s_lshl_b32 s56,s56,12\nv_mfma_f32_16x16x32_bf16 v[128:131],a[216:219],v[188:191],v[128:131]\ns_add_u32 s56,s56,s86\ns_add_u32 s56,s56,0x10000\nv_mfma_f32_16x16x32_bf16 v[132:135],a[220:223],v[188:191],v[132:135]\n"
"ds_read_b128 v[168:171],v245 offset:2048\nv_mfma_f32_16x16x32_bf16 v[132:135],a[212:215],v[184:187],v[132:135]\nds_read_b128 v[172:175],v245 offset:3072\nv_mfma_f32_16x16x32_bf16 v[132:135],a[204:207],v[180:183],v[132:135]\n"
"s_waitcnt lgkmcnt(4)\nv_mfma_f32_16x16x32_bf16 v[132:135],a[196:199],v[176:179],v[132:135]\nbuffer_load_dwordx4 v[20:23],v247,s[44:47],s56 offen sc1\nv_mfma_f32_16x16x32_bf16 v[132:135],a[228:231],v[192:195],v[132:135]\n"
"ds_read_b128 v[176:179],v245 offset:4096\nv_mfma_f32_16x16x32_bf16 v[132:135],a[236:239],v[196:199],v[132:135]\nds_read_b128 v[180:183],v245 offset:5120\nv_mfma_f32_16x16x32_bf16 v[132:135],a[244:247],v[200:203],v[132:135]\n"
"s_add_u32 s56,s80,3\ns_and_b32 s56,s56,3\ns_lshl_b32 s56,s56,12\nv_mfma_f32_16x16x32_bf16 v[132:135],a[252:255],v[204:207],v[132:135]\ns_add_u32 s56,s56,s86\ns_add_u32 s56,s56,0x10000\n"
"v_mfma_f32_16x16x32_bf16 v[128:131],a[248:251],v[204:207],v[128:131]\nds_read_b128 v[184:187],v245 offset:6144\nv_mfma_f32_16x16x32_bf16 v[128:131],a[240:243],v[200:203],v[128:131]\nds_read_b128 v[188:191],v245 offset:7168\n"
"v_mfma_f32_16x16x32_bf16 v[128:131],a[232:235],v[196:199],v[128:131]\ns_waitcnt lgkmcnt(4)\nv_mfma_f32_16x16x32_bf16 v[128:131],a[224:227],v[192:195],v[128:131]\nbuffer_load_dwordx4 v[24:27],v247,s[44:47],s56 offen sc1\n"
"v_mfma_f32_16x16x32_bf16 v[128:131],a[64:67],v[160:163],v[128:131]\nds_read_b128 v[192:195],v245 offset:8192\nv_mfma_f32_16x16x32_bf16 v[128:131],a[72:75],v[164:167],v[128:131]\nds_read_b128 v[196:199],v245 offset:9216\n"
"v_mfma_f32_16x16x32_bf16 v[128:131],a[80:83],v[168:171],v[128:131]\nv_mfma_f32_16x16x32_bf16 v[128:131],a[88:91],v[172:175],v[128:131]\nbuffer_store_dwordx4 v[0:3],v28,s[40:43],s73 offen\n"
"v_mfma_f32_16x16x32_bf16 v[132:135],a[92:95],v[172:175],v[132:135]\nds_read_b128 v[200:203],v245 offset:10240\nv_mfma_f32_16x16x32_bf16 v[132:135],a[84:87],v[168:171],v[132:135]\nds_read_b128 v[204:207],v245 offset:11264\n"
"v_mfma_f32_16x16x32_bf16 v[132:135],a[76:79],v[164:167],v[132:135]\ns_waitcnt lgkmcnt(4)\nv_mfma_f32_16x16x32_bf16 v[132:135],a[68:71],v[160:163],v[132:135]\nv_mfma_f32_16x16x32_bf16 v[132:135],a[100:103],v[176:179],v[132:135]\n"
"v_mfma_f32_16x16x32_bf16 v[132:135],a[108:111],v[180:183],v[132:135]\nv_mfma_f32_16x16x32_bf16 v[132:135],a[116:119],v[184:187],v[132:135]\nv_mfma_f32_16x16x32_bf16 v[132:135],a[124:127],v[188:191],v[132:135]\n"
"v_mfma_f32_16x16x32_bf16 v[128:131],a[120:123],v[188:191],v[128:131]\nv_mfma_f32_16x16x32_bf16 v[128:131],a[112:115],v[184:187],v[128:131]\nv_mfma_f32_16x16x32_bf16 v[128:131],a[104:107],v[180:183],v[128:131]\n"
"s_waitcnt lgkmcnt(0)\nv_mfma_f32_16x16x32_bf16 v[128:131],a[96:99],v[176:179],v[128:131]\nv_mfma_f32_16x16x32_bf16 v[128:131],v[32:35],v[192:195],v[128:131]\nv_mfma_f32_16x16x32_bf16 v[128:131],v[40:43],v[196:199],v[128:131]\n"
"v_mfma_f32_16x16x32_bf16 v[128:131],v[48:51],v[200:203],v[128:131]\nv_mfma_f32_16x16x32_bf16 v[128:131],v[56:59],v[204:207],v[128:131]\nv_mfma_f32_16x16x32_bf16 v[132:135],v[60:63],v[204:207],v[132:135]\n"
"v_mfma_f32_16x16x32_bf16 v[132:135],v[52:55],v[200:203],v[132:135]\nv_mfma_f32_16x16x32_bf16 v[132:135],v[44:47],v[196:199],v[132:135]\nv_mfma_f32_16x16x32_bf16 v[132:135],v[36:39],v[192:195],v[132:135]\ns_branch LFIN_%=\n"
"LLESS_%=:\ns_waitcnt vmcnt(21)\ns_cmp_lg_u32 s54,0\ns_cbranch_scc1 LW0as0_%=\nglobal_load_dwordx2 v[240:241],v237,s[60:61] offset:0 sc1\nLW0as0_%=:\nbuffer_load_dwordx4 a[124:127],v247,s[44:47],s55 offen offset:1024 sc1\n"
"s_add_u32 m0,s50,122880\ns_add_u32 s48,s22,28672\nbuffer_load_dwordx4 v247,s[44:47],s48 offen sc1 lds\ns_add_u32 m0,s50,126976\ns_add_u32 s48,s22,176128\nbuffer_load_dwordx4 v247,s[44:47],s48 offen sc1 lds\n"
"v_mfma_f32_16x16x32_bf16 v[212:215],a[228:231],v[160:163],v[212:215]\nds_read_b128 v[192:195],v246 offset:20480\ns_add_u32 m0,s50,0\nv_mfma_f32_16x16x32_bf16 v[212:215],a[236:239],v[164:167],v[212:215]\n"
"ds_read_b128 v[196:199],v246 offset:21504\ns_add_u32 s48,s22,323584\nv_mfma_f32_16x16x32_bf16 v[212:215],a[244:247],v[168:171],v[212:215]\nv_mfma_f32_16x16x32_bf16 v[212:215],a[252:255],v[172:175],v[212:215]\n"
"v_mfma_f32_16x16x32_bf16 v[208:211],a[248:251],v[172:175],v[208:211]\nds_read_b128 v[200:203],v246 offset:22528\nv_mfma_f32_16x16x32_bf16 v[208:211],a[240:243],v[168:171],v[208:211]\nds_read_b128 v[204:207],v246 offset:23552\n"
"v_mfma_f32_16x16x32_bf16 v[208:211],a[232:235],v[164:167],v[208:211]\ns_waitcnt vmcnt(10) lgkmcnt(4)\nv_mfma_f32_16x16x32_bf16 v[208:211],a[224:227],v[160:163],v[208:211]\nbuffer_load_dwordx4 v247,s[44:47],s48 offen sc1 lds\n"
"v_mfma_f32_16x16x32_bf16 v[216:219],a[224:227],v[176:179],v[216:219]\ns_barrier\ns_add_u32 s55,s20,262144\nv_mfma_f32_16x16x32_bf16 v[216:219],a[232:235],v[180:183],v[216:219]\nds_read_b128 v[160:163],v246 offset:24576\n"
"v_mfma_f32_16x16x32_bf16 v[216:219],a[240:243],v[184:187],v[216:219]\nds_read_b128 v[164:167],v246 offset:25600\nv_mfma_f32_16x16x32_bf16 v[216:219],a[248:251],v[188:191],v[216:219]\n"
"buffer_load_dwordx4 v[32:35],v247,s[44:47],s55 offen offset:0 sc1\nv_mfma_f32_16x16x32_bf16 v[220:223],a[252:255],v[188:191],v[220:223]\nds_read_b128 v[168:171],v246 offset:26624\n"
"v_mfma_f32_16x16x32_bf16 v[220:223],a[244:247],v[184:187],v[220:223]\nds_read_b128 v[172:175],v246 offset:27648\nv_mfma_f32_16x16x32_bf16 v[220:223],a[236:239],v[180:183],v[220:223]\ns_waitcnt lgkmcnt(4)\n"
"v_mfma_f32_16x16x32_bf16 v[220:223],a[228:231],v[176:179],v[220:223]\nbuffer_load_dwordx4 v[36:39],v247,s[44:47],s55 offen offset:1024 sc1\nv_mfma_f32_16x16x32_bf16 v[228:231],a[228:231],v[192:195],v[228:231]\n"
"ds_read_b128 v[176:179],v245 offset:24576\ns_add_u32 s55,s20,270336\nv_mfma_f32_16x16x32_bf16 v[228:231],a[236:239],v[196:199],v[228:231]\nds_read_b128 v[180:183],v245 offset:25600\n"
"v_mfma_f32_16x16x32_bf16 v[228:231],a[244:247],v[200:203],v[228:231]\nv_mfma_f32_16x16x32_bf16 v[228:231],a[252:255],v[204:207],v[228:231]\nbuffer_load_dwordx4 v[40:43],v247,s[44:47],s55 offen offset:0 sc1\n"
"v_mfma_f32_16x16x32_bf16 v[224:227],a[248:251],v[204:207],v[224:227]\nds_read_b128 v[184:187],v245 offset:26624\nv_mfma_f32_16x16x32_bf16 v[224:227],a[240:243],v[200:203],v[224:227]\nds_read_b128 v[188:191],v245 offset:27648\n"
"v_mfma_f32_16x16x32_bf16 v[224:227],a[232:235],v[196:199],v[224:227]\ns_waitcnt lgkmcnt(4)\nv_mfma_f32_16x16x32_bf16 v[224:227],a[224:227],v[192:195],v[224:227]\n"
"buffer_load_dwordx4 v[44:47],v247,s[44:47],s55 offen offset:1024 sc1\ns_waitcnt vmcnt(18)\ns_add_u32 s55,s20,278528\nbuffer_load_dwordx4 v[48:51],v247,s[44:47],s55 offen offset:0 sc1\n"
"buffer_load_dwordx4 v[52:55],v247,s[44:47],s55 offen offset:1024 sc1\ns_add_u32 s55,s20,286720\nbuffer_load_dwordx4 v[56:59],v247,s[44:47],s55 offen offset:0 sc1\n"
"buffer_load_dwordx4 v[60:63],v247,s[44:47],s55 offen offset:1024 sc1\ns_add_u32 m0,s50,4096\ns_add_u32 s48,s22,32768\nbuffer_load_dwordx4 v247,s[44:47],s48 offen sc1 lds\ns_add_u32 m0,s50,8192\ns_add_u32 s48,s22,180224\n"
"buffer_load_dwordx4 v247,s[44:47],s48 offen sc1 lds\nv_mfma_f32_16x16x32_bf16 v[208:211],a[64:67],v[160:163],v[208:211]\nds_read_b128 v[192:195],v245 offset:28672\ns_add_u32 m0,s50,12288\n"
"v_mfma_f32_16x16x32_bf16 v[208:211],a[72:75],v[164:167],v[208:211]\nds_read_b128 v[196:199],v245 offset:29696\ns_add_u32 s48,s22,327680\nv_mfma_f32_16x16x32_bf16 v[208:211],a[80:83],v[168:171],v[208:211]\n"
"v_mfma_f32_16x16x32_bf16 v[208:211],a[88:91],v[172:175],v[208:211]\nv_mfma_f32_16x16x32_bf16 v[212:215],a[92:95],v[172:175],v[212:215]\nds_read_b128 v[200:203],v245 offset:30720\n"
"v_mfma_f32_16x16x32_bf16 v[212:215],a[84:87],v[168:171],v[212:215]\nds_read_b128 v[204:207],v245 offset:31744\nv_mfma_f32_16x16x32_bf16 v[212:215],a[76:79],v[164:167],v[212:215]\ns_waitcnt vmcnt(10) lgkmcnt(4)\n"
"v_mfma_f32_16x16x32_bf16 v[212:215],a[68:71],v[160:163],v[212:215]\nbuffer_load_dwordx4 v247,s[44:47],s48 offen sc1 lds\ns_cmp_lg_u32 s54,0\ns_cbranch_scc1 LW0bs0_%=\ns_mov_b32 s70,0x40000\ns_waitcnt vmcnt(15)\n"
"s_branch LPCLs0_%=\nLPLLs0_%=:\nglobal_load_dwordx2 v[240:241],v237,s[60:61] offset:0 sc1\ns_waitcnt vmcnt(0)\nLPCLs0_%=:\nv_cmp_ne_u64 vcc,s[98:99],v[240:241]\ns_cmp_eq_u64 vcc,0\ns_cbranch_scc1 LPDLs0_%=\ns_sleep 2\n"
"s_sub_u32 s70,s70,1\ns_cmp_lg_u32 s70,0\ns_cbranch_scc1 LPLLs0_%=\nLPDLs0_%=:\nLW0bs0_%=:\nv_mfma_f32_16x16x32_bf16 v[220:223],a[68:71],v[176:179],v[220:223]\ns_barrier\ns_add_u32 m0,s50,16384\ns_add_u32 s48,s76,0\n"
"v_mfma_f32_16x16x32_bf16 v[220:223],a[76:79],v[180:183],v[220:223]\nds_read_b128 v[160:163],v245 offset:57344\nv_mfma_f32_16x16x32_bf16 v[220:223],a[84:87],v[184:187],v[220:223]\nds_read_b128 v[164:167],v245 offset:58368\n"
"v_mfma_f32_16x16x32_bf16 v[220:223],a[92:95],v[188:191],v[220:223]\nbuffer_load_dwordx4 v247,s[44:47],s48 offen sc1 lds\nv_mfma_f32_16x16x32_bf16 v[216:219],a[88:91],v[188:191],v[216:219]\n"
"ds_read_b128 v[168:171],v245 offset:59392\ns_add_u32 m0,s50,20480\nv_mfma_f32_16x16x32_bf16 v[216:219],a[80:83],v[184:187],v[216:219]\nds_read_b128 v[172:175],v245 offset:60416\ns_add_u32 s48,s76,4096\n"
"v_mfma_f32_16x16x32_bf16 v[216:219],a[72:75],v[180:183],v[216:219]\ns_waitcnt lgkmcnt(4)\nv_mfma_f32_16x16x32_bf16 v[216:219],a[64:67],v[176:179],v[216:219]\nbuffer_load_dwordx4 v247,s[44:47],s48 offen sc1 lds\n"
"v_mfma_f32_16x16x32_bf16 v[224:227],a[64:67],v[192:195],v[224:227]\nds_read_b128 v[176:179],v245 offset:61440\ns_add_u32 m0,s50,24576\nv_mfma_f32_16x16x32_bf16 v[224:227],a[72:75],v[196:199],v[224:227]\n"
"ds_read_b128 v[180:183],v245 offset:62464\ns_add_u32 s48,s76,8192\nv_mfma_f32_16x16x32_bf16 v[224:227],a[80:83],v[200:203],v[224:227]\nv_mfma_f32_16x16x32_bf16 v[224:227],a[88:91],v[204:207],v[224:227]\n"
"buffer_load_dwordx4 v247,s[44:47],s48 offen sc1 lds\nv_mfma_f32_16x16x32_bf16 v[228:231],a[92:95],v[204:207],v[228:231]\nds_read_b128 v[184:187],v245 offset:63488\ns_add_u32 m0,s50,28672\n"
"v_mfma_f32_16x16x32_bf16 v[228:231],a[84:87],v[200:203],v[228:231]\nds_read_b128 v[188:191],v245 offset:64512\ns_add_u32 s48,s76,12288\nv_mfma_f32_16x16x32_bf16 v[228:231],a[76:79],v[196:199],v[228:231]\ns_waitcnt lgkmcnt(4)\n"
"v_mfma_f32_16x16x32_bf16 v[228:231],a[68:71],v[192:195],v[228:231]\nbuffer_load_dwordx4 v247,s[44:47],s48 offen sc1 lds\ns_waitcnt vmcnt(18)\nv_mfma_f32_16x16x32_bf16 v[212:215],a[100:103],v[160:163],v[212:215]\n"
"ds_read_b128 v[192:195],v244 offset:0\ns_add_u32 m0,s50,32768\nv_mfma_f32_16x16x32_bf16 v[212:215],a[108:111],v[164:167],v[212:215]\nds_read_b128 v[196:199],v244 offset:1024\ns_add_u32 s48,s76,16384\n"
"v_mfma_f32_16x16x32_bf16 v[212:215],a[116:119],v[168:171],v[212:215]\nv_mfma_f32_16x16x32_bf16 v[212:215],a[124:127],v[172:175],v[212:215]\nbuffer_load_dwordx4 v247,s[44:47],s48 offen sc1 lds\n"
"v_mfma_f32_16x16x32_bf16 v[208:211],a[120:123],v[172:175],v[208:211]\nds_read_b128 v[200:203],v244 offset:2048\ns_add_u32 m0,s50,36864\nv_mfma_f32_16x16x32_bf16 v[208:211],a[112:115],v[168:171],v[208:211]\n"
"ds_read_b128 v[204:207],v244 offset:3072\ns_add_u32 s48,s76,20480\nv_mfma_f32_16x16x32_bf16 v[208:211],a[104:107],v[164:167],v[208:211]\ns_waitcnt vmcnt(5) lgkmcnt(4)\n"
"v_mfma_f32_16x16x32_bf16 v[208:211],a[96:99],v[160:163],v[208:211]\nbuffer_load_dwordx4 v247,s[44:47],s48 offen sc1 lds\nv_mfma_f32_16x16x32_bf16 v[216:219],a[96:99],v[176:179],v[216:219]\ns_barrier\ns_add_u32 m0,s50,40960\n"
"s_add_u32 s48,s76,24576\nv_mfma_f32_16x16x32_bf16 v[216:219],a[104:107],v[180:183],v[216:219]\nds_read_b128 v[160:163],v244 offset:4096\nv_mfma_f32_16x16x32_bf16 v[216:219],a[112:115],v[184:187],v[216:219]\n"
"ds_read_b128 v[164:167],v244 offset:5120\nv_mfma_f32_16x16x32_bf16 v[216:219],a[120:123],v[188:191],v[216:219]\nbuffer_load_dwordx4 v247,s[44:47],s48 offen sc1 lds\n"
"v_mfma_f32_16x16x32_bf16 v[220:223],a[124:127],v[188:191],v[220:223]\nds_read_b128 v[168:171],v244 offset:6144\ns_add_u32 m0,s50,45056\nv_mfma_f32_16x16x32_bf16 v[220:223],a[116:119],v[184:187],v[220:223]\n"
"ds_read_b128 v[172:175],v244 offset:7168\ns_add_u32 s48,s76,28672\nv_mfma_f32_16x16x32_bf16 v[220:223],a[108:111],v[180:183],v[220:223]\ns_waitcnt lgkmcnt(4)\nv_mfma_f32_16x16x32_bf16 v[220:223],a[100:103],v[176:179],v[220:223]\n"
"buffer_load_dwordx4 v247,s[44:47],s48 offen sc1 lds\nv_mfma_f32_16x16x32_bf16 v[228:231],a[100:103],v[192:195],v[228:231]\nds_read_b128 v[176:179],v244 offset:8192\ns_add_u32 m0,s50,49152\n"
"v_mfma_f32_16x16x32_bf16 v[228:231],a[108:111],v[196:199],v[228:231]\nds_read_b128 v[180:183],v244 offset:9216\ns_add_u32 s48,s76,32768\nv_mfma_f32_16x16x32_bf16 v[228:231],a[116:119],v[200:203],v[228:231]\n"
"v_mfma_f32_16x16x32_bf16 v[228:231],a[124:127],v[204:207],v[228:231]\nbuffer_load_dwordx4 v247,s[44:47],s48 offen sc1 lds\nv_mfma_f32_16x16x32_bf16 v[224:227],a[120:123],v[204:207],v[224:227]\n"
"ds_read_b128 v[184:187],v244 offset:10240\ns_add_u32 m0,s50,53248\nv_mfma_f32_16x16x32_bf16 v[224:227],a[112:115],v[200:203],v[224:227]\nds_read_b128 v[188:191],v244 offset:11264\ns_add_u32 s48,s77,0\n"
"v_mfma_f32_16x16x32_bf16 v[224:227],a[104:107],v[196:199],v[224:227]\ns_waitcnt lgkmcnt(4)\nv_mfma_f32_16x16x32_bf16 v[224:227],a[96:99],v[192:195],v[224:227]\nbuffer_load_dwordx4 v247,s[44:47],s48 offen sc1 lds\n"
"s_waitcnt vmcnt(13)\nv_mfma_f32_16x16x32_bf16 v[208:211],v[32:35],v[160:163],v[208:211]\nds_read_b128 v[192:195],v244 offset:12288\ns_add_u32 m0,s50,57344\nv_mfma_f32_16x16x32_bf16 v[208:211],v[40:43],v[164:167],v[208:211]\n"
"ds_read_b128 v[196:199],v244 offset:13312\ns_add_u32 s48,s77,4096\nv_mfma_f32_16x16x32_bf16 v[208:211],v[48:51],v[168:171],v[208:211]\nv_mfma_f32_16x16x32_bf16 v[208:211],v[56:59],v[172:175],v[208:211]\n"
"buffer_load_dwordx4 v247,s[44:47],s48 offen sc1 lds\nv_mfma_f32_16x16x32_bf16 v[212:215],v[60:63],v[172:175],v[212:215]\nds_read_b128 v[200:203],v244 offset:14336\ns_add_u32 m0,s50,61440\n"
"v_mfma_f32_16x16x32_bf16 v[212:215],v[52:55],v[168:171],v[212:215]\nds_read_b128 v[204:207],v244 offset:15360\ns_add_u32 s48,s77,8192\nv_mfma_f32_16x16x32_bf16 v[212:215],v[44:47],v[164:167],v[212:215]\n"
"s_waitcnt vmcnt(5) lgkmcnt(4)\nv_mfma_f32_16x16x32_bf16 v[212:215],v[36:39],v[160:163],v[212:215]\nbuffer_load_dwordx4 v247,s[44:47],s48 offen sc1 lds\nv_mfma_f32_16x16x32_bf16 v[220:223],v[36:39],v[176:179],v[220:223]\n"
"s_barrier\ns_add_u32 s57,s85,0\ns_add_u32 m0,s50,65536\nv_mfma_f32_16x16x32_bf16 v[220:223],v[44:47],v[180:183],v[220:223]\nds_read_b128 v[160:163],v244 offset:16384\ns_add_u32 s48,s77,12288\n"
"v_mfma_f32_16x16x32_bf16 v[220:223],v[52:55],v[184:187],v[220:223]\nds_read_b128 v[164:167],v244 offset:17408\nv_mfma_f32_16x16x32_bf16 v[220:223],v[60:63],v[188:191],v[220:223]\n"
"buffer_load_dwordx4 v247,s[44:47],s48 offen sc1 lds\nv_mfma_f32_16x16x32_bf16 v[216:219],v[56:59],v[188:191],v[216:219]\nds_read_b128 v[168:171],v244 offset:18432\ns_add_u32 m0,s50,69632\n"
"v_mfma_f32_16x16x32_bf16 v[216:219],v[48:51],v[184:187],v[216:219]\nds_read_b128 v[172:175],v244 offset:19456\ns_add_u32 s48,s77,16384\nv_mfma_f32_16x16x32_bf16 v[216:219],v[40:43],v[180:183],v[216:219]\ns_waitcnt lgkmcnt(4)\n"
"v_cvt_pk_bf16_f32 v208,v208,v209\nv_cvt_pk_bf16_f32 v209,v210,v211\nv_mfma_f32_16x16x32_bf16 v[216:219],v[32:35],v[176:179],v[216:219]\nbuffer_load_dwordx4 v247,s[44:47],s48 offen sc1 lds\n"
"v_mfma_f32_16x16x32_bf16 v[224:227],v[32:35],v[192:195],v[224:227]\nds_read_b128 v[176:179],v244 offset:20480\ns_add_u32 m0,s50,73728\nv_mfma_f32_16x16x32_bf16 v[224:227],v[40:43],v[196:199],v[224:227]\n"
"ds_read_b128 v[180:183],v244 offset:21504\ns_add_u32 s48,s77,20480\nv_mfma_f32_16x16x32_bf16 v[224:227],v[48:51],v[200:203],v[224:227]\nv_cvt_pk_bf16_f32 v210,v212,v213\nv_cvt_pk_bf16_f32 v211,v214,v215\n"
"v_mfma_f32_16x16x32_bf16 v[224:227],v[56:59],v[204:207],v[224:227]\nbuffer_store_dwordx4 v[208:211],v247,s[44:47],s57 offen sc1\nv_mfma_f32_16x16x32_bf16 v[228:231],v[60:63],v[204:207],v[228:231]\n"
"ds_read_b128 v[184:187],v244 offset:22528\ns_add_u32 s57,s85,16384\nv_mfma_f32_16x16x32_bf16 v[228:231],v[52:55],v[200:203],v[228:231]\nds_read_b128 v[188:191],v244 offset:23552\nv_cvt_pk_bf16_f32 v216,v216,v217\n"
"v_mfma_f32_16x16x32_bf16 v[228:231],v[44:47],v[196:199],v[228:231]\ns_waitcnt lgkmcnt(4)\nv_cvt_pk_bf16_f32 v217,v218,v219\nv_cvt_pk_bf16_f32 v218,v220,v221\nv_mfma_f32_16x16x32_bf16 v[228:231],v[36:39],v[192:195],v[228:231]\n"
"buffer_load_dwordx4 v247,s[44:47],s48 offen sc1 lds\nv_mfma_f32_16x16x32_bf16 v[104:107],a[0:3],v[160:163],0\nds_read_b128 v[192:195],v244 offset:24576\ns_add_u32 m0,s50,77824\n"
"v_mfma_f32_16x16x32_bf16 v[104:107],a[8:11],v[164:167],v[104:107]\nds_read_b128 v[196:199],v244 offset:25600\ns_add_u32 s48,s77,24576\nv_mfma_f32_16x16x32_bf16 v[104:107],a[16:19],v[168:171],v[104:107]\n"
"v_cvt_pk_bf16_f32 v219,v222,v223\nv_mfma_f32_16x16x32_bf16 v[104:107],a[24:27],v[172:175],v[104:107]\nbuffer_store_dwordx4 v[216:219],v247,s[44:47],s57 offen sc1\nv_mfma_f32_16x16x32_bf16 v[108:111],a[28:31],v[172:175],0\n"
"ds_read_b128 v[200:203],v244 offset:26624\ns_add_u32 s57,s85,32768\nv_mfma_f32_16x16x32_bf16 v[108:111],a[20:23],v[168:171],v[108:111]\nds_read_b128 v[204:207],v244 offset:27648\nv_cvt_pk_bf16_f32 v224,v224,v225\n"
"v_mfma_f32_16x16x32_bf16 v[108:111],a[12:15],v[164:167],v[108:111]\ns_waitcnt lgkmcnt(4)\nv_cvt_pk_bf16_f32 v225,v226,v227\nv_cvt_pk_bf16_f32 v226,v228,v229\nv_mfma_f32_16x16x32_bf16 v[108:111],a[4:7],v[160:163],v[108:111]\n"
"buffer_load_dwordx4 v247,s[44:47],s48 offen sc1 lds\nv_mfma_f32_16x16x32_bf16 v[108:111],a[36:39],v[176:179],v[108:111]\nds_read_b128 v[160:163],v244 offset:28672\ns_add_u32 m0,s50,81920\n"
"v_mfma_f32_16x16x32_bf16 v[108:111],a[44:47],v[180:183],v[108:111]\nds_read_b128 v[164:167],v244 offset:29696\ns_add_u32 s48,s77,28672\nv_mfma_f32_16x16x32_bf16 v[108:111],a[52:55],v[184:187],v[108:111]\n"
"v_cvt_pk_bf16_f32 v227,v230,v231\nv_mfma_f32_16x16x32_bf16 v[108:111],a[60:63],v[188:191],v[108:111]\nbuffer_store_dwordx4 v[224:227],v247,s[44:47],s57 offen sc1\n"
"v_mfma_f32_16x16x32_bf16 v[104:107],a[56:59],v[188:191],v[104:107]\nds_read_b128 v[168:171],v244 offset:30720\nv_mfma_f32_16x16x32_bf16 v[104:107],a[48:51],v[184:187],v[104:107]\nds_read_b128 v[172:175],v244 offset:31744\n"
"v_mfma_f32_16x16x32_bf16 v[104:107],a[40:43],v[180:183],v[104:107]\ns_waitcnt lgkmcnt(4)\nv_mfma_f32_16x16x32_bf16 v[104:107],a[32:35],v[176:179],v[104:107]\nbuffer_load_dwordx4 v247,s[44:47],s48 offen sc1 lds\n"
"v_mfma_f32_16x16x32_bf16 v[104:107],a[128:131],v[192:195],v[104:107]\nds_read_b128 v[176:179],v244 offset:32768\ns_add_u32 m0,s50,86016\nv_mfma_f32_16x16x32_bf16 v[104:107],a[136:139],v[196:199],v[104:107]\n"
"ds_read_b128 v[180:183],v244 offset:33792\ns_add_u32 s48,s77,32768\nv_mfma_f32_16x16x32_bf16 v[104:107],a[144:147],v[200:203],v[104:107]\nv_mfma_f32_16x16x32_bf16 v[104:107],a[152:155],v[204:207],v[104:107]\n"
"buffer_load_dwordx4 v247,s[44:47],s48 offen sc1 lds\nv_mfma_f32_16x16x32_bf16 v[108:111],a[156:159],v[204:207],v[108:111]\nds_read_b128 v[184:187],v244 offset:34816\ns_add_u32 m0,s50,98304\n"
"v_mfma_f32_16x16x32_bf16 v[108:111],a[148:151],v[200:203],v[108:111]\nds_read_b128 v[188:191],v244 offset:35840\ns_add_u32 s48,s78,0\nv_mfma_f32_16x16x32_bf16 v[108:111],a[140:143],v[196:199],v[108:111]\ns_waitcnt lgkmcnt(4)\n"
"v_mfma_f32_16x16x32_bf16 v[108:111],a[132:135],v[192:195],v[108:111]\nbuffer_load_dwordx4 v247,s[44:47],s48 offen sc1 lds\nv_mfma_f32_16x16x32_bf16 v[108:111],a[164:167],v[160:163],v[108:111]\n"
"ds_read_b128 v[192:195],v244 offset:36864\ns_add_u32 m0,s50,102400\nv_mfma_f32_16x16x32_bf16 v[108:111],a[172:175],v[164:167],v[108:111]\nds_read_b128 v[196:199],v244 offset:37888\ns_add_u32 s48,s78,4096\n"
"v_mfma_f32_16x16x32_bf16 v[108:111],a[180:183],v[168:171],v[108:111]\nv_mfma_f32_16x16x32_bf16 v[108:111],a[188:191],v[172:175],v[108:111]\nbuffer_load_dwordx4 v247,s[44:47],s48 offen sc1 lds\n"
"v_mfma_f32_16x16x32_bf16 v[104:107],a[184:187],v[172:175],v[104:107]\nds_read_b128 v[200:203],v244 offset:38912\ns_add_u32 m0,s50,106496\nv_mfma_f32_16x16x32_bf16 v[104:107],a[176:179],v[168:171],v[104:107]\n"
"ds_read_b128 v[204:207],v244 offset:39936\ns_add_u32 s48,s78,8192\nv_mfma_f32_16x16x32_bf16 v[104:107],a[168:171],v[164:167],v[104:107]\ns_waitcnt vmcnt(11) lgkmcnt(4)\n"
"v_mfma_f32_16x16x32_bf16 v[104:107],a[160:163],v[160:163],v[104:107]\nv_mfma_f32_16x16x32_bf16 v[104:107],a[192:195],v[176:179],v[104:107]\ns_barrier\nv_mfma_f32_16x16x32_bf16 v[104:107],a[200:203],v[180:183],v[104:107]\n"
"ds_read_b128 v[160:163],v244 offset:40960\nv_mfma_f32_16x16x32_bf16 v[104:107],a[208:211],v[184:187],v[104:107]\nds_read_b128 v[164:167],v244 offset:41984\nv_mfma_f32_16x16x32_bf16 v[104:107],a[216:219],v[188:191],v[104:107]\n"
"buffer_load_dwordx4 v247,s[44:47],s48 offen sc1 lds\nv_mfma_f32_16x16x32_bf16 v[108:111],a[220:223],v[188:191],v[108:111]\nds_read_b128 v[168:171],v244 offset:43008\ns_add_u32 m0,s50,110592\n"
"v_mfma_f32_16x16x32_bf16 v[108:111],a[212:215],v[184:187],v[108:111]\nds_read_b128 v[172:175],v244 offset:44032\ns_add_u32 s48,s78,12288\nv_mfma_f32_16x16x32_bf16 v[108:111],a[204:207],v[180:183],v[108:111]\ns_waitcnt lgkmcnt(4)\n"
"v_mfma_f32_16x16x32_bf16 v[108:111],a[196:199],v[176:179],v[108:111]\nv_mfma_f32_16x16x32_bf16 v[108:111],a[228:231],v[192:195],v[108:111]\nds_read_b128 v[176:179],v244 offset:45056\n"
"v_mfma_f32_16x16x32_bf16 v[108:111],a[236:239],v[196:199],v[108:111]\nds_read_b128 v[180:183],v244 offset:46080\nv_mfma_f32_16x16x32_bf16 v[108:111],a[244:247],v[200:203],v[108:111]\n"
"buffer_load_dwordx4 v247,s[44:47],s48 offen sc1 lds\nv_mfma_f32_16x16x32_bf16 v[108:111],a[252:255],v[204:207],v[108:111]\ns_add_u32 m0,s50,114688\ns_add_u32 s48,s78,16384\n"
"v_mfma_f32_16x16x32_bf16 v[104:107],a[248:251],v[204:207],v[104:107]\nds_read_b128 v[184:187],v244 offset:47104\nv_mfma_f32_16x16x32_bf16 v[104:107],a[240:243],v[200:203],v[104:107]\nds_read_b128 v[188:191],v244 offset:48128\n"
"v_mfma_f32_16x16x32_bf16 v[104:107],a[232:235],v[196:199],v[104:107]\ns_waitcnt lgkmcnt(4)\nv_mfma_f32_16x16x32_bf16 v[104:107],a[224:227],v[192:195],v[104:107]\ns_waitcnt vmcnt(6)\nv_mov_b32 v234,s98\nv_mov_b32 v235,s99\n"
"s_mov_b64 exec,1\nv_mov_b32 v233,s90\nglobal_store_dwordx2 v233,v[234:235],s[60:61] offset:0 sc1\ns_mov_b64 exec,-1\nv_mfma_f32_16x16x32_bf16 v[104:107],a[64:67],v[160:163],v[104:107]\nds_read_b128 v[192:195],v244 offset:49152\n"
"v_mfma_f32_16x16x32_bf16 v[104:107],a[72:75],v[164:167],v[104:107]\nds_read_b128 v[196:199],v244 offset:50176\nv_mfma_f32_16x16x32_bf16 v[104:107],a[80:83],v[168:171],v[104:107]\n"
"buffer_load_dwordx4 v247,s[44:47],s48 offen sc1 lds\nv_mfma_f32_16x16x32_bf16 v[104:107],a[88:91],v[172:175],v[104:107]\ns_add_u32 m0,s50,118784\ns_add_u32 s48,s78,20480\n"
"v_mfma_f32_16x16x32_bf16 v[108:111],a[92:95],v[172:175],v[108:111]\nds_read_b128 v[200:203],v244 offset:51200\nv_mfma_f32_16x16x32_bf16 v[108:111],a[84:87],v[168:171],v[108:111]\nds_read_b128 v[204:207],v244 offset:52224\n"
"v_mfma_f32_16x16x32_bf16 v[108:111],a[76:79],v[164:167],v[108:111]\ns_waitcnt lgkmcnt(4)\nv_mfma_f32_16x16x32_bf16 v[108:111],a[68:71],v[160:163],v[108:111]\nv_mfma_f32_16x16x32_bf16 v[108:111],a[100:103],v[176:179],v[108:111]\n"
"ds_read_b128 v[160:163],v244 offset:53248\nv_mfma_f32_16x16x32_bf16 v[108:111],a[108:111],v[180:183],v[108:111]\nds_read_b128 v[164:167],v244 offset:54272\nv_mfma_f32_16x16x32_bf16 v[108:111],a[116:119],v[184:187],v[108:111]\n"
"buffer_load_dwordx4 v247,s[44:47],s48 offen sc1 lds\nv_mfma_f32_16x16x32_bf16 v[108:111],a[124:127],v[188:191],v[108:111]\ns_add_u32 m0,s50,131072\ns_add_u32 s48,s78,24576\n"
"v_mfma_f32_16x16x32_bf16 v[104:107],a[120:123],v[188:191],v[104:107]\nds_read_b128 v[168:171],v244 offset:55296\nv_mfma_f32_16x16x32_bf16 v[104:107],a[112:115],v[184:187],v[104:107]\nds_read_b128 v[172:175],v244 offset:56320\n"
"v_mfma_f32_16x16x32_bf16 v[104:107],a[104:107],v[180:183],v[104:107]\ns_waitcnt lgkmcnt(4)\nv_mfma_f32_16x16x32_bf16 v[104:107],a[96:99],v[176:179],v[104:107]\nv_mfma_f32_16x16x32_bf16 v[104:107],v[32:35],v[192:195],v[104:107]\n"
"ds_read_b128 v[176:179],v244 offset:57344\nv_mfma_f32_16x16x32_bf16 v[104:107],v[40:43],v[196:199],v[104:107]\nds_read_b128 v[180:183],v244 offset:58368\nv_mfma_f32_16x16x32_bf16 v[104:107],v[48:51],v[200:203],v[104:107]\n"
"buffer_load_dwordx4 v247,s[44:47],s48 offen sc1 lds\nv_mfma_f32_16x16x32_bf16 v[104:107],v[56:59],v[204:207],v[104:107]\ns_add_u32 m0,s50,135168\ns_add_u32 s48,s78,28672\n"
"v_mfma_f32_16x16x32_bf16 v[108:111],v[60:63],v[204:207],v[108:111]\nds_read_b128 v[184:187],v244 offset:59392\nv_mfma_f32_16x16x32_bf16 v[108:111],v[52:55],v[200:203],v[108:111]\nds_read_b128 v[188:191],v244 offset:60416\n"
"v_mfma_f32_16x16x32_bf16 v[108:111],v[44:47],v[196:199],v[108:111]\ns_waitcnt lgkmcnt(4)\nv_mfma_f32_16x16x32_bf16 v[108:111],v[36:39],v[192:195],v[108:111]\nv_mfma_f32_16x16x32_bf16 v[112:115],a[0:3],v[160:163],0\n"
"ds_read_b128 v[192:195],v244 offset:61440\nv_mfma_f32_16x16x32_bf16 v[112:115],a[8:11],v[164:167],v[112:115]\nds_read_b128 v[196:199],v244 offset:62464\nv_mfma_f32_16x16x32_bf16 v[112:115],a[16:19],v[168:171],v[112:115]\n"
"s_add_u32 s57,s80,1\ns_and_b32 s57,s57,3\nv_mfma_f32_16x16x32_bf16 v[112:115],a[24:27],v[172:175],v[112:115]\ns_lshl_b32 s57,s57,14\ns_add_u32 s57,s57,s85\ns_add_u32 s57,s57,0x10000\n"
"v_mfma_f32_16x16x32_bf16 v[116:119],a[28:31],v[172:175],0\nds_read_b128 v[200:203],v244 offset:63488\nv_mfma_f32_16x16x32_bf16 v[116:119],a[20:23],v[168:171],v[116:119]\nds_read_b128 v[204:207],v244 offset:64512\n"
"v_cvt_pk_bf16_f32 v104,v104,v105\nv_mfma_f32_16x16x32_bf16 v[116:119],a[12:15],v[164:167],v[116:119]\ns_waitcnt vmcnt(7) lgkmcnt(4)\nv_cvt_pk_bf16_f32 v105,v106,v107\nv_cvt_pk_bf16_f32 v106,v108,v109\n"
"v_mfma_f32_16x16x32_bf16 v[116:119],a[4:7],v[160:163],v[116:119]\nbuffer_load_dwordx4 v247,s[44:47],s48 offen sc1 lds\nv_mfma_f32_16x16x32_bf16 v[116:119],a[36:39],v[176:179],v[116:119]\ns_barrier\ns_add_u32 m0,s50,139264\n"
"v_cvt_pk_bf16_f32 v107,v110,v111\nv_mfma_f32_16x16x32_bf16 v[116:119],a[44:47],v[180:183],v[116:119]\nds_read_b128 v[160:163],v245 offset:0\ns_add_u32 s48,s78,32768\n"
"v_mfma_f32_16x16x32_bf16 v[116:119],a[52:55],v[184:187],v[116:119]\nds_read_b128 v[164:167],v245 offset:1024\nv_mfma_f32_16x16x32_bf16 v[116:119],a[60:63],v[188:191],v[116:119]\n"
"buffer_store_dwordx4 v[104:107],v247,s[44:47],s57 offen sc1\nv_mfma_f32_16x16x32_bf16 v[112:115],a[56:59],v[188:191],v[112:115]\nds_read_b128 v[168:171],v245 offset:2048\n"
"v_mfma_f32_16x16x32_bf16 v[112:115],a[48:51],v[184:187],v[112:115]\nds_read_b128 v[172:175],v245 offset:3072\nv_mfma_f32_16x16x32_bf16 v[112:115],a[40:43],v[180:183],v[112:115]\ns_waitcnt lgkmcnt(4)\n"
"v_mfma_f32_16x16x32_bf16 v[112:115],a[32:35],v[176:179],v[112:115]\nbuffer_load_dwordx4 v247,s[44:47],s48 offen sc1 lds\nv_mfma_f32_16x16x32_bf16 v[112:115],a[128:131],v[192:195],v[112:115]\n"
"ds_read_b128 v[176:179],v245 offset:4096\ns_add_u32 m0,s50,143360\nv_mfma_f32_16x16x32_bf16 v[112:115],a[136:139],v[196:199],v[112:115]\nds_read_b128 v[180:183],v245 offset:5120\ns_add_u32 s48,s79,0\n"
"v_mfma_f32_16x16x32_bf16 v[112:115],a[144:147],v[200:203],v[112:115]\nv_mfma_f32_16x16x32_bf16 v[112:115],a[152:155],v[204:207],v[112:115]\nbuffer_load_dwordx4 v247,s[44:47],s48 offen sc1 lds\n"
"v_mfma_f32_16x16x32_bf16 v[116:119],a[156:159],v[204:207],v[116:119]\nds_read_b128 v[184:187],v245 offset:6144\ns_add_u32 m0,s50,147456\nv_mfma_f32_16x16x32_bf16 v[116:119],a[148:151],v[200:203],v[116:119]\n"
"ds_read_b128 v[188:191],v245 offset:7168\ns_add_u32 s48,s79,4096\nv_mfma_f32_16x16x32_bf16 v[116:119],a[140:143],v[196:199],v[116:119]\ns_waitcnt lgkmcnt(4)\nv_mfma_f32_16x16x32_bf16 v[116:119],a[132:135],v[192:195],v[116:119]\n"
"v_mfma_f32_16x16x32_bf16 v[116:119],a[164:167],v[160:163],v[116:119]\nds_read_b128 v[192:195],v245 offset:8192\nv_mfma_f32_16x16x32_bf16 v[116:119],a[172:175],v[164:167],v[116:119]\nds_read_b128 v[196:199],v245 offset:9216\n"
"v_mfma_f32_16x16x32_bf16 v[116:119],a[180:183],v[168:171],v[116:119]\nglobal_load_dwordx2 v[240:241],v238,s[60:61] offset:0 sc1\nv_mfma_f32_16x16x32_bf16 v[116:119],a[188:191],v[172:175],v[116:119]\n"
"v_mfma_f32_16x16x32_bf16 v[112:115],a[184:187],v[172:175],v[112:115]\nds_read_b128 v[200:203],v245 offset:10240\nv_mfma_f32_16x16x32_bf16 v[112:115],a[176:179],v[168:171],v[112:115]\nds_read_b128 v[204:207],v245 offset:11264\n"
"v_mfma_f32_16x16x32_bf16 v[112:115],a[168:171],v[164:167],v[112:115]\ns_waitcnt lgkmcnt(4)\nv_mfma_f32_16x16x32_bf16 v[112:115],a[160:163],v[160:163],v[112:115]\nbuffer_load_dwordx4 v247,s[44:47],s48 offen sc1 lds\n"
"v_mfma_f32_16x16x32_bf16 v[112:115],a[192:195],v[176:179],v[112:115]\nds_read_b128 v[160:163],v245 offset:12288\ns_add_u32 m0,s50,151552\nv_mfma_f32_16x16x32_bf16 v[112:115],a[200:203],v[180:183],v[112:115]\n"
"ds_read_b128 v[164:167],v245 offset:13312\ns_add_u32 s48,s79,8192\nv_mfma_f32_16x16x32_bf16 v[112:115],a[208:211],v[184:187],v[112:115]\nv_mfma_f32_16x16x32_bf16 v[112:115],a[216:219],v[188:191],v[112:115]\n"
"buffer_load_dwordx4 v247,s[44:47],s48 offen sc1 lds\nv_mfma_f32_16x16x32_bf16 v[116:119],a[220:223],v[188:191],v[116:119]\nds_read_b128 v[168:171],v245 offset:14336\ns_add_u32 m0,s50,155648\n"
"v_mfma_f32_16x16x32_bf16 v[116:119],a[212:215],v[184:187],v[116:119]\nds_read_b128 v[172:175],v245 offset:15360\ns_add_u32 s48,s79,12288\nv_mfma_f32_16x16x32_bf16 v[116:119],a[204:207],v[180:183],v[116:119]\ns_waitcnt lgkmcnt(4)\n"
"v_mfma_f32_16x16x32_bf16 v[116:119],a[196:199],v[176:179],v[116:119]\nv_mfma_f32_16x16x32_bf16 v[116:119],a[228:231],v[192:195],v[116:119]\nds_read_b128 v[176:179],v245 offset:16384\n"
"v_mfma_f32_16x16x32_bf16 v[116:119],a[236:239],v[196:199],v[116:119]\nds_read_b128 v[180:183],v245 offset:17408\nv_mfma_f32_16x16x32_bf16 v[116:119],a[244:247],v[200:203],v[116:119]\n"
"buffer_load_dwordx4 v247,s[44:47],s48 offen sc1 lds\nv_mfma_f32_16x16x32_bf16 v[116:119],a[252:255],v[204:207],v[116:119]\ns_add_u32 m0,s50,90112\ns_add_u32 s48,s79,16384\n"
"v_mfma_f32_16x16x32_bf16 v[112:115],a[248:251],v[204:207],v[112:115]\nds_read_b128 v[184:187],v245 offset:18432\nv_mfma_f32_16x16x32_bf16 v[112:115],a[240:243],v[200:203],v[112:115]\nds_read_b128 v[188:191],v245 offset:19456\n"
"v_mfma_f32_16x16x32_bf16 v[112:115],a[232:235],v[196:199],v[112:115]\ns_waitcnt lgkmcnt(4)\nv_mfma_f32_16x16x32_bf16 v[112:115],a[224:227],v[192:195],v[112:115]\nv_mfma_f32_16x16x32_bf16 v[112:115],a[64:67],v[160:163],v[112:115]\n"
"ds_read_b128 v[192:195],v245 offset:20480\nv_mfma_f32_16x16x32_bf16 v[112:115],a[72:75],v[164:167],v[112:115]\nds_read_b128 v[196:199],v245 offset:21504\nv_mfma_f32_16x16x32_bf16 v[112:115],a[80:83],v[168:171],v[112:115]\n"
"buffer_load_dwordx4 v247,s[44:47],s48 offen sc1 lds\nv_mfma_f32_16x16x32_bf16 v[112:115],a[88:91],v[172:175],v[112:115]\ns_add_u32 m0,s50,94208\ns_add_u32 s48,s79,20480\n"
"v_mfma_f32_16x16x32_bf16 v[116:119],a[92:95],v[172:175],v[116:119]\nds_read_b128 v[200:203],v245 offset:22528\nv_mfma_f32_16x16x32_bf16 v[116:119],a[84:87],v[168:171],v[116:119]\nds_read_b128 v[204:207],v245 offset:23552\n"
"v_mfma_f32_16x16x32_bf16 v[116:119],a[76:79],v[164:167],v[116:119]\ns_waitcnt vmcnt(10) lgkmcnt(4)\nv_mfma_f32_16x16x32_bf16 v[116:119],a[68:71],v[160:163],v[116:119]\n"
"v_mfma_f32_16x16x32_bf16 v[116:119],a[100:103],v[176:179],v[116:119]\ns_barrier\nv_mfma_f32_16x16x32_bf16 v[116:119],a[108:111],v[180:183],v[116:119]\nds_read_b128 v[160:163],v245 offset:32768\n"
"v_mfma_f32_16x16x32_bf16 v[116:119],a[116:119],v[184:187],v[116:119]\nds_read_b128 v[164:167],v245 offset:33792\nv_mfma_f32_16x16x32_bf16 v[116:119],a[124:127],v[188:191],v[116:119]\n"
"buffer_load_dwordx4 v247,s[44:47],s48 offen sc1 lds\nv_mfma_f32_16x16x32_bf16 v[112:115],a[120:123],v[188:191],v[112:115]\nds_read_b128 v[168:171],v245 offset:34816\ns_add_u32 m0,s50,122880\n"
"v_mfma_f32_16x16x32_bf16 v[112:115],a[112:115],v[184:187],v[112:115]\nds_read_b128 v[172:175],v245 offset:35840\ns_add_u32 s48,s79,24576\nv_mfma_f32_16x16x32_bf16 v[112:115],a[104:107],v[180:183],v[112:115]\ns_waitcnt lgkmcnt(4)\n"
"v_mfma_f32_16x16x32_bf16 v[112:115],a[96:99],v[176:179],v[112:115]\ns_waitcnt vmcnt(5)\nv_cmp_ne_u64 vcc,s[98:99],v[240:241]\ns_cmp_eq_u64 vcc,0\ns_cselect_b32 s72,0,0x40000000\n"
"v_mfma_f32_16x16x32_bf16 v[112:115],v[32:35],v[192:195],v[112:115]\nds_read_b128 v[176:179],v245 offset:36864\nv_and_b32 v28,0xf0,v247\nv_mfma_f32_16x16x32_bf16 v[112:115],v[40:43],v[196:199],v[112:115]\n"
"ds_read_b128 v[180:183],v245 offset:37888\nv_lshlrev_b32 v28,6,v28\nv_mfma_f32_16x16x32_bf16 v[112:115],v[48:51],v[200:203],v[112:115]\ns_add_u32 s56,s86,0\ns_add_u32 s56,s56,s72\nv_lshrrev_b32 v29,4,v247\n"
"v_mfma_f32_16x16x32_bf16 v[112:115],v[56:59],v[204:207],v[112:115]\nbuffer_load_dwordx4 v[0:3],v247,s[44:47],s56 offen sc1\nv_mfma_f32_16x16x32_bf16 v[116:119],v[60:63],v[204:207],v[116:119]\n"
"ds_read_b128 v[184:187],v245 offset:38912\nv_and_b32 v29,48,v29\nv_mfma_f32_16x16x32_bf16 v[116:119],v[52:55],v[200:203],v[116:119]\nds_read_b128 v[188:191],v245 offset:39936\nv_add_u32 v28,v28,v29\n"
"v_mfma_f32_16x16x32_bf16 v[116:119],v[44:47],v[196:199],v[116:119]\ns_waitcnt lgkmcnt(4)\ns_add_u32 s56,s86,4096\ns_add_u32 s56,s56,s72\nv_mfma_f32_16x16x32_bf16 v[116:119],v[36:39],v[192:195],v[116:119]\n"
"buffer_load_dwordx4 v[4:7],v247,s[44:47],s56 offen sc1\nv_mfma_f32_16x16x32_bf16 v[120:123],a[0:3],v[160:163],0\nds_read_b128 v[192:195],v245 offset:40960\nv_mfma_f32_16x16x32_bf16 v[120:123],a[8:11],v[164:167],v[120:123]\n"
"ds_read_b128 v[196:199],v245 offset:41984\nv_mfma_f32_16x16x32_bf16 v[120:123],a[16:19],v[168:171],v[120:123]\ns_add_u32 s56,s86,8192\ns_add_u32 s56,s56,s72\nv_mfma_f32_16x16x32_bf16 v[120:123],a[24:27],v[172:175],v[120:123]\n"
"buffer_load_dwordx4 v[8:11],v247,s[44:47],s56 offen sc1\nv_mfma_f32_16x16x32_bf16 v[124:127],a[28:31],v[172:175],0\nds_read_b128 v[200:203],v245 offset:43008\nv_mfma_f32_16x16x32_bf16 v[124:127],a[20:23],v[168:171],v[124:127]\n"
"ds_read_b128 v[204:207],v245 offset:44032\nv_cvt_pk_bf16_f32 v112,v112,v113\nv_mfma_f32_16x16x32_bf16 v[124:127],a[12:15],v[164:167],v[124:127]\ns_waitcnt lgkmcnt(4)\ns_add_u32 s56,s86,12288\ns_add_u32 s56,s56,s72\n"
"v_mfma_f32_16x16x32_bf16 v[124:127],a[4:7],v[160:163],v[124:127]\nbuffer_load_dwordx4 v[12:15],v247,s[44:47],s56 offen sc1\nv_mfma_f32_16x16x32_bf16 v[124:127],a[36:39],v[176:179],v[124:127]\n"
"ds_read_b128 v[160:163],v245 offset:45056\nv_cvt_pk_bf16_f32 v113,v114,v115\nv_mfma_f32_16x16x32_bf16 v[124:127],a[44:47],v[180:183],v[124:127]\nds_read_b128 v[164:167],v245 offset:46080\nv_cvt_pk_bf16_f32 v114,v116,v117\n"
"v_mfma_f32_16x16x32_bf16 v[124:127],a[52:55],v[184:187],v[124:127]\ns_add_u32 s57,s80,2\ns_and_b32 s57,s57,3\nv_cvt_pk_bf16_f32 v115,v118,v119\nv_mfma_f32_16x16x32_bf16 v[124:127],a[60:63],v[188:191],v[124:127]\n"
"s_lshl_b32 s57,s57,14\ns_add_u32 s57,s57,s85\ns_add_u32 s57,s57,0x10000\nv_mfma_f32_16x16x32_bf16 v[120:123],a[56:59],v[188:191],v[120:123]\nds_read_b128 v[168:171],v245 offset:47104\n"
"v_mfma_f32_16x16x32_bf16 v[120:123],a[48:51],v[184:187],v[120:123]\nds_read_b128 v[172:175],v245 offset:48128\nv_mfma_f32_16x16x32_bf16 v[120:123],a[40:43],v[180:183],v[120:123]\ns_waitcnt lgkmcnt(4)\n"
"v_mfma_f32_16x16x32_bf16 v[120:123],a[32:35],v[176:179],v[120:123]\nbuffer_store_dwordx4 v[112:115],v247,s[44:47],s57 offen sc1\nv_mfma_f32_16x16x32_bf16 v[120:123],a[128:131],v[192:195],v[120:123]\n"
"ds_read_b128 v[176:179],v245 offset:49152\nv_mfma_f32_16x16x32_bf16 v[120:123],a[136:139],v[196:199],v[120:123]\nds_read_b128 v[180:183],v245 offset:50176\nv_mfma_f32_16x16x32_bf16 v[120:123],a[144:147],v[200:203],v[120:123]\n"
"v_mfma_f32_16x16x32_bf16 v[120:123],a[152:155],v[204:207],v[120:123]\nbuffer_load_dwordx4 v247,s[44:47],s48 offen sc1 lds\nv_mfma_f32_16x16x32_bf16 v[124:127],a[156:159],v[204:207],v[124:127]\n"
"ds_read_b128 v[184:187],v245 offset:51200\ns_add_u32 m0,s50,126976\nv_mfma_f32_16x16x32_bf16 v[124:127],a[148:151],v[200:203],v[124:127]\nds_read_b128 v[188:191],v245 offset:52224\ns_add_u32 s48,s79,28672\n"
"v_mfma_f32_16x16x32_bf16 v[124:127],a[140:143],v[196:199],v[124:127]\ns_waitcnt lgkmcnt(4)\nv_mfma_f32_16x16x32_bf16 v[124:127],a[132:135],v[192:195],v[124:127]\nbuffer_load_dwordx4 v247,s[44:47],s48 offen sc1 lds\n"
"v_mfma_f32_16x16x32_bf16 v[124:127],a[164:167],v[160:163],v[124:127]\nds_read_b128 v[192:195],v245 offset:53248\ns_add_u32 m0,s50,0\nv_mfma_f32_16x16x32_bf16 v[124:127],a[172:175],v[164:167],v[124:127]\n"
"ds_read_b128 v[196:199],v245 offset:54272\ns_add_u32 s48,s79,32768\nv_mfma_f32_16x16x32_bf16 v[124:127],a[180:183],v[168:171],v[124:127]\nv_mfma_f32_16x16x32_bf16 v[124:127],a[188:191],v[172:175],v[124:127]\n"
"buffer_load_dwordx4 v247,s[44:47],s48 offen sc1 lds\nv_mfma_f32_16x16x32_bf16 v[120:123],a[184:187],v[172:175],v[120:123]\nds_read_b128 v[200:203],v245 offset:55296\n"
"v_mfma_f32_16x16x32_bf16 v[120:123],a[176:179],v[168:171],v[120:123]\nds_read_b128 v[204:207],v245 offset:56320\nv_mfma_f32_16x16x32_bf16 v[120:123],a[168:171],v[164:167],v[120:123]\ns_waitcnt vmcnt(11) lgkmcnt(4)\n"
"v_mfma_f32_16x16x32_bf16 v[120:123],a[160:163],v[160:163],v[120:123]\nv_mfma_f32_16x16x32_bf16 v[120:123],a[192:195],v[176:179],v[120:123]\ns_barrier\nv_mfma_f32_16x16x32_bf16 v[120:123],a[200:203],v[180:183],v[120:123]\n"
"ds_read_b128 v[160:163],v246 offset:0\nv_mfma_f32_16x16x32_bf16 v[120:123],a[208:211],v[184:187],v[120:123]\nds_read_b128 v[164:167],v246 offset:1024\nv_mfma_f32_16x16x32_bf16 v[120:123],a[216:219],v[188:191],v[120:123]\n"
"v_mfma_f32_16x16x32_bf16 v[124:127],a[220:223],v[188:191],v[124:127]\nds_read_b128 v[168:171],v246 offset:2048\nv_mfma_f32_16x16x32_bf16 v[124:127],a[212:215],v[184:187],v[124:127]\nds_read_b128 v[172:175],v246 offset:3072\n"
"v_mfma_f32_16x16x32_bf16 v[124:127],a[204:207],v[180:183],v[124:127]\ns_waitcnt lgkmcnt(4)\nv_mfma_f32_16x16x32_bf16 v[124:127],a[196:199],v[176:179],v[124:127]\n"
"v_mfma_f32_16x16x32_bf16 v[124:127],a[228:231],v[192:195],v[124:127]\nds_read_b128 v[176:179],v246 offset:4096\nv_mfma_f32_16x16x32_bf16 v[124:127],a[236:239],v[196:199],v[124:127]\nds_read_b128 v[180:183],v246 offset:5120\n"
"v_mfma_f32_16x16x32_bf16 v[124:127],a[244:247],v[200:203],v[124:127]\nv_mfma_f32_16x16x32_bf16 v[124:127],a[252:255],v[204:207],v[124:127]\nv_mfma_f32_16x16x32_bf16 v[120:123],a[248:251],v[204:207],v[120:123]\n"
"ds_read_b128 v[184:187],v246 offset:6144\nv_mfma_f32_16x16x32_bf16 v[120:123],a[240:243],v[200:203],v[120:123]\nds_read_b128 v[188:191],v246 offset:7168\nv_mfma_f32_16x16x32_bf16 v[120:123],a[232:235],v[196:199],v[120:123]\n"
"s_waitcnt lgkmcnt(4)\nv_mfma_f32_16x16x32_bf16 v[120:123],a[224:227],v[192:195],v[120:123]\nv_mfma_f32_16x16x32_bf16 v[120:123],a[64:67],v[160:163],v[120:123]\nds_read_b128 v[192:195],v246 offset:8192\n"
"v_mfma_f32_16x16x32_bf16 v[120:123],a[72:75],v[164:167],v[120:123]\nds_read_b128 v[196:199],v246 offset:9216\nv_mfma_f32_16x16x32_bf16 v[120:123],a[80:83],v[168:171],v[120:123]\n"
"v_mfma_f32_16x16x32_bf16 v[120:123],a[88:91],v[172:175],v[120:123]\nv_mfma_f32_16x16x32_bf16 v[124:127],a[92:95],v[172:175],v[124:127]\nds_read_b128 v[200:203],v246 offset:10240\n"
"v_mfma_f32_16x16x32_bf16 v[124:127],a[84:87],v[168:171],v[124:127]\nds_read_b128 v[204:207],v246 offset:11264\nv_mfma_f32_16x16x32_bf16 v[124:127],a[76:79],v[164:167],v[124:127]\ns_waitcnt lgkmcnt(4)\n"
"v_mfma_f32_16x16x32_bf16 v[124:127],a[68:71],v[160:163],v[124:127]\ns_waitcnt vmcnt(4)\ns_add_u32 s73,s72,s83\nv_lshlrev_b32 v64,16,v0\nv_mfma_f32_16x16x32_bf16 v[124:127],a[100:103],v[176:179],v[124:127]\n"
"ds_read_b128 v[160:163],v246 offset:12288\nv_and_b32 v65,0xffff0000,v0\nv_mfma_f32_16x16x32_bf16 v[124:127],a[108:111],v[180:183],v[124:127]\nds_read_b128 v[164:167],v246 offset:13312\nv_lshlrev_b32 v66,16,v1\n"
"v_mfma_f32_16x16x32_bf16 v[124:127],a[116:119],v[184:187],v[124:127]\nv_and_b32 v67,0xffff0000,v1\nv_lshlrev_b32 v68,16,v2\nv_mfma_f32_16x16x32_bf16 v[124:127],a[124:127],v[188:191],v[124:127]\nv_and_b32 v69,0xffff0000,v2\n"
"v_lshlrev_b32 v70,16,v3\nv_mfma_f32_16x16x32_bf16 v[120:123],a[120:123],v[188:191],v[120:123]\nds_read_b128 v[168:171],v246 offset:14336\nv_and_b32 v71,0xffff0000,v3\n"
"v_mfma_f32_16x16x32_bf16 v[120:123],a[112:115],v[184:187],v[120:123]\nds_read_b128 v[172:175],v246 offset:15360\nv_lshlrev_b32 v72,16,v4\nv_mfma_f32_16x16x32_bf16 v[120:123],a[104:107],v[180:183],v[120:123]\ns_waitcnt lgkmcnt(4)\n"
"v_and_b32 v73,0xffff0000,v4\nv_lshlrev_b32 v74,16,v5\nv_mfma_f32_16x16x32_bf16 v[120:123],a[96:99],v[176:179],v[120:123]\nv_and_b32 v75,0xffff0000,v5\nv_lshlrev_b32 v76,16,v6\n"
"v_mfma_f32_16x16x32_bf16 v[120:123],v[32:35],v[192:195],v[120:123]\nds_read_b128 v[176:179],v246 offset:16384\nv_and_b32 v77,0xffff0000,v6\nv_mfma_f32_16x16x32_bf16 v[120:123],v[40:43],v[196:199],v[120:123]\n"
"ds_read_b128 v[180:183],v246 offset:17408\nv_lshlrev_b32 v78,16,v7\nv_mfma_f32_16x16x32_bf16 v[120:123],v[48:51],v[200:203],v[120:123]\nv_and_b32 v79,0xffff0000,v7\nv_lshlrev_b32 v80,16,v8\n"
"v_mfma_f32_16x16x32_bf16 v[120:123],v[56:59],v[204:207],v[120:123]\nv_and_b32 v81,0xffff0000,v8\nv_lshlrev_b32 v82,16,v9\nv_mfma_f32_16x16x32_bf16 v[124:127],v[60:63],v[204:207],v[124:127]\n"
"ds_read_b128 v[184:187],v246 offset:18432\nv_and_b32 v83,0xffff0000,v9\nv_mfma_f32_16x16x32_bf16 v[124:127],v[52:55],v[200:203],v[124:127]\nds_read_b128 v[188:191],v246 offset:19456\nv_lshlrev_b32 v84,16,v10\n"
"v_mfma_f32_16x16x32_bf16 v[124:127],v[44:47],v[196:199],v[124:127]\ns_waitcnt lgkmcnt(4)\nv_and_b32 v85,0xffff0000,v10\nv_lshlrev_b32 v86,16,v11\nv_mfma_f32_16x16x32_bf16 v[124:127],v[36:39],v[192:195],v[124:127]\n"
"v_and_b32 v87,0xffff0000,v11\nv_lshlrev_b32 v88,16,v12\nv_mfma_f32_16x16x32_bf16 v[128:131],a[0:3],v[160:163],0\nds_read_b128 v[192:195],v246 offset:20480\nv_and_b32 v89,0xffff0000,v12\n"
"v_mfma_f32_16x16x32_bf16 v[128:131],a[8:11],v[164:167],v[128:131]\nds_read_b128 v[196:199],v246 offset:21504\nv_lshlrev_b32 v90,16,v13\nv_mfma_f32_16x16x32_bf16 v[128:131],a[16:19],v[168:171],v[128:131]\ns_add_u32 s57,s80,3\n"
"s_and_b32 s57,s57,3\nv_and_b32 v91,0xffff0000,v13\nv_mfma_f32_16x16x32_bf16 v[128:131],a[24:27],v[172:175],v[128:131]\ns_lshl_b32 s57,s57,14\ns_add_u32 s57,s57,s85\ns_add_u32 s57,s57,0x10000\n"
"v_mfma_f32_16x16x32_bf16 v[132:135],a[28:31],v[172:175],0\nds_read_b128 v[200:203],v246 offset:22528\nv_lshlrev_b32 v92,16,v14\nv_mfma_f32_16x16x32_bf16 v[132:135],a[20:23],v[168:171],v[132:135]\n"
"ds_read_b128 v[204:207],v246 offset:23552\nv_cvt_pk_bf16_f32 v120,v120,v121\nv_mfma_f32_16x16x32_bf16 v[132:135],a[12:15],v[164:167],v[132:135]\ns_waitcnt vmcnt(0) lgkmcnt(4)\nv_cvt_pk_bf16_f32 v121,v122,v123\n"
"v_cvt_pk_bf16_f32 v122,v124,v125\nv_mfma_f32_16x16x32_bf16 v[132:135],a[4:7],v[160:163],v[132:135]\nv_cvt_pk_bf16_f32 v123,v126,v127\nv_and_b32 v93,0xffff0000,v14\nv_lshlrev_b32 v94,16,v15\n"
"v_mfma_f32_16x16x32_bf16 v[132:135],a[36:39],v[176:179],v[132:135]\ns_barrier\nv_and_b32 v95,0xffff0000,v15\nv_add_f32 v88,v88,v64\nv_mfma_f32_16x16x32_bf16 v[132:135],a[44:47],v[180:183],v[132:135]\n"
"ds_read_b128 v[160:163],v246 offset:24576\nv_add_f32 v89,v89,v65\nv_mfma_f32_16x16x32_bf16 v[132:135],a[52:55],v[184:187],v[132:135]\nds_read_b128 v[164:167],v246 offset:25600\nv_add_f32 v90,v90,v66\n"
"v_mfma_f32_16x16x32_bf16 v[132:135],a[60:63],v[188:191],v[132:135]\nbuffer_store_dwordx4 v[120:123],v247,s[44:47],s57 offen sc1\nv_mfma_f32_16x16x32_bf16 v[128:131],a[56:59],v[188:191],v[128:131]\n"
"ds_read_b128 v[168:171],v246 offset:26624\nv_add_f32 v91,v91,v67\nv_mfma_f32_16x16x32_bf16 v[128:131],a[48:51],v[184:187],v[128:131]\nds_read_b128 v[172:175],v246 offset:27648\nv_add_f32 v92,v92,v68\n"
"v_mfma_f32_16x16x32_bf16 v[128:131],a[40:43],v[180:183],v[128:131]\ns_waitcnt lgkmcnt(4)\nv_add_f32 v93,v93,v69\nv_add_f32 v94,v94,v70\nv_mfma_f32_16x16x32_bf16 v[128:131],a[32:35],v[176:179],v[128:131]\nv_add_f32 v95,v95,v71\n"
"v_add_f32 v72,v72,v80\nv_mfma_f32_16x16x32_bf16 v[128:131],a[128:131],v[192:195],v[128:131]\nds_read_b128 v[176:179],v245 offset:24576\nv_add_f32 v73,v73,v81\nv_mfma_f32_16x16x32_bf16 v[128:131],a[136:139],v[196:199],v[128:131]\n"
"ds_read_b128 v[180:183],v245 offset:25600\nv_add_f32 v74,v74,v82\nv_mfma_f32_16x16x32_bf16 v[128:131],a[144:147],v[200:203],v[128:131]\nv_add_f32 v75,v75,v83\nv_add_f32 v76,v76,v84\n"
"v_mfma_f32_16x16x32_bf16 v[128:131],a[152:155],v[204:207],v[128:131]\nv_add_f32 v77,v77,v85\nv_add_f32 v78,v78,v86\nv_mfma_f32_16x16x32_bf16 v[132:135],a[156:159],v[204:207],v[132:135]\nds_read_b128 v[184:187],v245 offset:26624\n"
"v_add_f32 v79,v79,v87\nv_mfma_f32_16x16x32_bf16 v[132:135],a[148:151],v[200:203],v[132:135]\nds_read_b128 v[188:191],v245 offset:27648\nv_add_f32 v88,v88,v72\nv_mfma_f32_16x16x32_bf16 v[132:135],a[140:143],v[196:199],v[132:135]\n"
"s_waitcnt lgkmcnt(4)\nv_add_f32 v89,v89,v73\nv_add_f32 v90,v90,v74\nv_mfma_f32_16x16x32_bf16 v[132:135],a[132:135],v[192:195],v[132:135]\nv_add_f32 v91,v91,v75\nv_add_f32 v92,v92,v76\n"
"v_mfma_f32_16x16x32_bf16 v[132:135],a[164:167],v[160:163],v[132:135]\nds_read_b128 v[192:195],v245 offset:28672\nv_add_f32 v93,v93,v77\nv_mfma_f32_16x16x32_bf16 v[132:135],a[172:175],v[164:167],v[132:135]\n"
"ds_read_b128 v[196:199],v245 offset:29696\nv_add_f32 v94,v94,v78\nv_mfma_f32_16x16x32_bf16 v[132:135],a[180:183],v[168:171],v[132:135]\ns_add_u32 s56,s80,1\ns_and_b32 s56,s56,3\ns_lshl_b32 s56,s56,12\n"
"v_mfma_f32_16x16x32_bf16 v[132:135],a[188:191],v[172:175],v[132:135]\ns_add_u32 s56,s56,s86\ns_add_u32 s56,s56,0x10000\nv_add_f32 v95,v95,v79\nv_mfma_f32_16x16x32_bf16 v[128:131],a[184:187],v[172:175],v[128:131]\n"
"ds_read_b128 v[200:203],v245 offset:30720\nv_cvt_pk_bf16_f32 v0,v88,v89\nv_mfma_f32_16x16x32_bf16 v[128:131],a[176:179],v[168:171],v[128:131]\nds_read_b128 v[204:207],v245 offset:31744\nv_cvt_pk_bf16_f32 v1,v90,v91\n"
"v_mfma_f32_16x16x32_bf16 v[128:131],a[168:171],v[164:167],v[128:131]\ns_waitcnt lgkmcnt(4)\nv_cvt_pk_bf16_f32 v2,v92,v93\nv_cvt_pk_bf16_f32 v3,v94,v95\nv_mfma_f32_16x16x32_bf16 v[128:131],a[160:163],v[160:163],v[128:131]\n"
"buffer_load_dwordx4 v[16:19],v247,s[44:47],s56 offen sc1\nv_mfma_f32_16x16x32_bf16 v[128:131],a[192:195],v[176:179],v[128:131]\nds_read_b128 v[160:163],v245 offset:57344\n"
"v_mfma_f32_16x16x32_bf16 v[128:131],a[200:203],v[180:183],v[128:131]\nds_read_b128 v[164:167],v245 offset:58368\nv_mfma_f32_16x16x32_bf16 v[128:131],a[208:211],v[184:187],v[128:131]\ns_add_u32 s56,s80,2\ns_and_b32 s56,s56,3\n"
"s_lshl_b32 s56,s56,12\nv_mfma_f32_16x16x32_bf16 v[128:131],a[216:219],v[188:191],v[128:131]\ns_add_u32 s56,s56,s86\ns_add_u32 s56,s56,0x10000\nv_mfma_f32_16x16x32_bf16 v[132:135],a[220:223],v[188:191],v[132:135]\n"
"ds_read_b128 v[168:171],v245 offset:59392\nv_mfma_f32_16x16x32_bf16 v[132:135],a[212:215],v[184:187],v[132:135]\nds_read_b128 v[172:175],v245 offset:60416\nv_mfma_f32_16x16x32_bf16 v[132:135],a[204:207],v[180:183],v[132:135]\n"
"s_waitcnt lgkmcnt(4)\nv_mfma_f32_16x16x32_bf16 v[132:135],a[196:199],v[176:179],v[132:135]\nbuffer_load_dwordx4 v[20:23],v247,s[44:47],s56 offen sc1\nv_mfma_f32_16x16x32_bf16 v[132:135],a[228:231],v[192:195],v[132:135]\n"
"ds_read_b128 v[176:179],v245 offset:61440\nv_mfma_f32_16x16x32_bf16 v[132:135],a[236:239],v[196:199],v[132:135]\nds_read_b128 v[180:183],v245 offset:62464\nv_mfma_f32_16x16x32_bf16 v[132:135],a[244:247],v[200:203],v[132:135]\n"
"s_add_u32 s56,s80,3\ns_and_b32 s56,s56,3\ns_lshl_b32 s56,s56,12\nv_mfma_f32_16x16x32_bf16 v[132:135],a[252:255],v[204:207],v[132:135]\ns_add_u32 s56,s56,s86\ns_add_u32 s56,s56,0x10000\n"
"v_mfma_f32_16x16x32_bf16 v[128:131],a[248:251],v[204:207],v[128:131]\nds_read_b128 v[184:187],v245 offset:63488\nv_mfma_f32_16x16x32_bf16 v[128:131],a[240:243],v[200:203],v[128:131]\nds_read_b128 v[188:191],v245 offset:64512\n"
"v_mfma_f32_16x16x32_bf16 v[128:131],a[232:235],v[196:199],v[128:131]\ns_waitcnt lgkmcnt(4)\nv_mfma_f32_16x16x32_bf16 v[128:131],a[224:227],v[192:195],v[128:131]\nbuffer_load_dwordx4 v[24:27],v247,s[44:47],s56 offen sc1\n"
"v_mfma_f32_16x16x32_bf16 v[128:131],a[64:67],v[160:163],v[128:131]\nds_read_b128 v[192:195],v244 offset:0\nv_mfma_f32_16x16x32_bf16 v[128:131],a[72:75],v[164:167],v[128:131]\nds_read_b128 v[196:199],v244 offset:1024\n"
"v_mfma_f32_16x16x32_bf16 v[128:131],a[80:83],v[168:171],v[128:131]\nv_mfma_f32_16x16x32_bf16 v[128:131],a[88:91],v[172:175],v[128:131]\nbuffer_store_dwordx4 v[0:3],v28,s[40:43],s73 offen\n"
"v_mfma_f32_16x16x32_bf16 v[132:135],a[92:95],v[172:175],v[132:135]\nds_read_b128 v[200:203],v244 offset:2048\nv_mfma_f32_16x16x32_bf16 v[132:135],a[84:87],v[168:171],v[132:135]\nds_read_b128 v[204:207],v244 offset:3072\n"
"v_mfma_f32_16x16x32_bf16 v[132:135],a[76:79],v[164:167],v[132:135]\ns_waitcnt lgkmcnt(4)\nv_mfma_f32_16x16x32_bf16 v[132:135],a[68:71],v[160:163],v[132:135]\nv_mfma_f32_16x16x32_bf16 v[132:135],a[100:103],v[176:179],v[132:135]\n"
"v_mfma_f32_16x16x32_bf16 v[132:135],a[108:111],v[180:183],v[132:135]\nv_mfma_f32_16x16x32_bf16 v[132:135],a[116:119],v[184:187],v[132:135]\nv_mfma_f32_16x16x32_bf16 v[132:135],a[124:127],v[188:191],v[132:135]\n"
"v_mfma_f32_16x16x32_bf16 v[128:131],a[120:123],v[188:191],v[128:131]\nv_mfma_f32_16x16x32_bf16 v[128:131],a[112:115],v[184:187],v[128:131]\nv_mfma_f32_16x16x32_bf16 v[128:131],a[104:107],v[180:183],v[128:131]\n"
"s_waitcnt lgkmcnt(0)\nv_mfma_f32_16x16x32_bf16 v[128:131],a[96:99],v[176:179],v[128:131]\nv_mfma_f32_16x16x32_bf16 v[128:131],v[32:35],v[192:195],v[128:131]\nv_mfma_f32_16x16x32_bf16 v[128:131],v[40:43],v[196:199],v[128:131]\n"
"v_mfma_f32_16x16x32_bf16 v[128:131],v[48:51],v[200:203],v[128:131]\nv_mfma_f32_16x16x32_bf16 v[128:131],v[56:59],v[204:207],v[128:131]\nv_mfma_f32_16x16x32_bf16 v[132:135],v[60:63],v[204:207],v[132:135]\n"
"v_mfma_f32_16x16x32_bf16 v[132:135],v[52:55],v[200:203],v[132:135]\nv_mfma_f32_16x16x32_bf16 v[132:135],v[44:47],v[196:199],v[132:135]\nv_mfma_f32_16x16x32_bf16 v[132:135],v[36:39],v[192:195],v[132:135]\ns_branch LFIN_%=\n"
".Lgm_code_end:\nLEX_%=:\ns_waitcnt vmcnt(0)\nv_mov_b32 %[st],s62\nv_writelane_b32 %[st],s98,1\nv_writelane_b32 %[st],s99,2\nv_writelane_b32 %[st],s4,3\nv_writelane_b32 %[st],s5,4\ns_nop 1\n"
    : [st] "=v"(st)
    : [ka] "s"(ka), [bid_] "s"(blockIdx.x), [wid_] "s"(__builtin_amdgcn_readfirstlane(threadIdx.x >> 6))
    : "v0", "v1", "v2", "v3", "v4", "v5", "v6", "v7", "v8", "v9", "v10", "v11", "v12", "v13", "v14", "v15", "v16", "v17", "v18", "v19", "v20", "v21", "v22", "v23", "v24", "v25", "v26", "v27", "v28", "v29", "v30", "v31", "v32", "v33", "v34", "v35", "v36", "v37", "v38", "v39", "v40", "v41", "v42", "v43", "v44", "v45", "v46", "v47", "v48", "v49", "v50", "v51", "v52", "v53", "v54", "v55", "v56", "v57", "v58", "v59", "v60", "v61", "v62", "v63", "v64", "v65", "v66", "v67", "v68", "v69", "v70", "v71", "v72", "v73", "v74", "v75", "v76", "v77", "v78", "v79", "v80", "v81", "v82", "v83", "v84", "v85", "v86", "v87", "v88", "v89", "v90", "v91", "v92", "v93", "v94", "v95", "v96", "v97", "v98", "v99", "v100", "v101", "v102", "v103", "v104", "v105", "v106", "v107", "v108", "v109", "v110", "v111", "v112", "v113", "v114", "v115", "v116", "v117", "v118", "v119", "v120", "v121", "v122", "v123", "v124", "v125", "v126", "v127", "v128", "v129", "v130", "v131", "v132", "v133", "v134", "v135", "v136", "v137", "v138", "v139", "v140", "v141", "v142", "v143", "v144", "v145", "v146", "v147", "v148", "v149", "v150", "v151", "v152", "v153", "v154", "v155", "v156", "v157", "v158", "v159", "v160", "v161", "v162", "v163", "v164", "v165", "v166", "v167", "v168", "v169", "v170", "v171", "v172", "v173", "v174", "v175", "v176", "v177", "v178", "v179", "v180", "v181", "v182", "v183", "v184", "v185", "v186", "v187", "v188", "v189", "v190", "v191", "v192", "v193", "v194", "v195", "v196", "v197", "v198", "v199", "v200", "v201", "v202", "v203", "v204", "v205", "v206", "v207", "v208", "v209", "v210", "v211", "v212", "v213", "v214", "v215", "v216", "v217", "v218", "v219", "v220", "v221", "v222", "v223", "v224", "v225", "v226", "v227", "v228", "v229", "v230", "v231", "v232", "v233", "v234", "v235", "v236", "v237", "v238", "v239", "v240", "v241", "v242", "v243", "v244", "v245", "v246", "v247", "v248", "v249", "v250", "v251", "a0", "a1", "a2", "a3", "a4", "a5", "a6", "a7", "a8", "a9", "a10", "a11", "a12", "a13", "a14", "a15", "a16", "a17", "a18", "a19", "a20", "a21", "a22", "a23", "a24", "a25", "a26", "a27", "a28", "a29", "a30", "a31", "a32", "a33", "a34", "a35", "a36", "a37", "a38", "a39", "a40", "a41", "a42", "a43", "a44", "a45", "a46", "a47", "a48", "a49", "a50", "a51", "a52", "a53", "a54", "a55", "a56", "a57", "a58", "a59", "a60", "a61", "a62", "a63", "a64", "a65", "a66", "a67", "a68", "a69", "a70", "a71", "a72", "a73", "a74", "a75", "a76", "a77", "a78", "a79", "a80", "a81", "a82", "a83", "a84", "a85", "a86", "a87", "a88", "a89", "a90", "a91", "a92", "a93", "a94", "a95", "a96", "a97", "a98", "a99", "a100", "a101", "a102", "a103", "a104", "a105", "a106", "a107", "a108", "a109", "a110", "a111", "a112", "a113", "a114", "a115", "a116", "a117", "a118", "a119", "a120", "a121", "a122", "a123", "a124", "a125", "a126", "a127", "a128", "a129", "a130", "a131", "a132", "a133", "a134", "a135", "a136", "a137", "a138", "a139", "a140", "a141", "a142", "a143", "a144", "a145", "a146", "a147", "a148", "a149", "a150", "a151", "a152", "a153", "a154", "a155", "a156", "a157", "a158", "a159", "a160", "a161", "a162", "a163", "a164", "a165", "a166", "a167", "a168", "a169", "a170", "a171", "a172", "a173", "a174", "a175", "a176", "a177", "a178", "a179", "a180", "a181", "a182", "a183", "a184", "a185", "a186", "a187", "a188", "a189", "a190", "a191", "a192", "a193", "a194", "a195", "a196", "a197", "a198", "a199", "a200", "a201", "a202", "a203", "a204", "a205", "a206", "a207", "a208", "a209", "a210", "a211", "a212", "a213", "a214", "a215", "a216", "a217", "a218", "a219", "a220", "a221", "a222", "a223", "a224", "a225", "a226", "a227", "a228", "a229", "a230", "a231", "a232", "a233", "a234", "a235", "a236", "a237", "a238", "a239", "a240", "a241", "a242", "a243", "a244", "a245", "a246", "a247", "a248", "a249", "a250", "a251", "a252", "a253", "a254", "a255", "s4", "s5", "s6", "s7", "s8", "s9", "s10", "s11", "s12", "s13", "s14", "s15", "s16", "s17", "s18", "s19", "s20", "s21", "s22", "s23", "s24", "s25", "s26", "s27", "s28", "s29", "s30", "s31", "s34", "s35", "s36", "s37", "s38", "s39", "s40", "s41", "s42", "s43", "s44", "s45", "s46", "s47", "s48", "s49", "s50", "s51", "s52", "s53", "s54", "s55", "s56", "s57", "s58", "s59", "s60", "s61", "s62", "s63", "s64", "s65", "s66", "s67", "s68", "s69", "s70", "s71", "s72", "s73", "s74", "s75", "s76", "s77", "s78", "s79", "s80", "s81", "s82", "s83", "s84", "s85", "s86", "s87", "s88", "s89", "s90", "s91", "s92", "s93", "s94", "s95", "s96", "s97", "s98", "s99", "vcc", "scc", "memory");
  asm volatile("" : "+s"(ka));
  const __attribute__((address_space(4))) unsigned long long* kp = (const __attribute__((address_space(4))) unsigned long long*)ka;
  gu16 x = (gu16)kp[0], wq = (gu16)kp[4]; char* ws = (char*)kp[9]; u16* out = (u16*)kp[10];
  const unsigned bad = __builtin_amdgcn_readlane(st, 0), bid = __builtin_amdgcn_readlane(st, 3), wid = __builtin_amdgcn_readlane(st, 4);
  const unsigned long long tag = (unsigned)__builtin_amdgcn_readlane(st, 1) | ((unsigned long long)(unsigned)__builtin_amdgcn_readlane(st, 2) << 32);
  const unsigned h = (bid & 7) * 4 + ((bid >> 3) & 3), i = bid >> 5;
  const unsigned rx = (15 - i) * 8 + (((wid & 1) << 2) | (wid & 2)), re = i * 8 + 2 * wid;
  for (int b = 0; b < 4; ++b)
    if ((bad >> b) & 1) attn_rows_exact(x, wq, ws, h, ((b & 2) ? re : rx) + (b & 1));
  asm volatile(
"s_mov_b32 s4,%[bid_]\ns_mov_b32 s5,%[wid_]\ns_mov_b64 s[26:27],%[ws_]\ns_mov_b64 s[28:29],%[out_]\ns_and_b32 s24,s4,0xf8\ns_and_b32 s6,s4,7\ns_lshl_b32 s6,s6,2\ns_lshr_b32 s6,0x76543210,s6\ns_and_b32 s6,s6,7\ns_or_b32 s24,s24,s6\n"
"s_lshr_b32 s19,s4,5\ns_lshl_b32 s19,s19,2\ns_add_u32 s19,s19,s5\ns_lshl_b32 s19,s19,8\ns_and_b32 s6,s4,7\ns_lshl_b32 s6,s6,2\ns_lshr_b32 s7,s4,3\ns_and_b32 s7,s7,3\ns_add_u32 s6,s6,s7\ns_lshl_b32 s6,s6,3\ns_add_u32 s19,s19,s6\n"
"s_add_u32 s19,s19,0x1000\ns_lshr_b32 s18,s4,5\ns_lshl_b32 s18,s18,1\ns_and_b32 s6,s5,1\ns_add_u32 s18,s18,s6\ns_lshl_b32 s18,s18,9\ns_and_b32 s6,s4,7\ns_lshl_b32 s6,s6,2\ns_lshr_b32 s7,s4,3\ns_and_b32 s7,s7,3\ns_add_u32 s6,s6,s7\n"
"s_lshl_b32 s6,s6,4\ns_add_u32 s18,s18,s6\ns_lshr_b32 s6,s5,1\ns_lshl_b32 s6,s6,3\ns_add_u32 s18,s18,s6\ns_add_u32 s18,s18,0x3000\ns_lshr_b32 s20,s4,3\ns_and_b32 s20,s20,3\ns_mul_i32 s20,s20,0x90\ns_lshr_b32 s6,s24,1\n"
"s_and_b32 s6,s6,3\ns_mul_i32 s6,s6,36\ns_add_u32 s20,s20,s6\ns_lshl_b32 s20,s20,3\ns_lshl_b32 s6,s5,1\ns_add_u32 s20,s20,s6\ns_lshl_b32 s20,s20,10\ns_add_u32 s20,s20,0x2400000\ns_lshr_b32 s21,s4,5\ns_lshl_b32 s21,s21,1\n"
"s_and_b32 s6,s24,1\ns_add_u32 s21,s21,s6\ns_mul_i32 s21,s21,3\ns_lshr_b32 s21,s21,1\ns_lshl_b32 s21,s21,8\ns_add_u32 s21,s21,0x1000\ns_lshr_b32 s22,s4,5\ns_lshl_b32 s22,s22,1\ns_and_b32 s6,s24,1\ns_add_u32 s22,s22,s6\n"
"s_mul_i32 s22,s22,0x1b0\ns_lshr_b32 s6,s24,1\ns_and_b32 s6,s6,3\ns_mul_i32 s6,s6,36\ns_add_u32 s22,s22,s6\ns_add_u32 s22,s22,s5\ns_lshl_b32 s22,s22,10\ns_add_u32 s22,s22,0x3080000\ns_mov_b64 s[98:99],%[tag]\n"
"s_mov_b64 s[44:45],s[26:27]\ns_add_u32 s60,s44,70033408\ns_addc_u32 s61,s45,0\ns_and_b32 s45,s45,0xffff\ns_mov_b32 s46,70033408\ns_mov_b32 s47,0x20000\ns_mov_b32 s54,s5\ns_lshl_b32 s50,s54,10\ns_mov_b32 s49,0x40000000\n"
"s_mov_b32 s64,0\ns_mov_b32 s65,0\nv_mbcnt_lo_u32_b32 v233,-1,0\nv_mbcnt_hi_u32_b32 v233,-1,v233\nv_lshlrev_b32 v247,4,v233\ns_add_u32 s55,s20,0\nbuffer_load_dwordx4 a[0:3],v247,s[44:47],s55 offen offset:0 sc1\n"
"buffer_load_dwordx4 a[4:7],v247,s[44:47],s55 offen offset:1024 sc1\ns_add_u32 s55,s20,8192\nbuffer_load_dwordx4 a[8:11],v247,s[44:47],s55 offen offset:0 sc1\nbuffer_load_dwordx4 a[12:15],v247,s[44:47],s55 offen offset:1024 sc1\n"
"s_add_u32 s55,s20,16384\nbuffer_load_dwordx4 a[16:19],v247,s[44:47],s55 offen offset:0 sc1\nbuffer_load_dwordx4 a[20:23],v247,s[44:47],s55 offen offset:1024 sc1\ns_add_u32 s55,s20,24576\n"
"buffer_load_dwordx4 a[24:27],v247,s[44:47],s55 offen offset:0 sc1\nbuffer_load_dwordx4 a[28:31],v247,s[44:47],s55 offen offset:1024 sc1\ns_add_u32 s55,s20,32768\nbuffer_load_dwordx4 a[32:35],v247,s[44:47],s55 offen offset:0 sc1\n"
"buffer_load_dwordx4 a[36:39],v247,s[44:47],s55 offen offset:1024 sc1\ns_add_u32 s55,s20,40960\nbuffer_load_dwordx4 a[40:43],v247,s[44:47],s55 offen offset:0 sc1\n"
"buffer_load_dwordx4 a[44:47],v247,s[44:47],s55 offen offset:1024 sc1\ns_add_u32 s55,s20,49152\nbuffer_load_dwordx4 a[48:51],v247,s[44:47],s55 offen offset:0 sc1\n"
"buffer_load_dwordx4 a[52:55],v247,s[44:47],s55 offen offset:1024 sc1\ns_add_u32 s55,s20,57344\nbuffer_load_dwordx4 a[56:59],v247,s[44:47],s55 offen offset:0 sc1\n"
"buffer_load_dwordx4 a[60:63],v247,s[44:47],s55 offen offset:1024 sc1\ns_add_u32 s55,s20,65536\nbuffer_load_dwordx4 a[128:131],v247,s[44:47],s55 offen offset:0 sc1\n"
"buffer_load_dwordx4 a[132:135],v247,s[44:47],s55 offen offset:1024 sc1\ns_add_u32 s55,s20,73728\nbuffer_load_dwordx4 a[136:139],v247,s[44:47],s55 offen offset:0 sc1\n"
"buffer_load_dwordx4 a[140:143],v247,s[44:47],s55 offen offset:1024 sc1\ns_add_u32 s55,s20,81920\nbuffer_load_dwordx4 a[144:147],v247,s[44:47],s55 offen offset:0 sc1\n"
"buffer_load_dwordx4 a[148:151],v247,s[44:47],s55 offen offset:1024 sc1\ns_add_u32 s55,s20,90112\nbuffer_load_dwordx4 a[152:155],v247,s[44:47],s55 offen offset:0 sc1\n"
"buffer_load_dwordx4 a[156:159],v247,s[44:47],s55 offen offset:1024 sc1\ns_add_u32 s55,s20,98304\nbuffer_load_dwordx4 a[160:163],v247,s[44:47],s55 offen offset:0 sc1\n"
"buffer_load_dwordx4 a[164:167],v247,s[44:47],s55 offen offset:1024 sc1\ns_add_u32 s55,s20,106496\nbuffer_load_dwordx4 a[168:171],v247,s[44:47],s55 offen offset:0 sc1\n"
"buffer_load_dwordx4 a[172:175],v247,s[44:47],s55 offen offset:1024 sc1\ns_add_u32 s55,s20,114688\nbuffer_load_dwordx4 a[176:179],v247,s[44:47],s55 offen offset:0 sc1\n"
"buffer_load_dwordx4 a[180:183],v247,s[44:47],s55 offen offset:1024 sc1\ns_add_u32 s55,s20,122880\nbuffer_load_dwordx4 a[184:187],v247,s[44:47],s55 offen offset:0 sc1\n"
"buffer_load_dwordx4 a[188:191],v247,s[44:47],s55 offen offset:1024 sc1\ns_add_u32 s55,s20,131072\nbuffer_load_dwordx4 a[192:195],v247,s[44:47],s55 offen offset:0 sc1\n"
"buffer_load_dwordx4 a[196:199],v247,s[44:47],s55 offen offset:1024 sc1\ns_add_u32 s55,s20,139264\nbuffer_load_dwordx4 a[200:203],v247,s[44:47],s55 offen offset:0 sc1\n"
"buffer_load_dwordx4 a[204:207],v247,s[44:47],s55 offen offset:1024 sc1\ns_add_u32 s55,s20,147456\nbuffer_load_dwordx4 a[208:211],v247,s[44:47],s55 offen offset:0 sc1\n"
"buffer_load_dwordx4 a[212:215],v247,s[44:47],s55 offen offset:1024 sc1\ns_add_u32 s55,s20,155648\nbuffer_load_dwordx4 a[216:219],v247,s[44:47],s55 offen offset:0 sc1\n"
"buffer_load_dwordx4 a[220:223],v247,s[44:47],s55 offen offset:1024 sc1\ns_add_u32 s55,s20,163840\nbuffer_load_dwordx4 a[224:227],v247,s[44:47],s55 offen offset:0 sc1\n"
"buffer_load_dwordx4 a[228:231],v247,s[44:47],s55 offen offset:1024 sc1\ns_add_u32 s55,s20,172032\nbuffer_load_dwordx4 a[232:235],v247,s[44:47],s55 offen offset:0 sc1\n"
"buffer_load_dwordx4 a[236:239],v247,s[44:47],s55 offen offset:1024 sc1\ns_add_u32 s55,s20,180224\nbuffer_load_dwordx4 a[240:243],v247,s[44:47],s55 offen offset:0 sc1\n"
"buffer_load_dwordx4 a[244:247],v247,s[44:47],s55 offen offset:1024 sc1\ns_add_u32 s55,s20,188416\nbuffer_load_dwordx4 a[248:251],v247,s[44:47],s55 offen offset:0 sc1\n"
"buffer_load_dwordx4 a[252:255],v247,s[44:47],s55 offen offset:1024 sc1\ns_add_u32 s55,s20,196608\nbuffer_load_dwordx4 a[64:67],v247,s[44:47],s55 offen offset:0 sc1\n"
"buffer_load_dwordx4 a[68:71],v247,s[44:47],s55 offen offset:1024 sc1\ns_add_u32 s55,s20,204800\nbuffer_load_dwordx4 a[72:75],v247,s[44:47],s55 offen offset:0 sc1\n"
"buffer_load_dwordx4 a[76:79],v247,s[44:47],s55 offen offset:1024 sc1\ns_add_u32 s55,s20,212992\nbuffer_load_dwordx4 a[80:83],v247,s[44:47],s55 offen offset:0 sc1\n"
"buffer_load_dwordx4 a[84:87],v247,s[44:47],s55 offen offset:1024 sc1\ns_add_u32 s55,s20,221184\nbuffer_load_dwordx4 a[88:91],v247,s[44:47],s55 offen offset:0 sc1\n"
"buffer_load_dwordx4 a[92:95],v247,s[44:47],s55 offen offset:1024 sc1\ns_add_u32 s55,s20,229376\nbuffer_load_dwordx4 a[96:99],v247,s[44:47],s55 offen offset:0 sc1\n"
"buffer_load_dwordx4 a[100:103],v247,s[44:47],s55 offen offset:1024 sc1\ns_add_u32 s55,s20,237568\nbuffer_load_dwordx4 a[104:107],v247,s[44:47],s55 offen offset:0 sc1\n"
"buffer_load_dwordx4 a[108:111],v247,s[44:47],s55 offen offset:1024 sc1\ns_add_u32 s55,s20,245760\nbuffer_load_dwordx4 a[112:115],v247,s[44:47],s55 offen offset:0 sc1\n"
"buffer_load_dwordx4 a[116:119],v247,s[44:47],s55 offen offset:1024 sc1\ns_add_u32 s55,s20,253952\nbuffer_load_dwordx4 a[120:123],v247,s[44:47],s55 offen offset:0 sc1\n"
"buffer_load_dwordx4 a[124:127],v247,s[44:47],s55 offen offset:1024 sc1\ns_add_u32 s55,s20,262144\nbuffer_load_dwordx4 v[32:35],v247,s[44:47],s55 offen offset:0 sc1\n"
"buffer_load_dwordx4 v[36:39],v247,s[44:47],s55 offen offset:1024 sc1\ns_add_u32 s55,s20,270336\nbuffer_load_dwordx4 v[40:43],v247,s[44:47],s55 offen offset:0 sc1\n"
"buffer_load_dwordx4 v[44:47],v247,s[44:47],s55 offen offset:1024 sc1\ns_add_u32 s55,s20,278528\nbuffer_load_dwordx4 v[48:51],v247,s[44:47],s55 offen offset:0 sc1\n"
"buffer_load_dwordx4 v[52:55],v247,s[44:47],s55 offen offset:1024 sc1\ns_add_u32 s55,s20,286720\nbuffer_load_dwordx4 v[56:59],v247,s[44:47],s55 offen offset:0 sc1\n"
"buffer_load_dwordx4 v[60:63],v247,s[44:47],s55 offen offset:1024 sc1\ns_waitcnt vmcnt(0)\ns_branch .Lgm_p3\n"
    :: [ws_] "s"(ws), [out_] "s"(out), [bid_] "s"(bid), [wid_] "s"(wid), [tag] "s"(tag)
    : "v0", "v1", "v2", "v3", "v4", "v5", "v6", "v7", "v8", "v9", "v10", "v11", "v12", "v13", "v14", "v15", "v16", "v17", "v18", "v19", "v20", "v21", "v22", "v23", "v24", "v25", "v26", "v27", "v28", "v29", "v30", "v31", "v32", "v33", "v34", "v35", "v36", "v37", "v38", "v39", "v40", "v41", "v42", "v43", "v44", "v45", "v46", "v47", "v48", "v49", "v50", "v51", "v52", "v53", "v54", "v55", "v56", "v57", "v58", "v59", "v60", "v61", "v62", "v63", "v64", "v65", "v66", "v67", "v68", "v69", "v70", "v71", "v72", "v73", "v74", "v75", "v76", "v77", "v78", "v79", "v80", "v81", "v82", "v83", "v84", "v85", "v86", "v87", "v88", "v89", "v90", "v91", "v92", "v93", "v94", "v95", "v96", "v97", "v98", "v99", "v100", "v101", "v102", "v103", "v104", "v105", "v106", "v107", "v108", "v109", "v110", "v111", "v112", "v113", "v114", "v115", "v116", "v117", "v118", "v119", "v120", "v121", "v122", "v123", "v124", "v125", "v126", "v127", "v128", "v129", "v130", "v131", "v132", "v133", "v134", "v135", "v136", "v137", "v138", "v139", "v140", "v141", "v142", "v143", "v144", "v145", "v146", "v147", "v148", "v149", "v150", "v151", "v152", "v153", "v154", "v155", "v156", "v157", "v158", "v159", "v160", "v161", "v162", "v163", "v164", "v165", "v166", "v167", "v168", "v169", "v170", "v171", "v172", "v173", "v174", "v175", "v176", "v177", "v178", "v179", "v180", "v181", "v182", "v183", "v184", "v185", "v186", "v187", "v188", "v189", "v190", "v191", "v192", "v193", "v194", "v195", "v196", "v197", "v198", "v199", "v200", "v201", "v202", "v203", "v204", "v205", "v206", "v207", "v208", "v209", "v210", "v211", "v212", "v213", "v214", "v215", "v216", "v217", "v218", "v219", "v220", "v221", "v222", "v223", "v224", "v225", "v226", "v227", "v228", "v229", "v230", "v231", "v232", "v233", "v234", "v235", "v236", "v237", "v238", "v239", "v240", "v241", "v242", "v243", "v244", "v245", "v246", "v247", "v248", "v249", "v250", "v251", "a0", "a1", "a2", "a3", "a4", "a5", "a6", "a7", "a8", "a9", "a10", "a11", "a12", "a13", "a14", "a15", "a16", "a17", "a18", "a19", "a20", "a21", "a22", "a23", "a24", "a25", "a26", "a27", "a28", "a29", "a30", "a31", "a32", "a33", "a34", "a35", "a36", "a37", "a38", "a39", "a40", "a41", "a42", "a43", "a44", "a45", "a46", "a47", "a48", "a49", "a50", "a51", "a52", "a53", "a54", "a55", "a56", "a57", "a58", "a59", "a60", "a61", "a62", "a63", "a64", "a65", "a66", "a67", "a68", "a69", "a70", "a71", "a72", "a73", "a74", "a75", "a76", "a77", "a78", "a79", "a80", "a81", "a82", "a83", "a84", "a85", "a86", "a87", "a88", "a89", "a90", "a91", "a92", "a93", "a94", "a95", "a96", "a97", "a98", "a99", "a100", "a101", "a102", "a103", "a104", "a105", "a106", "a107", "a108", "a109", "a110", "a111", "a112", "a113", "a114", "a115", "a116", "a117", "a118", "a119", "a120", "a121", "a122", "a123", "a124", "a125", "a126", "a127", "a128", "a129", "a130", "a131", "a132", "a133", "a134", "a135", "a136", "a137", "a138", "a139", "a140", "a141", "a142", "a143", "a144", "a145", "a146", "a147", "a148", "a149", "a150", "a151", "a152", "a153", "a154", "a155", "a156", "a157", "a158", "a159", "a160", "a161", "a162", "a163", "a164", "a165", "a166", "a167", "a168", "a169", "a170", "a171", "a172", "a173", "a174", "a175", "a176", "a177", "a178", "a179", "a180", "a181", "a182", "a183", "a184", "a185", "a186", "a187", "a188", "a189", "a190", "a191", "a192", "a193", "a194", "a195", "a196", "a197", "a198", "a199", "a200", "a201", "a202", "a203", "a204", "a205", "a206", "a207", "a208", "a209", "a210", "a211", "a212", "a213", "a214", "a215", "a216", "a217", "a218", "a219", "a220", "a221", "a222", "a223", "a224", "a225", "a226", "a227", "a228", "a229", "a230", "a231", "a232", "a233", "a234", "a235", "a236", "a237", "a238", "a239", "a240", "a241", "a242", "a243", "a244", "a245", "a246", "a247", "a248", "a249", "a250", "a251", "a252", "a253", "a254", "a255", "s4", "s5", "s6", "s7", "s8", "s9", "s10", "s11", "s12", "s13", "s14", "s15", "s16", "s17", "s18", "s19", "s20", "s21", "s22", "s23", "s24", "s25", "s26", "s27", "s28", "s29", "s30", "s31", "s34", "s35", "s36", "s37", "s38", "s39", "s40", "s41", "s42", "s43", "s44", "s45", "s46", "s47", "s48", "s49", "s50", "s51", "s52", "s53", "s54", "s55", "s56", "s57", "s58", "s59", "s60", "s61", "s62", "s63", "s64", "s65", "s66", "s67", "s68", "s69", "vcc", "scc", "memory", "s98", "s99");
}

extern "C" void kernel_launch(void* const* d_in, const int* in_sizes, int n_in, void* d_out, int out_size, void* d_ws, size_t ws_size,
                              hipStream_t stream) {
  gated_mix_kernel<<<256, 256, 0, stream>>>((const u16*)d_in[0], (const u16*)d_in[1], (const u16*)d_in[2], (const u16*)d_in[4], (const u16*)d_in[5],
                                            (const u16*)d_in[6], (const u16*)d_in[7], (const u16*)d_in[8], (const u16*)d_in[9], (char*)d_ws, (u16*)d_out);
}
```
